```python
import torch

def _softmax(x, axis=-1):
    return torch.softmax(x, dim=axis)
def _gelu(x):
    return torch.nn.functional.gelu(x, approximate="tanh")
def _silu(x):
    return torch.nn.functional.silu(x)
def _sigmoid(x):
    return torch.sigmoid(x)
def _layernorm(x, axis=-1, eps=1e-5):
    m = x.mean(dim=axis, keepdim=True)
    v = x.var(dim=axis, keepdim=True, unbiased=False)
    return (x - m) / torch.sqrt(v + eps)
def _scatter_add(tgt, idx, src, axis=0):
    return tgt.clone().index_add_(axis, idx, src)
def _segment_sum(src, seg, n):
    out = torch.zeros((n,) + src.shape[1:], dtype=src.dtype)
    return out.index_add_(0, seg, src)
def _pad(x, pw):
    flat = tuple(v for pair in reversed(pw) for v in pair)
    return torch.nn.functional.pad(x, flat)

kernel_name = 'parallel_sum__ssm_conv_s5__swiglu_ffn__prompt'

def setup_inputs():

    _w = (lambda a: (lambda t: t.to(torch.bfloat16) if t.is_floating_point() else t)(torch.as_tensor(a)))
    def _A_setup():
        B, S, D = 1, 2048, 576
        P = D
        Lambda_re = -0.5 * (lambda _x: torch.exp(torch.as_tensor(_x)))((lambda *s: torch.randn(*s, dtype=torch.bfloat16) * 0.8)(P))
        Lambda_im = (lambda *s: torch.randn(*s, dtype=torch.bfloat16) * 0.8)(P) * 1.0
        B_re = (lambda *s: torch.randn(*s, dtype=torch.bfloat16) * 0.8)(P, D) * (D ** -0.5)
        B_im = (lambda *s: torch.randn(*s, dtype=torch.bfloat16) * 0.8)(P, D) * (D ** -0.5)
        C_re = (lambda *s: torch.randn(*s, dtype=torch.bfloat16) * 0.8)(D, P) * (P ** -0.5)
        C_im = (lambda *s: torch.randn(*s, dtype=torch.bfloat16) * 0.8)(D, P) * (P ** -0.5)
        log_dt = (lambda *s: torch.randn(*s, dtype=torch.bfloat16) * 0.8)(P) * 0.3 - 2.0
        Dskip = (lambda *s: torch.randn(*s, dtype=torch.bfloat16) * 0.8)(D)
        return {
            "x": _w((lambda *s: torch.randn(*s, dtype=torch.bfloat16) * 0.05)(B, S, D)),
            "Lambda_re": _w(Lambda_re),
            "Lambda_im": _w(Lambda_im),
            "B_re": _w(B_re),
            "B_im": _w(B_im),
            "C_re": _w(C_re),
            "C_im": _w(C_im),
            "log_dt": _w(log_dt),
            "Dskip": _w(Dskip),
        }
    _A_in = _A_setup()

    def _B_setup():
        B, S, D, F = 1, 2048, 576, 4608
        return {
            "x": _w((lambda *s: torch.randn(*s, dtype=torch.bfloat16) * 0.05)(B, S, D)),
            "w_gate": _w((lambda *s: torch.randn(*s, dtype=torch.bfloat16) * 0.05)(D, F)),
            "w_up": _w((lambda *s: torch.randn(*s, dtype=torch.bfloat16) * 0.05)(D, F)),
            "w_down": _w((lambda *s: torch.randn(*s, dtype=torch.bfloat16) * 0.05)(F, D)),
        }
    _B_in = _B_setup()

    return {
        "A_x": _A_in["x"],
        "A_Lambda_re": _A_in["Lambda_re"],
        "A_Lambda_im": _A_in["Lambda_im"],
        "A_B_re": _A_in["B_re"],
        "A_B_im": _A_in["B_im"],
        "A_C_re": _A_in["C_re"],
        "A_C_im": _A_in["C_im"],
        "A_log_dt": _A_in["log_dt"],
        "A_Dskip": _A_in["Dskip"],
        "B_x": _B_in["x"],
        "B_w_gate": _B_in["w_gate"],
        "B_w_up": _B_in["w_up"],
        "B_w_down": _B_in["w_down"],
    }

def reference(A_x, A_Lambda_re, A_Lambda_im, A_B_re, A_B_im, A_C_re, A_C_im, A_log_dt, A_Dskip, B_x, B_w_gate, B_w_up, B_w_down):
    def _A_ref(x, Lambda_re, Lambda_im, B_re, B_im, C_re, C_im, log_dt, Dskip):
        Bsz, L, H = x.shape
        Lam = Lambda_re + 1j * Lambda_im
        Bmat = B_re + 1j * B_im
        Cmat = C_re + 1j * C_im
        dt = torch.exp(log_dt)
        Lam_bar = torch.exp(dt * Lam)
        B_bar = ((Lam_bar - 1.0) / Lam)[:, None] * Bmat
        Bu = torch.einsum("bld,pd->blp", x + 0j, B_bar)
        h = torch.zeros_like(Bu[:, 0, :])
        hs = []
        for t in range(L):
            h = Lam_bar[None, :] * h + Bu[:, t, :]
            hs.append(h)
        hseq = torch.stack(hs, 1)
        y = 2.0 * torch.einsum("blp,dp->bld", hseq, Cmat).real + x * Dskip[None, None, :]
        return y

    def _B_ref(x, w_gate, w_up, w_down):
        g = _silu(x @ w_gate)
        u = x @ w_up
        return (g * u) @ w_down

    x = A_x
    xn = _layernorm(x, axis=-1)
    return x + _A_ref(xn, A_Lambda_re, A_Lambda_im, A_B_re, A_B_im, A_C_re, A_C_im, A_log_dt, A_Dskip) + _B_ref(xn, B_w_gate, B_w_up, B_w_down)
```

```cpp
#include <hip/hip_runtime.h>
#include <hip/hip_bf16.h>
#include <stdint.h>
#define MIMIC 1
typedef unsigned short u16;
constexpr int S=2048, D=576, F=4608, P=576;
__device__ __forceinline__ float bf2f(u16 v){ return __uint_as_float(((unsigned)v)<<16); }
__device__ __forceinline__ u16 f2bf(float f){ unsigned u=__float_as_uint(f); u += 0x7fffu + ((u>>16)&1u); return (u16)(u>>16); }
__device__ __forceinline__ float rbf(float f){ return bf2f(f2bf(f)); }

__global__ void k_ln(const u16* x, u16* xn, float* xnf){
  int row=blockIdx.x; int t=threadIdx.x;
  float v[9]; float s=0;
  for(int i=0;i<9;i++){ v[i]=bf2f(x[row*D+t+64*i]); s+=v[i]; }
  for(int o=32;o>0;o>>=1) s+=__shfl_xor(s,o);
  float mean=s/D; float q=0;
  for(int i=0;i<9;i++){ float d=v[i]-mean; q+=d*d; }
  for(int o=32;o>0;o>>=1) q+=__shfl_xor(q,o);
  float var=q/D;
  float m=rbf(mean), vv=rbf(var);
  float den=rbf(sqrtf(rbf(vv+1e-5f)));
  for(int i=0;i<9;i++){ float r=rbf(rbf(v[i]-m)/den); xn[row*D+t+64*i]=f2bf(r); xnf[row*D+t+64*i]=r; }
}
__global__ void k_ffn1(const float* xnf, const u16* wg, const u16* wu, float* H){
  int f=blockIdx.x*64+threadIdx.x; int s0=blockIdx.y*8;
  float g[8]={0},u[8]={0};
  for(int k=0;k<D;k++){ float a=bf2f(wg[(size_t)k*F+f]), b=bf2f(wu[(size_t)k*F+f]);
    for(int i=0;i<8;i++){ float xv=xnf[(s0+i)*D+k]; g[i]+=xv*a; u[i]+=xv*b; } }
  for(int i=0;i<8;i++){
    float gg=rbf(g[i]); float sg=rbf(gg/(1.0f+expf(-gg))); float uu=rbf(u[i]); H[(size_t)(s0+i)*F+f]=rbf(sg*uu);
  }
}
__global__ void k_bu(const float* xnf, const u16* bre, const u16* bim, float* bur, float* bui){
  int p=blockIdx.x*64+threadIdx.x; int s0=blockIdx.y*8;
  float g[8]={0},u[8]={0};
  for(int k=0;k<D;k++){ float a=bf2f(bre[(size_t)p*D+k]), b=bf2f(bim[(size_t)p*D+k]);
    for(int i=0;i<8;i++){ float xv=xnf[(s0+i)*D+k]; g[i]+=xv*a; u[i]+=xv*b; } }
  for(int i=0;i<8;i++){ bur[(s0+i)*P+p]=g[i]; bui[(s0+i)*P+p]=u[i]; }
}
__global__ void k_scan(const u16* lre, const u16* lim, const u16* logdt, float* bur, float* bui){
  int p=blockIdx.x*64+threadIdx.x;
  float lr=bf2f(lre[p]), li=bf2f(lim[p]);
  float dt=expf(bf2f(logdt[p]));
  dt=rbf(dt);
  float er=expf(dt*lr); float ar=er*cosf(dt*li), ai=er*sinf(dt*li);
  float nr=ar-1.0f, ni=ai; float den=lr*lr+li*li;
  float cr=(nr*lr+ni*li)/den, ci=(ni*lr-nr*li)/den;
  float hr=0,hi=0;
  for(int t=0;t<S;t++){ float xr=bur[t*P+p], xi=bui[t*P+p];
    float br=cr*xr-ci*xi, bi=cr*xi+ci*xr;
    float nhr=ar*hr-ai*hi+br, nhi=ar*hi+ai*hr+bi; hr=nhr; hi=nhi; bur[t*P+p]=hr; bui[t*P+p]=hi; }
}
__global__ void k_out(const u16* x, const float* xnf, const float* H, const u16* wd, const float* hr, const float* hi, const u16* cre, const u16* cim, const u16* dsk, float* out){
  int d=blockIdx.x*64+threadIdx.x; int s0=blockIdx.y*8;
  float a[8]={0}, y[8]={0};
  for(int k=0;k<F;k++){ float w=bf2f(wd[(size_t)k*D+d]); for(int i=0;i<8;i++) a[i]+=H[(size_t)(s0+i)*F+k]*w; }
  for(int p=0;p<P;p++){ float c=bf2f(cre[d*P+p]), e=bf2f(cim[d*P+p]); for(int i=0;i<8;i++) y[i]+=hr[(s0+i)*P+p]*c-hi[(s0+i)*P+p]*e; }
  for(int i=0;i<8;i++){
    float xv=bf2f(x[(s0+i)*D+d]); float xn=xnf[(s0+i)*D+d];
    float sk=rbf(xn*bf2f(dsk[d])); float ff=rbf(a[i]);
    out[(s0+i)*D+d]=xv+(2.0f*y[i]+sk)+ff;
  }
}
extern "C" void kernel_launch(void* const* d_in, const int* in_sizes, int n_in, void* d_out, int out_size, void* d_ws, size_t ws_size, hipStream_t stream){
  const u16* x=(const u16*)d_in[0]; const u16* lre=(const u16*)d_in[1]; const u16* lim=(const u16*)d_in[2];
  const u16* bre=(const u16*)d_in[3]; const u16* bim=(const u16*)d_in[4]; const u16* cre=(const u16*)d_in[5]; const u16* cim=(const u16*)d_in[6];
  const u16* logdt=(const u16*)d_in[7]; const u16* dsk=(const u16*)d_in[8];
  const u16* wg=(const u16*)d_in[10]; const u16* wu=(const u16*)d_in[11]; const u16* wd=(const u16*)d_in[12];
  char* ws=(char*)d_ws;
  u16* xn=(u16*)ws; ws+=S*D*2;
  float* xnf=(float*)ws; ws+=S*D*4;
  float* H=(float*)ws; ws+=(size_t)S*F*4;
  float* bur=(float*)ws; ws+=S*P*4;
  float* bui=(float*)ws; ws+=S*P*4;
  k_ln<<<S,64,0,stream>>>(x,xn,xnf);
  k_ffn1<<<dim3(F/64,S/8),64,0,stream>>>(xnf,wg,wu,H);
  k_bu<<<dim3(P/64,S/8),64,0,stream>>>(xnf,bre,bim,bur,bui);
  k_scan<<<P/64,64,0,stream>>>(lre,lim,logdt,bur,bui);
  k_out<<<dim3(D/64,S/8),64,0,stream>>>(x,xnf,H,wd,bur,bui,cre,cim,dsk,(float*)d_out);
}
```

```cpp
#include <hip/hip_runtime.h>
#include <stdint.h>

typedef unsigned short u16;
typedef unsigned long long u64;
typedef __bf16 bf16x8 __attribute__((ext_vector_type(8)));
typedef __bf16 bf16x2 __attribute__((ext_vector_type(2)));
typedef float f32x4 __attribute__((ext_vector_type(4)));
typedef float f32x2 __attribute__((ext_vector_type(2)));
typedef short s16x4 __attribute__((ext_vector_type(4)));
typedef unsigned u32x4 __attribute__((ext_vector_type(4)));
typedef unsigned u32x2 __attribute__((ext_vector_type(2)));
#define LAS __attribute__((address_space(3)))
#define GAS __attribute__((address_space(1)))
#define DI __device__ __forceinline__

constexpr int SEQ = 2048, DM = 576, FF = 4608;
constexpr int NKG = 180;
constexpr size_t WS_FLAGS = 0;
constexpr size_t FL_H = 0;
constexpr size_t FL_HS = 4096;
constexpr size_t FL_P = 8192;
constexpr size_t FL_E = 16384;
constexpr size_t FLAGS_BYTES = FL_E + 8 * 16 * 80 * 8;
constexpr size_t WS_CONST = 131072;
constexpr size_t WS_STATS = WS_CONST + 16384;
constexpr size_t WS_XT = 262144;
constexpr size_t WS_HT = WS_XT + (size_t)128 * 18 * 1024;
constexpr size_t WS_WT = WS_HT + (size_t)128 * NKG * 1024;
constexpr size_t WS_PART = WS_WT + (size_t)4 * NKG * 9 * 1024;
constexpr size_t WS_END = WS_PART + (size_t)64 * 4 * 8 * 9 * 1024;
#define STAMP(k)

DI float bf2f(u16 v) { return __uint_as_float(((unsigned)v) << 16); }
DI float rbf(float f) { return (float)(__bf16)f; }
DI unsigned pk2(float lo, float hi) { f32x2 v = {lo, hi}; bf16x2 c = __builtin_convertvector(v, bf16x2); return __builtin_bit_cast(unsigned, c); }

__host__ __device__ inline int kg_f0(int kg) {
  int s = kg / 36, j = kg % 36;
  if (j < 32) { int p = j >> 4, cg = 8 * s + ((j & 15) >> 1); return 64 * (2 * cg + p) + 32 * (j & 1); }
  return 4096 + 32 * (4 * s + (j - 32));
}

__global__ __launch_bounds__(256) void k0_prep(const u16* __restrict__ x, const u16* __restrict__ lre, const u16* __restrict__ lim,
                                               const u16* __restrict__ cre, const u16* __restrict__ cim, const u16* __restrict__ logdt,
                                               const u16* __restrict__ wd, char* __restrict__ ws) {
  __shared__ __attribute__((aligned(16))) char sm[32 * 1152 + 512];
  const int tid = threadIdx.x, b = blockIdx.x;
  if (b < 256) {
    if (tid < 24) *(u32x4*)(ws + WS_FLAGS + (size_t)b * 384 + tid * 16) = (u32x4){0, 0, 0, 0};
    const int r = tid >> 5, c = tid & 31, row = b * 8 + r;
    const unsigned* xr = (const unsigned*)(x + (size_t)row * DM);
    float v[18]; float s = 0.f;
#pragma unroll
    for (int i = 0; i < 9; i++) { unsigned u = xr[c + 32 * i]; v[2 * i] = __uint_as_float(u << 16); v[2 * i + 1] = __uint_as_float(u & 0xffff0000u); s += v[2 * i] + v[2 * i + 1]; }
#pragma unroll
    for (int o = 16; o > 0; o >>= 1) s += __shfl_xor(s, o);
    const float mean = s * (1.0f / DM); float q = 0.f;
#pragma unroll
    for (int i = 0; i < 18; i++) { float d = v[i] - mean; q += d * d; }
#pragma unroll
    for (int o = 16; o > 0; o >>= 1) q += __shfl_xor(q, o);
    const float m = rbf(mean), den = rbf(sqrtf(rbf(rbf(q * (1.0f / DM)) + 1e-5f)));
    if (c == 0) *(f32x2*)(ws + WS_STATS + (size_t)row * 8) = (f32x2){m, den};
    unsigned* smr = (unsigned*)(sm + r * 1168);
#pragma unroll
    for (int i = 0; i < 9; i++) smr[c + 32 * i] = pk2(rbf(v[2 * i] - m) / den, rbf(v[2 * i + 1] - m) / den);
    __syncthreads();
    const int tb = b >> 1, i0 = (b & 1) * 8;
    for (int p = tid; p < 576; p += 256) {
      const int i = p & 7, ch = p >> 3;
      u32x4 val = *(const u32x4*)(sm + i * 1168 + ch * 16);
      *(u32x4*)(ws + WS_XT + ((size_t)tb * 18 + (ch >> 2)) * 1024 + ((ch & 3) * 16 + i0 + i) * 16) = val;
    }
  } else if (b < 400) {
    const int kg = b - 256, f0 = kg_f0(kg);
    const u32x4* src = (const u32x4*)(wd + (size_t)f0 * DM);
    for (int p = tid; p < 2304; p += 256) *(u32x4*)(sm + p * 16) = src[p];
    __syncthreads();
    const int w = tid >> 6, lane = tid & 63, q = lane >> 4, r4 = (lane & 15) >> 2, c = lane & 3;
    for (int d16 = w; d16 < 36; d16 += 4) {
      const int a0 = (4 * q + r4) * 1152 + d16 * 32 + c * 8;
      s16x4 lo = __builtin_amdgcn_ds_read_tr16_b64_v4i16((LAS s16x4*)(sm + a0));
      s16x4 hi = __builtin_amdgcn_ds_read_tr16_b64_v4i16((LAS s16x4*)(sm + a0 + 16 * 1152));
      bf16x8 fr = __builtin_bit_cast(bf16x8, __builtin_shufflevector(lo, hi, 0, 1, 2, 3, 4, 5, 6, 7));
      const int dt = d16 / 9, db = d16 % 9;
      *(bf16x8*)(ws + WS_WT + (((size_t)dt * NKG + kg) * 9 + db) * 1024 + lane * 16) = fr;
    }
  } else if (b < 436) {
    const int d16 = b - 400, dt = d16 / 9, db = d16 % 9;
    for (int p = tid; p < 36 * 64; p += 256) {
      const int g = p >> 6, lane = p & 63, i = lane & 15, q = lane >> 4;
      const u16* src = ((q & 1) ? cim : cre) + (size_t)(16 * d16 + i) * DM + 8 * (2 * g + (q >> 1));
      *(u32x4*)(ws + WS_WT + (((size_t)dt * NKG + 144 + g) * 9 + db) * 1024 + lane * 16) = *(const u32x4*)src;
    }
  } else {
    for (int p = tid; p < DM; p += 256) {
      const float lr = bf2f(lre[p]), li = bf2f(lim[p]);
      const float dt = rbf(expf(bf2f(logdt[p])));
      const float er = expf(dt * lr), ar = er * cosf(dt * li), ai = er * sinf(dt * li);
      const float nr = ar - 1.0f, ni = ai, dn = lr * lr + li * li;
      *(f32x4*)(ws + WS_CONST + p * 16) = (f32x4){ar, ai, (nr * lr + ni * li) / dn, (ni * lr - nr * li) / dn};
    }
  }
}

DI void glds_s(const void* sbase, unsigned voff, unsigned ldsaddr) {
  unsigned keep;
  asm volatile("s_mov_b32 %0, m0\n\ts_mov_b32 m0, %3\n\ts_nop 0\n\tglobal_load_lds_dwordx4 %1, %2\n\ts_mov_b32 m0, %0"
               : "=&s"(keep) : "v"(voff), "s"(sbase), "s"(ldsaddr) : "memory");
}
DI void glds_v(const void* vaddr, unsigned ldsaddr) {
  unsigned keep;
  asm volatile("s_mov_b32 %0, m0\n\ts_mov_b32 m0, %2\n\ts_nop 0\n\tglobal_load_lds_dwordx4 %1, off\n\ts_mov_b32 m0, %0"
               : "=&s"(keep) : "v"(vaddr), "s"(ldsaddr) : "memory");
}
template <int OFF> DI void gld16(bf16x8& dst, const void* sbase, unsigned voff) {
  asm volatile("global_load_dwordx4 %0, %1, %2 offset:%3" : "=v"(dst) : "v"(voff), "s"(sbase), "i"(OFF) : "memory");
}
template <int OFF> DI void gld16_sc1(bf16x8& dst, const void* sbase, unsigned voff) {
  asm volatile("global_load_dwordx4 %0, %1, %2 offset:%3 sc1" : "=v"(dst) : "v"(voff), "s"(sbase), "i"(OFF) : "memory");
}
DI void gld4_sc1(unsigned& dst, const void* sbase, unsigned voff) {
  asm volatile("global_load_dword %0, %1, %2 sc1" : "=v"(dst) : "v"(voff), "s"(sbase) : "memory");
}
DI void gld8_sc1(u32x2& dst, const void* sbase, unsigned voff) {
  asm volatile("global_load_dwordx2 %0, %1, %2 sc1" : "=v"(dst) : "v"(voff), "s"(sbase) : "memory");
}
#define WAITV(n) asm volatile("s_waitcnt vmcnt(%0)" ::"i"(n) : "memory")
#define SCHED0 __builtin_amdgcn_sched_barrier(0)
DI void bar() { asm volatile("" ::: "memory"); __builtin_amdgcn_s_barrier(); asm volatile("" ::: "memory"); }
DI void tie(bf16x8& a) { asm volatile("" : "+v"(a)); }
DI void tie(unsigned& a) { asm volatile("" : "+v"(a)); }
DI void tie(float& a) { asm volatile("" : "+v"(a)); }
DI void tie(u32x2& a) { asm volatile("" : "+v"(a)); }
DI bf16x8 trfrag(unsigned addr) {
  s16x4 a = __builtin_amdgcn_ds_read_tr16_b64_v4i16((LAS s16x4*)(addr));
  s16x4 b = __builtin_amdgcn_ds_read_tr16_b64_v4i16((LAS s16x4*)(addr + 512));
  return __builtin_bit_cast(bf16x8, __builtin_shufflevector(a, b, 0, 1, 2, 3, 4, 5, 6, 7));
}
template <int O2> DI bf16x8 trfrag2(unsigned addr) {
  s16x4 a = __builtin_amdgcn_ds_read_tr16_b64_v4i16((LAS s16x4*)(addr));
  s16x4 b = __builtin_amdgcn_ds_read_tr16_b64_v4i16((LAS s16x4*)(addr + O2));
  return __builtin_bit_cast(bf16x8, __builtin_shufflevector(a, b, 0, 1, 2, 3, 4, 5, 6, 7));
}
DI bf16x8 ldsfrag(unsigned addr) { return *(LAS bf16x8*)(addr); }
#define MFMA(a, b, c) __builtin_amdgcn_mfma_f32_16x16x32_bf16((a), (b), (c), 0, 0, 0)
DI float silu_mul(float g, float u) {
  const float e = __builtin_amdgcn_exp2f(g * -1.44269504089f);
  return g * u * __builtin_amdgcn_rcpf(1.0f + e);
}
DI float dpp_ror8(float v) { return __int_as_float(__builtin_amdgcn_update_dpp(0, __float_as_int(v), 0x128, 0xf, 0xf, true)); }
typedef __amdgpu_buffer_rsrc_t rsrc_t;
DI rsrc_t mkrsrc(const void* p, unsigned bytes) { return __builtin_amdgcn_make_buffer_rsrc((void*)p, 0, bytes, 0x00020000); }
DI void st16_sc1(rsrc_t r, unsigned voff, unsigned soff, u32x4 v) { unsigned o = voff + soff; asm volatile("" : "+v"(o)); __builtin_amdgcn_raw_buffer_store_b128(v, r, o, 0, 16); }
DI void setflag(rsrc_t r, bool sel, unsigned off) { __builtin_amdgcn_raw_buffer_store_b32(1u, r, sel ? 0u : 0x7ffffff0u, off, 16); }
DI void spin_flag(const char* base, unsigned voff) {
  for (unsigned it = 0; it < (1u << 18); ++it) {
    unsigned v = __hip_atomic_load((GAS unsigned*)(base + voff), __ATOMIC_RELAXED, __HIP_MEMORY_SCOPE_AGENT);
    if (__all(v == 1u)) break;
    __builtin_amdgcn_s_sleep(2);
  }
  asm volatile("" ::: "memory");
}

#define PD 2
constexpr int FR = PD + 1;
constexpr int SLOT1 = 24576;
constexpr int SLOT2 = 18432;
constexpr int LDS_TOT = 163840;
constexpr int LDS_SCR = 147456;
constexpr int LDS_WTOT = LDS_SCR + 8192;
constexpr int LDS_WCAR = LDS_WTOT + 2560;

struct P1Ctx {
  const char* wsrc;
  unsigned wvoff;
  unsigned lds0;
  unsigned ab[4];
  int w;
};

template <int V> DI constexpr int p1_slot_off() { return (V % 5) * SLOT1; }
template <int U> DI constexpr int p1_b_off() { return U == 0 ? 122880 : (U == 1 ? 49152 : 79872); }

template <int V> DI void issue_L(const P1Ctx& c) {
#pragma unroll
  for (int n = 0; n < 3; n++)
    glds_s(c.wsrc + (V / 6) * 128 + (size_t)(3 * (V % 6) + n) * 32 * (FF * 2), c.wvoff, c.lds0 + p1_slot_off<V>() + n * 8192 + c.w * 1024);
}

__global__ __launch_bounds__(512, 2) void k_main(const u16* __restrict__ x, const u16* __restrict__ bre, const u16* __restrict__ bim,
                                                 const u16* __restrict__ dsk, const u16* __restrict__ wg, const u16* __restrict__ wu,
                                                 char* __restrict__ ws, float* __restrict__ out) {
  extern __shared__ __attribute__((aligned(16))) char lds[];
  const int tid = threadIdx.x, lane = tid & 63, w = __builtin_amdgcn_readfirstlane(tid >> 6);
  const int b = blockIdx.x, xcd = b & 7, idx = b >> 3;
  const int q = lane >> 4, li = lane & 15;
  unsigned lds0 = (unsigned)(size_t)(LAS char*)lds;
  asm volatile("" : "+s"(lds0));
  const unsigned l16 = lane * 16;
  const int rt = idx & 7, cg = xcd * 4 + (idx >> 3);
  const bool isY = cg & 1;
  const int yy = cg >> 1;
  const int sb = 9 * (yy >> 1) + ((yy & 1) ? 4 : 0);
  const int s2 = xcd >> 1, tt = (xcd & 1) * 8 + (idx & 7), dt = idx >> 3;
  const int tile = tt * 4 + dt;

  const rsrc_t rsHT = mkrsrc(ws + WS_HT, 128 * NKG * 1024);
  const rsrc_t rsFL = mkrsrc(ws, (unsigned)FLAGS_BYTES);

  STAMP(0);
  P1Ctx c;
  c.w = w; c.lds0 = lds0;
  {
    const int rg = w & 3, r8 = lane >> 3, ch = lane & 7;
    const int g = ((lane >> 4) & 1) | ((rg & 1) << 1);
    c.wvoff = (unsigned)((8 * rg + r8) * (FF * 2) + (((ch >> 1) ^ g) << 5) + ((ch & 1) << 4));
    c.wsrc = (const char*)((w >> 2) ? wu : wg) + (size_t)cg * 256;
    const int r4 = li >> 2, cc = lane & 3;
    const int gr = ((r4 >> 1) & 1) | ((q & 1) << 1);
    const unsigned a0 = (unsigned)((8 * q + r4) * 128 + (gr << 5) + 8 * cc);
#pragma unroll
    for (int k = 0; k < 4; k++) { c.ab[k] = lds0 + (a0 ^ (k << 5)); tie(c.ab[k]); }
  }
  bf16x8 xf[18][2];
  {
    const char* xb = ws + WS_XT + (size_t)(rt * 16 + 2 * w) * 18 * 1024;
#define XLD(u) do { _Pragma("unroll") for (int t_ = 0; t_ < 2; t_++) { const char* p_ = xb + (t_ * 18 + 3 * (u)) * 1024; \
      gld16<0>(xf[3 * (u)][t_], p_, l16); gld16<1024>(xf[3 * (u) + 1][t_], p_, l16); gld16<2048>(xf[3 * (u) + 2][t_], p_, l16); } } while (0)
    issue_L<0>(c); XLD(0); issue_L<1>(c); XLD(1); issue_L<2>(c); XLD(2); issue_L<3>(c); XLD(3); XLD(4); XLD(5);
  }
#define XTIE(u) do { _Pragma("unroll") for (int k_ = 0; k_ < 3; k_++) { tie(xf[3 * (u) + k_][0]); tie(xf[3 * (u) + k_][1]); } } while (0)

  unsigned tvoff;
  const char* tsrc; const char* tsrc4; unsigned tstep, tustep, tdst, tdstep, tdst4;
  {
    const unsigned vx = (unsigned)((lane >> 2) * (FF * 2) + ((((lane >> 1) & 1) ^ (lane >> 5)) << 5) + ((lane & 1) << 4));
    const unsigned vy = (unsigned)((lane >> 3) * (DM * 2) + (((lane & 7) ^ (4 * (w & 1) + (lane >> 4))) << 4));
    tvoff = isY ? vy : vx;
    const char* sx = (const char*)(((w >> 1) & 1) ? wu : wg) + (size_t)(32 * (w >> 2) + 16 * (w & 1)) * (FF * 2) + (size_t)(4096 + 32 * yy) * 2;
    const char* my = (const char*)((w & 1) ? bim : bre);
    const char* sy = my + (size_t)(8 * (sb + (w >> 1))) * (DM * 2);
    const int w4 = w < 6 ? w : (w & 1);
    const char* sy4 = my + (size_t)(8 * (sb + 4)) * (DM * 2) + (w4 >> 1) * 128;
    tsrc = isY ? sy : sx; tsrc4 = isY ? sy4 : sx;
    tstep = isY ? 128u : 64u * FF * 2; tustep = isY ? 384u : 192u * FF * 2;
    tdst = lds0 + w * 1024; tdstep = isY ? 10240u : 8192u;
    tdst4 = isY ? lds0 + (w4 >> 1) * 10240 + (8 + (w & 1)) * 1024 : tdst;
  }
  auto issue_tail = [&](auto Uc) {
    constexpr int U = decltype(Uc)::value;
    const unsigned uo = isY ? (unsigned)p1_b_off<U>() : (unsigned)p1_slot_off<12 + U>();
#pragma unroll
    for (int n = 0; n < 3; n++) glds_s(tsrc + (size_t)(U * tustep + n * tstep), tvoff, tdst + uo + n * tdstep);
    glds_s(tsrc4 + (size_t)(U * tustep), tvoff, tdst4 + uo);
  };

  f32x4 acc[8][2];
  bf16x8 fr[FR];
  const f32x4 zero4 = {0.f, 0.f, 0.f, 0.f};
  WAITV(39); XTIE(0); bar(); SCHED0;
  STAMP(1);
#define ABSTEP(V) do { _Pragma("unroll") for (int k_ = 0; k_ < 4; k_++) { c.ab[k_] += (unsigned)(p1_slot_off<(V) + 1>() - p1_slot_off<V>()); tie(c.ab[k_]); } } while (0)
#define LADDR(V, g) (c.ab[(g) & 3] + ((g) >> 3) * 8192 + (((g) >> 2) & 1) * 4096)
#define HADDR(U, g) (hb[(U) == 2][(g) & 1] + ((U) == 1 ? SLOT1 : 0) + ((g) >> 2) * 4096 + (((g) >> 1) & 1) * 2048)
#define HFRAG(U, g) trfrag2<256>(HADDR(U, g))
#define BOFF(U, g) (p1_b_off<U>() + ((g) % 5) * 2048 + (((g) / 5) >> 1) * 10240)
#define BADDR(U, g) ((U) == 0 ? eb[1][((g) / 5) & 1] + (BOFF(U, g) - 110592) : eb[0][((g) / 5) & 1] + (BOFF(U, g) - 49152))
#pragma unroll
  for (int d = 0; d < PD; d++) fr[d] = trfrag(LADDR(0, d));

  auto epi_L = [&](int kgbase) {
#pragma unroll
    for (int p = 0; p < 2; p++)
#pragma unroll
      for (int t = 0; t < 2; t++) {
        float h[8];
#pragma unroll
        for (int j = 0; j < 8; j++) h[j] = silu_mul(acc[2 * p + (j >> 2)][t][j & 3], acc[4 + 2 * p + (j >> 2)][t][j & 3]);
        u32x4 o = {pk2(h[0], h[1]), pk2(h[2], h[3]), pk2(h[4], h[5]), pk2(h[6], h[7])};
        st16_sc1(rsHT, l16, (unsigned)(((rt * 16 + 2 * w + t) * NKG + kgbase + p) * 1024), o);
        SCHED0;
      }
  };
  const int kg_l0 = 36 * (cg >> 3) + 2 * (cg & 7);
  const unsigned flH = (unsigned)FL_H + (unsigned)(((rt * 4 + (cg >> 3)) * 3) * 8 + (cg & 7)) * 4;
  unsigned f0v = 0;
  const char* flbase = ws + FL_H + (size_t)(((tt >> 1) * 4 + s2) * 3) * 32;

  auto interval_L = [&](auto Vc) {
    constexpr int V = decltype(Vc)::value;
    constexpr int U = V % 6;
#pragma unroll
    for (int g = 0; g < 24; g++) {
      if (g == 8) {
        SCHED0;
        if constexpr (V == 0) { WAITV(30); XTIE(1); }
        else if constexpr (V == 1) { WAITV(24); XTIE(2); }
        else if constexpr (V == 2) { WAITV(18); XTIE(3); }
        else if constexpr (V == 3) { WAITV(6); XTIE(4); XTIE(5); }
        else if constexpr (V >= 6 && V <= 8) { WAITV(10); }
        else if constexpr (V == 9) { WAITV(7); }
        else if constexpr (V >= 10) { WAITV(8); }
        else { WAITV(6); }
        bar();
        if constexpr (V == 9) { setflag(rsFL, tid == 0, flH); }
        SCHED0;
      }
      if (g == 10) {
        SCHED0;
        if constexpr (V + 4 < 12) issue_L<V + 4>(c);
        else if constexpr (V + 4 < 15) issue_tail(std::integral_constant<int, V + 4 - 12>{});
        SCHED0;
      }
      if (g + PD == 24 && V < 11) ABSTEP(V);
      if (g + PD < 24) fr[(g + PD) % FR] = trfrag(LADDR(V, g + PD));
      else if constexpr (V < 11) fr[(g + PD) % FR] = trfrag(LADDR(V + 1, g + PD - 24));
      const int ks = 3 * U + (g >> 3), blk = g & 7;
      if (ks == 0) { acc[blk][0] = MFMA(fr[g % FR], xf[ks][0], zero4); acc[blk][1] = MFMA(fr[g % FR], xf[ks][1], zero4); }
      else { acc[blk][0] = MFMA(fr[g % FR], xf[ks][0], acc[blk][0]); acc[blk][1] = MFMA(fr[g % FR], xf[ks][1], acc[blk][1]); }
      if (g + PD < 24 || V < 11) __builtin_amdgcn_sched_group_barrier(0x100, 2, 0);
      __builtin_amdgcn_sched_group_barrier(0x8, 2, 0);
    }
    SCHED0;
    if constexpr (U == 5) { epi_L(kg_l0 + 16 * (V / 6)); SCHED0; }
  };
#define IC(n) std::integral_constant<int, n>{}
  interval_L(IC(0)); interval_L(IC(1)); interval_L(IC(2)); interval_L(IC(3)); interval_L(IC(4)); interval_L(IC(5));
  STAMP(2);
  interval_L(IC(6)); interval_L(IC(7)); interval_L(IC(8)); interval_L(IC(9)); interval_L(IC(10)); interval_L(IC(11));

  STAMP(3);
  auto tail_sync = [&](auto Uc) {
    constexpr int U = decltype(Uc)::value;
    SCHED0;
    if constexpr (U == 0) { WAITV(8); bar(); }
    else if constexpr (U == 1) { WAITV(0); bar(); setflag(rsFL, tid == 0, flH + 32); }
    else { gld4_sc1(f0v, flbase, (lane & 7) * 4); }
    SCHED0;
  };
  if (!isY) {
    unsigned hb[2][2];
    {
      const unsigned h0 = (unsigned)((8 * q + (li >> 2)) * 64 + ((q & 1) << 5) + 8 * (lane & 3));
      hb[0][0] = lds0 + 2 * SLOT1 + h0; hb[0][1] = lds0 + 2 * SLOT1 + (h0 ^ 32u); hb[1][0] = lds0 + 4 * SLOT1 + h0; hb[1][1] = lds0 + 4 * SLOT1 + (h0 ^ 32u);
      tie(hb[0][0]); tie(hb[0][1]); tie(hb[1][0]); tie(hb[1][1]);
    }
#pragma unroll
    for (int d = 0; d < PD; d++) fr[d] = HFRAG(0, d);
    auto interval_H = [&](auto Uc) {
      constexpr int U = decltype(Uc)::value;
#pragma unroll
      for (int g = 0; g < 24; g++) {
        if (g == 8) tail_sync(Uc);
        if (g + PD < 24) fr[(g + PD) % FR] = HFRAG(U, g + PD);
        else if constexpr (U < 2) fr[(g + PD) % FR] = HFRAG(U + 1, g + PD - 24);
        const int ks = 6 * U + (g >> 2), blk = g & 3;
        if (ks == 0) { acc[blk][0] = MFMA(fr[g % FR], xf[ks][0], zero4); acc[blk][1] = MFMA(fr[g % FR], xf[ks][1], zero4); }
        else { acc[blk][0] = MFMA(fr[g % FR], xf[ks][0], acc[blk][0]); acc[blk][1] = MFMA(fr[g % FR], xf[ks][1], acc[blk][1]); }
        if (g + PD < 24 || U < 2) __builtin_amdgcn_sched_group_barrier(0x100, 2, 0);
        __builtin_amdgcn_sched_group_barrier(0x8, 2, 0);
      }
      SCHED0;
    };
    interval_H(IC(0)); interval_H(IC(1)); interval_H(IC(2));
  } else {
    unsigned eb[2][2];
    {
      const unsigned e0 = li * 128 + ((q ^ (li >> 1)) << 4);
      eb[0][0] = lds0 + 49152 + e0; eb[0][1] = lds0 + 49152 + (e0 ^ 64u); eb[1][0] = lds0 + 110592 + e0; eb[1][1] = lds0 + 110592 + (e0 ^ 64u);
      tie(eb[0][0]); tie(eb[0][1]); tie(eb[1][0]); tie(eb[1][1]);
    }
#pragma unroll
    for (int d = 0; d < PD; d++) fr[d] = ldsfrag(BADDR(0, d));
    auto interval_B = [&](auto Uc) {
      constexpr int U = decltype(Uc)::value;
#pragma unroll
      for (int g = 0; g < 30; g++) {
        if (g == 10) tail_sync(Uc);
        const int G = 30 * U + g;
        if (g + PD < 30) fr[(G + PD) % FR] = ldsfrag(BADDR(U, g + PD));
        else if constexpr (U < 2) fr[(G + PD) % FR] = ldsfrag(BADDR(U + 1, g + PD - 30));
        const int ks = 6 * U + g / 5, blk = g % 5;
        if (ks == 0) { acc[blk][0] = MFMA(xf[ks][0], fr[G % FR], zero4); acc[blk][1] = MFMA(xf[ks][1], fr[G % FR], zero4); }
        else { acc[blk][0] = MFMA(xf[ks][0], fr[G % FR], acc[blk][0]); acc[blk][1] = MFMA(xf[ks][1], fr[G % FR], acc[blk][1]); }
        if (g + PD < 30 || U < 2) __builtin_amdgcn_sched_group_barrier(0x100, 1, 0);
        __builtin_amdgcn_sched_group_barrier(0x8, 2, 0);
      }
      SCHED0;
    };
    interval_B(IC(0)); interval_B(IC(1)); interval_B(IC(2));
  }

  STAMP(4);
  const int tg = w & 3, kh = w >> 2;
  const char* wtb = ws + WS_WT + ((size_t)dt * NKG + 36 * s2) * 9 * 1024 + w * 1024;
  const char* hb0 = ws + WS_HT + ((size_t)(tt * 8 + 2 * tg) * NKG + 36 * s2 + kh) * 1024;
  const char* hb1 = hb0 + (size_t)NKG * 1024;
  bf16x8 hf[7][2];
  auto issue_A = [&](auto Uc) {
    constexpr int U = decltype(Uc)::value;
    const char* src = wtb + (size_t)U * SLOT2;
    const unsigned dst = lds0 + (U & 7) * SLOT2 + w * 1024;
    glds_s(src, l16, dst);
    glds_s(src + 8192, l16, dst + 8192);
    if (w < 2) glds_s(src + 16384, l16, dst + 16384);
  };
  auto issue_B = [&](auto Uc) {
    constexpr int U = decltype(Uc)::value;
    gld16_sc1<0>(hf[U % 7][0], hb0 + (size_t)U * 2048, l16);
    gld16_sc1<0>(hf[U % 7][1], hb1 + (size_t)U * 2048, l16);
  };
  WAITV(0); tie(f0v);
  bar();
  if (!__all(f0v == 1u)) spin_flag(flbase, (lane & 7) * 4);
  SCHED0;
  issue_A(IC(0)); issue_B(IC(0)); issue_A(IC(1)); issue_B(IC(1)); issue_A(IC(2)); issue_B(IC(2));
  issue_A(IC(3)); issue_B(IC(3)); issue_A(IC(4)); issue_B(IC(4)); issue_A(IC(5)); issue_B(IC(5));
  SCHED0;

  float sv[5][2][4];
  float car[5], cai[5];
  if (!isY) {
#pragma unroll
    for (int t = 0; t < 2; t++) {
      float h[8];
#pragma unroll
      for (int j = 0; j < 8; j++) h[j] = silu_mul(acc[j >> 2][t][j & 3], acc[2 + (j >> 2)][t][j & 3]);
      u32x4 o = {pk2(h[0], h[1]), pk2(h[2], h[3]), pk2(h[4], h[5]), pk2(h[6], h[7])};
      st16_sc1(rsHT, l16, (unsigned)(((rt * 16 + 2 * w + t) * NKG + 36 * (cg >> 3) + 32 + ((cg & 7) >> 1)) * 1024), o);
    }
#pragma unroll
    for (int k = 0; k < 5; k++) { car[k] = 0.f; cai[k] = 0.f;
#pragma unroll
      for (int j = 0; j < 4; j++) { sv[k][0][j] = 0.f; sv[k][1][j] = 0.f; } }
  } else {
    const float sg = (li & 8) ? 1.0f : -1.0f;
#pragma unroll
    for (int k = 0; k < 5; k++) {
      const f32x4 cs = *(const f32x4*)(ws + WS_CONST + (size_t)(8 * (sb + k) + (li & 7)) * 16);
      const float ar = cs[0], ai = sg * cs[1], cr = cs[2], ci = sg * cs[3];
      car[k] = ar; cai[k] = ai;
      const float a2r = ar * ar - ai * ai, a2i = 2.f * ar * ai;
      const float a4r = a2r * a2r - a2i * a2i, a4i = 2.f * a2r * a2i;
      const float a8r = a4r * a4r - a4i * a4i, a8i = 2.f * a4r * a4i;
      const float a16r = a8r * a8r - a8i * a8i, a16i = 2.f * a8r * a8i;
      float inc[2], spre[2];
#pragma unroll
      for (int t = 0; t < 2; t++) {
        float prev = 0.f;
#pragma unroll
        for (int j = 0; j < 4; j++) {
          const float u = acc[k][t][j];
          float v = cr * u + ci * dpp_ror8(u);
          if (j > 0) v += ar * prev + ai * dpp_ror8(prev);
          sv[k][t][j] = v; prev = v;
        }
        float e = prev;
        float u1 = __shfl_up(e, 16); u1 = (q >= 1) ? u1 : 0.f;
        e += a4r * u1 + a4i * dpp_ror8(u1);
        float u2 = __shfl_up(e, 32); u2 = (q >= 2) ? u2 : 0.f;
        e += a8r * u2 + a8i * dpp_ror8(u2);
        inc[t] = e;
        float ex = __shfl_up(e, 16); spre[t] = (q >= 1) ? ex : 0.f;
      }
      const float t0 = __shfl(inc[0], 48 + li), t1 = __shfl(inc[1], 48 + li);
      float pr = 1.f, pi = 0.f;
      if (q & 1) { pr = a4r; pi = a4i; }
      if (q & 2) { const float nr = pr * a8r - pi * a8i, ni = pr * a8i + pi * a8r; pr = nr; pi = ni; }
      spre[1] += pr * t0 + pi * dpp_ror8(t0);
#pragma unroll
      for (int t = 0; t < 2; t++) {
        float cc = spre[t];
#pragma unroll
        for (int j = 0; j < 4; j++) { cc = ar * cc + ai * dpp_ror8(cc); sv[k][t][j] += cc; }
      }
      const float tot = t1 + a16r * t0 + a16i * dpp_ror8(t0);
      if (q == 0) *(LAS float*)(lds0 + LDS_WTOT + (w * 80 + k * 16 + li) * 4) = tot;
      tie(car[k]); tie(cai[k]);
      SCHED0;
    }
  }
  SCHED0;

  STAMP(5);
  f32x4 ac2[9][2];
  unsigned a2b[3];
#pragma unroll
  for (int k = 0; k < 3; k++) { a2b[k] = lds0 + kh * 9216 + l16 + k * 55296; tie(a2b[k]); }
#define A2ADDR(U, g) (a2b[((U) & 7) / 3] + (((U) & 7) % 3) * SLOT2 + (g) * 1024)
  unsigned f1v = 0;
  u32x2 eg[7];
  float ltot = 0.f;
  asm volatile("s_waitcnt vmcnt(20) lgkmcnt(0)" ::: "memory"); tie(hf[0][0]); tie(hf[0][1]);
  bar(); SCHED0;
  const int col = tid;
  float pa_r = 0.f, pa_i = 0.f;
  if (isY && w < 2) {
    const int cl = col < 80 ? col : 79;
    const f32x4 cs = *(const f32x4*)(ws + WS_CONST + (size_t)(8 * (sb + (cl >> 4)) + (cl & 7)) * 16);
    float r = cs[0], i = ((cl & 8) ? 1.0f : -1.0f) * cs[1];
#pragma unroll
    for (int k = 0; k < 5; k++) { const float nr = r * r - i * i, ni = 2.f * r * i; r = nr; i = ni; }
    pa_r = r; pa_i = i;
    float e = 0.f;
#pragma unroll
    for (int k = 0; k < 8; k++) { const float tw = *(LAS float*)(lds0 + LDS_WTOT + (k * 80 + cl) * 4); e = tw + pa_r * e + pa_i * dpp_ror8(e); }
    if (col < 80) __hip_atomic_store((GAS u64*)(ws + FL_E + (size_t)((rt * 16 + yy) * 80 + col) * 8), (1ull << 32) | __float_as_uint(e), __ATOMIC_RELAXED, __HIP_MEMORY_SCOPE_AGENT);
  }
  SCHED0;
  STAMP(6);
#pragma unroll
  for (int d = 0; d < PD; d++) fr[d] = ldsfrag(A2ADDR(0, d));

  auto interval_2 = [&](auto Uc) {
    constexpr int U = decltype(Uc)::value;
#pragma unroll
    for (int g = 0; g < 9; g++) {
      if (g == 3 && U < 17) {
        SCHED0;
        constexpr int later = (U + 5 < 17 ? U + 5 : 17) - (U + 1);
        if constexpr (U == 2 || U == 10) { WAITV(8); tie(f1v); }
        else { WAITV(4 * later); }
        tie(hf[(U + 1) % 7][0]); tie(hf[(U + 1) % 7][1]);
        if constexpr (U == 8) {
          if (isY && w < 2) {
            WAITV(8);
#pragma unroll
            for (int r = 0; r < 7; r++) tie(eg[r]);
            const int cl = col < 80 ? col : 79;
            const char* eb = ws + FL_E + (size_t)(yy * 80) * 8;
            bool ok = true;
#pragma unroll
            for (int r = 0; r < 7; r++) ok &= (r >= rt) || (eg[r][1] == 1u);
            if (!__all(ok)) {
              for (unsigned it = 0; it < (1u << 18); ++it) {
                ok = true;
#pragma unroll
                for (int r = 0; r < 7; r++) {
                  const u64 v = __hip_atomic_load((GAS u64*)(eb + (size_t)r * 10240 + cl * 8), __ATOMIC_RELAXED, __HIP_MEMORY_SCOPE_AGENT);
                  eg[r][0] = (unsigned)v; eg[r][1] = (unsigned)(v >> 32); ok &= (r >= rt) || (eg[r][1] == 1u);
                }
                if (__all(ok)) break;
                __builtin_amdgcn_s_sleep(2);
              }
            }
            float r256 = pa_r, i256 = pa_i;
#pragma unroll
            for (int k = 0; k < 3; k++) { const float nr = r256 * r256 - i256 * i256, ni = 2.f * r256 * i256; r256 = nr; i256 = ni; }
            float e = 0.f;
#pragma unroll
            for (int r = 0; r < 7; r++) if (r < rt) e = __uint_as_float(eg[r][0]) + r256 * e + i256 * dpp_ror8(e);
#pragma unroll
            for (int k = 0; k < 8; k++) {
              if (col < 80) *(LAS float*)(lds0 + LDS_WCAR + (k * 80 + col) * 4) = e;
              const float tw = *(LAS float*)(lds0 + LDS_WTOT + (k * 80 + cl) * 4);
              e = tw + pa_r * e + pa_i * dpp_ror8(e);
            }
            asm volatile("s_waitcnt lgkmcnt(0)" ::: "memory");
          }
        }
        bar();
        if constexpr (U == 2) { if (!__all(f1v == 1u)) spin_flag(flbase + 32, (lane & 7) * 4); }
        if constexpr (U == 10) { if (!__all(f1v == 1u)) spin_flag(flbase + 64, (lane & 3) * 8); }
        if constexpr (U == 0) gld4_sc1(f1v, flbase + 32, (lane & 7) * 4);
        if constexpr (U == 8) gld4_sc1(f1v, flbase + 64, (lane & 3) * 8);
        if constexpr (U == 6) {
          setflag(rsFL, tid == 0 && !isY, flH + 64);
          if (isY && w < 2) {
            const int cl = col < 80 ? col : 79;
#pragma unroll
            for (int r = 0; r < 7; r++) gld8_sc1(eg[r], ws + FL_E + (size_t)(r * 16 + yy) * 640, cl * 8);
          }
        }
        if constexpr (U == 15) { setflag(rsFL, isY && tid == 0, (unsigned)FL_HS + (unsigned)(rt * 16 + yy) * 4); }
        if constexpr (U + 6 < 18) { issue_A(IC(U + 6)); issue_B(IC(U + 6)); }
        SCHED0;
      }
      const int G = 9 * U + g;
      if (g + PD < 9) fr[(G + PD) % FR] = ldsfrag(A2ADDR(U, g + PD));
      else if constexpr (U < 17) fr[(G + PD) % FR] = ldsfrag(A2ADDR(U + 1, g + PD - 9));
      if constexpr (U == 0) { ac2[g][0] = MFMA(fr[G % FR], hf[0][0], zero4); ac2[g][1] = MFMA(fr[G % FR], hf[0][1], zero4); }
      else { ac2[g][0] = MFMA(fr[G % FR], hf[U % 7][0], ac2[g][0]); ac2[g][1] = MFMA(fr[G % FR], hf[U % 7][1], ac2[g][1]); }
      if (g + PD < 9 || U < 17) __builtin_amdgcn_sched_group_barrier(0x100, 1, 0);
      __builtin_amdgcn_sched_group_barrier(0x8, 2, 0);
    }
    SCHED0;
    if constexpr (U == 8) {
      if (isY) {
        const unsigned scr = lds0 + LDS_SCR + w * 1024;
        const float osc = (li & 8) ? -2.0f : 2.0f;
#pragma unroll
        for (int k = 0; k < 5; k++) {
          const float ar = car[k], ai = cai[k];
          const float a2r = ar * ar - ai * ai, a2i = 2.f * ar * ai;
          const float a4r = a2r * a2r - a2i * a2i, a4i = 2.f * a2r * a2i;
          const float a8r = a4r * a4r - a4i * a4i, a8i = 2.f * a4r * a4i;
          const float a16r = a8r * a8r - a8i * a8i, a16i = 2.f * a8r * a8i;
          float pr = 1.f, pi = 0.f;
          if (q & 1) { pr = a4r; pi = a4i; }
          if (q & 2) { const float nr = pr * a8r - pi * a8i, ni = pr * a8i + pi * a8r; pr = nr; pi = ni; }
          const float wc = *(LAS float*)(lds0 + LDS_WCAR + (w * 80 + k * 16 + li) * 4);
          float cin = pr * wc + pi * dpp_ror8(wc);
#pragma unroll
          for (int t = 0; t < 2; t++) {
            float cc = cin;
            float hv[4];
#pragma unroll
            for (int j = 0; j < 4; j++) { cc = ar * cc + ai * dpp_ror8(cc); hv[j] = (sv[k][t][j] + cc) * osc; }
            *(LAS u32x2*)(scr + t * 512 + li * 32 + q * 8) = (u32x2){pk2(hv[0], hv[1]), pk2(hv[2], hv[3])};
            if (t == 0) cin = a16r * cin + a16i * dpp_ror8(cin);
          }
          const unsigned ra = scr + (q >> 1) * 512 + (8 * (q & 1) + (li >> 2)) * 32 + (lane & 3) * 8;
          s16x4 lo = __builtin_amdgcn_ds_read_tr16_b64_v4i16((LAS s16x4*)(ra));
          s16x4 hi = __builtin_amdgcn_ds_read_tr16_b64_v4i16((LAS s16x4*)(ra + 128));
          u32x4 o = __builtin_bit_cast(u32x4, __builtin_shufflevector(lo, hi, 0, 1, 2, 3, 4, 5, 6, 7));
          const int blk = sb + k;
          const unsigned voff = (unsigned)((q >> 1) * NKG * 1024 + ((q & 1) * 16 + li) * 16);
          st16_sc1(rsHT, voff, (unsigned)(((rt * 16 + 2 * w) * NKG + 144 + (blk >> 1)) * 1024 + (blk & 1) * 512), o);
          SCHED0;
        }
      }
      SCHED0;
    }
  };
  interval_2(IC(0)); interval_2(IC(1)); interval_2(IC(2)); interval_2(IC(3)); interval_2(IC(4)); interval_2(IC(5));
  interval_2(IC(6)); interval_2(IC(7)); interval_2(IC(8)); STAMP(7); interval_2(IC(9)); interval_2(IC(10)); interval_2(IC(11));
  interval_2(IC(12)); interval_2(IC(13)); interval_2(IC(14)); interval_2(IC(15)); interval_2(IC(16)); interval_2(IC(17));
  (void)ltot;
  STAMP(8);

  bar();
  constexpr int LX = 0, LOWN = 73728, LP3 = 92160;
#pragma unroll
  for (int g = 0; g < 9; g++) *(LAS f32x4*)(lds0 + LX + ((2 * tg + (kh ^ 1)) * 9 + g) * 1024 + l16) = kh ? ac2[g][0] : ac2[g][1];
  asm volatile("s_waitcnt lgkmcnt(0)" ::: "memory");
  bar();
  {
    const int tbl = 2 * tg + kh;
    const rsrc_t rsP = mkrsrc(ws + WS_PART, 64 * 4 * 8 * 9 * 1024);
#pragma unroll
    for (int g = 0; g < 9; g++) {
      const f32x4 o = (kh ? ac2[g][1] : ac2[g][0]) + *(LAS f32x4*)(lds0 + LX + (tbl * 9 + g) * 1024 + l16);
      if (tg == s2) *(LAS f32x4*)(lds0 + LOWN + (kh * 9 + g) * 1024 + l16) = o;
      else st16_sc1(rsP, l16, (unsigned)((((tile * 4 + s2) * 8 + tbl) * 9 + g) * 1024), __builtin_bit_cast(u32x4, o));
    }
  }
  {
    const int kq = w & 3, dg = w >> 2;
    spin_flag(ws + FL_HS + (size_t)(tt >> 1) * 64, (lane & 15) * 4);
    asm volatile("s_waitcnt vmcnt(0) lgkmcnt(0)" ::: "memory");
    bar();
    setflag(rsFL, tid == 0, (unsigned)FL_P + (unsigned)(tile * 4 + s2) * 4);
    STAMP(9);
    const char* cb = ws + WS_WT + (((size_t)dt * NKG + 144 + 9 * kq) * 9 + 5 * dg) * 1024;
    const char* hb = ws + WS_HT + ((size_t)(tt * 8 + 2 * s2) * NKG + 144 + 9 * kq) * 1024;
    f32x4 ac3[5][2];
#pragma unroll
    for (int k = 0; k < 5; k++) { ac3[k][0] = zero4; ac3[k][1] = zero4; }
    bf16x8 cf[3][3][5], h3[9][2];
#pragma unroll
    for (int g = 0; g < 9; g++) { gld16_sc1<0>(h3[g][0], hb + g * 1024, l16); gld16_sc1<0>(h3[g][1], hb + (size_t)NKG * 1024 + g * 1024, l16); }
    const int last = dg ? 3 : 4;
#define CLD(bt) do { _Pragma("unroll") for (int g_ = 0; g_ < 3; g_++) { const char* p_ = cb + (size_t)(3 * (bt) + g_) * 9216; \
      gld16<0>(cf[bt][g_][0], p_, l16); gld16<1024>(cf[bt][g_][1], p_, l16); gld16<2048>(cf[bt][g_][2], p_, l16); gld16<3072>(cf[bt][g_][3], p_, l16); \
      gld16<0>(cf[bt][g_][4], p_ + last * 1024, l16); } } while (0)
#define CTIE(bt) do { _Pragma("unroll") for (int g_ = 0; g_ < 3; g_++) _Pragma("unroll") for (int k_ = 0; k_ < 5; k_++) tie(cf[bt][g_][k_]); } while (0)
#define CMM(bt) do { _Pragma("unroll") for (int g_ = 0; g_ < 3; g_++) _Pragma("unroll") for (int k_ = 0; k_ < 5; k_++) { \
      ac3[k_][0] = MFMA(cf[bt][g_][k_], h3[3 * (bt) + g_][0], ac3[k_][0]); ac3[k_][1] = MFMA(cf[bt][g_][k_], h3[3 * (bt) + g_][1], ac3[k_][1]); } } while (0)
    CLD(0); CLD(1);
    WAITV(15);
#pragma unroll
    for (int g = 0; g < 9; g++) { tie(h3[g][0]); tie(h3[g][1]); }
    CTIE(0); SCHED0; CMM(0); SCHED0;
    CLD(2);
    WAITV(15); CTIE(1); SCHED0; CMM(1); SCHED0;
    WAITV(0); CTIE(2); SCHED0; CMM(2); SCHED0;
    const unsigned pb = lds0 + (kq < 2 ? LP3 + kq * 18432 : LX + (kq - 2) * 18432);
#pragma unroll
    for (int k = 0; k < 5; k++)
      if (k <= last) {
        *(LAS f32x4*)(pb + (5 * dg + k) * 1024 + l16) = ac3[k][0];
        *(LAS f32x4*)(pb + (9 + 5 * dg + k) * 1024 + l16) = ac3[k][1];
      }
    STAMP(10);
    spin_flag(ws + FL_P + (size_t)tile * 16, (lane & 3) * 4);
    asm volatile("s_waitcnt vmcnt(0) lgkmcnt(0)" ::: "memory");
    bar();
    constexpr int LT = 36864;
    const rsrc_t rsP = mkrsrc(ws + WS_PART, 64 * 4 * 8 * 9 * 1024);
#pragma unroll
    for (int r = 0; r < 3; r++) {
      const int fidx = w + 8 * r;
      if (fidx < 18) {
        f32x4 sum = *(LAS f32x4*)(lds0 + LOWN + fidx * 1024 + l16);
#pragma unroll
        for (int o = 1; o < 4; o++) {
          const int so = (s2 + o) & 3;
          sum += __builtin_bit_cast(f32x4, __builtin_amdgcn_raw_buffer_load_b128(rsP, l16, (unsigned)((((tile * 4 + so) * 8 + 2 * s2) * 9 + fidx) * 1024), 16));
        }
        sum += *(LAS f32x4*)(lds0 + LP3 + fidx * 1024 + l16) + *(LAS f32x4*)(lds0 + LP3 + 18432 + fidx * 1024 + l16);
        sum += *(LAS f32x4*)(lds0 + LX + fidx * 1024 + l16) + *(LAS f32x4*)(lds0 + LX + 18432 + fidx * 1024 + l16);
        const int tb = fidx / 9, db = fidx - 9 * tb;
        *(LAS f32x4*)(lds0 + LT + (tb * 16 + li) * 592 + (db * 16 + 4 * q) * 4) = sum;
      }
    }
    asm volatile("s_waitcnt lgkmcnt(0)" ::: "memory");
    bar();
#pragma unroll
    for (int r = 0; r < 3; r++) {
      const int e = tid + 512 * r;
      if (e < 1152) {
        const int t = e / 36, d4 = e - 36 * t;
        const int row = tt * 128 + 32 * s2 + t, dcol = dt * 144 + 4 * d4;
        const f32x4 sum = *(LAS f32x4*)(lds0 + LT + t * 592 + d4 * 16);
        const u32x2 xv = *(const u32x2*)(x + (size_t)row * DM + dcol);
        const u32x2 dv = *(const u32x2*)(dsk + dcol);
        const f32x2 st = *(const f32x2*)(ws + WS_STATS + (size_t)row * 8);
        f32x4 o;
#pragma unroll
        for (int j = 0; j < 4; j++) {
          const float xe = (j & 1) ? __uint_as_float(xv[j >> 1] & 0xffff0000u) : __uint_as_float(xv[j >> 1] << 16);
          const float de = (j & 1) ? __uint_as_float(dv[j >> 1] & 0xffff0000u) : __uint_as_float(dv[j >> 1] << 16);
          const float xn = rbf(rbf(xe - st[0]) / st[1]);
          o[j] = xe + rbf(xn * de) + sum[j];
        }
        *(f32x4*)(out + (size_t)row * DM + dcol) = o;
      }
    }
    STAMP(11);
  }
}

extern "C" void kernel_launch(void* const* d_in, const int* in_sizes, int n_in, void* d_out, int out_size, void* d_ws, size_t ws_size,
                              hipStream_t stream) {
  const u16* x = (const u16*)d_in[0]; const u16* lre = (const u16*)d_in[1]; const u16* lim = (const u16*)d_in[2];
  const u16* bre = (const u16*)d_in[3]; const u16* bim = (const u16*)d_in[4]; const u16* cre = (const u16*)d_in[5]; const u16* cim = (const u16*)d_in[6];
  const u16* logdt = (const u16*)d_in[7]; const u16* dsk = (const u16*)d_in[8];
  const u16* wg = (const u16*)d_in[10]; const u16* wu = (const u16*)d_in[11]; const u16* wd = (const u16*)d_in[12];
  char* ws = (char*)d_ws;
  hipFuncSetAttribute((const void*)k_main, hipFuncAttributeMaxDynamicSharedMemorySize, LDS_TOT);
  k0_prep<<<437, 256, 0, stream>>>(x, lre, lim, cre, cim, logdt, wd, ws);
  k_main<<<256, 512, LDS_TOT, stream>>>(x, bre, bim, dsk, wg, wu, ws, (float*)d_out);
}
```

```cpp
#include <hip/hip_runtime.h>
#include <stdint.h>
#include <type_traits>

typedef unsigned short u16;
typedef unsigned long long u64;
typedef __bf16 bf16x8 __attribute__((ext_vector_type(8)));
typedef __bf16 bf16x2 __attribute__((ext_vector_type(2)));
typedef float f32x4 __attribute__((ext_vector_type(4)));
typedef float f32x2 __attribute__((ext_vector_type(2)));
typedef short s16x4 __attribute__((ext_vector_type(4)));
typedef unsigned u32x4 __attribute__((ext_vector_type(4)));
typedef unsigned u32x2 __attribute__((ext_vector_type(2)));
#define LAS __attribute__((address_space(3)))
#define GAS __attribute__((address_space(1)))
#define DI __device__ __forceinline__

constexpr int SEQ = 2048, DM = 576, FF = 4608;
constexpr int KS2 = 23;
constexpr int NKG = 8 * KS2;
constexpr size_t FL_H = 0;
constexpr size_t FL_PO = 32768;
constexpr size_t FL_E = 40960;
constexpr size_t FLAGS_BYTES = FL_E + 8 * 32 * 48 * 8;
constexpr size_t WS_CONST = 147456;
constexpr size_t WS_STATS = WS_CONST + 147456;
constexpr size_t WS_XT = 327680;
constexpr size_t WS_HT = WS_XT + (size_t)128 * 18 * 1024;
constexpr size_t WS_WT = WS_HT + (size_t)128 * NKG * 1024;
constexpr size_t WS_PART = WS_WT + (size_t)4 * NKG * 9 * 1024;
constexpr size_t PART_BYTES = (size_t)8 * 4 * 8 * 8 * 9 * 1024;
constexpr size_t WS_END = WS_PART + PART_BYTES;

DI float bf2f(u16 v) { return __uint_as_float(((unsigned)v) << 16); }
DI float rbf(float f) { return (float)(__bf16)f; }
DI unsigned pk2(float lo, float hi) { f32x2 v = {lo, hi}; bf16x2 c = __builtin_convertvector(v, bf16x2); return __builtin_bit_cast(unsigned, c); }
DI float blo(unsigned u) { return __uint_as_float(u << 16); }
DI float bhi(unsigned u) { return __uint_as_float(u & 0xffff0000u); }

__host__ __device__ inline int wt_frag(int j, int db) { return j < 16 ? j * 9 + db : 144 + db * 7 + (j - 16); }
__host__ __device__ inline int kg_f0(int s, int j) {
  if (j < 16) { int p = j >> 3, cg = 4 * s + ((j & 7) >> 1); return 64 * (2 * cg + p) + 32 * (j & 1); }
  return 4096 + 32 * (2 * s + (j - 16));
}

__global__ __launch_bounds__(256) void k0_prep(const u16* __restrict__ x, const u16* __restrict__ lre, const u16* __restrict__ lim,
                                               const u16* __restrict__ cre, const u16* __restrict__ cim, const u16* __restrict__ logdt,
                                               const u16* __restrict__ wd, char* __restrict__ ws) {
  __shared__ __attribute__((aligned(16))) char sm[32 * 1152 + 512];
  const int tid = threadIdx.x, b = blockIdx.x;
  if (b < 256) {
    if (tid < 34) *(u32x4*)(ws + (size_t)b * 544 + tid * 16) = (u32x4){0, 0, 0, 0};
    const int r = tid >> 5, c = tid & 31, row = b * 8 + r;
    const unsigned* xr = (const unsigned*)(x + (size_t)row * DM);
    float v[18]; float s = 0.f;
#pragma unroll
    for (int i = 0; i < 9; i++) { unsigned u = xr[c + 32 * i]; v[2 * i] = blo(u); v[2 * i + 1] = bhi(u); s += v[2 * i] + v[2 * i + 1]; }
#pragma unroll
    for (int o = 16; o > 0; o >>= 1) s += __shfl_xor(s, o);
    const float mean = s * (1.0f / DM); float q = 0.f;
#pragma unroll
    for (int i = 0; i < 18; i++) { float d = v[i] - mean; q += d * d; }
#pragma unroll
    for (int o = 16; o > 0; o >>= 1) q += __shfl_xor(q, o);
    const float m = rbf(mean), den = rbf(sqrtf(rbf(rbf(q * (1.0f / DM)) + 1e-5f)));
    if (c == 0) *(f32x2*)(ws + WS_STATS + (size_t)row * 8) = (f32x2){m, den};
    unsigned* smr = (unsigned*)(sm + r * 1168);
#pragma unroll
    for (int i = 0; i < 9; i++) smr[c + 32 * i] = pk2(rbf(v[2 * i] - m) / den, rbf(v[2 * i + 1] - m) / den);
    __syncthreads();
    const int tb = b >> 1, i0 = (b & 1) * 8;
    for (int p = tid; p < 576; p += 256) {
      const int i = p & 7, ch = p >> 3;
      u32x4 val = *(const u32x4*)(sm + i * 1168 + ch * 16);
      *(u32x4*)(ws + WS_XT + ((size_t)tb * 18 + (ch >> 2)) * 1024 + ((ch & 3) * 16 + i0 + i) * 16) = val;
    }
  } else if (b < 400) {
    const int kg = b - 256, s = kg / 18, j = kg % 18, f0 = kg_f0(s, j);
    const u32x4* src = (const u32x4*)(wd + (size_t)f0 * DM);
    for (int p = tid; p < 2304; p += 256) *(u32x4*)(sm + p * 16) = src[p];
    __syncthreads();
    const int w = tid >> 6, lane = tid & 63, q = lane >> 4, r4 = (lane & 15) >> 2, c = lane & 3;
    for (int d16 = w; d16 < 36; d16 += 4) {
      const int a0 = (4 * q + r4) * 1152 + d16 * 32 + c * 8;
      s16x4 lo = __builtin_amdgcn_ds_read_tr16_b64_v4i16((LAS s16x4*)(sm + a0));
      s16x4 hi = __builtin_amdgcn_ds_read_tr16_b64_v4i16((LAS s16x4*)(sm + a0 + 16 * 1152));
      bf16x8 fr = __builtin_bit_cast(bf16x8, __builtin_shufflevector(lo, hi, 0, 1, 2, 3, 4, 5, 6, 7));
      const int dt = d16 / 9, db = d16 % 9;
      *(bf16x8*)(ws + WS_WT + (((size_t)dt * NKG + s * KS2) * 9 + wt_frag(j, db)) * 1024 + lane * 16) = fr;
    }
  } else if (b < 436) {
    const int d16 = b - 400, dt = d16 / 9, db = d16 % 9;
    for (int p = tid; p < 40 * 64; p += 256) {
      const int g = p >> 6, s = g / 5, jj = g % 5, lane = p & 63, i = lane & 15, q = lane >> 4;
      const int bl = 2 * jj + (q >> 1);
      u32x4 v = {0, 0, 0, 0};
      if (bl < 9) v = *(const u32x4*)(((q & 1) ? cim : cre) + (size_t)(16 * d16 + i) * DM + 8 * (9 * s + bl));
      *(u32x4*)(ws + WS_WT + (((size_t)dt * NKG + s * KS2) * 9 + wt_frag(18 + jj, db)) * 1024 + lane * 16) = v;
    }
  } else {
    for (int p = tid; p < DM; p += 256) {
      const float lr = bf2f(lre[p]), li = bf2f(lim[p]);
      const float dt = rbf(expf(bf2f(logdt[p])));
      const float er = expf(dt * lr), ar = er * cosf(dt * li), ai = er * sinf(dt * li);
      const float nr = ar - 1.0f, ni = ai, dn = lr * lr + li * li;
      float pr[14], pi[14];
      pr[0] = ar; pi[0] = ai;
      pr[1] = (nr * lr + ni * li) / dn; pi[1] = (ni * lr - nr * li) / dn;
#pragma unroll
      for (int k = 2; k < 5; k++) { pr[k] = pr[k - 1] * ar - pi[k - 1] * ai; pi[k] = pr[k - 1] * ai + pi[k - 1] * ar; }
      pr[5] = 1.f; pi[5] = 0.f;
      const float a2r = ar * ar - ai * ai, a2i = 2.f * ar * ai;
      pr[6] = a2r * a2r - a2i * a2i; pi[6] = 2.f * a2r * a2i;
      pr[7] = pr[6] * pr[6] - pi[6] * pi[6]; pi[7] = 2.f * pr[6] * pi[6];
      pr[8] = pr[7] * pr[6] - pi[7] * pi[6]; pi[8] = pr[7] * pi[6] + pi[7] * pr[6];
      pr[9] = pr[7] * pr[7] - pi[7] * pi[7]; pi[9] = 2.f * pr[7] * pi[7];
#pragma unroll
      for (int k = 10; k < 14; k++) { pr[k] = pr[k - 1] * pr[k - 1] - pi[k - 1] * pi[k - 1]; pi[k] = 2.f * pr[k - 1] * pi[k - 1]; }
      float* tre = (float*)(ws + WS_CONST) + (p >> 3) * 512 + (p & 7) * 2;
      float* tim = tre + 16;
#pragma unroll
      for (int k = 0; k < 14; k++) {
        if (k >= 1 && k <= 4) { tre[32 * k] = 2.f * pr[k]; tre[32 * k + 1] = -2.f * pi[k]; tim[32 * k] = -2.f * pr[k]; tim[32 * k + 1] = -2.f * pi[k]; }
        else { tre[32 * k] = pr[k]; tre[32 * k + 1] = pi[k]; tim[32 * k] = pr[k]; tim[32 * k + 1] = -pi[k]; }
      }
    }
  }
}

template <int N> using IC = std::integral_constant<int, N>;
template <int B, int E, class F> DI void sfor(F&& f) { if constexpr (B < E) { f(IC<B>{}); sfor<B + 1, E>(f); } }

DI void glds_s(const void* sbase, unsigned voff, unsigned ldsaddr) {
  asm volatile("s_mov_b32 m0, %2\n\ts_nop 0\n\tglobal_load_lds_dwordx4 %0, %1" :: "v"(voff), "s"(sbase), "s"(ldsaddr) : "memory");
}
template <class T> DI void gld16(T& dst, const void* sbase, unsigned voff) {
  asm volatile("global_load_dwordx4 %0, %1, %2" : "=v"(dst) : "v"(voff), "s"(sbase) : "memory");
}
template <class T> DI void gld16_sc1(T& dst, const void* sbase, unsigned voff) {
  asm volatile("global_load_dwordx4 %0, %1, %2 sc1" : "=v"(dst) : "v"(voff), "s"(sbase) : "memory");
}
template <class T> DI void gld16_sc1_io(T& dst, const void* sbase, unsigned voff) {
  asm volatile("global_load_dwordx4 %0, %1, %2 sc1" : "+v"(dst) : "v"(voff), "s"(sbase) : "memory");
}
DI void gld4_sc1(unsigned& dst, const void* sbase, unsigned voff) {
  asm volatile("global_load_dword %0, %1, %2 sc1" : "=v"(dst) : "v"(voff), "s"(sbase) : "memory");
}
DI void gld8_sc1(u32x2& dst, const void* sbase, unsigned voff) {
  asm volatile("global_load_dwordx2 %0, %1, %2 sc1" : "=v"(dst) : "v"(voff), "s"(sbase) : "memory");
}
DI void gld8(u32x2& dst, const void* sbase, unsigned voff) {
  asm volatile("global_load_dwordx2 %0, %1, %2" : "=v"(dst) : "v"(voff), "s"(sbase) : "memory");
}
template <int N> DI void waitv() { static_assert(N >= 0 && N < 64, ""); asm volatile("s_waitcnt vmcnt(%0)" ::"i"(N) : "memory"); }
#define SCHED0 __builtin_amdgcn_sched_barrier(0)
DI void bar() { asm volatile("" ::: "memory"); __builtin_amdgcn_s_barrier(); asm volatile("" ::: "memory"); }
template <class T> DI void tie(T& a) { asm volatile("" : "+v"(a)); }
template <int O2> DI bf16x8 trfrag(unsigned addr) {
  s16x4 a = __builtin_amdgcn_ds_read_tr16_b64_v4i16((LAS s16x4*)(addr));
  s16x4 b = __builtin_amdgcn_ds_read_tr16_b64_v4i16((LAS s16x4*)(addr + O2));
  return __builtin_bit_cast(bf16x8, __builtin_shufflevector(a, b, 0, 1, 2, 3, 4, 5, 6, 7));
}
DI bf16x8 ldsfrag(unsigned addr) { return *(LAS bf16x8*)(addr); }
template <bool FIRST> DI void mfma(f32x4& c, bf16x8 a, bf16x8 b) {
  if constexpr (FIRST) asm volatile("v_mfma_f32_16x16x32_bf16 %0, %1, %2, 0" : "=v"(c) : "v"(a), "v"(b));
  else asm volatile("v_mfma_f32_16x16x32_bf16 %0, %1, %2, %0" : "+v"(c) : "v"(a), "v"(b));
}
DI float silu_mul(float g, float u) {
  const float e = __builtin_amdgcn_exp2f(g * -1.44269504089f);
  return g * u * __builtin_amdgcn_rcpf(1.0f + e);
}
DI unsigned silu_mul2(f32x2 g, f32x2 u) {
  const f32x2 t = g * -1.44269504089f;
  f32x2 e; e.x = __builtin_amdgcn_exp2f(t.x); e.y = __builtin_amdgcn_exp2f(t.y);
  const f32x2 d = e + 1.0f;
  f32x2 r; r.x = __builtin_amdgcn_rcpf(d.x); r.y = __builtin_amdgcn_rcpf(d.y);
  const f32x2 h = (g * u) * r;
  return pk2(h.x, h.y);
}
DI float dpp_ror8(float v) { return __int_as_float(__builtin_amdgcn_update_dpp(0, __float_as_int(v), 0x128, 0xf, 0xf, true)); }
DI void cmac(float& d, float P, float Q, float x) {
  asm volatile("v_fmac_f32 %0, %1, %3\n\tv_fmac_f32_dpp %0, %3, %2 row_ror:8 row_mask:0xf bank_mask:0xf" : "+v"(d) : "v"(P), "v"(Q), "v"(x));
}
DI void cmul(float& d, float P, float Q, float x) {
  asm volatile("v_mul_f32 %0, %1, %3\n\tv_fmac_f32_dpp %0, %3, %2 row_ror:8 row_mask:0xf bank_mask:0xf" : "=&v"(d) : "v"(P), "v"(Q), "v"(x));
}
DI void cfma(float& d, float P, float Q, float x, float c) {
  asm volatile("v_fma_f32 %0, %1, %3, %4\n\tv_fmac_f32_dpp %0, %3, %2 row_ror:8 row_mask:0xf bank_mask:0xf" : "=&v"(d) : "v"(P), "v"(Q), "v"(x), "v"(c));
}
DI void v_mul(float& d, float a, float b) { asm volatile("v_mul_f32 %0, %1, %2" : "=v"(d) : "v"(a), "v"(b)); }
DI void v_mulc(float& d, float a) { asm volatile("v_mul_f32 %0, 0xbfb8aa3b, %1" : "=v"(d) : "v"(a)); }
DI void v_exp2(float& d) { asm volatile("v_exp_f32 %0, %0" : "+v"(d)); }
DI void v_add1(float& d) { asm volatile("v_add_f32 %0, 1.0, %0" : "+v"(d)); }
DI void v_rcp(float& d) { asm volatile("v_rcp_f32 %0, %0" : "+v"(d)); }
DI void v_cvt(unsigned& d, float a, float b) { asm volatile("v_cvt_pk_bf16_f32 %0, %1, %2" : "=v"(d) : "v"(a), "v"(b)); }
DI void dnop() { asm volatile("s_nop 1"); }
typedef __amdgpu_buffer_rsrc_t rsrc_t;
DI rsrc_t mkrsrc(const void* p, unsigned bytes) { return __builtin_amdgcn_make_buffer_rsrc((void*)p, 0, bytes, 0x00020000); }
DI void st8_sc1(rsrc_t r, unsigned voff, unsigned soff, u32x2 v) { unsigned o = voff + soff; tie(o); __builtin_amdgcn_raw_buffer_store_b64(v, r, o, 0, 16); }
DI void st16_sc1(rsrc_t r, unsigned voff, unsigned soff, u32x4 v) { unsigned o = voff + soff; tie(o); __builtin_amdgcn_raw_buffer_store_b128(v, r, o, 0, 16); }
DI void setflag(rsrc_t r, bool sel, unsigned off) { __builtin_amdgcn_raw_buffer_store_b32(1u, r, sel ? off : 0x7ffffff0u, 0, 16); }
DI void spin_flag(const char* base, unsigned voff) {
  for (unsigned it = 0; it < (1u << 18); ++it) {
    unsigned v = __hip_atomic_load((GAS unsigned*)(base + voff), __ATOMIC_RELAXED, __HIP_MEMORY_SCOPE_AGENT);
    if (__all(v == 1u)) break;
    __builtin_amdgcn_s_sleep(2);
  }
  asm volatile("" ::: "memory");
}

constexpr int PD = 2, FR = PD + 1;
constexpr int SLOT1 = 24576;
constexpr int SLOT2 = 18432;
constexpr int LDS_TOT = 163840;
constexpr int LDS_SCR = 40960;
constexpr int LDS_CST = 153600;
constexpr int LDS_WTOT = LDS_CST + 6144;

#define STAMP(k)
#define STAMPC(k)
#define STAMP_DUMP()

constexpr int p1_off(int v) { return v < 12 ? (v % 6) * SLOT1 : (v - 12) * 30720; }
constexpr int p2_off(int u) { return (u % 5) < 3 ? 92160 + (u % 5) * SLOT2 : ((u % 5) - 3) * SLOT2; }

constexpr int GT = 288, G2 = 378, GEND = 585;
constexpr int gl(int v, int g) { return v * 24 + g; }
constexpr int gt(int u, int g) { return GT + u * 30 + g; }
constexpr int g2(int u, int g) { return G2 + u * 18 + g; }
constexpr int SY1 = 8, SYT = 2, SY2 = 4;
constexpr int late_g(int f) { return g2(8, f); }
constexpr int PLAG = 4;

constexpr int SILU0[3] = {2 * (gl(4, 0) + 12), 2 * (gl(10, 0) + 12), 2 * (G2 + 6)};
constexpr int SILUN[3] = {32, 32, 8};
constexpr int silu_end(int s, int nel) { return SILU0[s] + 9 * (nel / 2) + 1; }
constexpr int S1A = 2 * (GT + 54);
constexpr int S1SEL = S1A + 64;
constexpr int SCE = 2 * g2(0, SY2) + 2;
constexpr int SCM = 2 * g2(1, 8);
constexpr int UFIN = 3;
constexpr int SFIN = 2 * g2(UFIN, SY2);

enum { OP_G = 0, OP_X, OP_ST, OP_PO, OP_PH, OP_A, OP_B, OP_FL, OP_EP, OP_HL, OP_PL, OP_EG };
struct Op { int pos, kind, a, n; };
struct Sched {
  Op op[320]; int cnt;
  constexpr void add(int pos, int kind, int a, int n) {
    int i = cnt; while (i > 0 && op[i - 1].pos > pos) { op[i] = op[i - 1]; i--; }
    op[i] = Op{pos, kind, a, n}; cnt++;
  }
  constexpr void unit(int g, int v, int step) { for (int n = 0; n < (v < 12 ? 3 : 4); n++) add(2 * (g + n * step), OP_G, v, n); }
  constexpr Sched() : op{}, cnt(0) {
    for (int n = 0; n < 3; n++) add(-1, OP_G, 0, n);
    for (int n = 0; n < 6; n++) add(-1, OP_X, 0, n);
    for (int n = 0; n < 3; n++) add(-1, OP_G, 1, n);
    for (int n = 0; n < 6; n++) add(-1, OP_X, 1, n);
    for (int n = 0; n < 3; n++) add(-1, OP_G, 2, n);
    for (int v = 0; v < 3; v++) for (int n = 0; n < 6; n++) add(2 * gl(v, 1 + n), OP_X, v + 2, n);
    for (int n = 0; n < 6; n++) add(2 * gl(2, 16 + n), OP_X, 5, n);
    unit(gl(0, 10), 3, 2); unit(gl(0, 16), 4, 2); unit(gl(1, 10), 5, 3); unit(gl(2, 10), 6, 2); unit(gl(3, 10), 7, 3); unit(gl(3, 19), 8, 2) ;
    unit(gl(4, 10), 9, 4); unit(gl(6, 10), 10, 4); unit(gl(7, 2), 11, 4); unit(gl(8, 10), 12, 3); unit(gl(9, 10), 13, 3); unit(gl(10, 10), 14, 3);
    for (int n = 0; n < 3; n++) add(2 * gl(3, 1 + 2 * n), OP_PH, 0, n);
    for (int n = 0; n < 9; n++) add(2 * (gl(4, 2) + 14 * n), OP_PO, 0, n);
    for (int s = 0; s < 2; s++) for (int h = 0; h < 2; h++) for (int t = 0; t < 2; t++) add((silu_end(s, 16 * (h + 1)) / 2) * 2 + 1, OP_ST, s, 2 * h + t);
    for (int t = 0; t < 2; t++) add((silu_end(2, 8) / 2) * 2 + 1, OP_ST, 2, t);
    add(2 * gt(0, 6), OP_FL, 0, 0);
    for (int n = 0; n < 2; n++) { add(2 * gt(0, 20 + 4 * n), OP_A, 0, n); add(2 * gt(1, 8 + 4 * n), OP_A, 1, n); add(2 * gt(1, 16 + 4 * n), OP_A, 2, n); }
    for (int n = 0; n < 4; n++) { add(2 * gt(2, 3 + 2 * n), OP_B, 0, n); add(2 * gt(2, 11 + 2 * n), OP_B, 1, n); add(2 * gt(2, 19 + 3 * n), OP_B, 2, n); }
    add(2 * g2(0, 1), OP_FL, 1, 0);
    for (int u = 0; u < 9; u++) {
      const int U = u + 3;
      for (int n = 0; n < (U < 11 ? 2 : 1); n++) add(2 * g2(u, 5 + 2 * n), OP_A, U, n);
      if (U < 8) for (int n = 0; n < 4; n++) add(2 * g2(u, 9 + 2 * n), OP_B, U, n);
    }
    for (int n = 0; n < 7; n++) add(2 * g2(UFIN - 1, 10) + 0, OP_EG, 0, n);
    add(2 * g2(4, 6), OP_FL, 2, 0);
    add(2 * g2(4, 8), OP_FL, 3, 0);
    for (int n = 0; n < 4; n++) add(2 * g2(5, 9 + 2 * n), OP_HL, 0, n);
    for (int n = 4; n < 14; n++) add(2 * g2(6, 8 + (n - 4)), OP_HL, 0, n);
    for (int n = 0; n < 9; n++) add(2 * g2(8, 6 + n), OP_EP, 0, n);
    for (int db = 0; db < 8; db++) add(2 * (late_g(7 * db + 6) + 3) + 1, OP_ST, 3, db);
    for (int db = 0; db + PLAG < 8; db++) add(2 * (late_g(7 * (db + PLAG) + 6) + 5), OP_PL, 0, db);
  }
  constexpr int first_at(int pos) const { int i = 0; while (i < cnt && op[i].pos < pos) i++; return i; }
  constexpr int after(int kind, int a, int g) const {
    int last = -1;
    for (int i = 0; i < cnt; i++) if (op[i].kind == kind && op[i].a == a) last = i;
    int n = 0;
    for (int i = last + 1; i < cnt; i++) if (op[i].pos < 2 * g) n++;
    return n;
  }
};
constexpr Sched S1{};
constexpr int cmin(int a, int b) { return a < b ? a : b; }
constexpr int cmin(int a, int b, int c) { return cmin(cmin(a, b), c); }
constexpr bool l_dbl(int v) { return v == 4 || v == 5 || v == 6 || v == 7 || v >= 10; }
struct LF { int unit, ks, blk; };
constexpr LF l_frag(int G) {
  const int v = G / 24, g = G % 24;
  if (!l_dbl(v)) return LF{v, 3 * (v % 6) + (g >> 3), g & 7};
  const int v0 = v & ~1, r = (v & 1) * 24 + g, p = r / 12, k6 = (r % 12) / 2;
  return LF{v0 + k6 / 3, 3 * (v0 % 6) + k6, p + 4 * (r & 1)};
}
constexpr int l_base(int v) { return l_dbl(v) ? (v & ~1) : v; }
constexpr int t_ks(int r) { return r < 54 ? r / 3 : (r - 54) / 2; }
constexpr int t_blk(int r) { return r < 54 ? 2 + r % 3 : (r - 54) & 1; }

__global__ __launch_bounds__(512, 2) void k_main(const u16* __restrict__ x, const u16* __restrict__ bre, const u16* __restrict__ bim,
                                                 const u16* __restrict__ dsk, const u16* __restrict__ wg, const u16* __restrict__ wu,
                                                 char* __restrict__ ws, float* __restrict__ out) {
  extern __shared__ __attribute__((aligned(16))) char lds[];
  const int tid = threadIdx.x, w = __builtin_amdgcn_readfirstlane(tid >> 6);
  const int b = blockIdx.x, sx = b & 7, idx = b >> 3;
  unsigned lds0 = (unsigned)(size_t)(LAS char*)lds;
  asm volatile("" : "+s"(lds0));
  const int rt = idx & 7, cgl = idx >> 3, cg = sx * 4 + cgl;
  const rsrc_t rsHT = mkrsrc(ws + WS_HT, 128 * NKG * 1024);
  const rsrc_t rsFL = mkrsrc(ws, (unsigned)FLAGS_BYTES);
  const rsrc_t rsPT = mkrsrc(ws + WS_PART, (unsigned)PART_BYTES);
  const unsigned htw = (unsigned)(((rt * 16 + 2 * w) * 8 + sx) * KS2) * 1024u;
  constexpr unsigned HTB = NKG * 1024;
  const unsigned flw = (unsigned)FL_H + (unsigned)((((rt * 8 + sx) * 4) * 8 + w) * 4) * 4u;
  const f32x4 zero4 = {0.f, 0.f, 0.f, 0.f};
  const unsigned l16 = (tid & 63) * 16;
  STAMP(0);

  f32x4 acc[8][2];
  f32x4 tac[5][2];
  f32x4 ac2[9][2];
  bf16x8 fr[FR];
  bf16x8 xf[18][2];
  constexpr int HR = 4;
  bf16x8 hf[HR][2][2];
  bf16x8 hl[7][2];
  u32x4 pv[9];
  unsigned flv[4] = {0, 0, 0, 0};
  u32x2 epv[9];
  u32x2 eg[7];
  unsigned wvoff, ab[4], tvA = 0, tvF = 0;
  const char* wsrc = (const char*)((w >> 2) ? wu : wg) + (size_t)cg * 256;
  {
    const int lane = tid & 63, q = lane >> 4, li = lane & 15;
    const int rg = w & 3, r8 = lane >> 3, ch = lane & 7;
    const int g = ((lane >> 4) & 1) | ((rg & 1) << 1);
    wvoff = (unsigned)((8 * rg + r8) * (FF * 2) + (((ch >> 1) ^ g) << 5) + ((ch & 1) << 4));
    const int r4 = li >> 2, cc = lane & 3;
    const int gr = ((r4 >> 1) & 1) | ((q & 1) << 1);
    const unsigned a0 = (unsigned)((8 * q + r4) * 128 + (gr << 5) + 8 * cc);
#pragma unroll
    for (int k = 0; k < 4; k++) { ab[k] = lds0 + (a0 ^ (k << 5)); tie(ab[k]); }
    if (tid < 384) *(LAS float*)(lds0 + LDS_WTOT + tid * 4) = 0.f;
  }
  const char* xb = ws + WS_XT + (size_t)(rt * 16 + 2 * w) * 18 * 1024;
  const unsigned pobase = (unsigned)(((rt * 4 + cgl) * 8 + sx) * 72) * 1024u;
  const unsigned pb = (unsigned)((((rt * 4 + cgl) * 8 + w) * 8 + sx) * 9) * 1024u;
  const char* wtb = ws + WS_WT + ((size_t)cgl * NKG + sx * KS2) * 9 * 1024 + w * 1024;
  const char* hb0 = ws + WS_HT + htw;
  const int erow = rt * 256 + 32 * sx;
  const int blk3 = w < 4 ? 2 * cgl + (w >> 1) : 8;
  const char* tB = (const char*)((w & 1) ? bim : bre) + (size_t)(8 * (9 * sx + blk3)) * (DM * 2);
  const size_t fcol = (size_t)(4096 + 64 * sx + 16 * cgl) * 2;
  auto fsrc = [&](int fp) { return (const char*)((fp & 1) ? wu : wg) + fcol + (size_t)(fp >> 1) * 32 * (FF * 2); };
  unsigned cla = 0;

  auto issue = [&](auto Ic) {
    constexpr Op o = S1.op[decltype(Ic)::value];
    if constexpr (o.kind == OP_G) {
      if constexpr (o.a < 12) {
        glds_s(wsrc + (o.a / 6) * 128 + (size_t)(3 * (o.a % 6) + o.n) * 32 * (FF * 2), wvoff, lds0 + p1_off(o.a) + o.n * 8192 + w * 1024);
      } else {
        constexpr int U = o.a - 12;
        if constexpr (U == 0 && o.n == 0) {
          int tl = tid; tie(tl);
          const int lane = tl & 63, p = lane >> 1;
          tvF = (unsigned)((p ^ (((p >> 3) & 1) << 2)) * (FF * 2) + ((lane & 1) << 4));
          const unsigned vb = (unsigned)((lane >> 3) * (DM * 2) + (((lane & 7) ^ (4 * (w & 1) + (lane >> 4))) << 4));
          tvA = w < 6 ? vb : tvF;
        }
        const unsigned tb = lds0 + p1_off(o.a);
        constexpr size_t FU = (size_t)U * 192 * (FF * 2);
        if constexpr (o.n < 3) {
          const int fp = 3 * w - 12 + o.n;
          const char* src = w < 6 ? tB + U * 384 + o.n * 128 : fsrc(fp) + FU;
          const unsigned dst = w < 6 ? tb + 12288 + o.n * 6144 + w * 1024 : tb + fp * 1024;
          glds_s(src, tvA, dst);
        } else {
          const int fp = w < 6 ? w : 3 * w - 12;
          glds_s(fsrc(fp) + FU, tvF, tb + fp * 1024);
        }
      }
    } else if constexpr (o.kind == OP_X) {
      constexpr int ks = 3 * o.a + o.n / 2, t = o.n & 1;
      gld16(xf[ks][t], xb + (t * 18 + ks) * 1024, l16);
    } else if constexpr (o.kind == OP_PO) {
      st16_sc1(rsPT, l16, pobase + (unsigned)(o.n * 8 + w) * 1024u, (u32x4){~0u, ~0u, ~0u, ~0u});
    } else if constexpr (o.kind == OP_PH) {
      if constexpr (o.n < 2) st16_sc1(rsHT, l16, htw + o.n * HTB + (unsigned)(18 + cgl) * 1024u, (u32x4){~0u, ~0u, ~0u, ~0u});
      else {
        const unsigned f = (l16 & 512) ? 0u : ~0u;
        st16_sc1(rsHT, cgl < 2 ? l16 : 0x70000000u, htw + (cgl & 1) * HTB + 22u * 1024u, (u32x4){f, f, f, f});
      }
    } else if constexpr (o.kind == OP_A) {
      constexpr int U = o.a;
      const char* src = wtb + (size_t)U * SLOT2;
      const unsigned dst = lds0 + p2_off(U) + w * 1024;
      glds_s(src + o.n * 8192, l16, dst + o.n * 8192);
      if constexpr (U < 11 && o.n == 1) { if (w < 2) glds_s(src + 16384, l16, dst + 16384); }
      if constexpr (U == 11) { if (w < 1) glds_s(src + 8192, l16, dst + 8192); }
    } else if constexpr (o.kind == OP_B) {
      gld16_sc1(hf[o.a % HR][o.n >> 1][o.n & 1], hb0 + (size_t)(o.n & 1) * HTB + (2 * o.a + (o.n >> 1)) * 1024, l16);
    } else if constexpr (o.kind == OP_HL) {
      gld16_sc1(hl[o.n >> 1][o.n & 1], hb0 + (size_t)(o.n & 1) * HTB + (16 + (o.n >> 1)) * 1024, l16);
    } else if constexpr (o.kind == OP_PL) {
      gld16_sc1(pv[o.n], ws + WS_PART + (size_t)pobase + (size_t)(w * 9 + o.n) * 1024, l16);
    } else if constexpr (o.kind == OP_EG) {
      const int r = rt - 7 + o.n;
      gld8_sc1(eg[o.n], ws + FL_E + (size_t)((r < 0 ? 0 : r) * 32 + cg) * 384, cla * 2);
    } else if constexpr (o.kind == OP_FL) {
      if constexpr (o.a < 3) gld4_sc1(flv[o.a], ws + flw + 128 * o.a, (l16 >> 2) & 12);
      else gld4_sc1(flv[3], ws + FL_PO + (size_t)((rt * 4 + cgl) * 8 + w) * 4, l16 & 0);
    } else if constexpr (o.kind == OP_EP) {
      constexpr int r = o.n / 3;
      int tl = tid; tie(tl);
      const int e = tl + 512 * r < 1152 ? tl + 512 * r : 1151, t = e / 36, d4 = e - 36 * t;
      if constexpr (o.n % 3 == 0) gld8(epv[o.n], x + (size_t)erow * DM + cgl * 144, (unsigned)(t * DM + 4 * d4) * 2u);
      else if constexpr (o.n % 3 == 1) gld8(epv[o.n], dsk + cgl * 144, (unsigned)(4 * d4) * 2u);
      else gld8(epv[o.n], ws + WS_STATS + (size_t)erow * 8, (unsigned)t * 8u);
    }
  };
  auto issue_at = [&](auto Pc) {
    constexpr int i0 = S1.first_at(decltype(Pc)::value), i1 = S1.first_at(decltype(Pc)::value + 1);
    sfor<i0, i1>([&](auto Ic) { issue(Ic); SCHED0; });
  };
  auto xtie = [&](auto Uc) { constexpr int u = decltype(Uc)::value;
#pragma unroll
    for (int k = 0; k < 3; k++) { tie(xf[3 * u + k][0]); tie(xf[3 * u + k][1]); } };
  auto htie = [&](auto Uc) { constexpr int U = decltype(Uc)::value; tie(hf[U % HR][0][0]); tie(hf[U % HR][0][1]); tie(hf[U % HR][1][0]); tie(hf[U % HR][1][1]); };
  auto chk_flag = [&](auto Ac) { constexpr int a = decltype(Ac)::value; tie(flv[a]);
    if (!__all(flv[a] == 1u)) { if constexpr (a < 3) spin_flag(ws + flw + 128 * a, (l16 >> 2) & 12); else spin_flag(ws + FL_PO + (size_t)((rt * 4 + cgl) * 8 + w) * 4, l16 & 0); } };

  float te = 0.f, pp = 0.f, hh[2];
  unsigned hp[2][4];
  float cm[3][8], ca4[3][2], se[3][2], eg4[3][2][4], spre[3][2], sv[3][2][4], cc1[3][2], cin[3][2];
  float tw[8], c32[2], wpre = 0.f, crt = 0.f, cz[3], cpw[3][2], c256[2];
  unsigned fwa = 0, fra = 0, fvo = 0;
  unsigned cs48 = 0;
#define CPAIR(base, pr) (*(LAS f32x2*)((base) + (pr) * 128))
  unsigned csa = 0, pkd[4];
  bool q1 = false, q2 = false;
  auto side = [&](auto Sc) {
    constexpr int sl = decltype(Sc)::value;
    sfor<0, 3>([&](auto Kc) {
      constexpr int s = decltype(Kc)::value, r = sl - SILU0[s], N = SILUN[s];
      if constexpr (r >= 0 && r <= 9 * (N / 2) + 1) {
        constexpr int pr2 = r / 9, ph = r % 9;
        auto G = [&](auto Ec) -> float { constexpr int e = decltype(Ec)::value; if constexpr (s < 2) return acc[e >> 3][(e >> 2) & 1][e & 3]; else return tac[0][e >> 2][e & 3]; };
        auto Uv = [&](auto Ec) -> float { constexpr int e = decltype(Ec)::value; if constexpr (s < 2) return acc[4 + (e >> 3)][(e >> 2) & 1][e & 3]; else return tac[1][e >> 2][e & 3]; };
        constexpr int e0 = 2 * pr2, e1 = e0 + 1;
        if constexpr (ph == 0) { if constexpr (pr2 > 0) v_mul(hh[1], pp, te); if constexpr (e0 < N) v_mulc(te, G(IC<e0 < N ? e0 : 0>{})); }
        if constexpr (ph == 1) {
          if constexpr (pr2 > 0) {
            constexpr int ep = e0 - 2, t = (ep >> 2) & 1;
            v_cvt(hp[t][s < 2 ? 2 * ((ep >> 3) & 1) + ((ep >> 1) & 1) : ((ep >> 1) & 1)], hh[0], hh[1]);
            if constexpr (s < 2 && (e0 & 15) == 0) {
#pragma unroll
              for (int tt = 0; tt < 2; tt++)
                st16_sc1(rsHT, l16, htw + tt * HTB + (unsigned)(8 * s + 2 * cgl + (ep >> 4)) * 1024u, (u32x4){hp[tt][0], hp[tt][1], hp[tt][2], hp[tt][3]});
            }
            if constexpr (s == 2 && e0 == 8) {
#pragma unroll
              for (int tt = 0; tt < 2; tt++) st8_sc1(rsHT, l16, htw + tt * HTB + (unsigned)((16 + (cgl >> 1)) * 1024 + (cgl & 1) * 8), (u32x2){hp[tt][0], hp[tt][1]});
            }
          }
        }
        if constexpr (e0 < N) {
          if constexpr (ph == 2 || ph == 6) v_exp2(te);
          if constexpr (ph == 3) { v_add1(te); v_mul(pp, G(IC<e0>{}), Uv(IC<e0>{})); }
          if constexpr (ph == 4 || ph == 8) v_rcp(te);
          if constexpr (ph == 5) { v_mul(hh[0], pp, te); v_mulc(te, G(IC<e1>{})); }
          if constexpr (ph == 7) { v_add1(te); v_mul(pp, G(IC<e1>{}), Uv(IC<e1>{})); }
        }
      }
    });
    if constexpr (sl == S1A - 12) {
      int tl = tid; tie(tl);
      const int lane = tl & 63;
      csa = lds0 + LDS_CST + (lane & 15) * 8; cla = (unsigned)(lane < 48 ? lane : lane - 16) * 4u;
      cs48 = csa + (cla >> 6) * 2048;
      q1 = lane & 16; q2 = lane & 32;
    }
    if constexpr (sl == S1A - 6 || sl == S1A + 2 || sl == S1A + 10) {
      constexpr int k = (sl - (S1A - 6)) / 8;
#pragma unroll
      for (int i = 0; i < 4; i++) { const f32x2 m = CPAIR(csa + k * 2048, 1 + i); cm[k][2 * i] = m[0]; cm[k][2 * i + 1] = m[1]; }
    }
    if constexpr (sl >= S1A && sl < S1A + 24) {
      constexpr int r = sl - S1A, k = r / 8, j = (r % 8) >> 1, t = r & 1;
      if constexpr (j == 0) cmul(se[k][t], cm[k][6], cm[k][7], tac[2 + k][t][0]);
      else cmac(se[k][t], cm[k][2 * (3 - j)], cm[k][2 * (3 - j) + 1], tac[2 + k][t][j]);
    }
    if constexpr (sl == S1A + 36) {
#pragma unroll
      for (int k = 0; k < 3; k++) { const f32x2 a4 = CPAIR(csa + k * 2048, 6); ca4[k][0] = a4[0]; ca4[k][1] = a4[1]; }
    }
    if constexpr (sl == SCM - 4) {
#pragma unroll
      for (int k = 0; k < 3; k++) { const f32x2 mm = CPAIR(csa + k * 2048, 1); cin[k][0] = mm[0]; cin[k][1] = mm[1]; }
    }
    if constexpr (sl >= S1A + 24 && sl < S1A + 42) {
      constexpr int r = sl - (S1A + 24), st = r / 6, k = (r % 6) >> 1, t = r & 1;
      if constexpr (st == 0) {
        const unsigned pu = __float_as_uint(se[k][t]);
        const auto s16 = __builtin_amdgcn_permlane16_swap(pu, pu, false, false);
        eg4[k][t][0] = __uint_as_float(s16[0]); eg4[k][t][1] = __uint_as_float(s16[1]);
      } else {
        const unsigned pu = __float_as_uint(eg4[k][t][st - 1]);
        const auto sw = __builtin_amdgcn_permlane32_swap(pu, pu, false, false);
        eg4[k][t][st - 1] = __uint_as_float(sw[0]); eg4[k][t][st + 1] = __uint_as_float(sw[1]);
      }
    }
    if constexpr (sl >= S1A + 42 && sl < S1A + 63) {
      constexpr int r = sl - (S1A + 42), sgm = 1 + r / 3, k = r % 3;
      cmac(eg4[k][sgm >> 2][sgm & 3], ca4[k][0], ca4[k][1], eg4[k][(sgm - 1) >> 2][(sgm - 1) & 3]);
    }
    if constexpr (sl == S1A + 63) {
#pragma unroll
      for (int k = 0; k < 3; k++) *(LAS float*)(lds0 + LDS_WTOT + (8 + w) * 192 + k * 64 + (cla & 60)) = eg4[k][1][3];
    }
    if constexpr (sl >= S1SEL && sl < S1SEL + 6) {
      constexpr int r = sl - S1SEL, k = r >> 1, t = r & 1;
      const float lo = q1 ? eg4[k][t][0] : (t ? eg4[k][0][3] : 0.f), hi = q1 ? eg4[k][t][2] : eg4[k][t][1];
      spre[k][t] = q2 ? hi : lo;
    }
    if constexpr (sl >= SCM && sl < SCM + 24) {
      constexpr int r = sl - SCM, k = r / 8, t = (r >> 2) & 1, j = r & 3;
      cmul(sv[k][t][j], cin[k][0], cin[k][1], tac[2 + k][t][j]);
    }
    if constexpr (sl == SFIN - 8) {
      const f32x2 a = CPAIR(cs48, 13), p0 = CPAIR(cs48, (w & 1) ? 10 : 5), p1 = CPAIR(cs48, (w & 2) ? 11 : 5), p2 = CPAIR(cs48, (w & 4) ? 12 : 5);
      c256[0] = a[0]; c256[1] = a[1]; cpw[0][0] = p0[0]; cpw[0][1] = p0[1]; cpw[1][0] = p1[0]; cpw[1][1] = p1[1]; cpw[2][0] = p2[0]; cpw[2][1] = p2[1];
    }
    if constexpr (sl >= SFIN - 6 && sl < SFIN - 3) {
      constexpr int k = sl - (SFIN - 6);
      unsigned lz = l16; tie(lz);
      const f32x2 a1 = CPAIR(csa + k * 2048, 0), pq = CPAIR(csa + k * 2048, 5 + (lz >> 8)), a16 = CPAIR(csa + k * 2048, 9);
      cc1[k][0] = a1[0]; cc1[k][1] = a1[1]; cin[k][0] = pq[0]; cin[k][1] = pq[1]; ca4[k][0] = a16[0]; ca4[k][1] = a16[1];
    }
    if constexpr (sl >= SFIN && sl < SFIN + 6) {
      constexpr int r = sl - SFIN + 1;
      float n = __uint_as_float(eg[r][0]); cmac(n, c256[0], c256[1], crt); crt = n;
    }
    if constexpr (sl == SFIN + 6) { float n; cmul(n, cpw[0][0], cpw[0][1], crt); crt = n; }
    if constexpr (sl == SFIN + 7) { float n; cmul(n, cpw[1][0], cpw[1][1], crt); crt = n; }
    if constexpr (sl == SFIN + 8) cmac(wpre, cpw[2][0], cpw[2][1], crt);
    if constexpr (sl == SFIN + 9) {
      unsigned lz = l16; tie(lz);
      const unsigned scr = lds0 + LDS_SCR + w * 1024, q = lz >> 8, li = (lz >> 4) & 15;
      fwa = scr + li * 32 + q * 8;
      fra = scr + (q >> 1) * 512 + (8 * (q & 1) + (li >> 2)) * 32 + (li & 3) * 8;
      fvo = (q >> 1) * HTB + ((q & 1) * 16 + li) * 16;
      tie(fwa); tie(fra); tie(fvo);
    }
    if constexpr (sl == SFIN + 10) {
      const unsigned pu = __float_as_uint(wpre);
      const auto s16 = __builtin_amdgcn_permlane16_swap(pu, pu, false, false);
      cz[0] = __uint_as_float(s16[0]); cz[1] = __uint_as_float(s16[1]);
    }
    if constexpr (sl == SFIN + 11) { const unsigned pu = __float_as_uint(cz[0]); const auto sw = __builtin_amdgcn_permlane32_swap(pu, pu, false, false); cz[0] = __uint_as_float(sw[0]); cz[2] = __uint_as_float(sw[1]); }
    if constexpr (sl == SFIN + 12) { const unsigned pu = __float_as_uint(cz[1]); const auto sw = __builtin_amdgcn_permlane32_swap(pu, pu, false, false); cz[1] = __uint_as_float(sw[0]); }
    if constexpr (sl >= SFIN + 13 && sl < SFIN + 16) { constexpr int k = (sl - (SFIN + 13)) == 1 ? 2 : ((sl - (SFIN + 13)) == 2 ? 1 : 0); float n; cmul(n, cin[k][0], cin[k][1], cz[k]); cz[k] = n; }
    if constexpr (sl >= SFIN + 16 && sl < SFIN + 19) { constexpr int k = (sl - (SFIN + 16)) == 1 ? 2 : ((sl - (SFIN + 16)) == 2 ? 1 : 0); cmac(spre[k][1], ca4[k][0], ca4[k][1], cz[k]); }
    if constexpr (sl == SFIN + 19) { asm volatile("v_add_f32 %0, %0, %1" : "+v"(spre[0][0]) : "v"(cz[0])); asm volatile("v_add_f32 %0, %0, %1" : "+v"(spre[1][0]) : "v"(cz[1])); asm volatile("v_add_f32 %0, %0, %1" : "+v"(spre[2][0]) : "v"(cz[2])); }
    if constexpr (sl >= SFIN + 20 && sl < SFIN + 44) {
      constexpr int r = sl - (SFIN + 20), j = r / 6, k = (r % 6) >> 1, t = (r & 1) ^ 1;
      cmac(sv[k][t][j], cc1[k][0], cc1[k][1], j == 0 ? spre[k][t] : sv[k][t][j > 0 ? j - 1 : 0]);
    }
    if constexpr (sl >= SFIN + 44 && sl < SFIN + 53) {
      constexpr int r = sl - (SFIN + 44), k = r / 3, ph = r % 3;
      if constexpr (ph < 2) {
        v_cvt(pkd[0], sv[k][ph][0], sv[k][ph][1]); v_cvt(pkd[1], sv[k][ph][2], sv[k][ph][3]);
        *(LAS u32x2*)(fwa + ph * 512) = (u32x2){pkd[0], pkd[1]};
      } else {
        const u32x4 o = __builtin_bit_cast(u32x4, trfrag<128>(fra));
        st16_sc1(rsHT, (k == 2 && cgl) ? 0x70000000u : fvo, htw + (k < 2 ? (unsigned)((18 + cgl) * 1024 + k * 512) : 22u * 1024u), o);
      }
    }
    if constexpr (sl >= 2 * late_g(9) && sl < 2 * GEND) {
      constexpr int r = sl - 2 * late_g(9), db = r / 14, ph = r % 14;
      if constexpr (db < 8) {
        if constexpr (ph == 0) { unsigned a, c; v_cvt(a, ac2[db][0][0], ac2[db][0][1]); v_cvt(c, ac2[db][0][2], ac2[db][0][3]); pkd[0] = a; pkd[1] = c; }
        if constexpr (ph == 1) { unsigned a, c; v_cvt(a, ac2[db][1][0], ac2[db][1][1]); v_cvt(c, ac2[db][1][2], ac2[db][1][3]); st16_sc1(rsPT, l16, pb + db * 1024, (u32x4){pkd[0], pkd[1], a, c}); }
      }
    }
  };

  unsigned fa[2], eb[2], a2b[2];
  auto frag = [&](auto Gc) -> bf16x8 {
    constexpr int G = decltype(Gc)::value;
    if constexpr (G < GT) {
      constexpr LF f = l_frag(G);
      return trfrag<512>(ab[f.blk & 3] + (p1_off(f.unit) - p1_off(l_base(G / 24))) + (f.ks % 3) * 8192 + (f.blk >> 2) * 4096);
    } else if constexpr (G < G2) {
      constexpr int ks = t_ks(G - GT), blk = t_blk(G - GT), uo = (ks / 6) * 30720, k6 = ks % 6;
      if constexpr (blk < 2) {
        constexpr int o = uo - (ks >= 12 ? 30720 : 0) + (k6 * 2 + blk) * 1024;
        s16x4 a = __builtin_amdgcn_ds_read_tr16_b64_v4i16((LAS s16x4*)(fa[0] + o));
        s16x4 c = __builtin_amdgcn_ds_read_tr16_b64_v4i16((LAS s16x4*)(fa[1] + o));
        return __builtin_bit_cast(bf16x8, __builtin_shufflevector(a, c, 0, 1, 2, 3, 4, 5, 6, 7));
      } else return ldsfrag(eb[k6 & 1] + uo - (ks >= 12 ? 30720 : 0) + 12288 + (k6 >> 1) * 6144 + (blk - 2) * 2048);
    } else {
      constexpr int U = (G - G2) / 18, g = (G - G2) % 18;
      return ldsfrag(a2b[(U % 5) < 3] + ((U % 5) < 3 ? p2_off(U) - 92160 : p2_off(U)) + g * 1024);
    }
  };
  auto addr_step = [&](auto Gc) {
    constexpr int G = decltype(Gc)::value;
    if constexpr (G < GT && G % 24 == 0 && G > 0 && l_base(G / 24) != l_base(G / 24 - 1)) {
#pragma unroll
      for (int k = 0; k < 4; k++) { ab[k] += (unsigned)(p1_off(l_base(G / 24)) - p1_off(l_base(G / 24 - 1))); tie(ab[k]); }
    }
    if constexpr (G == GT) {
      int tl = tid; tie(tl);
      const int lane = tl & 63, q = lane >> 4, li = lane & 15;
      const unsigned e0 = li * 128 + ((q ^ (li >> 1)) << 4);
      eb[0] = lds0 + e0; eb[1] = lds0 + (e0 ^ 64u); tie(eb[0]); tie(eb[1]);
    }
    if constexpr (G == GT + 36) { eb[0] += 30720; eb[1] += 30720; tie(eb[0]); tie(eb[1]); }
    if constexpr (G == GT + 54) {
      int tl = tid; tie(tl);
      const int lane = tl & 63, q = lane >> 4, li = lane & 15;
      const unsigned f0 = (unsigned)((((8 * q + (li >> 2)) ^ ((q & 1) << 2)) << 5) + 8 * (lane & 3));
      fa[0] = lds0 + f0; fa[1] = lds0 + (f0 ^ 128u); tie(fa[0]); tie(fa[1]);
    }
    if constexpr (G == GT + 78) { fa[0] += 30720; fa[1] += 30720; tie(fa[0]); tie(fa[1]); }
    if constexpr (G == G2) { a2b[0] = lds0 + l16; a2b[1] = lds0 + l16 + 92160; tie(a2b[0]); tie(a2b[1]); }
  };
  auto mma = [&](auto Gc, auto Tc) {
    constexpr int G = decltype(Gc)::value, t = decltype(Tc)::value;
    const bf16x8 a = fr[G % FR];
    if constexpr (G < GT) {
      constexpr LF f = l_frag(G);
      mfma<f.ks == 0>(acc[f.blk][t], a, xf[f.ks][t]);
    } else if constexpr (G < G2) {
      constexpr int ks = t_ks(G - GT), blk = t_blk(G - GT);
      if constexpr (blk < 2) mfma<ks == 0>(tac[blk][t], a, xf[ks][t]);
      else mfma<ks == 0>(tac[blk][t], xf[ks][t], a);
    } else if constexpr (G < late_g(0)) {
      constexpr int U = (G - G2) / 18, g = (G - G2) % 18, kk = g / 9, db = g % 9;
      mfma<(U == 0 && kk == 0)>(ac2[db][t], a, hf[U % HR][kk][t]);
    } else {
      constexpr int f = G - late_g(0), db = f / 7, kk = f % 7;
      mfma<false>(ac2[db][t], a, hl[kk][t]);
    }
  };

  auto sync = [&](auto Gc) {
    constexpr int G = decltype(Gc)::value;
    if constexpr (G < GT && G % 24 == SY1) {
      constexpr int V = G / 24;
      constexpr int need = V + 1 < 12 ? (l_dbl(V + 1) ? (((V + 1) & 1) ? -1 : V + 2) : V + 1) : 12;
      constexpr int ng = need >= 0 ? S1.after(OP_G, need, G) : 63;
      constexpr int xn = V < 3 ? V + 1 : (V == 3 ? 5 : -1);
      constexpr int nx = xn >= 0 ? S1.after(OP_X, xn, G) : 63;
      constexpr int ns = V == 10 ? cmin(S1.after(OP_ST, 0, G), S1.after(OP_PO, 0, G)) : 63;
      waitv<cmin(ng, nx, ns)>();
      if constexpr (V < 3) xtie(IC<V + 1>{});
      if constexpr (V == 3) { xtie(IC<4>{}); xtie(IC<5>{}); }
      if constexpr (V == 10) setflag(rsFL, l16 == 0, flw + cgl * 4);
      bar();
      if constexpr (V == 10) setflag(rsFL, tid == 0, (unsigned)FL_PO + (unsigned)((rt * 4 + cgl) * 8 + sx) * 4u);
      SCHED0;
    }
    if constexpr (G == gt(0, SYT)) { waitv<S1.after(OP_G, 13, G)>(); bar(); SCHED0; }
    if constexpr (G == gt(1, SYT)) { waitv<cmin(S1.after(OP_G, 14, G), S1.after(OP_FL, 0, G))>(); bar(); chk_flag(IC<0>{}); SCHED0; }
    if constexpr (G == gt(2, 2)) { waitv<S1.after(OP_ST, 1, G)>(); setflag(rsFL, l16 == 0, flw + 128 + cgl * 4); SCHED0; }
    if constexpr (G == gt(2, 27)) {
      waitv<cmin(S1.after(OP_A, 0, G), S1.after(OP_B, 0, G))>(); htie(IC<0>{});
      bar(); SCHED0;
    }
    if constexpr (G >= G2 && (G - G2) % 18 == SY2 && (G - G2) / 18 < 11) {
      constexpr int U = (G - G2) / 18;
      constexpr int nab = cmin(S1.after(OP_A, U + 1, G), U + 1 < 8 ? S1.after(OP_B, U + 1, G) : (U == 7 ? S1.after(OP_HL, 0, G) : 63));
      constexpr int nfl = U == 1 ? S1.after(OP_FL, 1, G) : (U == 3 ? cmin(S1.after(OP_ST, 2, G), S1.after(OP_EG, 0, G)) : (U == 5 ? S1.after(OP_FL, 2, G) : 63));
      static_assert(UFIN == 3, "");
      if constexpr (U == 0) asm volatile("s_waitcnt lgkmcnt(0)" ::: "memory");
      waitv<cmin(nab, nfl)>();
      if constexpr (U + 1 < 8) htie(IC<U + 1>{});
      if constexpr (U == 7) {
        tie(hl[0][0]); tie(hl[0][1]); tie(hl[1][0]); tie(hl[1][1]);
        for (unsigned it = 0; it < (1u << 16); ++it) {
          unsigned m = 0;
#pragma unroll
          for (int n = 4; n < 14; n++) { tie(hl[n >> 1][n & 1]); const u32x4 v = __builtin_bit_cast(u32x4, hl[n >> 1][n & 1]); m = max(max(m, max(v[0], v[1])), max(v[2], v[3])); }
          if (__all(m != ~0u)) break;
          __builtin_amdgcn_s_sleep(4);
#pragma unroll
          for (int n = 4; n < 14; n++) gld16_sc1_io(hl[n >> 1][n & 1], hb0 + (size_t)(n & 1) * HTB + (16 + (n >> 1)) * 1024, l16);
          waitv<0>();
        }
#pragma unroll
        for (int n = 4; n < 14; n++) tie(hl[n >> 1][n & 1]);
        chk_flag(IC<3>{});
      }
      if constexpr (U == UFIN) {
        for (unsigned it = 0; it < (1u << 18); ++it) {
          bool ok = true;
#pragma unroll
          for (int r = 0; r < 7; r++) { tie(eg[r]); ok &= (rt - 7 + r < 0) || (eg[r][1] == 1u); }
          if (__all(ok)) break;
          __builtin_amdgcn_s_sleep(1);
#pragma unroll
          for (int r = 0; r < 7; r++) { const int rr = rt - 7 + r;
            asm volatile("global_load_dwordx2 %0, %1, %2 sc1" : "+v"(eg[r]) : "v"(cla * 2), "s"(ws + FL_E + (size_t)((rr < 0 ? 0 : rr) * 32 + cg) * 384) : "memory"); }
          waitv<0>();
        }
#pragma unroll
        for (int r = 0; r < 7; r++) { tie(eg[r]); if (rt - 7 + r < 0) eg[r][0] = 0u; tie(eg[r]); }
        crt = __uint_as_float(eg[0][0]);
        setflag(rsFL, l16 == 0, flw + 256 + cgl * 4);
      }
      bar();
      if constexpr (U == 1) chk_flag(IC<1>{});
      if constexpr (U == 5) chk_flag(IC<2>{});
      if constexpr (U == 0) {
        const f32x2 c = CPAIR(cs48, 10);
        c32[0] = c[0]; c32[1] = c[1];
#pragma unroll
        for (int k = 0; k < 8; k++) tw[k] = *(LAS float*)(lds0 + LDS_WTOT + (w + 1 + k) * 192 + cla);
#pragma unroll
        for (int k = 1; k < 8; k++) { if (k == 7) { wpre = tw[6]; tie(wpre); } dnop(); cmac(tw[k], c32[0], c32[1], tw[k - 1]); }
        __builtin_amdgcn_raw_buffer_store_b64((u32x2){__float_as_uint(tw[7]), 1u}, rsFL, (w == 7 && l16 < 768) ? (unsigned)FL_E + (unsigned)((rt * 32 + cg) * 384) + cla * 2 : 0x7ffffff0u, 0, 16);
      }
      SCHED0;
    }
  };

  if (w < 6) glds_s(ws + WS_CONST + (size_t)(9 * sx + (w < 4 ? 2 * cgl + (w >> 1) : 8)) * 2048 + (w & 1) * 1024, l16, lds0 + LDS_CST + w * 1024);
  SCHED0;
  issue_at(IC<-1>{});
  waitv<S1.after(OP_X, 0, 0)>(); xtie(IC<0>{}); bar(); SCHED0;
  STAMP(1);
  fr[0] = frag(IC<0>{}); fr[1] = frag(IC<1>{});
  SCHED0;

  auto group = [&](auto Gc) {
    constexpr int G = decltype(Gc)::value;
    if constexpr (G == 144) { STAMP(2); }
    if constexpr (G == GT) { STAMP(3); }
    if constexpr (G == G2) { STAMP(4); STAMP(5); }
    if constexpr (G == g2(3, 0)) { STAMP(6); }
    if constexpr (G == g2(8, 0)) { STAMP(7); }
    sync(Gc);
    issue_at(IC<2 * G>{});
    mma(Gc, IC<0>{}); SCHED0;
    if constexpr (G + PD < GEND) { addr_step(IC<G + PD>{}); fr[(G + PD) % FR] = frag(IC<G + PD>{}); SCHED0; }
    side(IC<2 * G>{}); SCHED0;
    mma(Gc, IC<1>{}); SCHED0;
    side(IC<2 * G + 1>{}); SCHED0;
  };
  sfor<0, 13>([&](auto Bc) { sfor<0, 45>([&](auto Jc) { group(IC<decltype(Bc)::value * 45 + decltype(Jc)::value>{}); }); });
  STAMPC(27);
  STAMP(8);

  asm volatile("s_nop 7\n\ts_nop 1" : "+v"(ac2[8][0]), "+v"(ac2[8][1]));
  int tz = tid; tie(tz);
  const int lane = tz & 63, q = lane >> 4, li = lane & 15;
  st16_sc1(rsPT, l16, pb + 8 * 1024, (u32x4){pk2(ac2[8][0][0], ac2[8][0][1]), pk2(ac2[8][0][2], ac2[8][0][3]), pk2(ac2[8][1][0], ac2[8][1][1]), pk2(ac2[8][1][2], ac2[8][1][3])});
  SCHED0;
  const char* ib = ws + WS_PART + (size_t)pobase + (size_t)w * 9216;
#pragma unroll
  for (int db = 8 - PLAG; db < 9; db++) gld16_sc1(pv[db], ib + db * 1024, l16);
  bar();
  STAMP(9);
  {
    constexpr int LROW = 304, LSRC = 32 * LROW;
    for (unsigned it = 0; it < (1u << 16); ++it) {
      waitv<0>();
      unsigned bad = 0;
#pragma unroll
      for (int db = 0; db < 9; db++) {
        tie(pv[db]);
        const unsigned m = max(max(pv[db][0], pv[db][1]), max(pv[db][2], pv[db][3]));
        if (!__all(m != ~0u)) bad |= 1u << db;
      }
      if (!bad) break;
      __builtin_amdgcn_s_sleep(2);
#pragma unroll
      for (int db = 0; db < 9; db++) if (bad & (1u << db)) gld16_sc1_io(pv[db], ib + db * 1024, l16);
    }
    waitv<0>();
#pragma unroll
    for (int db = 0; db < 9; db++) {
      tie(pv[db]);
      *(LAS u32x2*)(lds0 + w * LSRC + li * LROW + db * 32 + q * 8) = (u32x2){pv[db][0], pv[db][1]};
      *(LAS u32x2*)(lds0 + w * LSRC + (16 + li) * LROW + db * 32 + q * 8) = (u32x2){pv[db][2], pv[db][3]};
    }
    asm volatile("s_waitcnt vmcnt(0) lgkmcnt(0)" ::: "memory");
    bar();
    STAMP(10);
#pragma unroll
    for (int r = 0; r < 3; r++) {
      tie(epv[3 * r]); tie(epv[3 * r + 1]); tie(epv[3 * r + 2]);
      const int e = tz + 512 * r;
      if (e < 1152) {
        const int t = e / 36, d4 = e - 36 * t;
        f32x4 sum = zero4;
#pragma unroll
        for (int sr = 0; sr < 8; sr++) {
          const u32x2 p = *(LAS u32x2*)(lds0 + sr * LSRC + t * LROW + d4 * 8);
          sum += (f32x4){blo(p[0]), bhi(p[0]), blo(p[1]), bhi(p[1])};
        }
        const float m = __uint_as_float(epv[3 * r + 2][0]), den = __uint_as_float(epv[3 * r + 2][1]);
        f32x4 o;
#pragma unroll
        for (int j = 0; j < 4; j++) {
          const float xe = (j & 1) ? bhi(epv[3 * r][j >> 1]) : blo(epv[3 * r][j >> 1]);
          const float de = (j & 1) ? bhi(epv[3 * r + 1][j >> 1]) : blo(epv[3 * r + 1][j >> 1]);
          const float xn = rbf(rbf(xe - m) / den);
          o[j] = xe + rbf(xn * de) + sum[j];
        }
        *(f32x4*)(out + (size_t)(erow + t) * DM + cgl * 144 + 4 * d4) = o;
      }
    }
    STAMP(11);
    STAMP_DUMP();
  }
}

extern "C" void kernel_launch(void* const* d_in, const int* in_sizes, int n_in, void* d_out, int out_size, void* d_ws, size_t ws_size,
                              hipStream_t stream) {
  const u16* x = (const u16*)d_in[0]; const u16* lre = (const u16*)d_in[1]; const u16* lim = (const u16*)d_in[2];
  const u16* bre = (const u16*)d_in[3]; const u16* bim = (const u16*)d_in[4]; const u16* cre = (const u16*)d_in[5]; const u16* cim = (const u16*)d_in[6];
  const u16* logdt = (const u16*)d_in[7]; const u16* dsk = (const u16*)d_in[8];
  const u16* wg = (const u16*)d_in[10]; const u16* wu = (const u16*)d_in[11]; const u16* wd = (const u16*)d_in[12];
  char* ws = (char*)d_ws;
  (void)hipFuncSetAttribute((const void*)k_main, hipFuncAttributeMaxDynamicSharedMemorySize, LDS_TOT);
  k0_prep<<<437, 256, 0, stream>>>(x, lre, lim, cre, cim, logdt, wd, ws);
  k_main<<<256, 512, LDS_TOT, stream>>>(x, bre, bim, dsk, wg, wu, ws, (float*)d_out);
}
```

```cpp
#include <hip/hip_runtime.h>
#include <stdint.h>
#include <type_traits>

typedef unsigned short u16;
typedef unsigned long long u64;
typedef __bf16 bf16x8 __attribute__((ext_vector_type(8)));
typedef __bf16 bf16x2 __attribute__((ext_vector_type(2)));
typedef float f32x4 __attribute__((ext_vector_type(4)));
typedef float f32x2 __attribute__((ext_vector_type(2)));
typedef short s16x4 __attribute__((ext_vector_type(4)));
typedef unsigned u32x4 __attribute__((ext_vector_type(4)));
typedef unsigned u32x2 __attribute__((ext_vector_type(2)));
#define LAS __attribute__((address_space(3)))
#define GAS __attribute__((address_space(1)))
#define DI __device__ __forceinline__

constexpr int SEQ = 2048, DM = 576, FF = 4608;
constexpr int KS2 = 23;
constexpr int NKG = 8 * KS2;
constexpr size_t FL_H = 0;
constexpr size_t FL_PO = 32768;
constexpr size_t FL_W = 36864;
constexpr size_t FL_EPOCH = 38912;
constexpr size_t FL_E = 40960;
constexpr size_t FL_S = 139264;
constexpr size_t FLAGS_BYTES = FL_S + 2048 * 8;
constexpr size_t WS_HT = 262144;
constexpr size_t WS_WT = WS_HT + (size_t)128 * NKG * 1024;
constexpr size_t WS_PART = WS_WT + (size_t)4 * NKG * 9 * 1024;
constexpr size_t PART_BYTES = (size_t)8 * 4 * 8 * 8 * 9 * 1024;
constexpr size_t WS_END = WS_PART + PART_BYTES;

DI float bf2f(u16 v) { return __uint_as_float(((unsigned)v) << 16); }
DI float rbf(float f) { return (float)(__bf16)f; }
DI unsigned pk2(float lo, float hi) { f32x2 v = {lo, hi}; bf16x2 c = __builtin_convertvector(v, bf16x2); return __builtin_bit_cast(unsigned, c); }
DI float blo(unsigned u) { return __uint_as_float(u << 16); }
DI float bhi(unsigned u) { return __uint_as_float(u & 0xffff0000u); }

__host__ __device__ inline int wt_frag(int j, int db) { return j < 16 ? j * 9 + db : 144 + db * 7 + (j - 16); }
__host__ __device__ inline int kg_f0(int s, int j) {
  if (j < 16) { int p = j >> 3, cg = 4 * s + ((j & 7) >> 1); return 64 * (2 * cg + p) + 32 * (j & 1); }
  return 4096 + 32 * (2 * s + (j - 16));
}

template <int N> using IC = std::integral_constant<int, N>;
template <int B, int E, class F> DI void sfor(F&& f) { if constexpr (B < E) { f(IC<B>{}); sfor<B + 1, E>(f); } }

DI void glds_sc1(const void* sbase, unsigned voff, unsigned ldsaddr) {
  asm volatile("s_mov_b32 m0, %2\n\ts_nop 0\n\tglobal_load_lds_dwordx4 %0, %1 sc1" :: "v"(voff), "s"(sbase), "s"(ldsaddr) : "memory");
}
DI void gld4(unsigned& dst, const void* sbase, unsigned voff) {
  asm volatile("global_load_dword %0, %1, %2" : "=v"(dst) : "v"(voff), "s"(sbase) : "memory");
}
DI void glds_s(const void* sbase, unsigned voff, unsigned ldsaddr) {
  asm volatile("s_mov_b32 m0, %2\n\ts_nop 0\n\tglobal_load_lds_dwordx4 %0, %1" :: "v"(voff), "s"(sbase), "s"(ldsaddr) : "memory");
}
template <class T> DI void gld16(T& dst, const void* sbase, unsigned voff) {
  asm volatile("global_load_dwordx4 %0, %1, %2" : "=v"(dst) : "v"(voff), "s"(sbase) : "memory");
}
template <class T> DI void gld16_sc1(T& dst, const void* sbase, unsigned voff) {
  asm volatile("global_load_dwordx4 %0, %1, %2 sc1" : "=v"(dst) : "v"(voff), "s"(sbase) : "memory");
}
template <class T> DI void gld16_sc1_io(T& dst, const void* sbase, unsigned voff) {
  asm volatile("global_load_dwordx4 %0, %1, %2 sc1" : "+v"(dst) : "v"(voff), "s"(sbase) : "memory");
}
DI void gld4_sc1(unsigned& dst, const void* sbase, unsigned voff) {
  asm volatile("global_load_dword %0, %1, %2 sc1" : "=v"(dst) : "v"(voff), "s"(sbase) : "memory");
}
DI void gld8_sc1(u32x2& dst, const void* sbase, unsigned voff) {
  asm volatile("global_load_dwordx2 %0, %1, %2 sc1" : "=v"(dst) : "v"(voff), "s"(sbase) : "memory");
}
DI void gld8(u32x2& dst, const void* sbase, unsigned voff) {
  asm volatile("global_load_dwordx2 %0, %1, %2" : "=v"(dst) : "v"(voff), "s"(sbase) : "memory");
}
template <int N> DI void waitv() { static_assert(N >= 0 && N < 64, ""); asm volatile("s_waitcnt vmcnt(%0)" ::"i"(N) : "memory"); }
#define SCHED0 __builtin_amdgcn_sched_barrier(0)
DI void bar() { asm volatile("" ::: "memory"); __builtin_amdgcn_s_barrier(); asm volatile("" ::: "memory"); }
template <class T> DI void tie(T& a) { asm volatile("" : "+v"(a)); }
template <int O2> DI bf16x8 trfrag(unsigned addr) {
  s16x4 a = __builtin_amdgcn_ds_read_tr16_b64_v4i16((LAS s16x4*)(addr));
  s16x4 b = __builtin_amdgcn_ds_read_tr16_b64_v4i16((LAS s16x4*)(addr + O2));
  return __builtin_bit_cast(bf16x8, __builtin_shufflevector(a, b, 0, 1, 2, 3, 4, 5, 6, 7));
}
DI bf16x8 ldsfrag(unsigned addr) { return *(LAS bf16x8*)(addr); }
template <bool FIRST> DI void mfma(f32x4& c, bf16x8 a, bf16x8 b) {
  if constexpr (FIRST) asm volatile("v_mfma_f32_16x16x32_bf16 %0, %1, %2, 0" : "=v"(c) : "v"(a), "v"(b));
  else asm volatile("v_mfma_f32_16x16x32_bf16 %0, %1, %2, %0" : "+v"(c) : "v"(a), "v"(b));
}
DI float silu_mul(float g, float u) {
  const float e = __builtin_amdgcn_exp2f(g * -1.44269504089f);
  return g * u * __builtin_amdgcn_rcpf(1.0f + e);
}
DI unsigned silu_mul2(f32x2 g, f32x2 u) {
  const f32x2 t = g * -1.44269504089f;
  f32x2 e; e.x = __builtin_amdgcn_exp2f(t.x); e.y = __builtin_amdgcn_exp2f(t.y);
  const f32x2 d = e + 1.0f;
  f32x2 r; r.x = __builtin_amdgcn_rcpf(d.x); r.y = __builtin_amdgcn_rcpf(d.y);
  const f32x2 h = (g * u) * r;
  return pk2(h.x, h.y);
}
DI float dpp_ror8(float v) { return __int_as_float(__builtin_amdgcn_update_dpp(0, __float_as_int(v), 0x128, 0xf, 0xf, true)); }
DI void cmac(float& d, float P, float Q, float x) {
  asm volatile("v_fmac_f32 %0, %1, %3\n\tv_fmac_f32_dpp %0, %3, %2 row_ror:8 row_mask:0xf bank_mask:0xf" : "+v"(d) : "v"(P), "v"(Q), "v"(x));
}
DI void cmul(float& d, float P, float Q, float x) {
  asm volatile("v_mul_f32 %0, %1, %3\n\tv_fmac_f32_dpp %0, %3, %2 row_ror:8 row_mask:0xf bank_mask:0xf" : "=&v"(d) : "v"(P), "v"(Q), "v"(x));
}
DI void cfma(float& d, float P, float Q, float x, float c) {
  asm volatile("v_fma_f32 %0, %1, %3, %4\n\tv_fmac_f32_dpp %0, %3, %2 row_ror:8 row_mask:0xf bank_mask:0xf" : "=&v"(d) : "v"(P), "v"(Q), "v"(x), "v"(c));
}
DI void v_mul(float& d, float a, float b) { asm volatile("v_mul_f32 %0, %1, %2" : "=v"(d) : "v"(a), "v"(b)); }
DI void v_mulc(float& d, float a) { asm volatile("v_mul_f32 %0, 0xbfb8aa3b, %1" : "=v"(d) : "v"(a)); }
DI void v_fmac(float& d, float a, float b) { asm volatile("v_fmac_f32 %0, %1, %2" : "+v"(d) : "v"(a), "v"(b)); }
DI void v_fmaa(float& d, float c) { asm volatile("v_fma_f32 %0, %0, %1, %1" : "+v"(d) : "v"(c)); }
DI void v_muli(float& d, float a) { asm volatile("v_mul_f32 %0, %0, %1" : "+v"(d) : "v"(a)); }
DI void lds_add(unsigned addr, float v) { asm volatile("ds_add_f32 %0, %1" :: "v"(addr), "v"(v) : "memory"); }
DI void v_exp2(float& d) { asm volatile("v_exp_f32 %0, %0" : "+v"(d)); }
DI void v_add1(float& d) { asm volatile("v_add_f32 %0, 1.0, %0" : "+v"(d)); }
DI void v_rcp(float& d) { asm volatile("v_rcp_f32 %0, %0" : "+v"(d)); }
DI void v_cvt(unsigned& d, float a, float b) { asm volatile("v_cvt_pk_bf16_f32 %0, %1, %2" : "=v"(d) : "v"(a), "v"(b)); }
DI float wave_sum(float v) {
#define DPPADD(c) v += __int_as_float(__builtin_amdgcn_update_dpp(0, __float_as_int(v), c, 0xf, 0xf, true))
  DPPADD(0x111); DPPADD(0x112); DPPADD(0x114); DPPADD(0x118);
  v += __int_as_float(__builtin_amdgcn_update_dpp(0, __float_as_int(v), 0x142, 0xa, 0xf, false));
  v += __int_as_float(__builtin_amdgcn_update_dpp(0, __float_as_int(v), 0x143, 0xc, 0xf, false));
  return __int_as_float(__builtin_amdgcn_readlane(__float_as_int(v), 63));
}
DI void dnop() { asm volatile("s_nop 1"); }
typedef __amdgpu_buffer_rsrc_t rsrc_t;
DI rsrc_t mkrsrc(const void* p, unsigned bytes) { return __builtin_amdgcn_make_buffer_rsrc((void*)p, 0, bytes, 0x00020000); }
DI unsigned vadd_s(unsigned s, unsigned v) { unsigned o; asm volatile("v_add_u32 %0, %1, %2" : "=v"(o) : "s"(s), "v"(v)); return o; }
DI void st8_sc1(rsrc_t r, unsigned voff, unsigned soff, u32x2 v) { __builtin_amdgcn_raw_buffer_store_b64(v, r, vadd_s(soff, voff), 0, 16); }
DI void st16_sc1(rsrc_t r, unsigned voff, unsigned soff, u32x4 v) { __builtin_amdgcn_raw_buffer_store_b128(v, r, vadd_s(soff, voff), 0, 16); }
DI u32x4 ones4() { u32x4 v; asm volatile("v_mov_b32 %0, -1\n\tv_mov_b32 %1, -1\n\tv_mov_b32 %2, -1\n\tv_mov_b32 %3, -1" : "=v"(v[0]), "=v"(v[1]), "=v"(v[2]), "=v"(v[3])); return v; }
DI void setflag(rsrc_t r, bool sel, unsigned off, unsigned tag) { __builtin_amdgcn_raw_buffer_store_b32(tag, r, sel ? off : 0x7ffffff0u, 0, 16); }
DI void spin_flag(const char* base, unsigned voff, unsigned tag) {
  for (unsigned it = 0; it < (1u << 18); ++it) {
    unsigned v = __hip_atomic_load((GAS unsigned*)(base + voff), __ATOMIC_RELAXED, __HIP_MEMORY_SCOPE_AGENT);
    if (__all(v == tag)) break;
    __builtin_amdgcn_s_sleep(2);
  }
  asm volatile("" ::: "memory");
}

constexpr int PD = 2, FR = PD + 1;
constexpr int SLOT1 = 24576;
constexpr int SLOT2 = 18432;
constexpr int LDS_TOT = 163840;
constexpr int LDS_SCR = 61440;
constexpr int LDS_PK = 122880;
constexpr int LDS_XS = 131072;
constexpr int LDS_ST = 147456;
constexpr int LDS_CS = 151552;
constexpr int LDS_CST = 153600;
constexpr int LDS_WTOT = LDS_CST + 6144;

#define STAMP(k)
#define STAMPC(k)
#define STAMP_DUMP()

constexpr int p1_off(int v) { return v < 10 ? (v % 5) * SLOT1 : (v == 10 ? 5 * SLOT1 : (v == 11 ? 0 : SLOT1 + (v - 12) * 30720)); }
constexpr int p2_off(int u, int g) {
  const int k = u % 5;
  return k == 0 ? 116736 + g * 1024 : k == 1 ? g * 1024 : k == 2 ? (g < 12 ? 135168 + g * 1024 : 18432 + (g - 12) * 1024) : 24576 + (k - 3) * SLOT2 + g * 1024;
}

constexpr int GT = 288, G2 = 378, GEND = 585;
constexpr int gl(int v, int g) { return v * 24 + g; }
constexpr int gt(int u, int g) { return GT + u * 30 + g; }
constexpr int g2(int u, int g) { return G2 + u * 18 + g; }
constexpr int SY1 = 8, SYT = 2, SY2 = 4;
constexpr int late_g(int f) { return g2(8, f); }
constexpr int PLAG = 4;

constexpr int SQC = 2 * (G2 + 9);
constexpr int SILU0[3] = {2 * (gl(4, 0) + 12), 2 * (gl(10, 0) + 12), SQC + 8};
constexpr int SILUN[3] = {32, 32, 8};
constexpr int silu_end(int s, int nel) { return SILU0[s] + 9 * (nel / 2) + 1; }
constexpr int S1R = 2 * (GT + 55);
constexpr int S1A = S1R + 12;
constexpr int S1SEL = 2 * g2(1, 14) + 2;
constexpr int SCM = S1SEL + 6;
constexpr int UFIN = 3;
constexpr int SFIN = 2 * g2(UFIN, SY2);
static_assert(S1A + 63 < 2 * g2(0, SY2) && silu_end(2, 8) < S1SEL && SCM + 24 < SFIN - 8, "");
constexpr int csx_n(int v) { return v == 16 ? 2 : 3; }
constexpr int csx_g(int v, int n) {
  const int base[17] = {gl(0, 4), gl(0, 19), gl(1, 12), gl(2, 11), gl(3, 0), gl(3, 3), gl(7, 16), gl(7, 19), gl(8, 1), gl(8, 12), gl(9, 3), gl(8, 18),
                        GT + 27, GT + 30, GT + 34, GT + 37, GT + 40};
  const int step = (v == 4 || v == 5 || v == 6 || v == 7 || v == 10 || v >= 12) ? 1 : 2;
  return base[v] + n * step;
}
constexpr int CSF[3] = {gl(3, SY1), gl(9, SY1), GT + 44};
constexpr int GPC = gl(8, 23);
constexpr int GPK = gl(7, 14);
constexpr int GSYC = GT + 46;
constexpr int xt_g(int c) { const int g[9] = {-1, gl(0, 9), gl(1, 2), gl(1, 18), gl(2, 6), gl(2, 22), gl(3, 16), gl(4, 0), gl(4, 4)}; return g[c]; }
constexpr int csx_at(int G) { for (int v = 0; v < 17; v++) for (int n = 0; n < csx_n(v); n++) if (csx_g(v, n) == G) return v * 4 + n; return -1; }
constexpr int xt_at(int G) { for (int c = 1; c < 9; c++) if (xt_g(c) == G) return c; return -1; }
constexpr int silu_at(int sl) { for (int k = 0; k < 3; k++) if (sl >= SILU0[k] && sl - SILU0[k] <= 9 * (SILUN[k] / 2) + 1) return k; return -1; }

enum { OP_G = 0, OP_X, OP_ST, OP_PO, OP_PH, OP_A, OP_B, OP_FL, OP_EP, OP_HL, OP_PL, OP_EG, OP_SG, OP_PK };
struct Op { int pos, kind, a, n; };
struct Sched {
  Op op[320]; int cnt;
  constexpr void add(int pos, int kind, int a, int n) {
    int i = cnt; while (i > 0 && op[i - 1].pos > pos) { op[i] = op[i - 1]; i--; }
    op[i] = Op{pos, kind, a, n}; cnt++;
  }
  constexpr void unit(int g, int v, int step) { for (int n = 0; n < (v < 12 ? 3 : 4); n++) add(2 * (g + n * step), OP_G, v, n); }
  constexpr Sched() : op{}, cnt(0) {
    for (int n = 0; n < 4; n++) add(-3, OP_X, 0, n);
    for (int n = 0; n < 3; n++) add(-3, OP_G, 0, n);
    for (int n = 0; n < 6; n++) add(-2, OP_ST, 8, n);
    for (int n = 0; n < 4; n++) add(-1, OP_X, 1, n);
    for (int n = 0; n < 3; n++) add(-1, OP_G, 1, n);
    for (int n = 0; n < 2; n++) add(-1, OP_ST, 7, n);
    add(-1, OP_ST, 6, 0);
    for (int n = 0; n < 4; n++) add(2 * gl(5, 10 + 3 * n), OP_PK, 0, n);
    add(2 * GPK + 2, OP_PK, 1, 0);
    add(2 * GPC - 1, OP_ST, 9, 0);
    { const int g[7] = {gl(0, 0), gl(0, 15), gl(1, 0), gl(1, 14), gl(2, 4), gl(2, 16), gl(3, 6)};
      for (int c = 2; c < 9; c++) for (int n = 0; n < 4; n++) add(2 * (g[c - 2] + n), OP_X, c, n); }
    unit(gl(0, 4), 2, 1);
    for (int n = 0; n < 4; n++) add(2 * GPK - 1, OP_ST, 4, n);
    add(2 * gl(1, 5), OP_SG, 0, 0);
    unit(gl(0, 9), 3, 2); unit(gl(0, 21), 4, 1); unit(gl(1, 9), 5, 1); unit(gl(2, 10), 6, 2); unit(gl(3, 17), 7, 2);
    unit(gl(4, 10), 8, 4); unit(gl(6, 10), 9, 2); unit(gl(6, 16), 11, 2); unit(gl(8, 9), 10, 1); unit(gl(8, 13), 12, 3); unit(gl(9, 10), 13, 3); unit(gl(10, 10), 14, 3);
    for (int n = 0; n < 3; n++) add(2 * gl(4, 1 + 2 * n), OP_PH, 0, n);
    for (int n = 0; n < 9; n++) add(2 * (gl(4, 21) + 13 * n), OP_PO, 0, n);
    for (int s = 0; s < 2; s++) for (int h = 0; h < 2; h++) for (int t = 0; t < 2; t++) add((silu_end(s, 16 * (h + 1)) / 2) * 2 + 1, OP_ST, s, 2 * h + t);
    for (int t = 0; t < 2; t++) add((silu_end(2, 8) / 2) * 2 + 1, OP_ST, 2, t);
    add(2 * gl(11, 16), OP_FL, 4, 0);
    add(2 * gt(0, 6), OP_FL, 0, 0);
    for (int n = 0; n < 2; n++) { add(2 * gt(0, 20 + 4 * n), OP_A, 0, n); add(2 * gt(1, 8 + 4 * n), OP_A, 1, n); add(2 * gt(1, 16 + 4 * n), OP_A, 2, n); }
    for (int n = 0; n < 4; n++) { add(2 * gt(2, 3 + 2 * n), OP_B, 0, n); add(2 * gt(2, 11 + 2 * n), OP_B, 1, n); add(2 * gt(2, 19 + 3 * n), OP_B, 2, n); }
    add(2 * g2(0, 1), OP_FL, 1, 0);
    for (int u = 0; u < 9; u++) {
      const int U = u + 3;
      for (int n = 0; n < (U < 11 ? 2 : 1); n++) add(2 * g2(u, 5 + 2 * n), OP_A, U, n);
      if (U < 8) for (int n = 0; n < 4; n++) add(2 * g2(u, 9 + 2 * n), OP_B, U, n);
    }
    for (int n = 0; n < 7; n++) add(2 * g2(UFIN - 1, 10) + 0, OP_EG, 0, n);
    add(2 * g2(4, 6), OP_FL, 2, 0);
    add(2 * g2(4, 8), OP_FL, 3, 0);
    for (int n = 0; n < 4; n++) add(2 * g2(5, 9 + 2 * n), OP_HL, 0, n);
    for (int n = 4; n < 14; n++) add(2 * g2(6, 8 + (n - 4)), OP_HL, 0, n);
    for (int n = 0; n < 6; n++) add(2 * g2(8, 6 + 2 * n), OP_EP, 0, n);
    for (int db = 0; db < 8; db++) add(2 * (late_g(7 * db + 6) + 3) + 1, OP_ST, 3, db);
    for (int db = 0; db + PLAG < 8; db++) add(2 * (late_g(7 * (db + PLAG) + 6) + 5), OP_PL, 0, db);
  }
  constexpr int first_at(int pos) const { int i = 0; while (i < cnt && op[i].pos < pos) i++; return i; }
  constexpr int after(int kind, int a, int g) const {
    int last = -1;
    for (int i = 0; i < cnt; i++) if (op[i].kind == kind && op[i].a == a) last = i;
    int n = 0;
    for (int i = last + 1; i < cnt; i++) if (op[i].pos < 2 * g) n++;
    return n;
  }
};
constexpr Sched S1{};
constexpr int cmin(int a, int b) { return a < b ? a : b; }
constexpr int cmin(int a, int b, int c) { return cmin(cmin(a, b), c); }
constexpr bool l_dbl(int v) { return v == 4 || v == 5 || v == 6 || v == 7 || v >= 10; }
struct LF { int unit, ks, blk; };
constexpr LF l_frag(int G) {
  const int v = G / 24, g = G % 24;
  if (!l_dbl(v)) return LF{v, 3 * (v % 6) + (g >> 3), g & 7};
  const int v0 = v & ~1, r = (v & 1) * 24 + g, p = r / 12, k6 = (r % 12) / 2;
  return LF{v0 + k6 / 3, 3 * (v0 % 6) + k6, p + 4 * (r & 1)};
}
constexpr int l_prev_unit(int G) {
  for (int g = G - 1; g >= 0; g--) if ((l_frag(g).blk & 3) == (l_frag(G).blk & 3)) return l_frag(g).unit;
  return 0;
}
struct Corr {
  int item[40];
  constexpr Corr(int pass) : item{} {
    const int g0 = gl(6 * pass + 3, SY1 + 2);
    bool done[16] = {};
    const int order[8] = {0, 4, 1, 5, 2, 6, 3, 7};
    for (int k = 0; k < 40; k++) {
      item[k] = -1;
      const int G = g0 + k;
      for (int o = 0; o < 16 && item[k] < 0; o++) {
        const int bk = order[o >> 1], it = bk * 2 + (o & 1);
        if (done[it]) continue;
        if (l_frag(G).blk == bk || l_frag(G - 1).blk == bk || l_frag(G - 2).blk == bk || l_frag(G + 1).blk == bk) continue;
        item[k] = it; done[it] = true;
      }
    }
  }
  constexpr int last() const { int l = 0; for (int k = 0; k < 40; k++) if (item[k] >= 0) l = k; return l; }
  constexpr int count() const { int c = 0; for (int k = 0; k < 40; k++) c += item[k] >= 0; return c; }
};
constexpr Corr CR{0};
static_assert(CR.count() == 16 && gl(3, SY1 + 2) + CR.last() < gl(4, 12), "");
constexpr int t_ks(int r) { return r < 54 ? r / 3 : (r - 54) / 2; }
constexpr int t_blk(int r) { return r < 54 ? 2 + r % 3 : (r - 54) & 1; }

__global__ __launch_bounds__(512, 2) void k_main(const u16* __restrict__ x, const u16* __restrict__ lre, const u16* __restrict__ lim,
                                                 const u16* __restrict__ bre, const u16* __restrict__ bim, const u16* __restrict__ cre,
                                                 const u16* __restrict__ cim, const u16* __restrict__ logdt, const u16* __restrict__ dsk,
                                                 const u16* __restrict__ wg, const u16* __restrict__ wu, const u16* __restrict__ wd,
                                                 char* __restrict__ ws, float* __restrict__ out) {
  extern __shared__ __attribute__((aligned(16))) char lds[];
  const int tid = threadIdx.x, w = __builtin_amdgcn_readfirstlane(tid >> 6);
  const int b = blockIdx.x, sx = b & 7, idx = b >> 3;
  unsigned lds0 = (unsigned)(size_t)(LAS char*)lds;
  asm volatile("" : "+s"(lds0));
  const int rt = idx & 7, cgl = idx >> 3, cg = sx * 4 + cgl;
  const rsrc_t rsW = mkrsrc(ws, (unsigned)WS_END);
  const unsigned htw = (unsigned)WS_HT + (unsigned)(((rt * 16 + 2 * w) * 8 + sx) * KS2) * 1024u;
  constexpr unsigned HTB = NKG * 1024;
  const unsigned flw = (unsigned)FL_H + (unsigned)((((rt * 8 + sx) * 4) * 8 + w) * 4) * 4u;
  const f32x4 zero4 = {0.f, 0.f, 0.f, 0.f};
  const unsigned l16 = (tid & 63) * 16;
  unsigned tag = 0;
  STAMP(0);

  f32x4 acc[8][2];
  f32x4 tac[5][2];
  f32x4 ac2[9][2];
  bf16x8 fr[FR];
  bf16x8 xf[18][2];
  constexpr int HR = 4;
  bf16x8 hf[HR][2][2];
  bf16x8 hl[7][2];
  u32x4 pv[9];
  unsigned flv[5] = {0, 0, 0, 0, 0};
  u32x2 epv[6];
  u32x2 eg[7];
  unsigned wvoff, ab[4], tvA = 0, tvF = 0;
  const char* wsrc = (const char*)((w >> 2) ? wu : wg) + (size_t)cg * 256;
  {
    const int lane = tid & 63, q = lane >> 4, li = lane & 15;
    const int rg = w & 3, r8 = lane >> 3, ch = lane & 7;
    const int g = ((lane >> 4) & 1) | ((rg & 1) << 1);
    wvoff = (unsigned)((8 * rg + r8) * (FF * 2) + (((ch >> 1) ^ g) << 5) + ((ch & 1) << 4));
    const int r4 = li >> 2, cc = lane & 3;
    const int gr = ((r4 >> 1) & 1) | ((q & 1) << 1);
    const unsigned a0 = (unsigned)((8 * q + r4) * 128 + (gr << 5) + 8 * cc);
#pragma unroll
    for (int k = 0; k < 4; k++) { ab[k] = lds0 + (a0 ^ (k << 5)); tie(ab[k]); }
    if (tid < 384) *(LAS float*)(lds0 + LDS_WTOT + tid * 4) = 0.f;
  }
  const char* xb = (const char*)x + (size_t)(rt * 256 + 32 * w) * (DM * 2);
  u32x4 xr[9][4];
  unsigned xvo, xwa, xra;
  {
    const int lane = tid & 63, q = lane >> 4, li = lane & 15, r8 = lane >> 3;
    xvo = (unsigned)(r8 * (DM * 2) + (lane & 7) * 16);
    xwa = lds0 + LDS_XS + w * 2048 + r8 * 128 + (((lane & 7) ^ (r8 >> 1)) << 4);
    xra = lds0 + LDS_XS + w * 2048 + li * 128 + ((q ^ (li >> 1)) << 4);
    tie(xvo); tie(xwa); tie(xra);
  }
  u32x2 sgr;
  const unsigned pobase = (unsigned)WS_PART + (unsigned)(((rt * 4 + cgl) * 8 + sx) * 72) * 1024u;
  const unsigned pb = (unsigned)WS_PART + (unsigned)((((rt * 4 + cgl) * 8 + w) * 8 + sx) * 9) * 1024u;
  const char* wtb = ws + WS_WT + ((size_t)cgl * NKG + sx * KS2) * 9 * 1024 + w * 1024;
  const char* hb0 = ws + htw;
  const int erow = rt * 256 + 32 * sx;
  const int blk3 = w < 4 ? 2 * cgl + (w >> 1) : 8;
  const char* tB = (const char*)((w & 1) ? bim : bre) + (size_t)(8 * (9 * sx + blk3)) * (DM * 2);
  const size_t fcol = (size_t)(4096 + 64 * sx + 16 * cgl) * 2;
  auto fsrc = [&](int fp) { return (const char*)((fp & 1) ? wu : wg) + fcol + (size_t)(fp >> 1) * 32 * (FF * 2); };
  unsigned cla = 0;

  u32x4 cfr; unsigned pl[3];
  const int widx = w * 32 + idx;
  const int wi = widx < 162 ? widx : widx - 162, pj = wi / 9, pln = wi - 9 * pj;
  const int ci = widx < 180 ? widx : widx - 180, cd16 = ci / 5, cjj = ci - 5 * cd16;
  auto issue = [&](auto Ic) {
    constexpr Op o = S1.op[decltype(Ic)::value];
    if constexpr (o.kind == OP_G) {
      if constexpr (o.a < 12) {
        glds_s(wsrc + (o.a / 6) * 128 + (size_t)(3 * (o.a % 6) + o.n) * 32 * (FF * 2), wvoff, lds0 + p1_off(o.a) + o.n * 8192 + w * 1024);
      } else {
        constexpr int U = o.a - 12;
        if constexpr (U == 0 && o.n == 0) {
          unsigned l_ = l16; tie(l_); const int tl = (int)(l_ >> 4) + 64 * w;
          const int lane = tl & 63, p = lane >> 1;
          tvF = (unsigned)((p ^ (((p >> 3) & 1) << 2)) * (FF * 2) + ((lane & 1) << 4));
          const unsigned vb = (unsigned)((lane >> 3) * (DM * 2) + (((lane & 7) ^ (4 * (w & 1) + (lane >> 4))) << 4));
          tvA = w < 6 ? vb : tvF;
        }
        const unsigned tb = lds0 + p1_off(o.a);
        constexpr size_t FU = (size_t)U * 192 * (FF * 2);
        if constexpr (o.n < 3) {
          const int fp = 6 + 2 * o.n + (w & 1);
          const char* src = w < 6 ? tB + U * 384 + o.n * 128 : fsrc(fp) + FU;
          const unsigned dst = w < 6 ? tb + 12288 + o.n * 6144 + w * 1024 : tb + fp * 1024;
          glds_s(src, tvA, dst);
        } else {
          const int fp = w;
          glds_s(fsrc(fp) + FU, tvF, tb + fp * 1024);
        }
      }
    } else if constexpr (o.kind == OP_X) {
      gld16(xr[o.a][o.n], xb + (size_t)o.n * 8 * (DM * 2) + o.a * 128, xvo);
    } else if constexpr (o.kind == OP_PK) {
      unsigned l_ = l16; tie(l_);
      const int lane = l_ >> 4, q = lane >> 4, li = lane & 15;
      if constexpr (o.a == 0) {
        glds_s((const char*)wd + (size_t)(kg_f0(sx, pj) + 8 * o.n) * (DM * 2) + pln * 128, (unsigned)((lane >> 3) * (DM * 2) + (lane & 7) * 16),
               lds0 + (w < 6 ? LDS_PK + w * 4096 + o.n * 1024 : LDS_TOT));
      } else {
        const int cbl = 2 * cjj + (q >> 1);
        const char* cp = (const char*)((q & 1) ? cim : cre) + (size_t)(16 * cd16 + li) * (DM * 2) + 16 * (9 * sx + (cbl < 9 ? cbl : 8));
        asm volatile("global_load_dwordx4 %0, %1, off" : "=v"(cfr) : "v"(cp) : "memory");
      }
    } else if constexpr (o.kind == OP_SG) {
      gld8_sc1(sgr, ws + FL_S + (size_t)(rt * 256 + 32 * w) * 8, (l16 >> 1) & 248);
    } else if constexpr (o.kind == OP_PO) {
      st16_sc1(rsW, l16, pobase + (unsigned)(o.n * 8 + w) * 1024u, ones4());
    } else if constexpr (o.kind == OP_PH) {
      if constexpr (o.n < 2) st16_sc1(rsW, l16, htw + o.n * HTB + (unsigned)(18 + cgl) * 1024u, ones4());
      else {
        const unsigned f = (l16 & 512) ? 0u : ~0u;
        st16_sc1(rsW, cgl < 2 ? l16 : 0x70000000u, htw + (cgl & 1) * HTB + 22u * 1024u, (u32x4){f, f, f, f});
      }
    } else if constexpr (o.kind == OP_A) {
      constexpr int U = o.a;
      const char* src = wtb + (size_t)U * SLOT2;
      const unsigned dst = lds0 + w * 1024;
      glds_sc1(src + o.n * 8192, l16, dst + ((o.n == 1 && w >= 4) ? p2_off(U, 12) - 4096 : p2_off(U, 8 * o.n)));
      if constexpr (U < 11 && o.n == 1) { if (w < 2) glds_sc1(src + 16384, l16, dst + p2_off(U, 16)); }
      if constexpr (U == 11) { if (w < 1) glds_sc1(src + 8192, l16, dst + p2_off(U, 8)); }
    } else if constexpr (o.kind == OP_B) {
      gld16_sc1(hf[o.a % HR][o.n >> 1][o.n & 1], hb0 + (size_t)(o.n & 1) * HTB + (2 * o.a + (o.n >> 1)) * 1024, l16);
    } else if constexpr (o.kind == OP_HL) {
      gld16_sc1(hl[o.n >> 1][o.n & 1], hb0 + (size_t)(o.n & 1) * HTB + (16 + (o.n >> 1)) * 1024, l16);
    } else if constexpr (o.kind == OP_PL) {
      gld16_sc1(pv[o.n], ws + (size_t)pobase + (size_t)(w * 9 + o.n) * 1024, l16);
    } else if constexpr (o.kind == OP_EG) {
      const int r = rt - 7 + o.n;
      gld8_sc1(eg[o.n], ws + FL_E + (size_t)((r < 0 ? 0 : r) * 32 + cg) * 384, cla * 2);
    } else if constexpr (o.kind == OP_FL) {
      unsigned lz = l16; tie(lz);
      if constexpr (o.a < 3) gld4_sc1(flv[o.a], ws + flw + 128 * o.a, (lz >> 2) & 12);
      else if constexpr (o.a == 3) gld4_sc1(flv[3], ws + FL_PO + (size_t)((rt * 4 + cgl) * 8 + w) * 4, lz & 0);
      else gld4_sc1(flv[4], ws + FL_W + (size_t)sx * 128, (lz >> 2) & 124);
    } else if constexpr (o.kind == OP_EP) {
      constexpr int r = o.n / 2;
      unsigned l_ = l16; tie(l_); const int tl = (int)(l_ >> 4) + 64 * w;
      const int e = tl + 512 * r < 1152 ? tl + 512 * r : 1151, t = e / 36, d4 = e - 36 * t;
      if constexpr (o.n % 2 == 0) gld8(epv[o.n], x + (size_t)erow * DM + cgl * 144, (unsigned)(t * DM + 4 * d4) * 2u);
      else gld8(epv[o.n], dsk + cgl * 144, (unsigned)(4 * d4) * 2u);
    }
  };
  auto issue_at = [&](auto Pc) {
    constexpr int i0 = S1.first_at(decltype(Pc)::value), i1 = S1.first_at(decltype(Pc)::value + 1);
    sfor<i0, i1>([&](auto Ic) { issue(Ic); SCHED0; });
  };
  auto xconv = [&](auto Cc) {
    constexpr int c = decltype(Cc)::value;
#pragma unroll
    for (int t = 0; t < 2; t++) {
      tie(xr[c][2 * t]); tie(xr[c][2 * t + 1]);
      *(LAS u32x4*)(xwa) = xr[c][2 * t];
      *(LAS u32x4*)((xwa ^ 64u) + 1024) = xr[c][2 * t + 1];
      xf[2 * c][t] = ldsfrag(xra);
      xf[2 * c + 1][t] = ldsfrag(xra ^ 64u);
      asm volatile("" ::: "memory");
    }
  };
  auto htie = [&](auto Uc) { constexpr int U = decltype(Uc)::value; tie(hf[U % HR][0][0]); tie(hf[U % HR][0][1]); tie(hf[U % HR][1][0]); tie(hf[U % HR][1][1]); };
  auto chk_flag = [&](auto Ac) { constexpr int a = decltype(Ac)::value; tie(flv[a]);
    if (!__all(flv[a] == tag)) {
      unsigned lz = l16; tie(lz);
      if constexpr (a < 3) spin_flag(ws + flw + 128 * a, (lz >> 2) & 12, tag);
      else if constexpr (a == 3) spin_flag(ws + FL_PO + (size_t)((rt * 4 + cgl) * 8 + w) * 4, lz & 0, tag);
      else spin_flag(ws + FL_W + (size_t)sx * 128, (lz >> 2) & 124, tag); } };

  float te = 0.f, pp = 0.f, hh[2], sc1[2], sc2[2], sng[2];
  f32x4 csw[2];
  f32x4 csv, tst[2];
  f32x4 csa0, csa1, qs[8];
  bf16x8 selA, selB; unsigned cra = 0, crb = 0;
  unsigned hp[2][4];
  float cm[3][8], ca4[3][2], se[3][2], eg4[3][2][4], spre[3][2], sv[3][2][4], cc1[3][2], cin[3][2];
  float tw[8], c32[2], wpre = 0.f, crt = 0.f, cz[3], cpw[3][2], c256[2];
  unsigned fwa = 0, fra = 0, fvo = 0;
  unsigned cs48 = 0;
#define CPAIR(base, pr) (*(LAS f32x2*)((base) + (pr) * 128))
  unsigned csa = 0, pkd[4];
  bool q1 = false, q2 = false;
  auto side = [&](auto Sc) {
    constexpr int sl = decltype(Sc)::value;
    {
      if constexpr (silu_at(sl) >= 0) {
        constexpr int s = silu_at(sl), r = sl - SILU0[s], N = SILUN[s];
        constexpr int pr2 = r / 9, ph = r % 9;
        auto G = [&](auto Ec) -> float { constexpr int e = decltype(Ec)::value; if constexpr (s < 2) return acc[e >> 3][(e >> 2) & 1][e & 3]; else return tac[0][e >> 2][e & 3]; };
        auto Uv = [&](auto Ec) -> float { constexpr int e = decltype(Ec)::value; if constexpr (s < 2) return acc[4 + (e >> 3)][(e >> 2) & 1][e & 3]; else return tac[1][e >> 2][e & 3]; };
        constexpr int e0 = 2 * pr2, e1 = e0 + 1;
        if constexpr (ph == 0) { if constexpr (pr2 > 0) v_mul(hh[1], pp, te); if constexpr (e0 < N) v_mul(te, G(IC<e0 < N ? e0 : 0>{}), sc1[(e0 >> 2) & 1]); }
        if constexpr (ph == 1) {
          if constexpr (pr2 > 0) {
            constexpr int ep = e0 - 2, t = (ep >> 2) & 1;
            v_cvt(hp[t][s < 2 ? 2 * ((ep >> 3) & 1) + ((ep >> 1) & 1) : ((ep >> 1) & 1)], hh[0], hh[1]);
            if constexpr (s < 2 && (e0 & 15) == 0) {
#pragma unroll
              for (int tt = 0; tt < 2; tt++)
                st16_sc1(rsW, l16, htw + tt * HTB + (unsigned)(8 * s + 2 * cgl + (ep >> 4)) * 1024u, (u32x4){hp[tt][0], hp[tt][1], hp[tt][2], hp[tt][3]});
            }
            if constexpr (s == 2 && e0 == 8) {
#pragma unroll
              for (int tt = 0; tt < 2; tt++) st8_sc1(rsW, l16, htw + tt * HTB + (unsigned)((16 + (cgl >> 1)) * 1024 + (cgl & 1) * 8), (u32x2){hp[tt][0], hp[tt][1]});
            }
          }
        }
        if constexpr (e0 < N) {
          if constexpr (ph == 2 || ph == 6) v_exp2(te);
          if constexpr (ph == 3) { v_fmaa(te, sc2[(e0 >> 2) & 1]); v_mul(pp, G(IC<e0>{}), Uv(IC<e0>{})); }
          if constexpr (ph == 4 || ph == 8) v_rcp(te);
          if constexpr (ph == 5) { v_mul(hh[0], pp, te); v_mul(te, G(IC<e1>{}), sc1[(e0 >> 2) & 1]); }
          if constexpr (ph == 7) { v_fmaa(te, sc2[(e0 >> 2) & 1]); v_mul(pp, G(IC<e1>{}), Uv(IC<e1>{})); }
        }
      }
    }
    if constexpr (sl == SILU0[0] - 4 || sl == SILU0[1] - 4 || sl == SILU0[2] - 4) {
      unsigned lz = l16; tie(lz);
#pragma unroll
      for (int t = 0; t < 2; t++) { const f32x2 f = *(LAS f32x2*)(lds0 + LDS_ST + w * 512 + t * 256 + (lz & 240) + 4); sc1[t] = f[0]; sc2[t] = f[1]; }
    }
    if constexpr (sl == 2 * gl(3, SY1) || sl == 2 * gl(9, SY1) || sl == SQC - 14) {
      unsigned lz = l16; tie(lz);
#pragma unroll
      for (int t = 0; t < 2; t++) sng[t] = *(LAS float*)(lds0 + LDS_ST + w * 512 + t * 256 + (lz & 240));
      cra = lds0 + LDS_CS + (lz >> 8) * 16; tie(cra);
    }
    {
      constexpr int pass = sl / 2 >= gl(6, 0) ? 1 : 0, k = sl / 2 - gl(6 * pass + 3, SY1 + 2), half = sl & 1;
      if constexpr (k >= 0 && k < 40) {
        constexpr int it = CR.item[k];
        if constexpr (it >= 0) { constexpr int bk = it >> 1, t = it & 1; float a0 = acc[bk][t][2 * half], a1 = acc[bk][t][2 * half + 1];
          v_fmac(a0, sng[t], csw[k & 1][2 * half]); v_fmac(a1, sng[t], csw[k & 1][2 * half + 1]); acc[bk][t][2 * half] = a0; acc[bk][t][2 * half + 1] = a1; }
      }
      if constexpr (k >= -2 && k < 38 && half == 1) {
        constexpr int nx = CR.item[k + 2];
        if constexpr (nx >= 0) csw[k & 1] = *(LAS f32x4*)(cra + pass * 512 + ((nx >> 1) >> 2) * 256 + ((nx >> 1) & 3) * 64);
      }
    }
    if constexpr (sl == SQC - 12) {
#pragma unroll
      for (int k = 0; k < 8; k++) qs[k] = *(LAS f32x4*)(cra + 1280 + k * 64);
    }
    if constexpr (sl >= SQC - 8 && sl < SQC) {
      constexpr int r = sl - (SQC - 8);
#pragma unroll
      for (int e = 3 * r; e < 3 * r + 3; e++) { const int k = 2 + e / 4, j = e & 3; float a = qs[k & 1][j]; asm volatile("v_add_f32 %0, %0, %1" : "+v"(a) : "v"(qs[k][j])); qs[k & 1][j] = a; }
      if constexpr (r == 7) { csv = qs[0]; csa0 = qs[1]; }
    }
    if constexpr (sl >= SQC && sl < SQC + 8) {
      constexpr int r = sl - SQC, mt = r >> 2, t = (r >> 1) & 1, half = r & 1;
      float a0 = tac[mt][t][2 * half], a1 = tac[mt][t][2 * half + 1];
      if constexpr (mt == 0) { v_fmac(a0, sng[t], csv[2 * half]); v_fmac(a1, sng[t], csv[2 * half + 1]); }
      else { v_fmac(a0, sng[t], csa0[2 * half]); v_fmac(a1, sng[t], csa0[2 * half + 1]); }
      tac[mt][t][2 * half] = a0; tac[mt][t][2 * half + 1] = a1;
    }
    if constexpr (sl == 2 * GSYC) {
      unsigned lz = l16; tie(lz);
      crb = lds0 + LDS_ST + w * 512 + (lz >> 8) * 64; tie(crb);
#pragma unroll
      for (int t = 0; t < 2; t++)
#pragma unroll
        for (int j = 0; j < 4; j++) tst[t][j] = *(LAS float*)(crb + t * 256 + j * 16);
#pragma unroll
      for (int k = 0; k < 3; k++) csv[k] = *(LAS float*)(lds0 + LDS_CS + 1024 + k * 64 + ((lz >> 2) & 60));
    }
    if constexpr (sl >= 2 * (GT + 48) && sl < 2 * (GT + 54)) {
      constexpr int r = sl / 2 - (GT + 48), k = (r + 1) % 3, t = r / 3, half = sl & 1;
      float a0 = tac[2 + k][t][2 * half], a1 = tac[2 + k][t][2 * half + 1];
      v_fmac(a0, tst[t][2 * half], csv[k]); v_fmac(a1, tst[t][2 * half + 1], csv[k]);
      if constexpr (half) dnop();
      tac[2 + k][t][2 * half] = a0; tac[2 + k][t][2 * half + 1] = a1;
    }
    if constexpr (sl == S1R - 2) {
#pragma unroll
      for (int t = 0; t < 2; t++)
#pragma unroll
        for (int j = 0; j < 4; j++) tst[t][j] = *(LAS float*)(crb + t * 256 + j * 16 + 12);
    }
    if constexpr (sl >= S1R && sl < S1R + 12) {
      constexpr int r = sl - S1R, k = r >> 2, t = (r >> 1) & 1, half = r & 1;
      float a0 = tac[2 + k][t][2 * half], a1 = tac[2 + k][t][2 * half + 1];
      v_muli(a0, tst[t][2 * half]); v_muli(a1, tst[t][2 * half + 1]);
      tac[2 + k][t][2 * half] = a0; tac[2 + k][t][2 * half + 1] = a1;
    }
    if constexpr (sl == S1A - 12) {
      unsigned l_ = l16; tie(l_); const int tl = (int)(l_ >> 4) + 64 * w;
      const int lane = tl & 63;
      csa = lds0 + LDS_CST + (lane & 15) * 8; cla = (unsigned)(lane < 48 ? lane : lane - 16) * 4u;
      cs48 = csa + (cla >> 6) * 2048;
      q1 = lane & 16; q2 = lane & 32;
    }
    if constexpr (sl == S1A - 6 || sl == S1A + 2 || sl == S1A + 10) {
      constexpr int k = (sl - (S1A - 6)) / 8;
#pragma unroll
      for (int i = 0; i < 4; i++) { const f32x2 m = CPAIR(csa + k * 2048, 1 + i); cm[k][2 * i] = m[0]; cm[k][2 * i + 1] = m[1]; }
    }
    if constexpr (sl >= S1A && sl < S1A + 24) {
      constexpr int r = sl - S1A, k = r / 8, j = (r % 8) >> 1, t = r & 1;
      if constexpr (j == 0) cmul(se[k][t], cm[k][6], cm[k][7], tac[2 + k][t][0]);
      else cmac(se[k][t], cm[k][2 * (3 - j)], cm[k][2 * (3 - j) + 1], tac[2 + k][t][j]);
    }
    if constexpr (sl == S1A + 36) {
#pragma unroll
      for (int k = 0; k < 3; k++) { const f32x2 a4 = CPAIR(csa + k * 2048, 6); ca4[k][0] = a4[0]; ca4[k][1] = a4[1]; }
    }
    if constexpr (sl == SCM - 4) {
#pragma unroll
      for (int k = 0; k < 3; k++) { const f32x2 mm = CPAIR(csa + k * 2048, 1); cin[k][0] = mm[0]; cin[k][1] = mm[1]; }
    }
    if constexpr (sl >= S1A + 24 && sl < S1A + 42) {
      constexpr int r = sl - (S1A + 24), st = r / 6, k = (r % 6) >> 1, t = r & 1;
      if constexpr (st == 0) {
        const unsigned pu = __float_as_uint(se[k][t]);
        const auto s16 = __builtin_amdgcn_permlane16_swap(pu, pu, false, false);
        eg4[k][t][0] = __uint_as_float(s16[0]); eg4[k][t][1] = __uint_as_float(s16[1]);
      } else {
        const unsigned pu = __float_as_uint(eg4[k][t][st - 1]);
        const auto sw = __builtin_amdgcn_permlane32_swap(pu, pu, false, false);
        eg4[k][t][st - 1] = __uint_as_float(sw[0]); eg4[k][t][st + 1] = __uint_as_float(sw[1]);
      }
    }
    if constexpr (sl >= S1A + 42 && sl < S1A + 63) {
      constexpr int r = sl - (S1A + 42), sgm = 1 + r / 3, k = r % 3;
      cmac(eg4[k][sgm >> 2][sgm & 3], ca4[k][0], ca4[k][1], eg4[k][(sgm - 1) >> 2][(sgm - 1) & 3]);
    }
    if constexpr (sl == S1A + 63) {
#pragma unroll
      for (int k = 0; k < 3; k++) *(LAS float*)(lds0 + LDS_WTOT + (8 + w) * 192 + k * 64 + (cla & 60)) = eg4[k][1][3];
    }
    if constexpr (sl >= S1SEL && sl < S1SEL + 6) {
      constexpr int r = sl - S1SEL, k = r >> 1, t = r & 1;
      const float lo = q1 ? eg4[k][t][0] : (t ? eg4[k][0][3] : 0.f), hi = q1 ? eg4[k][t][2] : eg4[k][t][1];
      spre[k][t] = q2 ? hi : lo;
    }
    if constexpr (sl >= SCM && sl < SCM + 24) {
      constexpr int r = sl - SCM, k = r / 8, t = (r >> 2) & 1, j = r & 3;
      cmul(sv[k][t][j], cin[k][0], cin[k][1], tac[2 + k][t][j]);
    }
    if constexpr (sl == SFIN - 8) {
      const f32x2 a = CPAIR(cs48, 13), p0 = CPAIR(cs48, (w & 1) ? 10 : 5), p1 = CPAIR(cs48, (w & 2) ? 11 : 5), p2 = CPAIR(cs48, (w & 4) ? 12 : 5);
      c256[0] = a[0]; c256[1] = a[1]; cpw[0][0] = p0[0]; cpw[0][1] = p0[1]; cpw[1][0] = p1[0]; cpw[1][1] = p1[1]; cpw[2][0] = p2[0]; cpw[2][1] = p2[1];
    }
    if constexpr (sl >= SFIN - 6 && sl < SFIN - 3) {
      constexpr int k = sl - (SFIN - 6);
      unsigned lz = l16; tie(lz);
      const f32x2 a1 = CPAIR(csa + k * 2048, 0), pq = CPAIR(csa + k * 2048, 5 + (lz >> 8)), a16 = CPAIR(csa + k * 2048, 9);
      cc1[k][0] = a1[0]; cc1[k][1] = a1[1]; cin[k][0] = pq[0]; cin[k][1] = pq[1]; ca4[k][0] = a16[0]; ca4[k][1] = a16[1];
    }
    if constexpr (sl >= SFIN && sl < SFIN + 6) {
      constexpr int r = sl - SFIN + 1;
      float n = __uint_as_float(eg[r][0]); cmac(n, c256[0], c256[1], crt); crt = n;
    }
    if constexpr (sl == SFIN + 6) { float n; cmul(n, cpw[0][0], cpw[0][1], crt); crt = n; }
    if constexpr (sl == SFIN + 7) { float n; cmul(n, cpw[1][0], cpw[1][1], crt); crt = n; }
    if constexpr (sl == SFIN + 8) cmac(wpre, cpw[2][0], cpw[2][1], crt);
    if constexpr (sl == SFIN + 9) {
      unsigned lz = l16; tie(lz);
      const unsigned scr = lds0 + LDS_SCR + w * 1024, q = lz >> 8, li = (lz >> 4) & 15;
      fwa = scr + li * 32 + q * 8;
      fra = scr + (q >> 1) * 512 + (8 * (q & 1) + (li >> 2)) * 32 + (li & 3) * 8;
      fvo = (q >> 1) * HTB + ((q & 1) * 16 + li) * 16;
      tie(fwa); tie(fra); tie(fvo);
    }
    if constexpr (sl == SFIN + 10) {
      const unsigned pu = __float_as_uint(wpre);
      const auto s16 = __builtin_amdgcn_permlane16_swap(pu, pu, false, false);
      cz[0] = __uint_as_float(s16[0]); cz[1] = __uint_as_float(s16[1]);
    }
    if constexpr (sl == SFIN + 11) { const unsigned pu = __float_as_uint(cz[0]); const auto sw = __builtin_amdgcn_permlane32_swap(pu, pu, false, false); cz[0] = __uint_as_float(sw[0]); cz[2] = __uint_as_float(sw[1]); }
    if constexpr (sl == SFIN + 12) { const unsigned pu = __float_as_uint(cz[1]); const auto sw = __builtin_amdgcn_permlane32_swap(pu, pu, false, false); cz[1] = __uint_as_float(sw[0]); }
    if constexpr (sl >= SFIN + 13 && sl < SFIN + 16) { constexpr int k = (sl - (SFIN + 13)) == 1 ? 2 : ((sl - (SFIN + 13)) == 2 ? 1 : 0); float n; cmul(n, cin[k][0], cin[k][1], cz[k]); cz[k] = n; }
    if constexpr (sl >= SFIN + 16 && sl < SFIN + 19) { constexpr int k = (sl - (SFIN + 16)) == 1 ? 2 : ((sl - (SFIN + 16)) == 2 ? 1 : 0); cmac(spre[k][1], ca4[k][0], ca4[k][1], cz[k]); }
    if constexpr (sl == SFIN + 19) { asm volatile("v_add_f32 %0, %0, %1" : "+v"(spre[0][0]) : "v"(cz[0])); asm volatile("v_add_f32 %0, %0, %1" : "+v"(spre[1][0]) : "v"(cz[1])); asm volatile("v_add_f32 %0, %0, %1" : "+v"(spre[2][0]) : "v"(cz[2])); }
    if constexpr (sl >= SFIN + 20 && sl < SFIN + 44) {
      constexpr int r = sl - (SFIN + 20), j = r / 6, k = (r % 6) >> 1, t = (r & 1) ^ 1;
      cmac(sv[k][t][j], cc1[k][0], cc1[k][1], j == 0 ? spre[k][t] : sv[k][t][j > 0 ? j - 1 : 0]);
    }
    if constexpr (sl >= SFIN + 44 && sl < SFIN + 53) {
      constexpr int r = sl - (SFIN + 44), k = r / 3, ph = r % 3;
      if constexpr (ph < 2) {
        v_cvt(pkd[0], sv[k][ph][0], sv[k][ph][1]); v_cvt(pkd[1], sv[k][ph][2], sv[k][ph][3]);
        *(LAS u32x2*)(fwa + ph * 512) = (u32x2){pkd[0], pkd[1]};
      } else {
        const u32x4 o = __builtin_bit_cast(u32x4, trfrag<128>(fra));
        st16_sc1(rsW, (k == 2 && cgl) ? 0x70000000u : fvo, htw + (k < 2 ? (unsigned)((18 + cgl) * 1024 + k * 512) : 22u * 1024u), o);
      }
    }
    if constexpr (sl >= 2 * late_g(9) && sl < 2 * GEND) {
      constexpr int r = sl - 2 * late_g(9), db = r / 14, ph = r % 14;
      if constexpr (db < 8) {
        if constexpr (ph == 0) { unsigned a, c; v_cvt(a, ac2[db][0][0], ac2[db][0][1]); v_cvt(c, ac2[db][0][2], ac2[db][0][3]); pkd[0] = a; pkd[1] = c; }
        if constexpr (ph == 1) { unsigned a, c; v_cvt(a, ac2[db][1][0], ac2[db][1][1]); v_cvt(c, ac2[db][1][2], ac2[db][1][3]); st16_sc1(rsW, l16, pb + db * 1024, (u32x4){pkd[0], pkd[1], a, c}); }
      }
    }
  };

  unsigned fa[2], eb[2], a2b[2];
  auto frag = [&](auto Gc) -> bf16x8 {
    constexpr int G = decltype(Gc)::value;
    if constexpr (G < GT) {
      constexpr LF f = l_frag(G);
      return trfrag<512>(ab[f.blk & 3] + (f.ks % 3) * 8192 + (f.blk >> 2) * 4096);
    } else if constexpr (G < G2) {
      constexpr int ks = t_ks(G - GT), blk = t_blk(G - GT), uo = (ks / 6) * 30720, k6 = ks % 6;
      if constexpr (blk < 2) {
        constexpr int o = uo - (ks >= 12 ? 30720 : 0) + (k6 * 2 + blk) * 1024;
        s16x4 a = __builtin_amdgcn_ds_read_tr16_b64_v4i16((LAS s16x4*)(fa[0] + o));
        s16x4 c = __builtin_amdgcn_ds_read_tr16_b64_v4i16((LAS s16x4*)(fa[1] + o));
        return __builtin_bit_cast(bf16x8, __builtin_shufflevector(a, c, 0, 1, 2, 3, 4, 5, 6, 7));
      } else return ldsfrag(eb[k6 & 1] + uo - (ks >= 12 ? 30720 : 0) + 12288 + (k6 >> 1) * 6144 + (blk - 2) * 2048);
    } else {
      constexpr int U = (G - G2) / 18, g = (G - G2) % 18;
      return ldsfrag(p2_off(U, g) >= 65536 ? a2b[1] + (p2_off(U, g) - 116736) : a2b[0] + p2_off(U, g));
    }
  };
  auto addr_step = [&](auto Gc) {
    constexpr int G = decltype(Gc)::value;
    if constexpr (G < GT) {
      constexpr int u = l_frag(G).unit, pu = l_prev_unit(G), k = l_frag(G).blk & 3;
      if constexpr (u != pu) { ab[k] += (unsigned)(p1_off(u) - p1_off(pu)); tie(ab[k]); }
    }
    if constexpr (G == GT) {
      unsigned l_ = l16; tie(l_); const int tl = (int)(l_ >> 4) + 64 * w;
      const int lane = tl & 63, q = lane >> 4, li = lane & 15;
      const unsigned e0 = li * 128 + ((q ^ (li >> 1)) << 4);
      eb[0] = lds0 + p1_off(12) + e0; eb[1] = lds0 + p1_off(12) + (e0 ^ 64u); tie(eb[0]); tie(eb[1]);
    }
    if constexpr (G == GT + 36) { eb[0] += 30720; eb[1] += 30720; tie(eb[0]); tie(eb[1]); }
    if constexpr (G == GT + 54) {
      unsigned l_ = l16; tie(l_); const int tl = (int)(l_ >> 4) + 64 * w;
      const int lane = tl & 63, q = lane >> 4, li = lane & 15;
      const unsigned f0 = (unsigned)((((8 * q + (li >> 2)) ^ ((q & 1) << 2)) << 5) + 8 * (lane & 3));
      fa[0] = lds0 + p1_off(12) + f0; fa[1] = lds0 + p1_off(12) + (f0 ^ 128u); tie(fa[0]); tie(fa[1]);
    }
    if constexpr (G == GT + 78) { fa[0] += 30720; fa[1] += 30720; tie(fa[0]); tie(fa[1]); }
    if constexpr (G == G2) { a2b[0] = lds0 + l16; a2b[1] = lds0 + l16 + 116736; tie(a2b[0]); tie(a2b[1]); }
  };
  auto mma = [&](auto Gc, auto Tc) {
    constexpr int G = decltype(Gc)::value, t = decltype(Tc)::value;
    const bf16x8 a = fr[G % FR];
    if constexpr (G < GT) {
      constexpr LF f = l_frag(G);
      mfma<f.ks == 0>(acc[f.blk][t], a, xf[f.ks][t]);
    } else if constexpr (G < G2) {
      constexpr int ks = t_ks(G - GT), blk = t_blk(G - GT);
      if constexpr (blk < 2) mfma<ks == 0>(tac[blk][t], a, xf[ks][t]);
      else mfma<ks == 0>(tac[blk][t], xf[ks][t], a);
    } else if constexpr (G < late_g(0)) {
      constexpr int U = (G - G2) / 18, g = (G - G2) % 18, kk = g / 9, db = g % 9;
      mfma<(U == 0 && kk == 0)>(ac2[db][t], a, hf[U % HR][kk][t]);
    } else {
      constexpr int f = G - late_g(0), db = f / 7, kk = f % 7;
      mfma<false>(ac2[db][t], a, hl[kk][t]);
    }
  };

  auto sync = [&](auto Gc) {
    constexpr int G = decltype(Gc)::value;
    if constexpr (G < GT && G % 24 == (G < 24 ? 16 : SY1)) {
      constexpr int V = G / 24;
      constexpr int need = V + 1 < 12 ? (l_dbl(V + 1) ? (((V + 1) & 1) ? -1 : V + 2) : V + 1) : 12;
      constexpr int ng = need >= 0 ? S1.after(OP_G, need, G) : 63;
      constexpr int ng2 = V == 9 ? S1.after(OP_G, 10, G) : (V == 8 ? S1.after(OP_G, 11, G) : 63);
      constexpr int ns = V == 10 ? cmin(S1.after(OP_ST, 0, G), S1.after(OP_PO, 0, G), S1.after(OP_ST, 9, G)) : 63;
      waitv<cmin(ng, ng2, ns)>();
      if constexpr (V == 3 || V == 9) asm volatile("s_waitcnt lgkmcnt(0)" ::: "memory");
      if constexpr (V == 10) setflag(rsW, l16 == 0, flw + cgl * 4, tag);
      bar();
      if constexpr (V == 10) setflag(rsW, l16 == 0 && w == 1, (unsigned)FL_W + (unsigned)(sx * 32 + idx) * 4u, tag);
      if constexpr (V == 10) setflag(rsW, l16 == 0 && w == 0, (unsigned)FL_PO + (unsigned)((rt * 4 + cgl) * 8 + sx) * 4u, tag);
      SCHED0;
    }
    if constexpr (G == gt(0, 18)) { waitv<S1.after(OP_FL, 4, G)>(); chk_flag(IC<4>{}); SCHED0; }
    if constexpr (G == gl(2, 14)) {
      waitv<S1.after(OP_SG, 0, G)>();
      for (unsigned it = 0; it < (1u << 18); ++it) {
        tie(sgr);
        if (__all(sgr[1] == tag)) break;
        __builtin_amdgcn_s_sleep(1);
        asm volatile("global_load_dwordx2 %0, %1, %2 sc1" : "+v"(sgr) : "v"((l16 >> 1) & 248), "s"(ws + FL_S + (size_t)(rt * 256 + 32 * w) * 8) : "memory");
        waitv<0>();
      }
      tie(sgr);
      const float den = bhi(sgr[0]), rs = 1.0f / den;
      *(LAS f32x4*)(lds0 + LDS_ST + w * 512 + (l16 & 496)) = (f32x4){-blo(sgr[0]), rs * -1.44269504089f, den * den, rs};
      SCHED0;
    }
    if constexpr (G == gl(2, 22)) { waitv<S1.after(OP_G, 5, G)>(); bar(); SCHED0; }
    if constexpr (G == gl(9, 1)) { waitv<S1.after(OP_G, 10, G)>(); bar(); SCHED0; }
    if constexpr (G == GSYC) { asm volatile("s_waitcnt lgkmcnt(0)" ::: "memory"); bar(); SCHED0; }
    if constexpr (G == gt(0, SYT)) { waitv<S1.after(OP_G, 13, G)>(); bar(); SCHED0; }
    if constexpr (G == gt(1, SYT)) { waitv<cmin(S1.after(OP_G, 14, G), S1.after(OP_FL, 0, G))>(); bar(); chk_flag(IC<0>{}); SCHED0; }
    if constexpr (G == gt(2, 2)) { waitv<S1.after(OP_ST, 1, G)>(); setflag(rsW, l16 == 0, flw + 128 + cgl * 4, tag); SCHED0; }
    if constexpr (G == gt(2, 27)) {
      waitv<cmin(S1.after(OP_A, 0, G), S1.after(OP_B, 0, G))>(); htie(IC<0>{});
      bar(); SCHED0;
    }
    if constexpr (G >= G2 && (G - G2) % 18 == SY2 && (G - G2) / 18 < 11) {
      constexpr int U = (G - G2) / 18;
      constexpr int nab = cmin(S1.after(OP_A, U + 1, G), U + 1 < 8 ? S1.after(OP_B, U + 1, G) : (U == 7 ? S1.after(OP_HL, 0, G) : 63));
      constexpr int nfl = U == 1 ? S1.after(OP_FL, 1, G) : (U == 3 ? cmin(S1.after(OP_ST, 2, G), S1.after(OP_EG, 0, G)) : (U == 5 ? S1.after(OP_FL, 2, G) : 63));
      static_assert(UFIN == 3, "");
      if constexpr (U == 0) asm volatile("s_waitcnt lgkmcnt(0)" ::: "memory");
      waitv<cmin(nab, nfl)>();
      if constexpr (U + 1 < 8) htie(IC<U + 1>{});
      if constexpr (U == 7) {
        tie(hl[0][0]); tie(hl[0][1]); tie(hl[1][0]); tie(hl[1][1]);
        for (unsigned it = 0; it < (1u << 16); ++it) {
          unsigned m = 0;
#pragma unroll
          for (int n = 4; n < 14; n++) { tie(hl[n >> 1][n & 1]); const u32x4 v = __builtin_bit_cast(u32x4, hl[n >> 1][n & 1]); m = max(max(m, max(v[0], v[1])), max(v[2], v[3])); }
          if (__all(m != ~0u)) break;
          __builtin_amdgcn_s_sleep(4);
#pragma unroll
          for (int n = 4; n < 14; n++) gld16_sc1_io(hl[n >> 1][n & 1], hb0 + (size_t)(n & 1) * HTB + (16 + (n >> 1)) * 1024, l16);
          waitv<0>();
        }
#pragma unroll
        for (int n = 4; n < 14; n++) tie(hl[n >> 1][n & 1]);
        chk_flag(IC<3>{});
      }
      if constexpr (U == UFIN) {
        for (unsigned it = 0; it < (1u << 18); ++it) {
          bool ok = true;
#pragma unroll
          for (int r = 0; r < 7; r++) { tie(eg[r]); ok &= (rt - 7 + r < 0) || (eg[r][1] == tag); }
          if (__all(ok)) break;
          __builtin_amdgcn_s_sleep(1);
#pragma unroll
          for (int r = 0; r < 7; r++) { const int rr = rt - 7 + r;
            asm volatile("global_load_dwordx2 %0, %1, %2 sc1" : "+v"(eg[r]) : "v"(cla * 2), "s"(ws + FL_E + (size_t)((rr < 0 ? 0 : rr) * 32 + cg) * 384) : "memory"); }
          waitv<0>();
        }
#pragma unroll
        for (int r = 0; r < 7; r++) { tie(eg[r]); if (rt - 7 + r < 0) eg[r][0] = 0u; tie(eg[r]); }
        crt = __uint_as_float(eg[0][0]);
        setflag(rsW, l16 == 0, flw + 256 + cgl * 4, tag);
      }
      bar();
      if constexpr (U == 1) chk_flag(IC<1>{});
      if constexpr (U == 5) chk_flag(IC<2>{});
      if constexpr (U == 0) {
        const f32x2 c = CPAIR(cs48, 10);
        c32[0] = c[0]; c32[1] = c[1];
#pragma unroll
        for (int k = 0; k < 8; k++) tw[k] = *(LAS float*)(lds0 + LDS_WTOT + (w + 1 + k) * 192 + cla);
#pragma unroll
        for (int k = 1; k < 8; k++) { if (k == 7) { wpre = tw[6]; tie(wpre); } dnop(); cmac(tw[k], c32[0], c32[1], tw[k - 1]); }
        __builtin_amdgcn_raw_buffer_store_b64((u32x2){__float_as_uint(tw[7]), tag}, rsW, (w == 7 && l16 < 768) ? (unsigned)FL_E + (unsigned)((rt * 32 + cg) * 384) + cla * 2 : 0x7ffffff0u, 0, 16);
      }
      SCHED0;
    }
  };

  unsigned wm[2];
  {
    unsigned l_ = l16; tie(l_); const int tl = (int)(l_ >> 4) + 64 * w;
    const int lane = tl & 63, q = lane >> 4, li = lane & 15;
    unsigned ep; u32x4 xr0, xr1;
    issue_at(IC<-3>{});
    bar();
    gld4_sc1(ep, ws + FL_EPOCH, l16 & 0);
    const int row = b * 8 + w;
    gld16(xr0, x + (size_t)row * DM, l16);
    gld16(xr1, x + (size_t)row * DM, 1024u + (l16 & 112));
    {
      const int chn = 8 * (9 * sx + (lane < 16 ? 2 * cgl + (lane >> 3) : 8)) + (lane & 7);
      asm volatile("global_load_ushort %0, %1, %2" : "=v"(pl[0]) : "v"(chn * 2), "s"(lre) : "memory");
      asm volatile("global_load_ushort %0, %1, %2" : "=v"(pl[1]) : "v"(chn * 2), "s"(lim) : "memory");
      asm volatile("global_load_ushort %0, %1, %2" : "=v"(pl[2]) : "v"(chn * 2), "s"(logdt) : "memory");
    }
    SCHED0;
    issue_at(IC<-1>{});
    {
      const int n = tl < 432 ? tl : tl - 432, r = 72 * rt + n / 6, c6 = n % 6;
      const char* p0 = (const char*)((c6 & 1) ? wu : wg) + (size_t)r * (FF * 2) + (c6 < 4 ? cg * 256 + (c6 >> 1) * 128 : fcol);
      asm volatile("global_load_dword %0, %1, off" : "=v"(wm[0]) : "v"(p0) : "memory");
      const int m = (rt * 64 + lane) % 432, rb = m / 9;
      const char* p1 = (const char*)((rb & 8) ? bim : bre) + (size_t)(8 * (9 * sx + ((rb >> 4) < 2 ? 2 * cgl + (rb >> 4) : 8)) + (rb & 7)) * (DM * 2) + (m - 9 * rb) * 128;
      asm volatile("global_load_dword %0, %1, off" : "=v"(wm[1]) : "v"(p1) : "memory");
    }
    SCHED0;
    waitv<9>();
    tie(ep); tie(xr0); tie(xr1); tie(pl[0]); tie(pl[1]); tie(pl[2]);
    tag = (unsigned)__builtin_amdgcn_readfirstlane(ep) + 1u;
    STAMP(12);
    {
      float v[16];
      const bool ex = lane < 8;
#pragma unroll
      for (int k = 0; k < 4; k++) { v[2 * k] = blo(xr0[k]); v[2 * k + 1] = bhi(xr0[k]); v[8 + 2 * k] = ex ? blo(xr1[k]) : 0.f; v[9 + 2 * k] = ex ? bhi(xr1[k]) : 0.f; }
      float sm = 0.f;
#pragma unroll
      for (int k = 0; k < 16; k++) sm += v[k];
      sm = wave_sum(sm);
      const float mean = sm * (1.0f / DM);
      float qq = 0.f;
#pragma unroll
      for (int k = 0; k < 16; k++) { const float d = (k < 8 || ex) ? v[k] - mean : 0.f; qq += d * d; }
      qq = wave_sum(qq);
      const unsigned md = pk2(mean, sqrtf(rbf(rbf(qq * (1.0f / DM)) + 1e-5f)));
      __builtin_amdgcn_raw_buffer_store_b64((u32x2){md, tag}, rsW, lane == 0 ? (unsigned)FL_S + row * 8u : 0x7ffffff0u, 0, 16);
    }
    if (w == 0 && lane < 24) {
      const float lr = bf2f((u16)pl[0]), lm = bf2f((u16)pl[1]);
      const float dt = rbf(__builtin_amdgcn_exp2f(bf2f((u16)pl[2]) * 1.44269504089f));
      const float rev = __builtin_amdgcn_fractf(dt * lm * 0.15915494309f);
      const float er = __builtin_amdgcn_exp2f(dt * lr * 1.44269504089f), ar = er * __builtin_amdgcn_cosf(rev), ai = er * __builtin_amdgcn_sinf(rev);
      const float nr = ar - 1.0f, ni = ai, dn = lr * lr + lm * lm;
      const unsigned tre = lds0 + LDS_CST + (lane >> 3) * 2048 + (lane & 7) * 8;
      auto put = [&](int k, float pr, float pi) {
        if (k >= 1 && k <= 4) { *(LAS f32x2*)(tre + k * 128) = (f32x2){2.f * pr, -2.f * pi}; *(LAS f32x2*)(tre + k * 128 + 64) = (f32x2){-2.f * pr, -2.f * pi}; }
        else { *(LAS f32x2*)(tre + k * 128) = (f32x2){pr, pi}; *(LAS f32x2*)(tre + k * 128 + 64) = (f32x2){pr, -pi}; }
      };
      put(0, ar, ai); put(5, 1.f, 0.f);
      float cr = (nr * lr + ni * lm) / dn, cm_ = (ni * lr - nr * lm) / dn;
#pragma unroll
      for (int k = 1; k < 5; k++) { put(k, cr, cm_); const float t = cr * ar - cm_ * ai; cm_ = cr * ai + cm_ * ar; cr = t; }
      const float a2r = ar * ar - ai * ai, a2i = 2.f * ar * ai;
      const float a4r = a2r * a2r - a2i * a2i, a4i = 2.f * a2r * a2i;
      float pr = a4r * a4r - a4i * a4i, pi = 2.f * a4r * a4i;
      put(6, a4r, a4i); put(7, pr, pi); put(8, pr * a4r - pi * a4i, pr * a4i + pi * a4r);
#pragma unroll
      for (int k = 9; k < 14; k++) { const float t = pr * pr - pi * pi; pi = 2.f * pr * pi; pr = t; put(k, pr, pi); }
    }
    SCHED0;
    STAMP(13);
  }
  auto pack = [&]() {
    unsigned l_ = l16; tie(l_); const int tl = (int)(l_ >> 4) + 64 * w;
    const int lane = tl & 63, q = lane >> 4, li = lane & 15;
        const unsigned ra = lds0 + LDS_PK + (w < 6 ? w : 0) * 4096 + (4 * q + (li >> 2)) * 128 + (lane & 3) * 8;
#pragma unroll
    for (int n = 0; n < 4; n++) {
      const u32x4 f = __builtin_bit_cast(u32x4, trfrag<2048>(ra + n * 32));
      const int d16 = 4 * pln + n, dt = d16 / 9, db = d16 - 9 * dt;
      st16_sc1(rsW, widx < 162 ? l16 : 0x70000000u, (unsigned)WS_WT + (unsigned)((dt * NKG + sx * KS2) * 9 + wt_frag(pj, db)) * 1024u, f);
    }
    asm volatile("s_waitcnt lgkmcnt(0)" ::: "memory");
  };
  auto pack_c = [&]() {
    unsigned l_ = l16; tie(l_); tie(cfr);
    const int dt = cd16 / 9, db = cd16 - 9 * dt;
    if (2 * cjj + (l_ >> 9) >= 9) cfr = (u32x4){0, 0, 0, 0};
    st16_sc1(rsW, widx < 180 ? l_ : 0x70000000u, (unsigned)WS_WT + (unsigned)((dt * NKG + sx * KS2) * 9 + wt_frag(18 + cjj, db)) * 1024u, cfr);
  };
  waitv<S1.after(OP_X, 0, 0)>(); xconv(IC<0>{});
  waitv<S1.after(OP_G, 0, 0)>();
  STAMP(14);
  bar();
  SCHED0;
  STAMP(1);
  fr[0] = frag(IC<0>{}); fr[1] = frag(IC<1>{});
  SCHED0;

  bf16x8 cpb[2];
  unsigned csp = 0, csq = 0;
  auto extra = [&](auto Gc) {
    constexpr int G = decltype(Gc)::value;
    if constexpr (xt_at(G) >= 0) { waitv<S1.after(OP_X, xt_at(G), G + 1)>(); xconv(IC<xt_at(G)>{}); SCHED0; }
    if constexpr (G == csx_g(0, 0) - 3 || G == csx_g(6, 0) - 3) {
      unsigned l_ = l16; tie(l_); const int tl = (int)(l_ >> 4) + 64 * w;
      const int lane = tl & 63, q = lane >> 4, li = lane & 15, r4 = li >> 2;
      const int gr = ((r4 >> 1) & 1) | ((q & 1) << 1);
      csp = lds0 + (w >> 2) * 4096 + (8 * q + r4) * 128 + ((gr ^ (w & 3)) << 5) + 8 * (lane & 3); tie(csp);
      const u32x4 so = {0x3f803f80u, 0x3f803f80u, 0x3f803f80u, 0x3f803f80u};
      selA = __builtin_bit_cast(bf16x8, so); tie(selA);
      SCHED0;
    }
    if constexpr (G == csx_g(12, 0) - 3) {
      unsigned l_ = l16; tie(l_); const int tl = (int)(l_ >> 4) + 64 * w;
      const int lane = tl & 63, q = lane >> 4, li = lane & 15;
      const u32x4 so = {0x3f803f80u, 0x3f803f80u, 0x3f803f80u, 0x3f803f80u}, sz = {0, 0, 0, 0};
      selA = __builtin_bit_cast(bf16x8, so); selB = __builtin_bit_cast(bf16x8, w < 4 ? so : sz); tie(selA); tie(selB);
      csp = lds0 + p1_off(12) + 12288 + (w < 6 ? w : 0) * 1024 + (li & 7) * 128 + (2 * q + (li >> 3)) * 16;
      csq = (unsigned)((((8 * q + (li >> 2)) ^ ((q & 1) << 2)) << 5) + 8 * (lane & 3));
      tie(csp); tie(csq);
      SCHED0;
    }
    if constexpr (csx_at(G) >= 0) {
      constexpr int v = csx_at(G) >> 2, n = csx_at(G) & 3;
      const bf16x8 cpc = cpb[G & 1];
      if constexpr (v < 12) mfma<(v % 6 == 0 && n == 0)>(csa0, cpc, selA);
      else if constexpr (v < 15) mfma<(v == 12 && n == 0)>(csa0, selA, cpc);
      else mfma<(v == 15 && n == 0)>(csa1, cpc, (v == 16 && n == 1) ? selB : selA);
      SCHED0;
    }
    if constexpr (csx_at(G + 2) >= 0) {
      constexpr int v = csx_at(G + 2) >> 2, n = csx_at(G + 2) & 3;
      bf16x8& cpc = cpb[G & 1];
      if constexpr (v < 12) { unsigned a = csp + p1_off(v); cpc = trfrag<512>(a + n * 8192); }
      else if constexpr (v < 15) { unsigned a = csp + (v - 12) * 30720; cpc = ldsfrag(a + n * 6144); }
      else {
        constexpr int m = 3 * (v - 15) + n;
        const int ks = (w >> 1) + 4 * m < 18 ? (w >> 1) + 4 * m : 0, ku = ks / 6;
        const unsigned o = lds0 + p1_off(12) + ku * 30720 + ((ks - 6 * ku) * 2 + (w & 1)) * 1024;
        s16x4 a = __builtin_amdgcn_ds_read_tr16_b64_v4i16((LAS s16x4*)(o + csq));
        s16x4 c = __builtin_amdgcn_ds_read_tr16_b64_v4i16((LAS s16x4*)(o + (csq ^ 128u)));
        cpc = __builtin_bit_cast(bf16x8, __builtin_shufflevector(a, c, 0, 1, 2, 3, 4, 5, 6, 7));
      }
      SCHED0;
    }
  };
  auto cs_flush = [&](auto Gc) {
    constexpr int G = decltype(Gc)::value;
    if constexpr (G == CSF[0] || G == CSF[1]) {
      unsigned lz = l16; tie(lz);
      *(LAS f32x4*)(lds0 + LDS_CS + (G == CSF[1] ? 512 : 0) + w * 64 + (lz >> 8) * 16) = csa0;
      SCHED0;
    }
    if constexpr (G == CSF[2]) {
      unsigned lz = l16; tie(lz);
      *(LAS float*)(lds0 + LDS_CS + 1024 + w * 32 + ((lz >> 4) & 7) * 4) = csa0[0] + dpp_ror8(csa0[0]);
      *(LAS f32x4*)(lds0 + LDS_CS + 1280 + w * 64 + (lz >> 8) * 16) = csa1;
      SCHED0;
    }
  };

  auto group = [&](auto Gc) {
    constexpr int G = decltype(Gc)::value;
    if constexpr (G == 144) { STAMP(2); }
    if constexpr (G == GT) { STAMP(3); }
    if constexpr (G == G2) { STAMP(4); STAMP(5); }
    if constexpr (G == g2(3, 0)) { STAMP(6); }
    if constexpr (G == g2(8, 0)) { STAMP(7); }
    if constexpr (G == GPK) { waitv<S1.after(OP_PK, 0, G) - 4>(); pack(); SCHED0; }
    if constexpr (G == GPC) { waitv<S1.after(OP_PK, 1, G) - 1>(); pack_c(); SCHED0; }
    if constexpr (G == gl(1, 3)) { tie(wm[0]); tie(wm[1]); }
    cs_flush(Gc);
    sync(Gc);
    issue_at(IC<2 * G>{});
    extra(Gc);
    mma(Gc, IC<0>{}); SCHED0;
    if constexpr (G + PD < GEND) { addr_step(IC<G + PD>{}); fr[(G + PD) % FR] = frag(IC<G + PD>{}); SCHED0; }
    side(IC<2 * G>{}); SCHED0;
    mma(Gc, IC<1>{}); SCHED0;
    side(IC<2 * G + 1>{}); SCHED0;
  };
  sfor<0, 13>([&](auto Bc) { sfor<0, 45>([&](auto Jc) { group(IC<decltype(Bc)::value * 45 + decltype(Jc)::value>{}); }); });
  STAMPC(27);
  STAMP(8);

  asm volatile("s_nop 7\n\ts_nop 1" : "+v"(ac2[8][0]), "+v"(ac2[8][1]));
  unsigned l_ = l16; tie(l_); const int tz = (int)(l_ >> 4) + 64 * w;
  const int lane = tz & 63, q = lane >> 4, li = lane & 15;
  st16_sc1(rsW, l16, pb + 8 * 1024, (u32x4){pk2(ac2[8][0][0], ac2[8][0][1]), pk2(ac2[8][0][2], ac2[8][0][3]), pk2(ac2[8][1][0], ac2[8][1][1]), pk2(ac2[8][1][2], ac2[8][1][3])});
  SCHED0;
  const char* ib = ws + (size_t)pobase + (size_t)w * 9216;
#pragma unroll
  for (int db = 8 - PLAG; db < 9; db++) gld16_sc1(pv[db], ib + db * 1024, l16);
  bar();
  STAMP(9);
  {
    constexpr int LROW = 304, LSRC = 32 * LROW;
    for (unsigned it = 0; it < (1u << 16); ++it) {
      waitv<0>();
      unsigned bad = 0;
#pragma unroll
      for (int db = 0; db < 9; db++) {
        tie(pv[db]);
        const unsigned m = max(max(pv[db][0], pv[db][1]), max(pv[db][2], pv[db][3]));
        if (!__all(m != ~0u)) bad |= 1u << db;
      }
      if (!bad) break;
      __builtin_amdgcn_s_sleep(2);
#pragma unroll
      for (int db = 0; db < 9; db++) if (bad & (1u << db)) gld16_sc1_io(pv[db], ib + db * 1024, l16);
    }
    waitv<0>();
#pragma unroll
    for (int db = 0; db < 9; db++) {
      tie(pv[db]);
      *(LAS u32x2*)(lds0 + w * LSRC + li * LROW + db * 32 + q * 8) = (u32x2){pv[db][0], pv[db][1]};
      *(LAS u32x2*)(lds0 + w * LSRC + (16 + li) * LROW + db * 32 + q * 8) = (u32x2){pv[db][2], pv[db][3]};
    }
    asm volatile("s_waitcnt vmcnt(0) lgkmcnt(0)" ::: "memory");
    bar();
    STAMP(10);
#pragma unroll
    for (int r = 0; r < 3; r++) {
      tie(epv[2 * r]); tie(epv[2 * r + 1]);
      const int e = tz + 512 * r;
      if (e < 1152) {
        const int t = e / 36, d4 = e - 36 * t;
        f32x4 sum = zero4;
#pragma unroll
        for (int sr = 0; sr < 8; sr++) {
          const u32x2 p = *(LAS u32x2*)(lds0 + sr * LSRC + t * LROW + d4 * 8);
          sum += (f32x4){blo(p[0]), bhi(p[0]), blo(p[1]), bhi(p[1])};
        }
        const f32x4 st = *(LAS f32x4*)(lds0 + LDS_ST + sx * 512 + t * 16);
        f32x4 o;
#pragma unroll
        for (int j = 0; j < 4; j++) {
          const float xe = (j & 1) ? bhi(epv[2 * r][j >> 1]) : blo(epv[2 * r][j >> 1]);
          const float de = (j & 1) ? bhi(epv[2 * r + 1][j >> 1]) : blo(epv[2 * r + 1][j >> 1]);
          const float xn = rbf(rbf(xe + st[0]) * st[3]);
          o[j] = xe + rbf(xn * de) + sum[j];
        }
        *(f32x4*)(out + (size_t)(erow + t) * DM + cgl * 144 + 4 * d4) = o;
      }
    }
    if (b == 0 && w == 0) {
#pragma unroll
      for (int k = 0; k < 4; k++) spin_flag(ws + FL_W + k * 256, l16 >> 2, tag);
      setflag(rsW, l16 == 0, (unsigned)FL_EPOCH, tag);
    }
    STAMP(11);
    STAMP_DUMP();
  }
}

extern "C" void kernel_launch(void* const* d_in, const int* in_sizes, int n_in, void* d_out, int out_size, void* d_ws, size_t ws_size,
                              hipStream_t stream) {
  const u16* x = (const u16*)d_in[0]; const u16* lre = (const u16*)d_in[1]; const u16* lim = (const u16*)d_in[2];
  const u16* bre = (const u16*)d_in[3]; const u16* bim = (const u16*)d_in[4]; const u16* cre = (const u16*)d_in[5]; const u16* cim = (const u16*)d_in[6];
  const u16* logdt = (const u16*)d_in[7]; const u16* dsk = (const u16*)d_in[8];
  const u16* wg = (const u16*)d_in[10]; const u16* wu = (const u16*)d_in[11]; const u16* wd = (const u16*)d_in[12];
  char* ws = (char*)d_ws;
  (void)hipFuncSetAttribute((const void*)k_main, hipFuncAttributeMaxDynamicSharedMemorySize, LDS_TOT);
  k_main<<<256, 512, LDS_TOT, stream>>>(x, lre, lim, bre, bim, cre, cim, logdt, dsk, wg, wu, wd, ws, (float*)d_out);
}
```

```cpp
#include <hip/hip_runtime.h>
#include <stdint.h>
#include <type_traits>

typedef unsigned short u16;
typedef unsigned long long u64;
typedef __bf16 bf16x8 __attribute__((ext_vector_type(8)));
typedef __bf16 bf16x2 __attribute__((ext_vector_type(2)));
typedef float f32x4 __attribute__((ext_vector_type(4)));
typedef float f32x2 __attribute__((ext_vector_type(2)));
typedef short s16x4 __attribute__((ext_vector_type(4)));
typedef unsigned u32x4 __attribute__((ext_vector_type(4)));
typedef unsigned u32x2 __attribute__((ext_vector_type(2)));
#define LAS __attribute__((address_space(3)))
#define GAS __attribute__((address_space(1)))
#define DI __device__ __forceinline__

constexpr int SEQ = 2048, DM = 576, FF = 4608;
constexpr int KS2 = 23;
constexpr int NKG = 8 * KS2;
constexpr size_t FL_H = 0;
constexpr size_t FL_PO = 32768;
constexpr size_t FL_W = 36864;
constexpr size_t FL_EPOCH = 38912;
constexpr size_t FL_E = 160000 + 3840;
constexpr size_t FL_S = 139264;
constexpr size_t WS_HT = 524288;
constexpr size_t WS_WT = WS_HT + (size_t)128 * NKG * 1024;
constexpr size_t WS_PART = WS_WT + (size_t)4 * NKG * 9 * 1024;
constexpr size_t PART_BYTES = (size_t)8 * 4 * 8 * 8 * 9 * 1024;
constexpr size_t WS_END = WS_PART + PART_BYTES;

DI float bf2f(u16 v) { return __uint_as_float(((unsigned)v) << 16); }
DI float rbf(float f) { return (float)(__bf16)f; }
DI unsigned pk2(float lo, float hi) { f32x2 v = {lo, hi}; bf16x2 c = __builtin_convertvector(v, bf16x2); return __builtin_bit_cast(unsigned, c); }
DI float blo(unsigned u) { return __uint_as_float(u << 16); }
DI float bhi(unsigned u) { return __uint_as_float(u & 0xffff0000u); }

__host__ __device__ inline int wt_frag(int j, int db) { return j < 16 ? j * 9 + db : 144 + db * 7 + (j - 16); }
DI int kg_f0b(int s, int j) {
  const int a = 512 * s + 128 * ((j & 7) >> 1) + 64 * (j >> 3) + 32 * (j & 1), c = 4096 + 64 * s + 32 * (j & 1);
  return j < 16 ? a : c;
}
__host__ __device__ inline int kg_f0(int s, int j) {
  if (j < 16) { int p = j >> 3, cg = 4 * s + ((j & 7) >> 1); return 64 * (2 * cg + p) + 32 * (j & 1); }
  return 4096 + 32 * (2 * s + (j - 16));
}

template <int N> using IC = std::integral_constant<int, N>;
template <int B, int E, class F> DI void sfor(F&& f) { if constexpr (B < E) { f(IC<B>{}); sfor<B + 1, E>(f); } }

DI void glds_sc1(const void* sbase, unsigned voff, unsigned ldsaddr) {
  asm volatile("s_mov_b32 m0, %2\n\ts_nop 0\n\tglobal_load_lds_dwordx4 %0, %1 sc1" :: "v"(voff), "s"(sbase), "s"(ldsaddr) : "memory");
}
DI void gld4(unsigned& dst, const void* sbase, unsigned voff) {
  asm volatile("global_load_dword %0, %1, %2" : "=v"(dst) : "v"(voff), "s"(sbase) : "memory");
}
template <int C, int L, bool SC1> DI void glds_c(const void* sbase, unsigned voff, unsigned ldsw) {
  unsigned t;
  if constexpr (SC1) asm volatile("s_add_u32 m0, %3, %5\n\tv_add_u32 %0, %4, %1\n\tglobal_load_lds_dwordx4 %0, %2 sc1" : "=&v"(t) : "v"(voff), "s"(sbase), "s"(ldsw), "i"(C), "i"(L) : "memory", "scc");
  else asm volatile("s_add_u32 m0, %3, %5\n\tv_add_u32 %0, %4, %1\n\tglobal_load_lds_dwordx4 %0, %2" : "=&v"(t) : "v"(voff), "s"(sbase), "s"(ldsw), "i"(C), "i"(L) : "memory", "scc");
}
template <int OFF, class T> DI void gld16o_sc1(T& dst, const void* sbase, unsigned voff) {
  asm volatile("global_load_dwordx4 %0, %1, %2 offset:%3 sc1" : "=v"(dst) : "v"(voff), "s"(sbase), "i"(OFF) : "memory");
}
template <int OFF, class T> DI void gld16o_io(T& dst, const void* sbase, unsigned voff) {
  asm volatile("global_load_dwordx4 %0, %1, %2 offset:%3 sc1" : "+v"(dst) : "v"(voff), "s"(sbase), "i"(OFF) : "memory");
}
template <int OFF, bool IO> DI void gld8o(u32x2& dst, const void* sbase, unsigned voff) {
  if constexpr (IO) asm volatile("global_load_dwordx2 %0, %1, %2 offset:%3 sc1" : "+v"(dst) : "v"(voff), "s"(sbase), "i"(OFF) : "memory");
  else asm volatile("global_load_dwordx2 %0, %1, %2 offset:%3 sc1" : "=v"(dst) : "v"(voff), "s"(sbase), "i"(OFF) : "memory");
}
template <int L> DI void glds_run(unsigned& voff, const void* sbase, unsigned step, unsigned ldsa) {
  asm volatile("s_add_u32 m0, %3, %4\n\ts_nop 0\n\tglobal_load_lds_dwordx4 %0, %1\n\tv_add_u32 %0, %2, %0" : "+v"(voff) : "s"(sbase), "s"(step), "s"(ldsa), "i"(L) : "memory", "scc");
}
DI void glds_s(const void* sbase, unsigned voff, unsigned ldsaddr) {
  asm volatile("s_mov_b32 m0, %2\n\ts_nop 0\n\tglobal_load_lds_dwordx4 %0, %1" :: "v"(voff), "s"(sbase), "s"(ldsaddr) : "memory");
}
template <class T> DI void gld16(T& dst, const void* sbase, unsigned voff) {
  asm volatile("global_load_dwordx4 %0, %1, %2" : "=v"(dst) : "v"(voff), "s"(sbase) : "memory");
}
template <class T> DI void gld16_sc1(T& dst, const void* sbase, unsigned voff) {
  asm volatile("global_load_dwordx4 %0, %1, %2 sc1" : "=v"(dst) : "v"(voff), "s"(sbase) : "memory");
}
template <class T> DI void gld16_sc1_io(T& dst, const void* sbase, unsigned voff) {
  asm volatile("global_load_dwordx4 %0, %1, %2 sc1" : "+v"(dst) : "v"(voff), "s"(sbase) : "memory");
}
DI void gld4_sc1(unsigned& dst, const void* sbase, unsigned voff) {
  asm volatile("global_load_dword %0, %1, %2 sc1" : "=v"(dst) : "v"(voff), "s"(sbase) : "memory");
}
DI void gld8_sc1(u32x2& dst, const void* sbase, unsigned voff) {
  asm volatile("global_load_dwordx2 %0, %1, %2 sc1" : "=v"(dst) : "v"(voff), "s"(sbase) : "memory");
}
DI void gld8(u32x2& dst, const void* sbase, unsigned voff) {
  asm volatile("global_load_dwordx2 %0, %1, %2" : "=v"(dst) : "v"(voff), "s"(sbase) : "memory");
}
template <int N> DI void waitv() { static_assert(N >= 0 && N < 64, ""); asm volatile("s_waitcnt vmcnt(%0)" ::"i"(N) : "memory"); }
#define SCHED0 __builtin_amdgcn_sched_barrier(0)
DI void bar() { asm volatile("" ::: "memory"); __builtin_amdgcn_s_barrier(); asm volatile("" ::: "memory"); }
template <class T> DI void tie(T& a) { asm volatile("" : "+v"(a)); }
template <int O2> DI bf16x8 trfrag(unsigned addr) {
  s16x4 a = __builtin_amdgcn_ds_read_tr16_b64_v4i16((LAS s16x4*)(addr));
  s16x4 b = __builtin_amdgcn_ds_read_tr16_b64_v4i16((LAS s16x4*)(addr + O2));
  return __builtin_bit_cast(bf16x8, __builtin_shufflevector(a, b, 0, 1, 2, 3, 4, 5, 6, 7));
}
DI bf16x8 ldsfrag(unsigned addr) { return *(LAS bf16x8*)(addr); }
template <bool FIRST> DI void mfma(f32x4& c, bf16x8 a, bf16x8 b) {
  if constexpr (FIRST) asm volatile("v_mfma_f32_16x16x32_bf16 %0, %1, %2, 0" : "=v"(c) : "v"(a), "v"(b));
  else asm volatile("v_mfma_f32_16x16x32_bf16 %0, %1, %2, %0" : "+v"(c) : "v"(a), "v"(b));
}
DI float silu_mul(float g, float u) {
  const float e = __builtin_amdgcn_exp2f(g * -1.44269504089f);
  return g * u * __builtin_amdgcn_rcpf(1.0f + e);
}
DI unsigned silu_mul2(f32x2 g, f32x2 u) {
  const f32x2 t = g * -1.44269504089f;
  f32x2 e; e.x = __builtin_amdgcn_exp2f(t.x); e.y = __builtin_amdgcn_exp2f(t.y);
  const f32x2 d = e + 1.0f;
  f32x2 r; r.x = __builtin_amdgcn_rcpf(d.x); r.y = __builtin_amdgcn_rcpf(d.y);
  const f32x2 h = (g * u) * r;
  return pk2(h.x, h.y);
}
DI float dpp_ror8(float v) { return __int_as_float(__builtin_amdgcn_update_dpp(0, __float_as_int(v), 0x128, 0xf, 0xf, true)); }
DI void cmac(float& d, float P, float Q, float x) {
  asm volatile("v_fmac_f32 %0, %1, %3\n\tv_fmac_f32_dpp %0, %3, %2 row_ror:8 row_mask:0xf bank_mask:0xf" : "+v"(d) : "v"(P), "v"(Q), "v"(x));
}
DI void cmul(float& d, float P, float Q, float x) {
  asm volatile("v_mul_f32 %0, %1, %3\n\tv_fmac_f32_dpp %0, %3, %2 row_ror:8 row_mask:0xf bank_mask:0xf" : "=&v"(d) : "v"(P), "v"(Q), "v"(x));
}
DI void cfma(float& d, float P, float Q, float x, float c) {
  asm volatile("v_fma_f32 %0, %1, %3, %4\n\tv_fmac_f32_dpp %0, %3, %2 row_ror:8 row_mask:0xf bank_mask:0xf" : "=&v"(d) : "v"(P), "v"(Q), "v"(x), "v"(c));
}
DI void v_mul(float& d, float a, float b) { asm volatile("v_mul_f32 %0, %1, %2" : "=v"(d) : "v"(a), "v"(b)); }
DI void v_mulc(float& d, float a) { asm volatile("v_mul_f32 %0, 0xbfb8aa3b, %1" : "=v"(d) : "v"(a)); }
DI void v_fmac(float& d, float a, float b) { asm volatile("v_fmac_f32 %0, %1, %2" : "+v"(d) : "v"(a), "v"(b)); }
DI void v_fmaa(float& d, float c) { asm volatile("v_fma_f32 %0, %0, %1, %1" : "+v"(d) : "v"(c)); }
DI void v_muli(float& d, float a) { asm volatile("v_mul_f32 %0, %0, %1" : "+v"(d) : "v"(a)); }
DI void lds_add(unsigned addr, float v) { asm volatile("ds_add_f32 %0, %1" :: "v"(addr), "v"(v) : "memory"); }
DI void v_exp2(float& d) { asm volatile("v_exp_f32 %0, %0" : "+v"(d)); }
DI void v_add1(float& d) { asm volatile("v_add_f32 %0, 1.0, %0" : "+v"(d)); }
DI void v_rcp(float& d) { asm volatile("v_rcp_f32 %0, %0" : "+v"(d)); }
DI void v_cvt(unsigned& d, float a, float b) { asm volatile("v_cvt_pk_bf16_f32 %0, %1, %2" : "=v"(d) : "v"(a), "v"(b)); }
DI float wave_sum(float v) {
#define DPPADD(c) v += __int_as_float(__builtin_amdgcn_update_dpp(0, __float_as_int(v), c, 0xf, 0xf, true))
  DPPADD(0x111); DPPADD(0x112); DPPADD(0x114); DPPADD(0x118);
  v += __int_as_float(__builtin_amdgcn_update_dpp(0, __float_as_int(v), 0x142, 0xa, 0xf, false));
  v += __int_as_float(__builtin_amdgcn_update_dpp(0, __float_as_int(v), 0x143, 0xc, 0xf, false));
  return __int_as_float(__builtin_amdgcn_readlane(__float_as_int(v), 63));
}
DI void dnop() { asm volatile("s_nop 1"); }
typedef __amdgpu_buffer_rsrc_t rsrc_t;
DI rsrc_t mkrsrc(const void* p, unsigned bytes) { return __builtin_amdgcn_make_buffer_rsrc((void*)p, 0, bytes, 0x00020000); }
DI unsigned vadd_s(unsigned s, unsigned v) { unsigned o; asm volatile("v_add_u32 %0, %1, %2" : "=v"(o) : "s"(s), "v"(v)); return o; }
DI void st8_sc1(rsrc_t r, unsigned voff, unsigned soff, u32x2 v) { __builtin_amdgcn_raw_buffer_store_b64(v, r, vadd_s(soff, voff), 0, 16); }
DI void st16_sc1(rsrc_t r, unsigned voff, unsigned soff, u32x4 v) { __builtin_amdgcn_raw_buffer_store_b128(v, r, vadd_s(soff, voff), 0, 16); }
DI u32x4 ones4() { u32x4 v; asm volatile("v_mov_b32 %0, -1\n\tv_mov_b32 %1, -1\n\tv_mov_b32 %2, -1\n\tv_mov_b32 %3, -1" : "=v"(v[0]), "=v"(v[1]), "=v"(v[2]), "=v"(v[3])); return v; }
DI void setflag(rsrc_t r, bool sel, unsigned off, unsigned tag) { __builtin_amdgcn_raw_buffer_store_b32(tag, r, sel ? off : 0x7ffffff0u, 0, 16); }
DI void spin_flag(const char* base, unsigned voff, unsigned tag) {
  for (unsigned it = 0; it < (1u << 18); ++it) {
    unsigned v = __hip_atomic_load((GAS unsigned*)(base + voff), __ATOMIC_RELAXED, __HIP_MEMORY_SCOPE_AGENT);
    if (__all(v == tag)) break;
    __builtin_amdgcn_s_sleep(2);
  }
  asm volatile("" ::: "memory");
}

constexpr int PD = 2, FR = PD + 1;
constexpr int SLOT1 = 24576;
constexpr int SLOT2 = 18432;
constexpr int LDS_TOT = 163840;
constexpr int LDS_SCR = 90112;
constexpr int LDS_PK = 122880;
constexpr int LDS_XS = 98304;
constexpr int LDS_ST = 147456;
constexpr int LDS_CS = 151552;
constexpr int LDS_CST = 153600;
constexpr int LDS_WTOT = LDS_CST + 6144;

#define STAMP(k)
#define STAMPC(k)
#define STAMP_DUMP()

constexpr int p1_off(int v) { return v < 8 ? (v % 4) * SLOT1 : (v < 10 ? (v - 4) * SLOT1 : (v < 12 ? (v - 10) * SLOT1 : 2 * SLOT1 + (v - 12) * 30720)); }
constexpr int p2_off(int u, int g) {
  const int k = u % 5;
  return k < 2 ? k * SLOT2 + g * 1024 : k == 2 ? (g < 12 ? 36864 + g * 1024 : 141312 + (g - 12) * 1024) : 49152 + (k - 3) * SLOT2 + g * 1024;
}

constexpr int GT = 288, G2 = 378, GEND = 585;
constexpr int gl(int v, int g) { return v * 24 + g; }
constexpr int gt(int u, int g) { return GT + u * 30 + g; }
constexpr int g2(int u, int g) { return G2 + u * 18 + g; }
constexpr int SY1 = 8, SYT = 2, SY2 = 4;
constexpr int late_g(int f) { return g2(8, f); }
constexpr int PLAG = 4;

constexpr int SCE = 2 * g2(0, SY2) + 3;
constexpr int SQC = 2 * (G2 + 13);
constexpr int SILU0[3] = {2 * (gl(4, 0) + 12), 2 * (gl(10, 0) + 12), SQC + 8};
constexpr int SILUN[3] = {32, 32, 8};
constexpr int silu_end(int s, int nel) { return SILU0[s] + 9 * (nel / 2) + 1; }
constexpr int S1R = 2 * (GT + 55);
constexpr int S1A = S1R + 12;
constexpr int S1SEL = 2 * g2(2, 0) + 2;
constexpr int SCM = S1SEL + 6;
constexpr int UFIN = 3;
constexpr int SFIN = 2 * g2(UFIN, SY2);
static_assert(S1A + 63 < 2 * g2(0, SY2) && silu_end(2, 8) < S1SEL && SCM + 24 < SFIN - 8, "");
constexpr int csx_n(int v) { return v == 16 ? 2 : 3; }
constexpr int csx_g(int v, int n) {
  const int base[17] = {gl(0, 4), gl(0, 19), gl(1, 12), gl(2, 11), gl(2, 16), gl(3, 3), gl(7, 16), gl(7, 19), gl(8, 1), gl(8, 12), gl(8, 18), gl(9, 0),
                        GT + 27, GT + 30, GT + 34, GT + 37, GT + 40};
  const int step = (v == 5 || v == 6 || v == 7 || v >= 12) ? 1 : 2;
  return base[v] + n * step;
}
constexpr int CSF[3] = {gl(3, SY1), gl(9, SY1), GT + 44};
constexpr int GPC = gl(8, 23);
constexpr int GPK = gl(6, 6);
constexpr int GSYC = GT + 46;
constexpr int xt_g(int h) {
  const int g[18] = {-1, -1, gl(0, 6), gl(0, 12), gl(0, 20), gl(1, 3), gl(1, 11), gl(1, 18), gl(2, 2), gl(2, 10), gl(2, 17), gl(3, 1), gl(3, 9), gl(3, 14), gl(3, 19), gl(4, 0), gl(4, 3), gl(4, 5)};
  return g[h];
}
constexpr int xd_g(int h) { return h < 3 ? -1 : (h < 5 ? h - 2 : xt_g(h - 3) + 3); }

constexpr int csx_at(int G) { for (int v = 0; v < 17; v++) for (int n = 0; n < csx_n(v); n++) if (csx_g(v, n) == G) return v * 4 + n; return -1; }
constexpr int xt_at(int G) { for (int c = 2; c < 18; c++) if (xt_g(c) == G) return c; return -1; }
constexpr int silu_at(int sl) { for (int k = 0; k < 3; k++) if (sl >= SILU0[k] && sl - SILU0[k] <= 9 * (SILUN[k] / 2) + 1) return k; return -1; }

enum { OP_G = 0, OP_X, OP_ST, OP_PO, OP_PH, OP_A, OP_B, OP_FL, OP_EP, OP_HL, OP_PL, OP_EG, OP_SG, OP_PK };
struct Op { int pos, kind, a, n; };
struct Sched {
  Op op[320]; int cnt;
  constexpr void add(int pos, int kind, int a, int n) {
    int i = cnt; while (i > 0 && op[i - 1].pos > pos) { op[i] = op[i - 1]; i--; }
    op[i] = Op{pos, kind, a, n}; cnt++;
  }
  constexpr void unit(int g, int v, int step) { for (int n = 0; n < (v < 12 ? 3 : 4); n++) add(2 * (g + n * step), OP_G, v, n); }
  constexpr Sched() : op{}, cnt(0) {
    for (int h = 0; h < 2; h++) for (int n = 0; n < 2; n++) add(-3, OP_X, h, n);
    for (int n = 0; n < 3; n++) add(-3, OP_G, 0, n);
    for (int n = 0; n < 6; n++) add(-2, OP_ST, 8, n);
    for (int n = 0; n < 2; n++) add(-1, OP_X, 2, n);
    for (int n = 0; n < 3; n++) add(-1, OP_G, 1, n);
    for (int n = 0; n < 2; n++) add(-1, OP_ST, 7, n);
    add(-1, OP_ST, 6, 0);
    for (int h = 3; h < 18; h++) for (int n = 0; n < 2; n++) add(2 * xd_g(h), OP_X, h, n);
    for (int n = 0; n < 4; n++) add(2 * gl(4, 10 + 3 * n), OP_PK, 0, n);
    add(2 * gl(7, 15), OP_PK, 1, 0);
    add(2 * GPC - 1, OP_ST, 9, 0);
    unit(gl(0, 4), 2, 1);
    for (int n = 0; n < 4; n++) add(2 * GPK - 1, OP_ST, 4, n);
    add(2 * gl(1, 5), OP_SG, 0, 0);
    unit(gl(0, 9), 3, 3); unit(gl(1, 9), 4, 3); unit(gl(2, 9), 5, 1); unit(gl(3, 10), 6, 3); unit(gl(4, 9), 7, 3);
    unit(gl(5, 10), 8, 4); unit(gl(6, 9), 9, 1); unit(gl(6, 13), 10, 2); unit(gl(6, 19), 11, 2); unit(gl(8, 10), 12, 3); unit(gl(9, 10), 13, 3); unit(gl(10, 10), 14, 3);
    for (int n = 0; n < 3; n++) add(2 * gl(4, 1 + 2 * n), OP_PH, 0, n);
    for (int n = 0; n < 9; n++) add(2 * (gl(4, 21) + 13 * n), OP_PO, 0, n);
    for (int s = 0; s < 2; s++) for (int h = 0; h < 2; h++) for (int t = 0; t < 2; t++) add((silu_end(s, 16 * (h + 1)) / 2) * 2 + 1, OP_ST, s, 2 * h + t);
    for (int t = 0; t < 2; t++) add((silu_end(2, 8) / 2) * 2 + 1, OP_ST, 2, t);
    add(2 * gl(11, 16), OP_FL, 4, 0);
    add(2 * gt(0, 6), OP_FL, 0, 0);
    for (int n = 0; n < 2; n++) { add(2 * gt(0, 20 + 4 * n), OP_A, 0, n); add(2 * gt(1, 8 + 4 * n), OP_A, 1, n); add(2 * gt(1, 16 + 4 * n), OP_A, 2, n); }
    for (int n = 0; n < 4; n++) { add(2 * gt(2, 3 + 2 * n), OP_B, 0, n); add(2 * gt(2, 11 + 2 * n), OP_B, 1, n); add(2 * gt(2, 19 + 3 * n), OP_B, 2, n); }
    add(2 * g2(0, 1), OP_FL, 1, 0);
    for (int u = 0; u < 9; u++) {
      const int U = u + 3;
      for (int n = 0; n < (U < 11 ? 2 : 1); n++) add(2 * g2(u, 5 + 2 * n), OP_A, U, n);
      if (U < 8) for (int n = 0; n < 4; n++) add(2 * g2(u, 9 + 2 * n), OP_B, U, n);
    }
    for (int n = 0; n < 7; n++) add(2 * g2(UFIN - 1, 10) + 0, OP_EG, 0, n);
    add(2 * g2(4, 6), OP_FL, 2, 0);
    add(2 * g2(4, 8), OP_FL, 3, 0);
    for (int n = 0; n < 4; n++) add(2 * g2(5, 9 + 2 * n), OP_HL, 0, n);
    for (int n = 4; n < 14; n++) add(2 * g2(6, 8 + (n - 4)), OP_HL, 0, n);
    for (int n = 0; n < 6; n++) add(2 * g2(8, 6 + 2 * n), OP_EP, 0, n);
    for (int db = 0; db < 8; db++) add(2 * (late_g(7 * db + 6) + 3) + 1, OP_ST, 3, db);
    for (int db = 0; db + PLAG < 8; db++) add(2 * (late_g(7 * (db + PLAG) + 6) + 5), OP_PL, 0, db);
  }
  static constexpr bool droppable(const Op& o) { return (o.kind == OP_PH && o.n == 2) || (o.kind == OP_ST && (o.a == 4 || o.a == 9)) || (o.kind == OP_PK && o.a == 0); }
  constexpr int first_at(int pos) const { int i = 0; while (i < cnt && op[i].pos < pos) i++; return i; }
  constexpr int after(int kind, int a, int g) const {
    int last = -1;
    for (int i = 0; i < cnt; i++) if (op[i].kind == kind && op[i].a == a) last = i;
    int n = 0;
    for (int i = last + 1; i < cnt; i++) if (op[i].pos < 2 * g && !droppable(op[i])) n++;
    return n;
  }
};
constexpr Sched S1{};
constexpr int cmin(int a, int b) { return a < b ? a : b; }
constexpr int cmin(int a, int b, int c) { return cmin(cmin(a, b), c); }
constexpr bool l_dbl(int v) { return v == 4 || v == 5 || v == 6 || v == 7 || v >= 10; }
struct LF { int unit, ks, blk; };
constexpr LF l_frag(int G) {
  const int v = G / 24, g = G % 24;
  if (!l_dbl(v)) return LF{v, 3 * (v % 6) + (g >> 3), g & 7};
  const int v0 = v & ~1, r = (v & 1) * 24 + g, p = r / 12, k6 = (r % 12) / 2;
  return LF{v0 + k6 / 3, 3 * (v0 % 6) + k6, p + 4 * (r & 1)};
}
constexpr int l_prev_unit(int G) {
  for (int g = G - 1; g >= 0; g--) if ((l_frag(g).blk & 3) == (l_frag(G).blk & 3)) return l_frag(g).unit;
  return 0;
}
constexpr int l_unit0(int G) { for (int g = G - 1; g >= 0; g--) if ((l_frag(g).blk & 3) == 0) return l_frag(g).unit; return 0; }
struct Corr {
  int item[40];
  constexpr Corr(int pass) : item{} {
    const int g0 = gl(6 * pass + 3, SY1 + 2);
    bool done[16] = {};
    const int order[8] = {0, 4, 1, 5, 2, 6, 3, 7};
    for (int k = 0; k < 40; k++) {
      item[k] = -1;
      const int G = g0 + k;
      for (int o = 0; o < 16 && item[k] < 0; o++) {
        const int bk = order[o >> 1], it = bk * 2 + (o & 1);
        if (done[it]) continue;
        if (l_frag(G).blk == bk || l_frag(G - 1).blk == bk || l_frag(G - 2).blk == bk || l_frag(G + 1).blk == bk) continue;
        item[k] = it; done[it] = true;
      }
    }
  }
  constexpr int last() const { int l = 0; for (int k = 0; k < 40; k++) if (item[k] >= 0) l = k; return l; }
  constexpr int count() const { int c = 0; for (int k = 0; k < 40; k++) c += item[k] >= 0; return c; }
};
constexpr Corr CR{0};
static_assert(CR.count() == 16 && gl(3, SY1 + 2) + CR.last() < gl(4, 12), "");
constexpr int t_ks(int r) { return r < 54 ? r / 3 : (r - 54) / 2; }
constexpr int t_blk(int r) { return r < 54 ? 2 + r % 3 : (r - 54) & 1; }

__global__ __launch_bounds__(512, 2) void k_main(const u16* __restrict__ x, const u16* __restrict__ lre, const u16* __restrict__ lim,
                                                 const u16* __restrict__ bre, const u16* __restrict__ bim, const u16* __restrict__ cre,
                                                 const u16* __restrict__ cim, const u16* __restrict__ logdt, const u16* __restrict__ dsk,
                                                 const u16* __restrict__ wg, const u16* __restrict__ wu, const u16* __restrict__ wd,
                                                 char* __restrict__ ws, float* __restrict__ out) {
  extern __shared__ __attribute__((aligned(16))) char lds[];
  const int tid = threadIdx.x, w = __builtin_amdgcn_readfirstlane(tid >> 6);
  const int b = blockIdx.x, sx = b & 7, idx = b >> 3;
  unsigned lds0 = (unsigned)(size_t)(LAS char*)lds;
  asm volatile("" : "+s"(lds0));
  const int rt = idx & 7, cgl = idx >> 3, cg = sx * 4 + cgl;
  const rsrc_t rsW = mkrsrc(ws, (unsigned)WS_END);
  const unsigned htw = (unsigned)WS_HT + (unsigned)(((rt * 16 + 2 * w) * 8 + sx) * KS2) * 1024u;
  constexpr unsigned HTB = NKG * 1024;
  const unsigned flw = (unsigned)FL_H + (unsigned)((((rt * 8 + sx) * 4) * 8 + w) * 4) * 4u;
  const f32x4 zero4 = {0.f, 0.f, 0.f, 0.f};
  const unsigned l16 = (tid & 63) * 16;
  unsigned tag = 0;
  unsigned ldsw = lds0 + w * 1024; asm volatile("" : "+s"(ldsw));
  STAMP(0);

  f32x4 acc[8][2];
  f32x4 tac[5][2];
  f32x4 ac2[9][2];
  bf16x8 fr[FR];
  bf16x8 xf[18][2];
  constexpr int HR = 4;
  bf16x8 hf[HR][2][2];
  bf16x8 hl[7][2];
  u32x4 pv[9];
  unsigned flv[5] = {0, 0, 0, 0, 0};
  u32x2 epv[6];
  u32x2 eg[7];
  unsigned ept[3], epd[3];
  unsigned wvoff, ab[4], tvA = 0, tvF = 0;
  const char* wsrc = (const char*)((w >> 2) ? wu : wg) + (size_t)cg * 256;
  {
    const int lane = tid & 63, q = lane >> 4, li = lane & 15;
    const int rg = w & 3, r8 = lane >> 3, ch = lane & 7;
    const int g = ((lane >> 4) & 1) | ((rg & 1) << 1);
    wvoff = (unsigned)((8 * rg + r8) * (FF * 2) + (((ch >> 1) ^ g) << 5) + ((ch & 1) << 4));
    const int r4 = li >> 2, cc = lane & 3;
    const int gr = ((r4 >> 1) & 1) | ((q & 1) << 1);
    const unsigned a0 = (unsigned)((8 * q + r4) * 128 + (gr << 5) + 8 * cc);
#pragma unroll
    for (int k = 0; k < 4; k++) { ab[k] = lds0 + (a0 ^ (k << 5)); tie(ab[k]); }
    if (tid < 384) *(LAS float*)(lds0 + LDS_WTOT + tid * 4) = 0.f;
  }
  const char* xb = (const char*)x + (size_t)(rt * 256 + 32 * w) * (DM * 2);
  unsigned xvo, xvo1, xra, xra1;
  unsigned ldsx = lds0 + w * 6144; asm volatile("" : "+s"(ldsx));
  {
    const int lane = tid & 63, q = lane >> 4, li = lane & 15, r8 = lane >> 3;
    xvo = (unsigned)(r8 * (DM * 2) + (((lane & 7) ^ (r8 >> 1)) << 4));
    xra = lds0 + LDS_XS + w * 6144 + li * 128 + ((q ^ (li >> 1)) << 4);
    xvo1 = xvo ^ 64u; xra1 = xra ^ 64u;
    tie(xvo); tie(xra); tie(xvo1); tie(xra1);
  }
  u32x2 sgr;
  const unsigned pobase = (unsigned)WS_PART + (unsigned)(((rt * 4 + cgl) * 8 + sx) * 72) * 1024u;
  const unsigned pb = (unsigned)WS_PART + (unsigned)((((rt * 4 + cgl) * 8 + w) * 8 + sx) * 9) * 1024u;
  const char* wtb = ws + WS_WT + ((size_t)cgl * NKG + sx * KS2) * 9 * 1024 + w * 1024;
  const char* hb0 = ws + htw;
  const int erow = rt * 256 + 32 * sx;
  const int blk3 = w < 4 ? 2 * cgl + (w >> 1) : 8;
  const char* tB = (const char*)((w & 1) ? bim : bre) + (size_t)(8 * (9 * sx + blk3)) * (DM * 2);
  const size_t fcol = (size_t)(4096 + 64 * sx + 16 * cgl) * 2;
  auto fsrc = [&](int fp) { return (const char*)((fp & 1) ? wu : wg) + fcol + (size_t)(fp >> 1) * 32 * (FF * 2); };
  const char* tsF = nullptr; const char* tsA = nullptr; unsigned tsn = 0, tsu = 0, tdA = 0, tdn = 0;
  unsigned cla2 = 0;
  unsigned cla = 0;

  u32x4 cfr; unsigned pl[3], pkv = 0, pkl = 0;
  const int widx = w * 32 + idx;
  const int wi = widx < 162 ? widx : widx - 162, pj = (wi * 57) >> 9, pln = wi - 9 * pj;
  const int ci = widx < 180 ? widx : widx - 180, cd16 = (ci * 205) >> 10, cjj = ci - 5 * cd16;
  auto issue = [&](auto Ic) {
    constexpr Op o = S1.op[decltype(Ic)::value];
    if constexpr (o.kind == OP_G) {
      if constexpr (o.a < 12) {
        glds_c<(o.a / 6) * 128 + (3 * (o.a % 6) + o.n) * 32 * (FF * 2), p1_off(o.a) + o.n * 8192, false>(wsrc, wvoff, ldsw);
      } else {
        constexpr int U = o.a - 12;
        if constexpr (U == 0 && o.n == 0) {
          unsigned l_ = l16; tie(l_);
          const int lane = l_ >> 4, p = lane >> 1;
          tvF = (unsigned)((p ^ (((p >> 3) & 1) << 2)) * (FF * 2) + ((lane & 1) << 4));
          const unsigned vb = (unsigned)((lane >> 3) * (DM * 2) + (((lane & 7) ^ (4 * (w & 1) + (lane >> 4))) << 4));
          tvA = w < 6 ? vb : tvF; tie(tvA); tie(tvF);
          tsF = fsrc(w); tsA = w < 6 ? tB : tsF;
          tsn = w < 6 ? 128u : 32u * (FF * 2); tsu = w < 6 ? 128u : 128u * (FF * 2);
          tdA = ldsw + (w < 6 ? 12288 : 0); tdn = w < 6 ? 6144u : 2048u;
          asm volatile("" : "+s"(tsF), "+s"(tsA), "+s"(tsn), "+s"(tsu), "+s"(tdA), "+s"(tdn));
        }
        if constexpr (o.n < 3) {
          glds_run<p1_off(o.a)>(tvA, tsA, o.n < 2 ? tsn : tsu, tdA + o.n * tdn);
        } else glds_c<U * 192 * (FF * 2), p1_off(o.a), false>(tsF, tvF, ldsw);
      }
    } else if constexpr (o.kind == OP_X) {
      glds_c<((o.a & 1) * 16 + o.n * 8) * (DM * 2) + (o.a >> 1) * 128, LDS_XS + (o.a % 3) * 2048 + o.n * 1024, false>(xb, o.n ? xvo1 : xvo, ldsx);
    } else if constexpr (o.kind == OP_PK) {
      if constexpr (o.a == 0) {
        if constexpr (o.n == 0) {
          unsigned l_ = l16; tie(l_);
          pkv = (l_ >> 7) * (DM * 2) + (l_ & 112) + (unsigned)(kg_f0b(sx, pj) * (DM * 2) + pln * 128); tie(pkv);
          pkl = lds0 + (w < 6 ? LDS_PK + w * 4096 : LDS_TOT); asm volatile("" : "+s"(pkl));
        }
        glds_c<o.n * 8 * (DM * 2), o.n * 1024, false>(wd, pkv, pkl);
      } else {
        unsigned l_ = l16; tie(l_);
        const int lane = l_ >> 4, q = lane >> 4, li = lane & 15;
        const int cbl = 2 * cjj + (q >> 1);
        const char* cp = (const char*)((q & 1) ? cim : cre) + (unsigned)((16 * cd16 + li) * (DM * 2) + 16 * (9 * sx + (cbl < 9 ? cbl : 8)));
        asm volatile("global_load_dwordx4 %0, %1, off" : "=v"(cfr) : "v"(cp) : "memory");
      }
    } else if constexpr (o.kind == OP_SG) {
      gld8_sc1(sgr, ws + FL_S + (size_t)(rt * 256 + 32 * w) * 8, (l16 >> 1) & 248);
    } else if constexpr (o.kind == OP_PO) {
      st16_sc1(rsW, l16, pobase + (unsigned)(o.n * 8 + w) * 1024u, ones4());
    } else if constexpr (o.kind == OP_PH) {
      if constexpr (o.n < 2) st16_sc1(rsW, l16, htw + o.n * HTB + (unsigned)(18 + cgl) * 1024u, ones4());
      else {
        const unsigned f = (l16 & 512) ? 0u : ~0u;
        st16_sc1(rsW, cgl < 2 ? l16 : 0x70000000u, htw + (cgl & 1) * HTB + 22u * 1024u, (u32x4){f, f, f, f});
      }
    } else if constexpr (o.kind == OP_A) {
      constexpr int U = o.a;
      if constexpr (U % 5 == 2 && o.n == 1) glds_c<U * SLOT2 + 8192, 0, true>(wtb, l16, ldsw + (w >= 4 ? p2_off(U, 12) - 4096 : p2_off(U, 8)));
      else glds_c<U * SLOT2 + o.n * 8192, p2_off(U, 8 * o.n), true>(wtb, l16, ldsw);
      if constexpr (U < 11 && o.n == 1) { if (w < 2) glds_c<U * SLOT2 + 16384, p2_off(U, 16), true>(wtb, l16, ldsw); }
      if constexpr (U == 11) { if (w < 1) glds_c<U * SLOT2 + 8192, p2_off(U, 8), true>(wtb, l16, ldsw); }
    } else if constexpr (o.kind == OP_B) {
      constexpr int kg = 2 * o.a + (o.n >> 1);
      gld16o_sc1<(kg % 8 - 4) * 1024>(hf[o.a % HR][o.n >> 1][o.n & 1], hb0 + (size_t)(o.n & 1) * HTB + (kg / 8) * 8192 + 4096, l16);
    } else if constexpr (o.kind == OP_HL) {
      gld16o_sc1<((o.n >> 1) - 4) * 1024>(hl[o.n >> 1][o.n & 1], hb0 + (size_t)(o.n & 1) * HTB + 16384 + 4096, l16);
    } else if constexpr (o.kind == OP_PL) {
      gld16o_sc1<(o.n - 4) * 1024>(pv[o.n], ws + (size_t)pobase + (size_t)w * 9216 + 4096, l16);
    } else if constexpr (o.kind == OP_EG) {
      gld8o<o.n * 384, false>(eg[o.n], ws + FL_E + (size_t)(cg * 16 + rt + 1) * 384, cla2);
    } else if constexpr (o.kind == OP_FL) {
      unsigned lz = l16; tie(lz);
      if constexpr (o.a < 3) gld4_sc1(flv[o.a], ws + flw + 128 * o.a, (lz >> 2) & 12);
      else if constexpr (o.a == 3) gld4_sc1(flv[3], ws + FL_W + (size_t)(w * 32 + idx) * 4, lz & 0);
      else gld4_sc1(flv[4], ws + FL_W + (size_t)sx * 128, (lz >> 2) & 124);
    } else if constexpr (o.kind == OP_EP) {
      constexpr int r = o.n / 2;
      if constexpr (o.n % 2 == 0) {
        unsigned l_ = l16; tie(l_);
        const unsigned e = min((l_ >> 4) + 64u * w + 512u * r, 1151u), t = (e * 1821u) >> 16;
        ept[r] = t; epd[r] = (e - 36u * t) * 8u; tie(ept[r]); tie(epd[r]);
        gld8(epv[o.n], x + (size_t)erow * DM + cgl * 144, t * (DM * 2) + epd[r]);
      } else gld8(epv[o.n], dsk + cgl * 144, epd[r]);
    }
  };
  auto issue_at = [&](auto Pc) {
    constexpr int i0 = S1.first_at(decltype(Pc)::value), i1 = S1.first_at(decltype(Pc)::value + 1);
    sfor<i0, i1>([&](auto Ic) { issue(Ic); SCHED0; });
  };
  auto xconv = [&](auto Hc) {
    constexpr int h = decltype(Hc)::value, c = h >> 1, t = h & 1;
    xf[2 * c][t] = ldsfrag(xra + (h % 3) * 2048);
    xf[2 * c + 1][t] = ldsfrag(xra1 + (h % 3) * 2048);
  };
  auto htie = [&](auto Uc) { constexpr int U = decltype(Uc)::value; tie(hf[U % HR][0][0]); tie(hf[U % HR][0][1]); tie(hf[U % HR][1][0]); tie(hf[U % HR][1][1]); };
  auto chk_flag = [&](auto Ac) { constexpr int a = decltype(Ac)::value; tie(flv[a]);
    if (!__all(flv[a] == tag)) {
      unsigned lz = l16; tie(lz);
      if constexpr (a < 3) spin_flag(ws + flw + 128 * a, (lz >> 2) & 12, tag);
      else if constexpr (a == 3) spin_flag(ws + FL_W + (size_t)(w * 32 + idx) * 4, lz & 0, tag);
      else spin_flag(ws + FL_W + (size_t)sx * 128, (lz >> 2) & 124, tag); } };

  float te = 0.f, pp = 0.f, hh[2], sc1[2], sc2[2], sng[2];
  f32x4 csw[2];
  f32x4 csv, tst[2];
  f32x4 csa0, csa1, qs[8];
  bf16x8 selA, selB; unsigned cra = 0, crb = 0;
  unsigned hp[2][4];
  float cm[3][8], ca4[3][2], se[3][2], eg4[3][2][4], spre[3][2], sv[3][2][4], cc1[3][2], cin[3][2];
  float tw[8], c32[2], wpre = 0.f, crt = 0.f, cz[3], cpw[3][2], c256[2];
  unsigned fwa = 0, fra = 0, fvo = 0;
  unsigned cs48 = 0;
#define CPAIR(base, pr) (*(LAS f32x2*)((base) + (pr) * 128))
  unsigned csa = 0, pkd[4];
  bool q1 = false, q2 = false;
  auto side = [&](auto Sc) {
    constexpr int sl = decltype(Sc)::value;
    {
      if constexpr (silu_at(sl) >= 0) {
        constexpr int s = silu_at(sl), r = sl - SILU0[s], N = SILUN[s];
        constexpr int pr2 = r / 9, ph = r % 9;
        auto G = [&](auto Ec) -> float { constexpr int e = decltype(Ec)::value; if constexpr (s < 2) return acc[e >> 3][(e >> 2) & 1][e & 3]; else return tac[0][e >> 2][e & 3]; };
        auto Uv = [&](auto Ec) -> float { constexpr int e = decltype(Ec)::value; if constexpr (s < 2) return acc[4 + (e >> 3)][(e >> 2) & 1][e & 3]; else return tac[1][e >> 2][e & 3]; };
        constexpr int e0 = 2 * pr2, e1 = e0 + 1;
        if constexpr (ph == 0) { if constexpr (pr2 > 0) v_mul(hh[1], pp, te); if constexpr (e0 < N) v_mul(te, G(IC<e0 < N ? e0 : 0>{}), sc1[(e0 >> 2) & 1]); }
        if constexpr (ph == 1) {
          if constexpr (pr2 > 0) {
            constexpr int ep = e0 - 2, t = (ep >> 2) & 1;
            v_cvt(hp[t][s < 2 ? 2 * ((ep >> 3) & 1) + ((ep >> 1) & 1) : ((ep >> 1) & 1)], hh[0], hh[1]);
            if constexpr (s < 2 && (e0 & 15) == 0) {
#pragma unroll
              for (int tt = 0; tt < 2; tt++)
                st16_sc1(rsW, l16, htw + tt * HTB + (unsigned)(8 * s + 2 * cgl + (ep >> 4)) * 1024u, (u32x4){hp[tt][0], hp[tt][1], hp[tt][2], hp[tt][3]});
            }
            if constexpr (s == 2 && e0 == 8) {
#pragma unroll
              for (int tt = 0; tt < 2; tt++) st8_sc1(rsW, l16, htw + tt * HTB + (unsigned)((16 + (cgl >> 1)) * 1024 + (cgl & 1) * 8), (u32x2){hp[tt][0], hp[tt][1]});
            }
          }
        }
        if constexpr (e0 < N) {
          if constexpr (ph == 2 || ph == 6) v_exp2(te);
          if constexpr (ph == 3) { v_fmaa(te, sc2[(e0 >> 2) & 1]); v_mul(pp, G(IC<e0>{}), Uv(IC<e0>{})); }
          if constexpr (ph == 4 || ph == 8) v_rcp(te);
          if constexpr (ph == 5) { v_mul(hh[0], pp, te); v_mul(te, G(IC<e1>{}), sc1[(e0 >> 2) & 1]); }
          if constexpr (ph == 7) { v_fmaa(te, sc2[(e0 >> 2) & 1]); v_mul(pp, G(IC<e1>{}), Uv(IC<e1>{})); }
        }
      }
    }
    if constexpr (sl == SILU0[0] - 4 || sl == SILU0[1] - 4 || sl == SILU0[2] - 4) {
      unsigned lz = l16; tie(lz);
#pragma unroll
      for (int t = 0; t < 2; t++) { const f32x2 f = *(LAS f32x2*)(lds0 + LDS_ST + w * 512 + t * 256 + (lz & 240)); sc1[t] = f[0]; sc2[t] = f[1]; }
    }
    if constexpr (sl == 2 * gl(3, SY1) || sl == 2 * gl(9, SY1) || sl == SQC - 14) {
      unsigned lz = l16; tie(lz);
#pragma unroll
      for (int t = 0; t < 2; t++) sng[t] = *(LAS float*)(lds0 + LDS_ST + w * 512 + t * 256 + (lz & 240) + 8);
      cra = lds0 + LDS_CS + (lz >> 8) * 16; tie(cra);
    }
    {
      constexpr int pass = sl / 2 >= gl(6, 0) ? 1 : 0, k = sl / 2 - gl(6 * pass + 3, SY1 + 2), half = sl & 1;
      if constexpr (k >= 0 && k < 40) {
        constexpr int it = CR.item[k];
        if constexpr (it >= 0) { constexpr int bk = it >> 1, t = it & 1; float a0 = acc[bk][t][2 * half], a1 = acc[bk][t][2 * half + 1];
          v_fmac(a0, sng[t], csw[k & 1][2 * half]); v_fmac(a1, sng[t], csw[k & 1][2 * half + 1]); acc[bk][t][2 * half] = a0; acc[bk][t][2 * half + 1] = a1; }
      }
      if constexpr (k >= -2 && k < 38 && half == 1) {
        constexpr int nx = CR.item[k + 2];
        if constexpr (nx >= 0) csw[k & 1] = *(LAS f32x4*)(cra + pass * 512 + ((nx >> 1) >> 2) * 256 + ((nx >> 1) & 3) * 64);
      }
    }
    if constexpr (sl == SQC - 12) {
#pragma unroll
      for (int k = 0; k < 8; k++) qs[k] = *(LAS f32x4*)(cra + 1280 + k * 64);
    }
    if constexpr (sl >= SQC - 8 && sl < SQC) {
      constexpr int r = sl - (SQC - 8);
#pragma unroll
      for (int e = 3 * r; e < 3 * r + 3; e++) { const int k = 2 + e / 4, j = e & 3; float a = qs[k & 1][j]; asm volatile("v_add_f32 %0, %0, %1" : "+v"(a) : "v"(qs[k][j])); qs[k & 1][j] = a; }
      if constexpr (r == 7) { csv = qs[0]; csa0 = qs[1]; }
    }
    if constexpr (sl >= SQC && sl < SQC + 8) {
      constexpr int r = sl - SQC, mt = r >> 2, t = (r >> 1) & 1, half = r & 1;
      float a0 = tac[mt][t][2 * half], a1 = tac[mt][t][2 * half + 1];
      if constexpr (mt == 0) { v_fmac(a0, sng[t], csv[2 * half]); v_fmac(a1, sng[t], csv[2 * half + 1]); }
      else { v_fmac(a0, sng[t], csa0[2 * half]); v_fmac(a1, sng[t], csa0[2 * half + 1]); }
      tac[mt][t][2 * half] = a0; tac[mt][t][2 * half + 1] = a1;
    }
    if constexpr (sl == 2 * GSYC) {
      unsigned lz = l16; tie(lz);
      crb = lds0 + LDS_ST + w * 512 + (lz >> 8) * 64; tie(crb);
#pragma unroll
      for (int t = 0; t < 2; t++)
#pragma unroll
        for (int j = 0; j < 4; j++) tst[t][j] = *(LAS float*)(crb + t * 256 + j * 16 + 8);
#pragma unroll
      for (int k = 0; k < 3; k++) csv[k] = *(LAS float*)(lds0 + LDS_CS + 1024 + k * 64 + ((lz >> 2) & 60));
    }
    if constexpr (sl >= 2 * (GT + 48) && sl < 2 * (GT + 54)) {
      constexpr int r = sl / 2 - (GT + 48), k = (r + 1) % 3, t = r / 3, half = sl & 1;
      float a0 = tac[2 + k][t][2 * half], a1 = tac[2 + k][t][2 * half + 1];
      v_fmac(a0, tst[t][2 * half], csv[k]); v_fmac(a1, tst[t][2 * half + 1], csv[k]);
      if constexpr (half) dnop();
      tac[2 + k][t][2 * half] = a0; tac[2 + k][t][2 * half + 1] = a1;
    }
    if constexpr (sl == S1R - 2) {
#pragma unroll
      for (int t = 0; t < 2; t++)
#pragma unroll
        for (int j = 0; j < 4; j++) tst[t][j] = *(LAS float*)(crb + t * 256 + j * 16 + 12);
    }
    if constexpr (sl >= S1R && sl < S1R + 12) {
      constexpr int r = sl - S1R, k = r >> 2, t = (r >> 1) & 1, half = r & 1;
      float a0 = tac[2 + k][t][2 * half], a1 = tac[2 + k][t][2 * half + 1];
      v_muli(a0, tst[t][2 * half]); v_muli(a1, tst[t][2 * half + 1]);
      tac[2 + k][t][2 * half] = a0; tac[2 + k][t][2 * half + 1] = a1;
    }
    if constexpr (sl == S1A - 12) {
      unsigned l_ = l16; tie(l_); const int tl = (int)(l_ >> 4) + 64 * w;
      const int lane = tl & 63;
      csa = lds0 + LDS_CST + (lane & 15) * 8; cla = (unsigned)(lane < 48 ? lane : lane - 16) * 4u;
      cs48 = csa + (cla >> 6) * 2048; cla2 = cla * 2; tie(cla2);
      q1 = lane & 16; q2 = lane & 32;
    }
    if constexpr (sl == S1A - 6 || sl == S1A + 2 || sl == S1A + 10) {
      constexpr int k = (sl - (S1A - 6)) / 8;
#pragma unroll
      for (int i = 0; i < 4; i++) { const f32x2 m = CPAIR(csa + k * 2048, 1 + i); cm[k][2 * i] = m[0]; cm[k][2 * i + 1] = m[1]; }
    }
    if constexpr (sl >= S1A && sl < S1A + 24) {
      constexpr int r = sl - S1A, k = r / 8, j = (r % 8) >> 1, t = r & 1;
      if constexpr (j == 0) cmul(se[k][t], cm[k][6], cm[k][7], tac[2 + k][t][0]);
      else cmac(se[k][t], cm[k][2 * (3 - j)], cm[k][2 * (3 - j) + 1], tac[2 + k][t][j]);
    }
    if constexpr (sl == S1A + 36) {
#pragma unroll
      for (int k = 0; k < 3; k++) { const f32x2 a4 = CPAIR(csa + k * 2048, 6); ca4[k][0] = a4[0]; ca4[k][1] = a4[1]; }
    }
    if constexpr (sl == SCM - 4) {
#pragma unroll
      for (int k = 0; k < 3; k++) { const f32x2 mm = CPAIR(csa + k * 2048, 1); cin[k][0] = mm[0]; cin[k][1] = mm[1]; }
    }
    if constexpr (sl >= S1A + 24 && sl < S1A + 42) {
      constexpr int r = sl - (S1A + 24), st = r / 6, k = (r % 6) >> 1, t = r & 1;
      if constexpr (st == 0) {
        const unsigned pu = __float_as_uint(se[k][t]);
        const auto s16 = __builtin_amdgcn_permlane16_swap(pu, pu, false, false);
        eg4[k][t][0] = __uint_as_float(s16[0]); eg4[k][t][1] = __uint_as_float(s16[1]);
      } else {
        const unsigned pu = __float_as_uint(eg4[k][t][st - 1]);
        const auto sw = __builtin_amdgcn_permlane32_swap(pu, pu, false, false);
        eg4[k][t][st - 1] = __uint_as_float(sw[0]); eg4[k][t][st + 1] = __uint_as_float(sw[1]);
      }
    }
    if constexpr (sl >= S1A + 42 && sl < S1A + 63) {
      constexpr int r = sl - (S1A + 42), sgm = 1 + r / 3, k = r % 3;
      cmac(eg4[k][sgm >> 2][sgm & 3], ca4[k][0], ca4[k][1], eg4[k][(sgm - 1) >> 2][(sgm - 1) & 3]);
    }
    if constexpr (sl == S1A + 63) {
      *(LAS float*)(lds0 + LDS_WTOT + (8 + w) * 192 + cla) = q2 ? eg4[2][1][3] : (q1 ? eg4[1][1][3] : eg4[0][1][3]);
    }
    if constexpr (sl >= S1SEL && sl < S1SEL + 6) {
      constexpr int r = sl - S1SEL, k = r >> 1, t = r & 1;
      const float lo = q1 ? eg4[k][t][0] : (t ? eg4[k][0][3] : 0.f), hi = q1 ? eg4[k][t][2] : eg4[k][t][1];
      spre[k][t] = q2 ? hi : lo; tie(spre[k][t]);
    }
    if constexpr (sl >= SCE && sl < SCE + 7) {
      constexpr int k = sl - SCE + 1;
      cmac(tw[k], c32[0], c32[1], tw[k - 1]);
      if constexpr (k == 7) {
        wpre = tw[6];
        __builtin_amdgcn_raw_buffer_store_b64((u32x2){__float_as_uint(tw[7]), tag}, rsW, (w == 7 && l16 < 768) ? (unsigned)FL_E + (unsigned)((cg * 16 + 8 + rt) * 384) + cla2 : 0x7ffffff0u, 0, 16);
      }
    }
    if constexpr (sl >= SCM && sl < SCM + 24) {
      constexpr int r = sl - SCM, k = r / 8, t = (r >> 2) & 1, j = r & 3;
      cmul(sv[k][t][j], cin[k][0], cin[k][1], tac[2 + k][t][j]);
    }
    if constexpr (sl == SFIN - 8) {
      const f32x2 a = CPAIR(cs48, 13), p0 = CPAIR(cs48, (w & 1) ? 10 : 5), p1 = CPAIR(cs48, (w & 2) ? 11 : 5), p2 = CPAIR(cs48, (w & 4) ? 12 : 5);
      c256[0] = a[0]; c256[1] = a[1]; cpw[0][0] = p0[0]; cpw[0][1] = p0[1]; cpw[1][0] = p1[0]; cpw[1][1] = p1[1]; cpw[2][0] = p2[0]; cpw[2][1] = p2[1];
    }
    if constexpr (sl >= SFIN - 6 && sl < SFIN - 3) {
      constexpr int k = sl - (SFIN - 6);
      unsigned lz = l16; tie(lz);
      const f32x2 a1 = CPAIR(csa + k * 2048, 0), pq = CPAIR(csa + k * 2048, 5 + (lz >> 8)), a16 = CPAIR(csa + k * 2048, 9);
      cc1[k][0] = a1[0]; cc1[k][1] = a1[1]; cin[k][0] = pq[0]; cin[k][1] = pq[1]; ca4[k][0] = a16[0]; ca4[k][1] = a16[1];
    }
    if constexpr (sl >= SFIN && sl < SFIN + 6) {
      constexpr int r = sl - SFIN + 1;
      float n = __uint_as_float(eg[r][0]); cmac(n, c256[0], c256[1], crt); crt = n;
    }
    if constexpr (sl == SFIN + 6) { float n; cmul(n, cpw[0][0], cpw[0][1], crt); crt = n; }
    if constexpr (sl == SFIN + 7) { float n; cmul(n, cpw[1][0], cpw[1][1], crt); crt = n; }
    if constexpr (sl == SFIN + 8) cmac(wpre, cpw[2][0], cpw[2][1], crt);
    if constexpr (sl == SFIN + 9) {
      unsigned lz = l16; tie(lz);
      const unsigned scr = lds0 + LDS_SCR + w * 1024, q = lz >> 8, li = (lz >> 4) & 15;
      fwa = scr + li * 32 + q * 8;
      fra = scr + (q >> 1) * 512 + (8 * (q & 1) + (li >> 2)) * 32 + (li & 3) * 8;
      fvo = (q >> 1) * HTB + ((q & 1) * 16 + li) * 16;
      tie(fwa); tie(fra); tie(fvo);
    }
    if constexpr (sl == SFIN + 10) {
      const unsigned pu = __float_as_uint(wpre);
      const auto s16 = __builtin_amdgcn_permlane16_swap(pu, pu, false, false);
      cz[0] = __uint_as_float(s16[0]); cz[1] = __uint_as_float(s16[1]);
    }
    if constexpr (sl == SFIN + 11) { const unsigned pu = __float_as_uint(cz[0]); const auto sw = __builtin_amdgcn_permlane32_swap(pu, pu, false, false); cz[0] = __uint_as_float(sw[0]); cz[2] = __uint_as_float(sw[1]); }
    if constexpr (sl == SFIN + 12) { const unsigned pu = __float_as_uint(cz[1]); const auto sw = __builtin_amdgcn_permlane32_swap(pu, pu, false, false); cz[1] = __uint_as_float(sw[0]); }
    if constexpr (sl >= SFIN + 13 && sl < SFIN + 16) { constexpr int k = (sl - (SFIN + 13)) == 1 ? 2 : ((sl - (SFIN + 13)) == 2 ? 1 : 0); float n; cmul(n, cin[k][0], cin[k][1], cz[k]); cz[k] = n; }
    if constexpr (sl >= SFIN + 16 && sl < SFIN + 19) { constexpr int k = (sl - (SFIN + 16)) == 1 ? 2 : ((sl - (SFIN + 16)) == 2 ? 1 : 0); cmac(spre[k][1], ca4[k][0], ca4[k][1], cz[k]); }
    if constexpr (sl == SFIN + 19) { asm volatile("v_add_f32 %0, %0, %1" : "+v"(spre[0][0]) : "v"(cz[0])); asm volatile("v_add_f32 %0, %0, %1" : "+v"(spre[1][0]) : "v"(cz[1])); asm volatile("v_add_f32 %0, %0, %1" : "+v"(spre[2][0]) : "v"(cz[2])); }
    if constexpr (sl >= SFIN + 20 && sl < SFIN + 44) {
      constexpr int r = sl - (SFIN + 20), j = r / 6, k = (r % 6) >> 1, t = (r & 1) ^ 1;
      cmac(sv[k][t][j], cc1[k][0], cc1[k][1], j == 0 ? spre[k][t] : sv[k][t][j > 0 ? j - 1 : 0]);
    }
    if constexpr (sl >= SFIN + 44 && sl < SFIN + 53) {
      constexpr int r = sl - (SFIN + 44), k = r / 3, ph = r % 3;
      if constexpr (ph < 2) {
        v_cvt(pkd[0], sv[k][ph][0], sv[k][ph][1]); v_cvt(pkd[1], sv[k][ph][2], sv[k][ph][3]);
        *(LAS u32x2*)(fwa + ph * 512) = (u32x2){pkd[0], pkd[1]};
      } else {
        const u32x4 o = __builtin_bit_cast(u32x4, trfrag<128>(fra));
        st16_sc1(rsW, (k == 2 && cgl) ? 0x70000000u : fvo, htw + (k < 2 ? (unsigned)((18 + cgl) * 1024 + k * 512) : 22u * 1024u), o);
      }
    }
    if constexpr (sl >= 2 * late_g(9) && sl < 2 * GEND) {
      constexpr int r = sl - 2 * late_g(9), db = r / 14, ph = r % 14;
      if constexpr (db < 8) {
        if constexpr (ph == 0) { unsigned a, c; v_cvt(a, ac2[db][0][0], ac2[db][0][1]); v_cvt(c, ac2[db][0][2], ac2[db][0][3]); pkd[0] = a; pkd[1] = c; }
        if constexpr (ph == 1) { unsigned a, c; v_cvt(a, ac2[db][1][0], ac2[db][1][1]); v_cvt(c, ac2[db][1][2], ac2[db][1][3]); st16_sc1(rsW, l16, pb + db * 1024, (u32x4){pkd[0], pkd[1], a, c}); }
      }
    }
  };

  unsigned fa[2], eb[2], a2b[3];
  s16x4 flo;
  auto trl = [&](unsigned a) { return __builtin_amdgcn_ds_read_tr16_b64_v4i16((LAS s16x4*)(a)); };
  auto join = [&](s16x4 a, s16x4 c) { return __builtin_bit_cast(bf16x8, __builtin_shufflevector(a, c, 0, 1, 2, 3, 4, 5, 6, 7)); };
  auto frag = [&](auto Gc, auto Pc) {
    constexpr int G = decltype(Gc)::value, part = decltype(Pc)::value;
    if constexpr (G < GT) {
      constexpr LF f = l_frag(G);
      constexpr int o = (f.ks % 3) * 8192 + (f.blk >> 2) * 4096;
      if constexpr (part == 0) flo = trl(ab[f.blk & 3] + o); else fr[G % FR] = join(flo, trl(ab[f.blk & 3] + o + 512));
    } else if constexpr (G < G2) {
      constexpr int ks = t_ks(G - GT), blk = t_blk(G - GT), uo = (ks / 6) * 30720, k6 = ks % 6;
      if constexpr (blk < 2) {
        constexpr int o = uo - (ks >= 12 ? 30720 : 0) + (k6 * 2 + blk) * 1024;
        if constexpr (part == 0) flo = trl(fa[0] + o); else fr[G % FR] = join(flo, trl(fa[1] + o));
      } else if constexpr (part == 1) fr[G % FR] = ldsfrag(eb[k6 & 1] + uo - (ks >= 12 ? 30720 : 0) + 12288 + (k6 >> 1) * 6144 + (blk - 2) * 2048);
    } else if constexpr (part == 1) {
      constexpr int U = (G - G2) / 18, g = (G - G2) % 18;
      fr[G % FR] = ldsfrag(p2_off(U, g) >= 65536 ? (p2_off(U, g) >= 131072 ? a2b[2] + (p2_off(U, g) - 131072) : a2b[1] + (p2_off(U, g) - 65536)) : a2b[0] + p2_off(U, g));
    }
  };
  auto addr_step = [&](auto Gc) {
    constexpr int G = decltype(Gc)::value;
    if constexpr (G < GT) {
      constexpr int u = l_frag(G).unit, pu = l_prev_unit(G), k = l_frag(G).blk & 3;
      if constexpr (u != pu) { ab[k] += (unsigned)(p1_off(u) - p1_off(pu)); tie(ab[k]); }
    }
    if constexpr (G == GT) {
      unsigned l_ = l16; tie(l_); const int tl = (int)(l_ >> 4) + 64 * w;
      const int lane = tl & 63, q = lane >> 4, li = lane & 15;
      const unsigned e0 = li * 128 + ((q ^ (li >> 1)) << 4);
      eb[0] = lds0 + p1_off(12) + e0; eb[1] = lds0 + p1_off(12) + (e0 ^ 64u); tie(eb[0]); tie(eb[1]);
    }
    if constexpr (G == GT + 36) { eb[0] += 30720; eb[1] += 30720; tie(eb[0]); tie(eb[1]); }
    if constexpr (G == GT + 78) { fa[0] += 30720; fa[1] += 30720; tie(fa[0]); tie(fa[1]); }
    if constexpr (G == G2) { a2b[0] = lds0 + l16; a2b[1] = lds0 + l16 + 65536; a2b[2] = lds0 + l16 + 131072; tie(a2b[0]); tie(a2b[1]); tie(a2b[2]); }
  };
  auto mma = [&](auto Gc, auto Tc) {
    constexpr int G = decltype(Gc)::value, t = decltype(Tc)::value;
    const bf16x8 a = fr[G % FR];
    if constexpr (G < GT) {
      constexpr LF f = l_frag(G);
      mfma<f.ks == 0>(acc[f.blk][t], a, xf[f.ks][t]);
    } else if constexpr (G < G2) {
      constexpr int ks = t_ks(G - GT), blk = t_blk(G - GT);
      if constexpr (blk < 2) mfma<ks == 0>(tac[blk][t], a, xf[ks][t]);
      else mfma<ks == 0>(tac[blk][t], xf[ks][t], a);
    } else if constexpr (G < late_g(0)) {
      constexpr int U = (G - G2) / 18, g = (G - G2) % 18, kk = g / 9, db = g % 9;
      mfma<(U == 0 && kk == 0)>(ac2[db][t], a, hf[U % HR][kk][t]);
    } else {
      constexpr int f = G - late_g(0), db = f / 7, kk = f % 7;
      mfma<false>(ac2[db][t], a, hl[kk][t]);
    }
  };

  auto sync = [&](auto Gc) {
    constexpr int G = decltype(Gc)::value;
    if constexpr (G < GT && G % 24 == (G < 24 ? 16 : SY1)) {
      constexpr int V = G / 24;
      constexpr int need = V + 1 < 12 ? (l_dbl(V + 1) ? (((V + 1) & 1) ? -1 : V + 2) : V + 1) : 12;
      constexpr int ng = need >= 0 ? S1.after(OP_G, need, G) : 63;
      constexpr int ng2 = V == 2 ? S1.after(OP_G, 4, G) : (V == 8 ? S1.after(OP_G, 11, G) : 63);
      constexpr int ns = V == 10 ? cmin(S1.after(OP_ST, 0, G), S1.after(OP_PO, 0, G), S1.after(OP_ST, 9, G)) : 63;
      waitv<cmin(ng, ng2, ns)>();
      if constexpr (V == 3 || V == 9) asm volatile("s_waitcnt lgkmcnt(0)" ::: "memory");
      if constexpr (V == 10) setflag(rsW, l16 == 0, flw + cgl * 4, tag);
      bar();
      if constexpr (V == 10) setflag(rsW, l16 == 0 && w == 1, (unsigned)FL_W + (unsigned)(sx * 32 + idx) * 4u, tag);
      SCHED0;
    }
    if constexpr (G == gt(0, 18)) { waitv<S1.after(OP_FL, 4, G)>(); chk_flag(IC<4>{}); SCHED0; }
    if constexpr (G == gl(2, 14)) {
      waitv<S1.after(OP_SG, 0, G)>();
      for (unsigned it = 0; it < (1u << 18); ++it) {
        tie(sgr);
        if (__all(sgr[1] == tag)) break;
        __builtin_amdgcn_s_sleep(1);
        asm volatile("global_load_dwordx2 %0, %1, %2 sc1" : "+v"(sgr) : "v"((l16 >> 1) & 248), "s"(ws + FL_S + (size_t)(rt * 256 + 32 * w) * 8) : "memory");
        waitv<0>();
      }
      tie(sgr);
      const float den = bhi(sgr[0]), rs = __builtin_amdgcn_rcpf(den);
      *(LAS f32x4*)(lds0 + LDS_ST + w * 512 + (l16 & 496)) = (f32x4){rs * -1.44269504089f, den * den, -blo(sgr[0]), rs};
      SCHED0;
    }
    if constexpr (G == gl(3, 1)) { waitv<S1.after(OP_G, 5, G)>(); bar(); SCHED0; }
    if constexpr (G == GSYC) { asm volatile("s_waitcnt lgkmcnt(0)" ::: "memory"); bar(); SCHED0; }
    if constexpr (G == gt(0, SYT)) { waitv<S1.after(OP_G, 13, G)>(); bar(); SCHED0; }
    if constexpr (G == gt(1, SYT)) { waitv<cmin(S1.after(OP_G, 14, G), S1.after(OP_FL, 0, G))>(); bar(); chk_flag(IC<0>{}); SCHED0; }
    if constexpr (G == gt(2, 2)) { waitv<S1.after(OP_ST, 1, G)>(); setflag(rsW, l16 == 0, flw + 128 + cgl * 4, tag); SCHED0; }
    if constexpr (G == gt(2, 27)) {
      waitv<cmin(S1.after(OP_A, 0, G), S1.after(OP_B, 0, G))>(); htie(IC<0>{});
      bar(); SCHED0;
    }
    if constexpr (G >= G2 && (G - G2) % 18 == SY2 && (G - G2) / 18 < 11) {
      constexpr int U = (G - G2) / 18;
      constexpr int nab = cmin(S1.after(OP_A, U + 1, G), U + 1 < 8 ? S1.after(OP_B, U + 1, G) : (U == 7 ? S1.after(OP_HL, 0, G) : 63));
      constexpr int nfl = U == 1 ? S1.after(OP_FL, 1, G) : (U == 3 ? cmin(S1.after(OP_ST, 2, G), S1.after(OP_EG, 0, G)) : (U == 5 ? S1.after(OP_FL, 2, G) : 63));
      static_assert(UFIN == 3, "");
      if constexpr (U == 0) asm volatile("s_waitcnt lgkmcnt(0)" ::: "memory");
      waitv<cmin(nab, nfl)>();
      if constexpr (U + 1 < 8) htie(IC<U + 1>{});
      if constexpr (U == 7) {
        tie(hl[0][0]); tie(hl[0][1]); tie(hl[1][0]); tie(hl[1][1]);
        for (unsigned it = 0; it < (1u << 16); ++it) {
          unsigned m = 0;
#pragma unroll
          for (int n = 4; n < 14; n++) { tie(hl[n >> 1][n & 1]); const u32x4 v = __builtin_bit_cast(u32x4, hl[n >> 1][n & 1]); asm("v_max3_u32 %0, %0, %1, %2" : "+v"(m) : "v"(v[0]), "v"(v[1])); asm("v_max3_u32 %0, %0, %1, %2" : "+v"(m) : "v"(v[2]), "v"(v[3])); }
          if (__all(m != ~0u)) break;
          __builtin_amdgcn_s_sleep(4);
          sfor<4, 14>([&](auto Nc) { constexpr int n = decltype(Nc)::value;
            gld16o_io<((n >> 1) - 4) * 1024>(hl[n >> 1][n & 1], hb0 + (size_t)(n & 1) * HTB + 16384 + 4096, l16); });
          waitv<0>();
        }
#pragma unroll
        for (int n = 4; n < 14; n++) tie(hl[n >> 1][n & 1]);
        chk_flag(IC<3>{});
      }
      if constexpr (U == UFIN) {
        for (unsigned it = 0; it < (1u << 18); ++it) {
          unsigned d = 0;
#pragma unroll
          for (int r = 0; r < 7; r++) { tie(eg[r]); d |= eg[r][1] ^ tag; }
          if (__all(d == 0)) break;
          __builtin_amdgcn_s_sleep(1);
          sfor<0, 7>([&](auto Rc) { gld8o<decltype(Rc)::value * 384, true>(eg[decltype(Rc)::value], ws + FL_E + (size_t)(cg * 16 + rt + 1) * 384, cla2); });
          waitv<0>();
        }
#pragma unroll
        for (int r = 0; r < 7; r++) tie(eg[r]);
        crt = __uint_as_float(eg[0][0]);
        setflag(rsW, l16 == 0, flw + 256 + cgl * 4, tag);
      }
      bar();
      if constexpr (U == 1) chk_flag(IC<1>{});
      if constexpr (U == 5) chk_flag(IC<2>{});
      if constexpr (U == 0) {
        const f32x2 c = CPAIR(cs48, 10);
        c32[0] = c[0]; c32[1] = c[1];
#pragma unroll
        for (int k = 0; k < 8; k++) tw[k] = *(LAS float*)(lds0 + LDS_WTOT + (w + 1 + k) * 192 + cla);
      }
      SCHED0;
    }
  };

  unsigned wm[3];
  {
    unsigned l_ = l16; tie(l_); const int tl = (int)(l_ >> 4) + 64 * w;
    const int lane = tl & 63, q = lane >> 4, li = lane & 15;
    unsigned ep; u32x4 xr0, xr1;
    issue_at(IC<-3>{});
    bar();
    gld4_sc1(ep, ws + FL_EPOCH, l16 & 0);
    const int row = b * 8 + w;
    gld16(xr0, x + (size_t)row * DM, l16);
    gld16(xr1, x + (size_t)row * DM, 1024u + (l16 & 112));
    {
      const int chn = 8 * (9 * sx + (lane < 16 ? 2 * cgl + (lane >> 3) : 8)) + (lane & 7);
      asm volatile("global_load_ushort %0, %1, %2" : "=v"(pl[0]) : "v"(chn * 2), "s"(lre) : "memory");
      asm volatile("global_load_ushort %0, %1, %2" : "=v"(pl[1]) : "v"(chn * 2), "s"(lim) : "memory");
      asm volatile("global_load_ushort %0, %1, %2" : "=v"(pl[2]) : "v"(chn * 2), "s"(logdt) : "memory");
    }
    SCHED0;
    issue_at(IC<-1>{});
    {
      const int n = tl, r = 72 * rt + n / 6, c6 = n % 6;
      const char* p0 = (const char*)((c6 & 1) ? wu : wg) + (size_t)r * (FF * 2) + (c6 < 4 ? cg * 256 + (c6 >> 1) * 128 : fcol);
      const int m = rt * 54 + (tl - 432 < 54 ? tl - 432 : 0), rb = m / 9;
      const char* p1 = (const char*)((rb & 8) ? bim : bre) + (size_t)(8 * (9 * sx + ((rb >> 4) < 2 ? 2 * cgl + (rb >> 4) : 8)) + (rb & 7)) * (DM * 2) + (m - 9 * rb) * 128;
      asm volatile("global_load_dword %0, %1, off" : "=v"(wm[0]) : "v"(tl < 432 ? p0 : p1) : "memory");
      asm volatile("global_load_dword %0, %1, %2" : "=v"(wm[2]) : "v"((unsigned)((8 * cgl + (lane >> 3)) * (DM * 2) + 128 + (lane & 7) * 128)), "s"(xb) : "memory");
    }
    SCHED0;
    waitv<7>();
    tie(ep); tie(xr0); tie(xr1); tie(pl[0]); tie(pl[1]); tie(pl[2]);
    tag = (unsigned)__builtin_amdgcn_readfirstlane(ep) + 1u;
    STAMP(12);
    {
      float v[16];
      const bool ex = lane < 8;
#pragma unroll
      for (int k = 0; k < 4; k++) { v[2 * k] = blo(xr0[k]); v[2 * k + 1] = bhi(xr0[k]); v[8 + 2 * k] = ex ? blo(xr1[k]) : 0.f; v[9 + 2 * k] = ex ? bhi(xr1[k]) : 0.f; }
      float sm = 0.f;
#pragma unroll
      for (int k = 0; k < 16; k++) sm += v[k];
      sm = wave_sum(sm);
      const float mean = sm * (1.0f / DM);
      float qq = 0.f;
#pragma unroll
      for (int k = 0; k < 16; k++) { const float d = (k < 8 || ex) ? v[k] - mean : 0.f; qq += d * d; }
      qq = wave_sum(qq);
      const unsigned md = pk2(mean, sqrtf(rbf(rbf(qq * (1.0f / DM)) + 1e-5f)));
      const unsigned zo = (w == 1 && lane < 48) ? (unsigned)FL_E + (unsigned)((cg * 16 + rt) * 384 + lane * 8) : 0x7ffffff0u;
      __builtin_amdgcn_raw_buffer_store_b64((u32x2){lane == 63 ? md : 0u, tag}, rsW, lane == 63 ? (unsigned)FL_S + row * 8u : zo, 0, 16);
    }
    if (w == 0 && lane < 24) {
      const float lr = bf2f((u16)pl[0]), lm = bf2f((u16)pl[1]);
      const float dt = rbf(__builtin_amdgcn_exp2f(bf2f((u16)pl[2]) * 1.44269504089f));
      const float rev = __builtin_amdgcn_fractf(dt * lm * 0.15915494309f);
      const float er = __builtin_amdgcn_exp2f(dt * lr * 1.44269504089f), ar = er * __builtin_amdgcn_cosf(rev), ai = er * __builtin_amdgcn_sinf(rev);
      const float nr = ar - 1.0f, ni = ai, dn = lr * lr + lm * lm;
      const unsigned tre = lds0 + LDS_CST + (lane >> 3) * 2048 + (lane & 7) * 8;
      auto put = [&](int k, float pr, float pi) {
        if (k >= 1 && k <= 4) { *(LAS f32x2*)(tre + k * 128) = (f32x2){2.f * pr, -2.f * pi}; *(LAS f32x2*)(tre + k * 128 + 64) = (f32x2){-2.f * pr, -2.f * pi}; }
        else { *(LAS f32x2*)(tre + k * 128) = (f32x2){pr, pi}; *(LAS f32x2*)(tre + k * 128 + 64) = (f32x2){pr, -pi}; }
      };
      put(0, ar, ai); put(5, 1.f, 0.f);
      float cr = (nr * lr + ni * lm) / dn, cm_ = (ni * lr - nr * lm) / dn;
#pragma unroll
      for (int k = 1; k < 5; k++) { put(k, cr, cm_); const float t = cr * ar - cm_ * ai; cm_ = cr * ai + cm_ * ar; cr = t; }
      const float a2r = ar * ar - ai * ai, a2i = 2.f * ar * ai;
      const float a4r = a2r * a2r - a2i * a2i, a4i = 2.f * a2r * a2i;
      float pr = a4r * a4r - a4i * a4i, pi = 2.f * a4r * a4i;
      put(6, a4r, a4i); put(7, pr, pi); put(8, pr * a4r - pi * a4i, pr * a4i + pi * a4r);
#pragma unroll
      for (int k = 9; k < 14; k++) { const float t = pr * pr - pi * pi; pi = 2.f * pr * pi; pr = t; put(k, pr, pi); }
    }
    SCHED0;
    STAMP(13);
  }
  auto pack = [&]() {
    unsigned l_ = l16; tie(l_); const int tl = (int)(l_ >> 4) + 64 * w;
    const int lane = tl & 63, q = lane >> 4, li = lane & 15;
        const unsigned ra = lds0 + LDS_PK + (w < 6 ? w : 0) * 4096 + (4 * q + (li >> 2)) * 128 + (lane & 3) * 8;
    const int fa_ = pj < 16 ? 1 : 7, fb_ = (pj < 16 ? 9 * pj : 128 + pj) + sx * (KS2 * 9);
    const unsigned vo = widx < 162 ? l_ : 0x70000000u;
#pragma unroll
    for (int n = 0; n < 4; n++) {
      const u32x4 f = __builtin_bit_cast(u32x4, trfrag<2048>(ra + n * 32));
      const int d16 = 4 * pln + n, dt = (d16 * 57) >> 9, db = d16 - 9 * dt;
      st16_sc1(rsW, vo, (unsigned)WS_WT + (unsigned)(dt * (NKG * 9) + fb_ + fa_ * db) * 1024u, f);
    }
    asm volatile("s_waitcnt lgkmcnt(0)" ::: "memory");
  };
  auto pack_c = [&]() {
    unsigned l_ = l16; tie(l_); tie(cfr);
    const int dt = (cd16 * 57) >> 9, db = cd16 - 9 * dt;
    if (2 * cjj + (l_ >> 9) >= 9) cfr = (u32x4){0, 0, 0, 0};
    st16_sc1(rsW, widx < 180 ? l_ : 0x70000000u, (unsigned)WS_WT + (unsigned)((dt * NKG + sx * KS2) * 9 + 146 + cjj + 7 * db) * 1024u, cfr);
  };
  waitv<S1.after(OP_G, 0, 0)>(); xconv(IC<0>{}); xconv(IC<1>{});
  STAMP(14);
  bar();
  SCHED0;
  STAMP(1);
  frag(IC<0>{}, IC<0>{}); frag(IC<0>{}, IC<1>{}); frag(IC<1>{}, IC<0>{}); frag(IC<1>{}, IC<1>{});
  SCHED0;

  bf16x8 cpb[2];
  unsigned csp = 0, csq = 0, csr = 0;
  auto extra = [&](auto Gc) {
    constexpr int G = decltype(Gc)::value;
    if constexpr (xt_at(G) >= 0) { waitv<S1.after(OP_X, xt_at(G), G + 1)>(); xconv(IC<xt_at(G)>{}); SCHED0; }
    if constexpr (G == csx_g(0, 0) - 3 || G == csx_g(6, 0) - 3) {
      constexpr int cur = p1_off(l_unit0(G + PD));
      csp = (ab[0] ^ (unsigned)((w & 3) << 5)) + (unsigned)((w >> 2) * 4096 - cur); tie(csp);
      const u32x4 so = {0x3f803f80u, 0x3f803f80u, 0x3f803f80u, 0x3f803f80u};
      selA = __builtin_bit_cast(bf16x8, so); tie(selA);
      SCHED0;
    }
    if constexpr (G == csx_g(12, 0) - 3) {
      unsigned l_ = l16; tie(l_); const int tl = (int)(l_ >> 4) + 64 * w;
      const int lane = tl & 63, q = lane >> 4, li = lane & 15;
      const u32x4 so = {0x3f803f80u, 0x3f803f80u, 0x3f803f80u, 0x3f803f80u}, sz = {0, 0, 0, 0};
      selA = __builtin_bit_cast(bf16x8, so); selB = __builtin_bit_cast(bf16x8, w < 6 ? so : sz); tie(selA); tie(selB);
      csp = lds0 + p1_off(12) + 12288 + (w < 6 ? w : 0) * 1024 + (li & 7) * 128 + (2 * q + (li >> 3)) * 16;
      const unsigned f0 = lds0 + p1_off(12) + (unsigned)((((8 * q + (li >> 2)) ^ ((q & 1) << 2)) << 5) + 8 * (lane & 3));
      csq = f0 + w * 1024; csr = f0 + (w < 6 ? (w >> 1) * 30720 : 0) + (8 + (w & 1)) * 1024;
      fa[0] = f0; fa[1] = f0 ^ 128u;
      tie(csp); tie(csq); tie(csr); tie(fa[0]); tie(fa[1]);
      SCHED0;
    }
    if constexpr (csx_at(G) >= 0) {
      constexpr int v = csx_at(G) >> 2, n = csx_at(G) & 3;
      const bf16x8 cpc = cpb[G & 1];
      if constexpr (v < 12) mfma<(v % 6 == 0 && n == 0)>(csa0, cpc, selA);
      else if constexpr (v < 15) mfma<(v == 12 && n == 0)>(csa0, selA, cpc);
      else mfma<(v == 15 && n == 0)>(csa1, cpc, v == 16 ? selB : selA);
      SCHED0;
    }
    if constexpr (csx_at(G + 2) >= 0) {
      constexpr int v = csx_at(G + 2) >> 2, n = csx_at(G + 2) & 3;
      bf16x8& cpc = cpb[G & 1];
      if constexpr (v < 12) { unsigned a = csp + p1_off(v); cpc = trfrag<512>(a + n * 8192); }
      else if constexpr (v < 15) { unsigned a = csp + (v - 12) * 30720; cpc = ldsfrag(a + n * 6144); }
      else {
        const unsigned o = v == 15 ? csq + n * 30720 : csr + n * 2048;
        s16x4 a = __builtin_amdgcn_ds_read_tr16_b64_v4i16((LAS s16x4*)(o));
        s16x4 c = __builtin_amdgcn_ds_read_tr16_b64_v4i16((LAS s16x4*)(o ^ 128u));
        cpc = __builtin_bit_cast(bf16x8, __builtin_shufflevector(a, c, 0, 1, 2, 3, 4, 5, 6, 7));
      }
      SCHED0;
    }
  };
  auto cs_flush = [&](auto Gc) {
    constexpr int G = decltype(Gc)::value;
    if constexpr (G == CSF[0] || G == CSF[1]) {
      unsigned lz = l16; tie(lz);
      if ((lz & 240) == 0) *(LAS f32x4*)(lds0 + LDS_CS + (G == CSF[1] ? 512 : 0) + w * 64 + (lz >> 8) * 16) = csa0;
      SCHED0;
    }
    if constexpr (G == CSF[2]) {
      unsigned lz = l16; tie(lz); tie(csa0);
      const float rsum = csa0[0] + dpp_ror8(csa0[0]);
      if (lz < 128) *(LAS float*)(lds0 + LDS_CS + 1024 + w * 32 + (lz >> 2)) = rsum;
      if ((lz & 240) == 0) *(LAS f32x4*)(lds0 + LDS_CS + 1280 + w * 64 + (lz >> 8) * 16) = csa1;
      SCHED0;
    }
  };

  auto group = [&](auto Gc) {
    constexpr int G = decltype(Gc)::value;
    if constexpr (G == 144) { STAMP(2); }
    if constexpr (G == GT) { STAMP(3); }
    if constexpr (G == G2) { STAMP(4); STAMP(5); }
    if constexpr (G == g2(3, 0)) { STAMP(6); }
    if constexpr (G == g2(8, 0)) { STAMP(7); }
    if constexpr (G == GPK) { waitv<S1.after(OP_PK, 0, G)>(); pack(); SCHED0; }
    if constexpr (G == GPC) { waitv<S1.after(OP_PK, 1, G)>(); pack_c(); SCHED0; }
    if constexpr (G == gl(1, 4)) { tie(wm[0]); tie(wm[2]); }
    cs_flush(Gc);
    sync(Gc);
    issue_at(IC<2 * G>{});
    extra(Gc);
    mma(Gc, IC<0>{}); SCHED0;
    if constexpr (G + PD < GEND) { addr_step(IC<G + PD>{}); frag(IC<G + PD>{}, IC<0>{}); SCHED0; }
    side(IC<2 * G>{}); SCHED0;
    if constexpr (G + PD < GEND) { frag(IC<G + PD>{}, IC<1>{}); SCHED0; }
    mma(Gc, IC<1>{}); SCHED0;
    side(IC<2 * G + 1>{}); SCHED0;
  };
  sfor<0, 13>([&](auto Bc) { sfor<0, 45>([&](auto Jc) { group(IC<decltype(Bc)::value * 45 + decltype(Jc)::value>{}); }); });
  STAMPC(27);
  STAMP(8);

  asm volatile("s_nop 7\n\ts_nop 1" : "+v"(ac2[8][0]), "+v"(ac2[8][1]));
  unsigned l_ = l16; tie(l_); const int tz = (int)(l_ >> 4) + 64 * w;
  const int lane = tz & 63, q = lane >> 4, li = lane & 15;
  st16_sc1(rsW, l16, pb + 8 * 1024, (u32x4){pk2(ac2[8][0][0], ac2[8][0][1]), pk2(ac2[8][0][2], ac2[8][0][3]), pk2(ac2[8][1][0], ac2[8][1][1]), pk2(ac2[8][1][2], ac2[8][1][3])});
  SCHED0;
  const char* ib = ws + (size_t)pobase + (size_t)w * 9216;
#pragma unroll
  for (int db = 8 - PLAG; db < 9; db++) gld16_sc1(pv[db], ib + db * 1024, l16);
  bar();
  STAMP(9);
  {
    constexpr int LROW = 304, LSRC = 32 * LROW;
    for (unsigned it = 0; it < (1u << 16); ++it) {
      waitv<0>();
      unsigned bad = 0;
#pragma unroll
      for (int db = 0; db < 9; db++) {
        tie(pv[db]);
        const unsigned m = max(max(pv[db][0], pv[db][1]), max(pv[db][2], pv[db][3]));
        if (!__all(m != ~0u)) bad |= 1u << db;
      }
      if (!bad) break;
      __builtin_amdgcn_s_sleep(2);
#pragma unroll
      for (int db = 0; db < 9; db++) if (bad & (1u << db)) gld16_sc1_io(pv[db], ib + db * 1024, l16);
    }
    waitv<0>();
#pragma unroll
    for (int db = 0; db < 9; db++) {
      tie(pv[db]);
      *(LAS u32x2*)(lds0 + w * LSRC + li * LROW + db * 32 + q * 8) = (u32x2){pv[db][0], pv[db][1]};
      *(LAS u32x2*)(lds0 + w * LSRC + (16 + li) * LROW + db * 32 + q * 8) = (u32x2){pv[db][2], pv[db][3]};
    }
    asm volatile("s_waitcnt vmcnt(0) lgkmcnt(0)" ::: "memory");
    bar();
    STAMP(10);
#pragma unroll
    for (int r = 0; r < 3; r++) {
      tie(epv[2 * r]); tie(epv[2 * r + 1]);
      const int e = tz + 512 * r;
      if (e < 1152) {
        const unsigned t = ept[r], d8 = epd[r];
        f32x4 sum = zero4;
#pragma unroll
        for (int sr = 0; sr < 8; sr++) {
          const u32x2 p = *(LAS u32x2*)(lds0 + sr * LSRC + t * LROW + d8);
          sum += (f32x4){blo(p[0]), bhi(p[0]), blo(p[1]), bhi(p[1])};
        }
        const f32x4 st = *(LAS f32x4*)(lds0 + LDS_ST + sx * 512 + t * 16);
        f32x4 o;
#pragma unroll
        for (int j = 0; j < 4; j++) {
          const float xe = (j & 1) ? bhi(epv[2 * r][j >> 1]) : blo(epv[2 * r][j >> 1]);
          const float de = (j & 1) ? bhi(epv[2 * r + 1][j >> 1]) : blo(epv[2 * r + 1][j >> 1]);
          const float xn = rbf(rbf(xe + st[2]) * st[3]);
          o[j] = xe + rbf(xn * de) + sum[j];
        }
        *(f32x4*)((char*)(out + (size_t)erow * DM + cgl * 144) + t * (DM * 4) + d8 * 2) = o;
      }
    }
    if (b == 0 && w == 0) {
#pragma unroll
      for (int k = 0; k < 4; k++) spin_flag(ws + FL_W + k * 256, l16 >> 2, tag);
      setflag(rsW, l16 == 0, (unsigned)FL_EPOCH, tag);
    }
    STAMP(11);
    STAMP_DUMP();
  }
}

extern "C" void kernel_launch(void* const* d_in, const int* in_sizes, int n_in, void* d_out, int out_size, void* d_ws, size_t ws_size,
                              hipStream_t stream) {
  const u16* x = (const u16*)d_in[0]; const u16* lre = (const u16*)d_in[1]; const u16* lim = (const u16*)d_in[2];
  const u16* bre = (const u16*)d_in[3]; const u16* bim = (const u16*)d_in[4]; const u16* cre = (const u16*)d_in[5]; const u16* cim = (const u16*)d_in[6];
  const u16* logdt = (const u16*)d_in[7]; const u16* dsk = (const u16*)d_in[8];
  const u16* wg = (const u16*)d_in[10]; const u16* wu = (const u16*)d_in[11]; const u16* wd = (const u16*)d_in[12];
  char* ws = (char*)d_ws;
  (void)hipFuncSetAttribute((const void*)k_main, hipFuncAttributeMaxDynamicSharedMemorySize, LDS_TOT);
  k_main<<<256, 512, LDS_TOT, stream>>>(x, lre, lim, bre, bim, cre, cim, logdt, dsk, wg, wu, wd, ws, (float*)d_out);
}
```

```cpp
#include <hip/hip_runtime.h>
#include <stdint.h>
#include <type_traits>

typedef unsigned short u16;
typedef unsigned long long u64;
typedef __bf16 bf16x8 __attribute__((ext_vector_type(8)));
typedef __bf16 bf16x2 __attribute__((ext_vector_type(2)));
typedef float f32x4 __attribute__((ext_vector_type(4)));
typedef float f32x2 __attribute__((ext_vector_type(2)));
typedef short s16x4 __attribute__((ext_vector_type(4)));
typedef unsigned u32x4 __attribute__((ext_vector_type(4)));
typedef unsigned u32x2 __attribute__((ext_vector_type(2)));
#define LAS __attribute__((address_space(3)))
#define GAS __attribute__((address_space(1)))
#define DI __device__ __forceinline__

constexpr int SEQ = 2048, DM = 576, FF = 4608;
constexpr int KS2 = 23;
constexpr int NKG = 8 * KS2;
constexpr size_t FL_H = 0;
constexpr size_t FL_P = 40960;
constexpr size_t FL_W = 36864;
constexpr size_t FL_EPOCH = 38912;
constexpr size_t FL_E = 262144;
constexpr size_t FL_S = 180224;
constexpr size_t WS_HT = 524288;
constexpr size_t WS_WT = WS_HT + (size_t)128 * NKG * 1024;
constexpr size_t WS_PART = WS_WT + (size_t)4 * NKG * 9 * 1024;
constexpr size_t PART_BYTES = (size_t)8 * 4 * 8 * 8 * 9 * 1024;
constexpr size_t WS_END = WS_PART + PART_BYTES;

DI float bf2f(u16 v) { return __uint_as_float(((unsigned)v) << 16); }
DI float rbf(float f) { return (float)(__bf16)f; }
DI unsigned pk2(float lo, float hi) { f32x2 v = {lo, hi}; bf16x2 c = __builtin_convertvector(v, bf16x2); return __builtin_bit_cast(unsigned, c); }
DI float blo(unsigned u) { return __uint_as_float(u << 16); }
DI float bhi(unsigned u) { return __uint_as_float(u & 0xffff0000u); }

__host__ __device__ inline int wt_frag(int j, int db) { return j < 16 ? j * 9 + db : 144 + db * 7 + (j - 16); }
DI int kg_f0b(int s, int j) {
  const int a = 512 * s + 128 * ((j & 7) >> 1) + 64 * (j >> 3) + 32 * (j & 1), c = 4096 + 64 * s + 32 * (j & 1);
  return j < 16 ? a : c;
}
__host__ __device__ inline int kg_f0(int s, int j) {
  if (j < 16) { int p = j >> 3, cg = 4 * s + ((j & 7) >> 1); return 64 * (2 * cg + p) + 32 * (j & 1); }
  return 4096 + 32 * (2 * s + (j - 16));
}

template <int N> using IC = std::integral_constant<int, N>;
template <int B, int E, class F> DI void sfor(F&& f) { if constexpr (B < E) { f(IC<B>{}); sfor<B + 1, E>(f); } }

DI void glds_sc1(const void* sbase, unsigned voff, unsigned ldsaddr) {
  asm volatile("s_mov_b32 m0, %2\n\ts_nop 0\n\tglobal_load_lds_dwordx4 %0, %1 sc1" :: "v"(voff), "s"(sbase), "s"(ldsaddr) : "memory");
}
DI void gld4(unsigned& dst, const void* sbase, unsigned voff) {
  asm volatile("global_load_dword %0, %1, %2" : "=v"(dst) : "v"(voff), "s"(sbase) : "memory");
}
template <int C, int L, bool SC1> DI void glds_c(const void* sbase, unsigned voff, unsigned ldsw) {
  unsigned t;
  if constexpr (SC1) asm volatile("s_add_u32 m0, %3, %5\n\tv_add_u32 %0, %4, %1\n\tglobal_load_lds_dwordx4 %0, %2 sc1" : "=&v"(t) : "v"(voff), "s"(sbase), "s"(ldsw), "i"(C), "i"(L) : "memory", "scc");
  else asm volatile("s_add_u32 m0, %3, %5\n\tv_add_u32 %0, %4, %1\n\tglobal_load_lds_dwordx4 %0, %2" : "=&v"(t) : "v"(voff), "s"(sbase), "s"(ldsw), "i"(C), "i"(L) : "memory", "scc");
}
template <int OFF, class T> DI void gld16o_sc1(T& dst, const void* sbase, unsigned voff) {
  asm volatile("global_load_dwordx4 %0, %1, %2 offset:%3 sc1" : "=v"(dst) : "v"(voff), "s"(sbase), "i"(OFF) : "memory");
}
template <int OFF, class T> DI void gld16o_io(T& dst, const void* sbase, unsigned voff) {
  asm volatile("global_load_dwordx4 %0, %1, %2 offset:%3 sc1" : "+v"(dst) : "v"(voff), "s"(sbase), "i"(OFF) : "memory");
}
template <int OFF, bool IO> DI void gld8o(u32x2& dst, const void* sbase, unsigned voff) {
  if constexpr (IO) asm volatile("global_load_dwordx2 %0, %1, %2 offset:%3 sc1" : "+v"(dst) : "v"(voff), "s"(sbase), "i"(OFF) : "memory");
  else asm volatile("global_load_dwordx2 %0, %1, %2 offset:%3 sc1" : "=v"(dst) : "v"(voff), "s"(sbase), "i"(OFF) : "memory");
}
template <int L> DI void glds_run(unsigned& voff, const void* sbase, unsigned step, unsigned ldsa) {
  asm volatile("s_add_u32 m0, %3, %4\n\ts_nop 0\n\tglobal_load_lds_dwordx4 %0, %1\n\tv_add_u32 %0, %2, %0" : "+v"(voff) : "s"(sbase), "s"(step), "s"(ldsa), "i"(L) : "memory", "scc");
}
DI void glds_s(const void* sbase, unsigned voff, unsigned ldsaddr) {
  asm volatile("s_mov_b32 m0, %2\n\ts_nop 0\n\tglobal_load_lds_dwordx4 %0, %1" :: "v"(voff), "s"(sbase), "s"(ldsaddr) : "memory");
}
template <class T> DI void gld16(T& dst, const void* sbase, unsigned voff) {
  asm volatile("global_load_dwordx4 %0, %1, %2" : "=v"(dst) : "v"(voff), "s"(sbase) : "memory");
}
template <class T> DI void gld16_sc1(T& dst, const void* sbase, unsigned voff) {
  asm volatile("global_load_dwordx4 %0, %1, %2 sc1" : "=v"(dst) : "v"(voff), "s"(sbase) : "memory");
}
template <class T> DI void gld16_sc1_io(T& dst, const void* sbase, unsigned voff) {
  asm volatile("global_load_dwordx4 %0, %1, %2 sc1" : "+v"(dst) : "v"(voff), "s"(sbase) : "memory");
}
DI void gld4_sc1(unsigned& dst, const void* sbase, unsigned voff) {
  asm volatile("global_load_dword %0, %1, %2 sc1" : "=v"(dst) : "v"(voff), "s"(sbase) : "memory");
}
DI void gld8_sc1(u32x2& dst, const void* sbase, unsigned voff) {
  asm volatile("global_load_dwordx2 %0, %1, %2 sc1" : "=v"(dst) : "v"(voff), "s"(sbase) : "memory");
}
DI void gld8(u32x2& dst, const void* sbase, unsigned voff) {
  asm volatile("global_load_dwordx2 %0, %1, %2" : "=v"(dst) : "v"(voff), "s"(sbase) : "memory");
}
template <int N> DI void waitv() { static_assert(N >= 0 && N < 64, ""); asm volatile("s_waitcnt vmcnt(%0)" ::"i"(N) : "memory"); }
#define SCHED0 __builtin_amdgcn_sched_barrier(0)
DI void bar() { asm volatile("" ::: "memory"); __builtin_amdgcn_s_barrier(); asm volatile("" ::: "memory"); }
template <class T> DI void tie(T& a) { asm volatile("" : "+v"(a)); }
template <int O2> DI bf16x8 trfrag(unsigned addr) {
  s16x4 a = __builtin_amdgcn_ds_read_tr16_b64_v4i16((LAS s16x4*)(addr));
  s16x4 b = __builtin_amdgcn_ds_read_tr16_b64_v4i16((LAS s16x4*)(addr + O2));
  return __builtin_bit_cast(bf16x8, __builtin_shufflevector(a, b, 0, 1, 2, 3, 4, 5, 6, 7));
}
DI bf16x8 ldsfrag(unsigned addr) { return *(LAS bf16x8*)(addr); }
template <bool FIRST> DI void mfma(f32x4& c, bf16x8 a, bf16x8 b) {
  if constexpr (FIRST) asm volatile("v_mfma_f32_16x16x32_bf16 %0, %1, %2, 0" : "=v"(c) : "v"(a), "v"(b));
  else asm volatile("v_mfma_f32_16x16x32_bf16 %0, %1, %2, %0" : "+v"(c) : "v"(a), "v"(b));
}
DI float silu_mul(float g, float u) {
  const float e = __builtin_amdgcn_exp2f(g * -1.44269504089f);
  return g * u * __builtin_amdgcn_rcpf(1.0f + e);
}
DI unsigned silu_mul2(f32x2 g, f32x2 u) {
  const f32x2 t = g * -1.44269504089f;
  f32x2 e; e.x = __builtin_amdgcn_exp2f(t.x); e.y = __builtin_amdgcn_exp2f(t.y);
  const f32x2 d = e + 1.0f;
  f32x2 r; r.x = __builtin_amdgcn_rcpf(d.x); r.y = __builtin_amdgcn_rcpf(d.y);
  const f32x2 h = (g * u) * r;
  return pk2(h.x, h.y);
}
DI float dpp_ror8(float v) { return __int_as_float(__builtin_amdgcn_update_dpp(0, __float_as_int(v), 0x128, 0xf, 0xf, true)); }
DI void cmac(float& d, float P, float Q, float x) {
  asm volatile("v_fmac_f32 %0, %1, %3\n\tv_fmac_f32_dpp %0, %3, %2 row_ror:8 row_mask:0xf bank_mask:0xf" : "+v"(d) : "v"(P), "v"(Q), "v"(x));
}
DI void cmul(float& d, float P, float Q, float x) {
  asm volatile("v_mul_f32 %0, %1, %3\n\tv_fmac_f32_dpp %0, %3, %2 row_ror:8 row_mask:0xf bank_mask:0xf" : "=&v"(d) : "v"(P), "v"(Q), "v"(x));
}
DI void cfma(float& d, float P, float Q, float x, float c) {
  asm volatile("v_fma_f32 %0, %1, %3, %4\n\tv_fmac_f32_dpp %0, %3, %2 row_ror:8 row_mask:0xf bank_mask:0xf" : "=&v"(d) : "v"(P), "v"(Q), "v"(x), "v"(c));
}
DI void v_mul(float& d, float a, float b) { asm volatile("v_mul_f32 %0, %1, %2" : "=v"(d) : "v"(a), "v"(b)); }
DI void v_mulc(float& d, float a) { asm volatile("v_mul_f32 %0, 0xbfb8aa3b, %1" : "=v"(d) : "v"(a)); }
DI void v_fmac(float& d, float a, float b) { asm volatile("v_fmac_f32 %0, %1, %2" : "+v"(d) : "v"(a), "v"(b)); }
DI void v_fmaa(float& d, float c) { asm volatile("v_fma_f32 %0, %0, %1, %1" : "+v"(d) : "v"(c)); }
DI void v_muli(float& d, float a) { asm volatile("v_mul_f32 %0, %0, %1" : "+v"(d) : "v"(a)); }
DI void lds_add(unsigned addr, float v) { asm volatile("ds_add_f32 %0, %1" :: "v"(addr), "v"(v) : "memory"); }
DI void v_exp2(float& d) { asm volatile("v_exp_f32 %0, %0" : "+v"(d)); }
DI void v_add1(float& d) { asm volatile("v_add_f32 %0, 1.0, %0" : "+v"(d)); }
DI void v_rcp(float& d) { asm volatile("v_rcp_f32 %0, %0" : "+v"(d)); }
DI void v_cvt(unsigned& d, float a, float b) { asm volatile("v_cvt_pk_bf16_f32 %0, %1, %2" : "=v"(d) : "v"(a), "v"(b)); }
DI float wave_sum(float v) {
#define DPPADD(c) v += __int_as_float(__builtin_amdgcn_update_dpp(0, __float_as_int(v), c, 0xf, 0xf, true))
  DPPADD(0x111); DPPADD(0x112); DPPADD(0x114); DPPADD(0x118);
  v += __int_as_float(__builtin_amdgcn_update_dpp(0, __float_as_int(v), 0x142, 0xa, 0xf, false));
  v += __int_as_float(__builtin_amdgcn_update_dpp(0, __float_as_int(v), 0x143, 0xc, 0xf, false));
  return __int_as_float(__builtin_amdgcn_readlane(__float_as_int(v), 63));
}
DI void dnop() { asm volatile("s_nop 1"); }
typedef __amdgpu_buffer_rsrc_t rsrc_t;
DI rsrc_t mkrsrc(const void* p, unsigned bytes) { return __builtin_amdgcn_make_buffer_rsrc((void*)p, 0, bytes, 0x00020000); }
DI unsigned vadd_s(unsigned s, unsigned v) { unsigned o; asm volatile("v_add_u32 %0, %1, %2" : "=v"(o) : "s"(s), "v"(v)); return o; }
DI void st8_sc1(rsrc_t r, unsigned voff, unsigned soff, u32x2 v) { __builtin_amdgcn_raw_buffer_store_b64(v, r, vadd_s(soff, voff), 0, 16); }
DI void st16_sc1(rsrc_t r, unsigned voff, unsigned soff, u32x4 v) { __builtin_amdgcn_raw_buffer_store_b128(v, r, vadd_s(soff, voff), 0, 16); }
DI u32x4 ones4() { u32x4 v; asm volatile("v_mov_b32 %0, -1\n\tv_mov_b32 %1, -1\n\tv_mov_b32 %2, -1\n\tv_mov_b32 %3, -1" : "=v"(v[0]), "=v"(v[1]), "=v"(v[2]), "=v"(v[3])); return v; }
DI void setflag(rsrc_t r, bool sel, unsigned off, unsigned tag) { __builtin_amdgcn_raw_buffer_store_b32(tag, r, sel ? off : 0x7ffffff0u, 0, 16); }
DI void spin_flag(const char* base, unsigned voff, unsigned tag) {
  for (unsigned it = 0; it < (1u << 18); ++it) {
    unsigned v = __hip_atomic_load((GAS unsigned*)(base + voff), __ATOMIC_RELAXED, __HIP_MEMORY_SCOPE_AGENT);
    if (__all(v == tag)) break;
    __builtin_amdgcn_s_sleep(1);
  }
  asm volatile("" ::: "memory");
}

constexpr int PD = 2, FR = PD + 1;
constexpr int SLOT1 = 24576;
constexpr int SLOT2 = 18432;
constexpr int LDS_TOT = 163840;
constexpr int LDS_SCR = 90112;
constexpr int LDS_PK = 122880;
constexpr int LDS_XS = 98304;
constexpr int LDS_ST = 147456;
constexpr int LDS_CS = 151552;
constexpr int LDS_CST = 153600;
constexpr int LDS_WTOT = LDS_CST + 6144;

#define STAMP(k)
#define STAMPC(k)
#define STAMP_DUMP()

constexpr int p1_off(int v) { return v < 8 ? (v % 4) * SLOT1 : (v < 10 ? (v - 4) * SLOT1 : (v < 12 ? (v - 10) * SLOT1 : 2 * SLOT1 + (v - 12) * 30720)); }
constexpr int p2_off(int u, int g) {
  const int k = u % 5;
  return k < 2 ? k * SLOT2 + g * 1024 : k == 2 ? (g < 12 ? 36864 + g * 1024 : 141312 + (g - 12) * 1024) : 49152 + (k - 3) * SLOT2 + g * 1024;
}

constexpr int GT = 288, G2 = 378, GEND = 585;
constexpr int gl(int v, int g) { return v * 24 + g; }
constexpr int gt(int u, int g) { return GT + u * 30 + g; }
constexpr int g2(int u, int g) { return G2 + u * 18 + g; }
constexpr int SY1 = 8, SYT = 2, SY2 = 4;
constexpr int late_g(int f) { return g2(8, f); }
constexpr int PLAG = 4;

constexpr int SCE = 2 * g2(0, SY2) + 3;
constexpr int SQC = 2 * (G2 + 13);
constexpr int SILU0[3] = {2 * (gl(4, 0) + 12), 2 * (gl(10, 0) + 12), SQC + 8};
constexpr int SILUN[3] = {32, 32, 8};
constexpr int silu_end(int s, int nel) { return SILU0[s] + 9 * (nel / 2) + 1; }
constexpr int S1R = 2 * (GT + 55);
constexpr int S1A = S1R + 12;
constexpr int S1SEL = 2 * g2(2, 0) + 2;
constexpr int SCM = S1SEL + 6;
constexpr int UFIN = 3;
constexpr int SFIN = 2 * g2(UFIN, SY2);
static_assert(S1A + 63 < 2 * g2(0, SY2) && silu_end(2, 8) < S1SEL && SCM + 24 < SFIN - 8, "");
constexpr int csx_n(int v) { return v == 16 ? 2 : 3; }
constexpr int csx_g(int v, int n) {
  const int base[17] = {gl(0, 4), gl(0, 19), gl(1, 12), gl(2, 11), gl(2, 16), gl(3, 3), gl(7, 16), gl(7, 19), gl(8, 1), gl(8, 12), gl(8, 18), gl(9, 0),
                        GT + 27, GT + 30, GT + 34, GT + 37, GT + 40};
  const int step = (v == 5 || v == 6 || v == 7 || v >= 12) ? 1 : 2;
  return base[v] + n * step;
}
constexpr int CSF[3] = {gl(3, SY1), gl(9, SY1), GT + 44};
constexpr int GPC = gl(8, 23);
constexpr int GPK = gl(6, 6);
constexpr int GSYC = GT + 46;
constexpr int xt_g(int h) {
  const int g[18] = {-1, -1, gl(0, 6), gl(0, 12), gl(0, 20), gl(1, 3), gl(1, 11), gl(1, 18), gl(2, 2), gl(2, 10), gl(2, 17), gl(3, 1), gl(3, 9), gl(3, 14), gl(3, 19), gl(4, 0), gl(4, 3), gl(4, 5)};
  return g[h];
}
constexpr int xd_g(int h) { return h < 3 ? -1 : (h < 5 ? h - 2 : xt_g(h - 3) + 3); }

constexpr int csx_at(int G) { for (int v = 0; v < 17; v++) for (int n = 0; n < csx_n(v); n++) if (csx_g(v, n) == G) return v * 4 + n; return -1; }
constexpr int xt_at(int G) { for (int c = 2; c < 18; c++) if (xt_g(c) == G) return c; return -1; }
constexpr int silu_at(int sl) { for (int k = 0; k < 3; k++) if (sl >= SILU0[k] && sl - SILU0[k] <= 9 * (SILUN[k] / 2) + 1) return k; return -1; }

enum { OP_G = 0, OP_X, OP_ST, OP_PO, OP_PH, OP_A, OP_B, OP_FL, OP_EP, OP_HL, OP_PL, OP_EG, OP_SG, OP_PK };
struct Op { int pos, kind, a, n; };
struct Sched {
  Op op[320]; int cnt;
  constexpr void add(int pos, int kind, int a, int n) {
    int i = cnt; while (i > 0 && op[i - 1].pos > pos) { op[i] = op[i - 1]; i--; }
    op[i] = Op{pos, kind, a, n}; cnt++;
  }
  constexpr void unit(int g, int v, int step) { for (int n = 0; n < (v < 12 ? 3 : 4); n++) add(2 * (g + n * step), OP_G, v, n); }
  constexpr Sched() : op{}, cnt(0) {
    for (int h = 0; h < 2; h++) for (int n = 0; n < 2; n++) add(-3, OP_X, h, n);
    for (int n = 0; n < 3; n++) add(-3, OP_G, 0, n);
    for (int n = 0; n < 6; n++) add(-2, OP_ST, 8, n);
    for (int n = 0; n < 2; n++) add(-1, OP_X, 2, n);
    for (int n = 0; n < 3; n++) add(-1, OP_G, 1, n);
    for (int n = 0; n < 2; n++) add(-1, OP_ST, 7, n);
    add(-1, OP_ST, 6, 0);
    for (int h = 3; h < 18; h++) for (int n = 0; n < 2; n++) add(2 * xd_g(h), OP_X, h, n);
    for (int n = 0; n < 4; n++) add(2 * gl(4, 10 + 3 * n), OP_PK, 0, n);
    add(2 * gl(7, 15), OP_PK, 1, 0);
    add(2 * GPC - 1, OP_ST, 9, 0);
    unit(gl(0, 4), 2, 1);
    for (int n = 0; n < 4; n++) add(2 * GPK - 1, OP_ST, 4, n);
    add(2 * gl(1, 5), OP_SG, 0, 0);
    unit(gl(0, 9), 3, 3); unit(gl(1, 9), 4, 3); unit(gl(2, 9), 5, 1); unit(gl(3, 10), 6, 3); unit(gl(4, 9), 7, 3);
    unit(gl(5, 10), 8, 4); unit(gl(6, 9), 9, 1); unit(gl(6, 13), 10, 2); unit(gl(6, 19), 11, 2); unit(gl(8, 10), 12, 3); unit(gl(9, 10), 13, 3); unit(gl(10, 10), 14, 3);
    for (int n = 0; n < 3; n++) add(2 * gl(4, 1 + 2 * n), OP_PH, 0, n);
    for (int s = 0; s < 2; s++) for (int h = 0; h < 2; h++) for (int t = 0; t < 2; t++) add((silu_end(s, 16 * (h + 1)) / 2) * 2 + 1, OP_ST, s, 2 * h + t);
    for (int t = 0; t < 2; t++) add((silu_end(2, 8) / 2) * 2 + 1, OP_ST, 2, t);
    add(2 * gl(11, 16), OP_FL, 4, 0);
    add(2 * gt(0, 6), OP_FL, 0, 0);
    for (int n = 0; n < 2; n++) { add(2 * gt(0, 20 + 4 * n), OP_A, 0, n); add(2 * gt(1, 8 + 4 * n), OP_A, 1, n); add(2 * gt(1, 16 + 4 * n), OP_A, 2, n); }
    for (int n = 0; n < 4; n++) { add(2 * gt(2, 3 + 2 * n), OP_B, 0, n); add(2 * gt(2, 11 + 2 * n), OP_B, 1, n); add(2 * gt(2, 19 + 3 * n), OP_B, 2, n); }
    add(2 * g2(0, 1), OP_FL, 1, 0);
    for (int u = 0; u < 9; u++) {
      const int U = u + 3;
      for (int n = 0; n < (U < 11 ? 2 : 1); n++) add(2 * g2(u, 5 + 2 * n), OP_A, U, n);
      if (U < 8) for (int n = 0; n < 4; n++) add(2 * g2(u, 9 + 2 * n), OP_B, U, n);
    }
    for (int n = 0; n < 7; n++) add(2 * g2(UFIN - 1, 10) + 0, OP_EG, 0, n);
    add(2 * g2(4, 6), OP_FL, 2, 0);
    for (int n = 0; n < 4; n++) add(2 * g2(5, 9 + 2 * n), OP_HL, 0, n);
    for (int n = 4; n < 14; n++) add(2 * g2(6, 8 + (n - 4)), OP_HL, 0, n);
    for (int n = 0; n < 6; n++) add(2 * g2(8, 6 + 2 * n), OP_EP, 0, n);
    for (int db = 0; db < 8; db++) add(2 * (late_g(7 * db + 6) + 3) + 1, OP_ST, 3, db);
  }
  static constexpr bool droppable(const Op& o) { return (o.kind == OP_PH && o.n == 2) || (o.kind == OP_ST && (o.a == 4 || o.a == 9)) || (o.kind == OP_PK && o.a == 0); }
  constexpr int first_at(int pos) const { int i = 0; while (i < cnt && op[i].pos < pos) i++; return i; }
  constexpr int after(int kind, int a, int g) const {
    int last = -1;
    for (int i = 0; i < cnt; i++) if (op[i].kind == kind && op[i].a == a) last = i;
    int n = 0;
    for (int i = last + 1; i < cnt; i++) if (op[i].pos < 2 * g && !droppable(op[i])) n++;
    return n;
  }
};
constexpr Sched S1{};
constexpr int cmin(int a, int b) { return a < b ? a : b; }
constexpr int cmin(int a, int b, int c) { return cmin(cmin(a, b), c); }
constexpr bool l_dbl(int v) { return v == 4 || v == 5 || v == 6 || v == 7 || v >= 10; }
struct LF { int unit, ks, blk; };
constexpr LF l_frag(int G) {
  const int v = G / 24, g = G % 24;
  if (!l_dbl(v)) return LF{v, 3 * (v % 6) + (g >> 3), g & 7};
  const int v0 = v & ~1, r = (v & 1) * 24 + g, p = r / 12, k6 = (r % 12) / 2;
  return LF{v0 + k6 / 3, 3 * (v0 % 6) + k6, p + 4 * (r & 1)};
}
constexpr int l_prev_unit(int G) {
  for (int g = G - 1; g >= 0; g--) if ((l_frag(g).blk & 3) == (l_frag(G).blk & 3)) return l_frag(g).unit;
  return 0;
}
constexpr int l_unit0(int G) { for (int g = G - 1; g >= 0; g--) if ((l_frag(g).blk & 3) == 0) return l_frag(g).unit; return 0; }
struct Corr {
  int item[40];
  constexpr Corr(int pass) : item{} {
    const int g0 = gl(6 * pass + 3, SY1 + 2);
    bool done[16] = {};
    const int order[8] = {0, 4, 1, 5, 2, 6, 3, 7};
    for (int k = 0; k < 40; k++) {
      item[k] = -1;
      const int G = g0 + k;
      for (int o = 0; o < 16 && item[k] < 0; o++) {
        const int bk = order[o >> 1], it = bk * 2 + (o & 1);
        if (done[it]) continue;
        if (l_frag(G).blk == bk || l_frag(G - 1).blk == bk || l_frag(G - 2).blk == bk || l_frag(G + 1).blk == bk) continue;
        item[k] = it; done[it] = true;
      }
    }
  }
  constexpr int last() const { int l = 0; for (int k = 0; k < 40; k++) if (item[k] >= 0) l = k; return l; }
  constexpr int count() const { int c = 0; for (int k = 0; k < 40; k++) c += item[k] >= 0; return c; }
};
constexpr Corr CR{0};
static_assert(CR.count() == 16 && gl(3, SY1 + 2) + CR.last() < gl(4, 12), "");
constexpr int t_ks(int r) { return r < 54 ? r / 3 : (r - 54) / 2; }
constexpr int t_blk(int r) { return r < 54 ? 2 + r % 3 : (r - 54) & 1; }

__global__ __launch_bounds__(512, 2) void k_main(const u16* __restrict__ x, const u16* __restrict__ lre, const u16* __restrict__ lim,
                                                 const u16* __restrict__ bre, const u16* __restrict__ bim, const u16* __restrict__ cre,
                                                 const u16* __restrict__ cim, const u16* __restrict__ logdt, const u16* __restrict__ dsk,
                                                 const u16* __restrict__ wg, const u16* __restrict__ wu, const u16* __restrict__ wd,
                                                 char* __restrict__ ws, float* __restrict__ out) {
  extern __shared__ __attribute__((aligned(16))) char lds[];
  const int tid = threadIdx.x, w = __builtin_amdgcn_readfirstlane(tid >> 6);
  const int b = blockIdx.x, sx = b & 7, idx = b >> 3;
  unsigned lds0 = (unsigned)(size_t)(LAS char*)lds;
  asm volatile("" : "+s"(lds0));
  const int rt = idx & 7, cgl = idx >> 3, cg = sx * 4 + cgl;
  const rsrc_t rsW = mkrsrc(ws, (unsigned)WS_END);
  const unsigned htw = (unsigned)WS_HT + (unsigned)(((rt * 16 + 2 * w) * 8 + sx) * KS2) * 1024u;
  constexpr unsigned HTB = NKG * 1024;
  const unsigned flw = (unsigned)FL_H + (unsigned)((((rt * 8 + sx) * 4) * 8 + w) * 4) * 4u;
  const f32x4 zero4 = {0.f, 0.f, 0.f, 0.f};
  const unsigned l16 = (tid & 63) * 16;
  unsigned tag = 0;
  unsigned ldsw = lds0 + w * 1024; asm volatile("" : "+s"(ldsw));
  STAMP(0);

  f32x4 acc[8][2];
  f32x4 tac[5][2];
  f32x4 ac2[9][2];
  bf16x8 fr[FR];
  bf16x8 xf[18][2];
  constexpr int HR = 4;
  bf16x8 hf[HR][2][2];
  bf16x8 hl[7][2];
  u32x4 pv[9];
  unsigned flv[5] = {0, 0, 0, 0, 0};
  u32x2 epv[6];
  u32x2 eg[7];
  unsigned ept[3], epd[3];
  unsigned wvoff, ab[4], tvA = 0, tvF = 0;
  const char* wsrc = (const char*)((w >> 2) ? wu : wg) + (size_t)cg * 256;
  {
    const int lane = tid & 63, q = lane >> 4, li = lane & 15;
    const int rg = w & 3, r8 = lane >> 3, ch = lane & 7;
    const int g = ((lane >> 4) & 1) | ((rg & 1) << 1);
    wvoff = (unsigned)((8 * rg + r8) * (FF * 2) + (((ch >> 1) ^ g) << 5) + ((ch & 1) << 4));
    const int r4 = li >> 2, cc = lane & 3;
    const int gr = ((r4 >> 1) & 1) | ((q & 1) << 1);
    const unsigned a0 = (unsigned)((8 * q + r4) * 128 + (gr << 5) + 8 * cc);
#pragma unroll
    for (int k = 0; k < 4; k++) { ab[k] = lds0 + (a0 ^ (k << 5)); tie(ab[k]); }
    if (tid < 384) *(LAS float*)(lds0 + LDS_WTOT + tid * 4) = 0.f;
  }
  const char* xb = (const char*)x + (size_t)(rt * 256 + 32 * w) * (DM * 2);
  unsigned xvo, xvo1, xra, xra1;
  unsigned ldsx = lds0 + w * 6144; asm volatile("" : "+s"(ldsx));
  {
    const int lane = tid & 63, q = lane >> 4, li = lane & 15, r8 = lane >> 3;
    xvo = (unsigned)(r8 * (DM * 2) + (((lane & 7) ^ (r8 >> 1)) << 4));
    xra = lds0 + LDS_XS + w * 6144 + li * 128 + ((q ^ (li >> 1)) << 4);
    xvo1 = xvo ^ 64u; xra1 = xra ^ 64u;
    tie(xvo); tie(xra); tie(xvo1); tie(xra1);
  }
  u32x2 sgr;
  const unsigned pobase = (unsigned)WS_PART + (unsigned)(((rt * 4 + cgl) * 8 + sx) * 72) * 1024u;
  const unsigned pb = (unsigned)WS_PART + (unsigned)((((rt * 4 + cgl) * 8 + w) * 8 + sx) * 9) * 1024u;
  const char* wtb = ws + WS_WT + ((size_t)cgl * NKG + sx * KS2) * 9 * 1024 + w * 1024;
  const char* hb0 = ws + htw;
  const int erow = rt * 256 + 32 * sx;
  const int blk3 = w < 4 ? 2 * cgl + (w >> 1) : 8;
  const char* tB = (const char*)((w & 1) ? bim : bre) + (size_t)(8 * (9 * sx + blk3)) * (DM * 2);
  const size_t fcol = (size_t)(4096 + 64 * sx + 16 * cgl) * 2;
  auto fsrc = [&](int fp) { return (const char*)((fp & 1) ? wu : wg) + fcol + (size_t)(fp >> 1) * 32 * (FF * 2); };
  const char* tsF = nullptr; const char* tsA = nullptr; unsigned tsn = 0, tsu = 0, tdA = 0, tdn = 0;
  unsigned cla2 = 0;
  unsigned cla = 0;

  u32x4 cfr; unsigned pl[3], pkv = 0, pkl = 0;
  const int widx = w * 32 + idx;
  const int wi = widx < 162 ? widx : widx - 162, pj = (wi * 57) >> 9, pln = wi - 9 * pj;
  const int ci = widx < 180 ? widx : widx - 180, cd16 = (ci * 205) >> 10, cjj = ci - 5 * cd16;
  auto issue = [&](auto Ic) {
    constexpr Op o = S1.op[decltype(Ic)::value];
    if constexpr (o.kind == OP_G) {
      if constexpr (o.a < 12) {
        glds_c<(o.a / 6) * 128 + (3 * (o.a % 6) + o.n) * 32 * (FF * 2), p1_off(o.a) + o.n * 8192, false>(wsrc, wvoff, ldsw);
      } else {
        constexpr int U = o.a - 12;
        if constexpr (U == 0 && o.n == 0) {
          unsigned l_ = l16; tie(l_);
          const int lane = l_ >> 4, p = lane >> 1;
          tvF = (unsigned)((p ^ (((p >> 3) & 1) << 2)) * (FF * 2) + ((lane & 1) << 4));
          const unsigned vb = (unsigned)((lane >> 3) * (DM * 2) + (((lane & 7) ^ (4 * (w & 1) + (lane >> 4))) << 4));
          tvA = w < 6 ? vb : tvF; tie(tvA); tie(tvF);
          tsF = fsrc(w); tsA = w < 6 ? tB : tsF;
          tsn = w < 6 ? 128u : 32u * (FF * 2); tsu = w < 6 ? 128u : 128u * (FF * 2);
          tdA = ldsw + (w < 6 ? 12288 : 0); tdn = w < 6 ? 6144u : 2048u;
          asm volatile("" : "+s"(tsF), "+s"(tsA), "+s"(tsn), "+s"(tsu), "+s"(tdA), "+s"(tdn));
        }
        if constexpr (o.n < 3) {
          glds_run<p1_off(o.a)>(tvA, tsA, o.n < 2 ? tsn : tsu, tdA + o.n * tdn);
        } else glds_c<U * 192 * (FF * 2), p1_off(o.a), false>(tsF, tvF, ldsw);
      }
    } else if constexpr (o.kind == OP_X) {
      glds_c<((o.a & 1) * 16 + o.n * 8) * (DM * 2) + (o.a >> 1) * 128, LDS_XS + (o.a % 3) * 2048 + o.n * 1024, false>(xb, o.n ? xvo1 : xvo, ldsx);
    } else if constexpr (o.kind == OP_PK) {
      if constexpr (o.a == 0) {
        if constexpr (o.n == 0) {
          unsigned l_ = l16; tie(l_);
          pkv = (l_ >> 7) * (DM * 2) + (l_ & 112) + (unsigned)(kg_f0b(sx, pj) * (DM * 2) + pln * 128); tie(pkv);
          pkl = lds0 + (w < 6 ? LDS_PK + w * 4096 : LDS_TOT); asm volatile("" : "+s"(pkl));
        }
        glds_c<o.n * 8 * (DM * 2), o.n * 1024, false>(wd, pkv, pkl);
      } else {
        unsigned l_ = l16; tie(l_);
        const int lane = l_ >> 4, q = lane >> 4, li = lane & 15;
        const int cbl = 2 * cjj + (q >> 1);
        const char* cp = (const char*)((q & 1) ? cim : cre) + (unsigned)((16 * cd16 + li) * (DM * 2) + 16 * (9 * sx + (cbl < 9 ? cbl : 8)));
        asm volatile("global_load_dwordx4 %0, %1, off" : "=v"(cfr) : "v"(cp) : "memory");
      }
    } else if constexpr (o.kind == OP_SG) {
      gld8_sc1(sgr, ws + FL_S + (size_t)(rt * 256 + 32 * w) * 8, (l16 >> 1) & 248);
    } else if constexpr (o.kind == OP_PO) {
      st16_sc1(rsW, l16, pobase + (unsigned)(o.n * 8 + w) * 1024u, ones4());
    } else if constexpr (o.kind == OP_PH) {
      if constexpr (o.n < 2) st16_sc1(rsW, l16, htw + o.n * HTB + (unsigned)(18 + cgl) * 1024u, ones4());
      else {
        const unsigned f = (l16 & 512) ? 0u : ~0u;
        st16_sc1(rsW, cgl < 2 ? l16 : 0x70000000u, htw + (cgl & 1) * HTB + 22u * 1024u, (u32x4){f, f, f, f});
      }
    } else if constexpr (o.kind == OP_A) {
      constexpr int U = o.a;
      if constexpr (U % 5 == 2 && o.n == 1) glds_c<U * SLOT2 + 8192, 0, true>(wtb, l16, ldsw + (w >= 4 ? p2_off(U, 12) - 4096 : p2_off(U, 8)));
      else glds_c<U * SLOT2 + o.n * 8192, p2_off(U, 8 * o.n), true>(wtb, l16, ldsw);
      if constexpr (U < 11 && o.n == 1) { if (w < 2) glds_c<U * SLOT2 + 16384, p2_off(U, 16), true>(wtb, l16, ldsw); }
      if constexpr (U == 11) { if (w < 1) glds_c<U * SLOT2 + 8192, p2_off(U, 8), true>(wtb, l16, ldsw); }
    } else if constexpr (o.kind == OP_B) {
      constexpr int kg = 2 * o.a + (o.n >> 1);
      gld16o_sc1<(kg % 8 - 4) * 1024>(hf[o.a % HR][o.n >> 1][o.n & 1], hb0 + (size_t)(o.n & 1) * HTB + (kg / 8) * 8192 + 4096, l16);
    } else if constexpr (o.kind == OP_HL) {
      gld16o_sc1<((o.n >> 1) - 4) * 1024>(hl[o.n >> 1][o.n & 1], hb0 + (size_t)(o.n & 1) * HTB + 16384 + 4096, l16);
    } else if constexpr (o.kind == OP_PL) {
      gld16o_sc1<(o.n - 4) * 1024>(pv[o.n], ws + (size_t)pobase + (size_t)w * 9216 + 4096, l16);
    } else if constexpr (o.kind == OP_EG) {
      gld8o<o.n * 384, false>(eg[o.n], ws + FL_E + (size_t)(cg * 16 + rt + 1) * 384, cla2);
    } else if constexpr (o.kind == OP_FL) {
      unsigned lz = l16; tie(lz);
      if constexpr (o.a < 3) gld4_sc1(flv[o.a], ws + flw + 128 * o.a, (lz >> 2) & 12);
      else if constexpr (o.a == 3) gld4_sc1(flv[3], ws + FL_W + (size_t)(w * 32 + idx) * 4, lz & 0);
      else gld4_sc1(flv[4], ws + FL_W + (size_t)sx * 128, (lz >> 2) & 124);
    } else if constexpr (o.kind == OP_EP) {
      constexpr int r = o.n / 2;
      if constexpr (o.n % 2 == 0) {
        unsigned l_ = l16; tie(l_);
        const unsigned e = min((l_ >> 4) + 64u * w + 512u * r, 1151u), t = (e * 1821u) >> 16;
        ept[r] = t; epd[r] = (e - 36u * t) * 8u; tie(ept[r]); tie(epd[r]);
        gld8(epv[o.n], x + (size_t)erow * DM + cgl * 144, t * (DM * 2) + epd[r]);
      } else gld8(epv[o.n], dsk + cgl * 144, epd[r]);
    }
  };
  auto issue_at = [&](auto Pc) {
    constexpr int i0 = S1.first_at(decltype(Pc)::value), i1 = S1.first_at(decltype(Pc)::value + 1);
    sfor<i0, i1>([&](auto Ic) { issue(Ic); SCHED0; });
  };
  auto xconv = [&](auto Hc) {
    constexpr int h = decltype(Hc)::value, c = h >> 1, t = h & 1;
    xf[2 * c][t] = ldsfrag(xra + (h % 3) * 2048);
    xf[2 * c + 1][t] = ldsfrag(xra1 + (h % 3) * 2048);
  };
  auto htie = [&](auto Uc) { constexpr int U = decltype(Uc)::value; tie(hf[U % HR][0][0]); tie(hf[U % HR][0][1]); tie(hf[U % HR][1][0]); tie(hf[U % HR][1][1]); };
  auto chk_flag = [&](auto Ac) { constexpr int a = decltype(Ac)::value; tie(flv[a]);
    if (!__all(flv[a] == tag)) {
      unsigned lz = l16; tie(lz);
      if constexpr (a < 3) spin_flag(ws + flw + 128 * a, (lz >> 2) & 12, tag);
      else if constexpr (a == 3) spin_flag(ws + FL_W + (size_t)(w * 32 + idx) * 4, lz & 0, tag);
      else spin_flag(ws + FL_W + (size_t)sx * 128, (lz >> 2) & 124, tag); } };

  float te = 0.f, pp = 0.f, hh[2], sc1[2], sc2[2], sng[2];
  f32x4 csw[2];
  f32x4 csv, tst[2];
  f32x4 csa0, csa1, qs[8];
  bf16x8 selA, selB; unsigned cra = 0, crb = 0;
  unsigned hp[2][4];
  float cm[3][8], ca4[3][2], se[3][2], eg4[3][2][4], spre[3][2], sv[3][2][4], cc1[3][2], cin[3][2];
  float tw[8], c32[2], wpre = 0.f, crt = 0.f, cz[3], cpw[3][2], c256[2];
  unsigned fwa = 0, fra = 0, fvo = 0;
  unsigned cs48 = 0;
#define CPAIR(base, pr) (*(LAS f32x2*)((base) + (pr) * 128))
  unsigned csa = 0, pkd[4];
  bool q1 = false, q2 = false;
  auto side = [&](auto Sc) {
    constexpr int sl = decltype(Sc)::value;
    {
      if constexpr (silu_at(sl) >= 0) {
        constexpr int s = silu_at(sl), r = sl - SILU0[s], N = SILUN[s];
        constexpr int pr2 = r / 9, ph = r % 9;
        auto G = [&](auto Ec) -> float { constexpr int e = decltype(Ec)::value; if constexpr (s < 2) return acc[e >> 3][(e >> 2) & 1][e & 3]; else return tac[0][e >> 2][e & 3]; };
        auto Uv = [&](auto Ec) -> float { constexpr int e = decltype(Ec)::value; if constexpr (s < 2) return acc[4 + (e >> 3)][(e >> 2) & 1][e & 3]; else return tac[1][e >> 2][e & 3]; };
        constexpr int e0 = 2 * pr2, e1 = e0 + 1;
        if constexpr (ph == 0) { if constexpr (pr2 > 0) v_mul(hh[1], pp, te); if constexpr (e0 < N) v_mul(te, G(IC<e0 < N ? e0 : 0>{}), sc1[(e0 >> 2) & 1]); }
        if constexpr (ph == 1) {
          if constexpr (pr2 > 0) {
            constexpr int ep = e0 - 2, t = (ep >> 2) & 1;
            v_cvt(hp[t][s < 2 ? 2 * ((ep >> 3) & 1) + ((ep >> 1) & 1) : ((ep >> 1) & 1)], hh[0], hh[1]);
            if constexpr (s < 2 && (e0 & 15) == 0) {
#pragma unroll
              for (int tt = 0; tt < 2; tt++)
                st16_sc1(rsW, l16, htw + tt * HTB + (unsigned)(8 * s + 2 * cgl + (ep >> 4)) * 1024u, (u32x4){hp[tt][0], hp[tt][1], hp[tt][2], hp[tt][3]});
            }
            if constexpr (s == 2 && e0 == 8) {
#pragma unroll
              for (int tt = 0; tt < 2; tt++) st8_sc1(rsW, l16, htw + tt * HTB + (unsigned)((16 + (cgl >> 1)) * 1024 + (cgl & 1) * 8), (u32x2){hp[tt][0], hp[tt][1]});
            }
          }
        }
        if constexpr (e0 < N) {
          if constexpr (ph == 2 || ph == 6) v_exp2(te);
          if constexpr (ph == 3) { v_fmaa(te, sc2[(e0 >> 2) & 1]); v_mul(pp, G(IC<e0>{}), Uv(IC<e0>{})); }
          if constexpr (ph == 4 || ph == 8) v_rcp(te);
          if constexpr (ph == 5) { v_mul(hh[0], pp, te); v_mul(te, G(IC<e1>{}), sc1[(e0 >> 2) & 1]); }
          if constexpr (ph == 7) { v_fmaa(te, sc2[(e0 >> 2) & 1]); v_mul(pp, G(IC<e1>{}), Uv(IC<e1>{})); }
        }
      }
    }
    if constexpr (sl == SILU0[0] - 4 || sl == SILU0[1] - 4 || sl == SILU0[2] - 4) {
      unsigned lz = l16; tie(lz);
#pragma unroll
      for (int t = 0; t < 2; t++) { const f32x2 f = *(LAS f32x2*)(lds0 + LDS_ST + w * 512 + t * 256 + (lz & 240)); sc1[t] = f[0]; sc2[t] = f[1]; }
    }
    if constexpr (sl == 2 * gl(3, SY1) || sl == 2 * gl(9, SY1) || sl == SQC - 14) {
      unsigned lz = l16; tie(lz);
#pragma unroll
      for (int t = 0; t < 2; t++) sng[t] = *(LAS float*)(lds0 + LDS_ST + w * 512 + t * 256 + (lz & 240) + 8);
      cra = lds0 + LDS_CS + (lz >> 8) * 16; tie(cra);
    }
    {
      constexpr int pass = sl / 2 >= gl(6, 0) ? 1 : 0, k = sl / 2 - gl(6 * pass + 3, SY1 + 2), half = sl & 1;
      if constexpr (k >= 0 && k < 40) {
        constexpr int it = CR.item[k];
        if constexpr (it >= 0) { constexpr int bk = it >> 1, t = it & 1; float a0 = acc[bk][t][2 * half], a1 = acc[bk][t][2 * half + 1];
          v_fmac(a0, sng[t], csw[k & 1][2 * half]); v_fmac(a1, sng[t], csw[k & 1][2 * half + 1]); acc[bk][t][2 * half] = a0; acc[bk][t][2 * half + 1] = a1; }
      }
      if constexpr (k >= -2 && k < 38 && half == 1) {
        constexpr int nx = CR.item[k + 2];
        if constexpr (nx >= 0) csw[k & 1] = *(LAS f32x4*)(cra + pass * 512 + ((nx >> 1) >> 2) * 256 + ((nx >> 1) & 3) * 64);
      }
    }
    if constexpr (sl == SQC - 12) {
#pragma unroll
      for (int k = 0; k < 8; k++) qs[k] = *(LAS f32x4*)(cra + 1280 + k * 64);
    }
    if constexpr (sl >= SQC - 8 && sl < SQC) {
      constexpr int r = sl - (SQC - 8);
#pragma unroll
      for (int e = 3 * r; e < 3 * r + 3; e++) { const int k = 2 + e / 4, j = e & 3; float a = qs[k & 1][j]; asm volatile("v_add_f32 %0, %0, %1" : "+v"(a) : "v"(qs[k][j])); qs[k & 1][j] = a; }
      if constexpr (r == 7) { csv = qs[0]; csa0 = qs[1]; }
    }
    if constexpr (sl >= SQC && sl < SQC + 8) {
      constexpr int r = sl - SQC, mt = r >> 2, t = (r >> 1) & 1, half = r & 1;
      float a0 = tac[mt][t][2 * half], a1 = tac[mt][t][2 * half + 1];
      if constexpr (mt == 0) { v_fmac(a0, sng[t], csv[2 * half]); v_fmac(a1, sng[t], csv[2 * half + 1]); }
      else { v_fmac(a0, sng[t], csa0[2 * half]); v_fmac(a1, sng[t], csa0[2 * half + 1]); }
      tac[mt][t][2 * half] = a0; tac[mt][t][2 * half + 1] = a1;
    }
    if constexpr (sl == 2 * GSYC) {
      unsigned lz = l16; tie(lz);
      crb = lds0 + LDS_ST + w * 512 + (lz >> 8) * 64; tie(crb);
#pragma unroll
      for (int t = 0; t < 2; t++)
#pragma unroll
        for (int j = 0; j < 4; j++) tst[t][j] = *(LAS float*)(crb + t * 256 + j * 16 + 8);
#pragma unroll
      for (int k = 0; k < 3; k++) csv[k] = *(LAS float*)(lds0 + LDS_CS + 1024 + k * 64 + ((lz >> 2) & 60));
    }
    if constexpr (sl >= 2 * (GT + 48) && sl < 2 * (GT + 54)) {
      constexpr int r = sl / 2 - (GT + 48), k = (r + 1) % 3, t = r / 3, half = sl & 1;
      float a0 = tac[2 + k][t][2 * half], a1 = tac[2 + k][t][2 * half + 1];
      v_fmac(a0, tst[t][2 * half], csv[k]); v_fmac(a1, tst[t][2 * half + 1], csv[k]);
      if constexpr (half) dnop();
      tac[2 + k][t][2 * half] = a0; tac[2 + k][t][2 * half + 1] = a1;
    }
    if constexpr (sl == S1R - 2) {
#pragma unroll
      for (int t = 0; t < 2; t++)
#pragma unroll
        for (int j = 0; j < 4; j++) tst[t][j] = *(LAS float*)(crb + t * 256 + j * 16 + 12);
    }
    if constexpr (sl >= S1R && sl < S1R + 12) {
      constexpr int r = sl - S1R, k = r >> 2, t = (r >> 1) & 1, half = r & 1;
      float a0 = tac[2 + k][t][2 * half], a1 = tac[2 + k][t][2 * half + 1];
      v_muli(a0, tst[t][2 * half]); v_muli(a1, tst[t][2 * half + 1]);
      tac[2 + k][t][2 * half] = a0; tac[2 + k][t][2 * half + 1] = a1;
    }
    if constexpr (sl == S1A - 12) {
      unsigned l_ = l16; tie(l_); const int tl = (int)(l_ >> 4) + 64 * w;
      const int lane = tl & 63;
      csa = lds0 + LDS_CST + (lane & 15) * 8; cla = (unsigned)(lane < 48 ? lane : lane - 16) * 4u;
      cs48 = csa + (cla >> 6) * 2048; cla2 = cla * 2; tie(cla2);
      q1 = lane & 16; q2 = lane & 32;
    }
    if constexpr (sl == S1A - 6 || sl == S1A + 2 || sl == S1A + 10) {
      constexpr int k = (sl - (S1A - 6)) / 8;
#pragma unroll
      for (int i = 0; i < 4; i++) { const f32x2 m = CPAIR(csa + k * 2048, 1 + i); cm[k][2 * i] = m[0]; cm[k][2 * i + 1] = m[1]; }
    }
    if constexpr (sl >= S1A && sl < S1A + 24) {
      constexpr int r = sl - S1A, k = r / 8, j = (r % 8) >> 1, t = r & 1;
      if constexpr (j == 0) cmul(se[k][t], cm[k][6], cm[k][7], tac[2 + k][t][0]);
      else cmac(se[k][t], cm[k][2 * (3 - j)], cm[k][2 * (3 - j) + 1], tac[2 + k][t][j]);
    }
    if constexpr (sl == S1A + 36) {
#pragma unroll
      for (int k = 0; k < 3; k++) { const f32x2 a4 = CPAIR(csa + k * 2048, 6); ca4[k][0] = a4[0]; ca4[k][1] = a4[1]; }
    }
    if constexpr (sl == SCM - 4) {
#pragma unroll
      for (int k = 0; k < 3; k++) { const f32x2 mm = CPAIR(csa + k * 2048, 1); cin[k][0] = mm[0]; cin[k][1] = mm[1]; }
    }
    if constexpr (sl >= S1A + 24 && sl < S1A + 42) {
      constexpr int r = sl - (S1A + 24), st = r / 6, k = (r % 6) >> 1, t = r & 1;
      if constexpr (st == 0) {
        const unsigned pu = __float_as_uint(se[k][t]);
        const auto s16 = __builtin_amdgcn_permlane16_swap(pu, pu, false, false);
        eg4[k][t][0] = __uint_as_float(s16[0]); eg4[k][t][1] = __uint_as_float(s16[1]);
      } else {
        const unsigned pu = __float_as_uint(eg4[k][t][st - 1]);
        const auto sw = __builtin_amdgcn_permlane32_swap(pu, pu, false, false);
        eg4[k][t][st - 1] = __uint_as_float(sw[0]); eg4[k][t][st + 1] = __uint_as_float(sw[1]);
      }
    }
    if constexpr (sl >= S1A + 42 && sl < S1A + 63) {
      constexpr int r = sl - (S1A + 42), sgm = 1 + r / 3, k = r % 3;
      cmac(eg4[k][sgm >> 2][sgm & 3], ca4[k][0], ca4[k][1], eg4[k][(sgm - 1) >> 2][(sgm - 1) & 3]);
    }
    if constexpr (sl == S1A + 63) {
      *(LAS float*)(lds0 + LDS_WTOT + (8 + w) * 192 + cla) = q2 ? eg4[2][1][3] : (q1 ? eg4[1][1][3] : eg4[0][1][3]);
    }
    if constexpr (sl >= S1SEL && sl < S1SEL + 6) {
      constexpr int r = sl - S1SEL, k = r >> 1, t = r & 1;
      const float lo = q1 ? eg4[k][t][0] : (t ? eg4[k][0][3] : 0.f), hi = q1 ? eg4[k][t][2] : eg4[k][t][1];
      spre[k][t] = q2 ? hi : lo; tie(spre[k][t]);
    }
    if constexpr (sl >= SCE && sl < SCE + 7) {
      constexpr int k = sl - SCE + 1;
      cmac(tw[k], c32[0], c32[1], tw[k - 1]);
      if constexpr (k == 7) {
        wpre = tw[6];
        __builtin_amdgcn_raw_buffer_store_b64((u32x2){__float_as_uint(tw[7]), tag}, rsW, (w == 7 && l16 < 768) ? (unsigned)FL_E + (unsigned)((cg * 16 + 8 + rt) * 384) + cla2 : 0x7ffffff0u, 0, 16);
      }
    }
    if constexpr (sl >= SCM && sl < SCM + 24) {
      constexpr int r = sl - SCM, k = r / 8, t = (r >> 2) & 1, j = r & 3;
      cmul(sv[k][t][j], cin[k][0], cin[k][1], tac[2 + k][t][j]);
    }
    if constexpr (sl == SFIN - 8) {
      const f32x2 a = CPAIR(cs48, 13), p0 = CPAIR(cs48, (w & 1) ? 10 : 5), p1 = CPAIR(cs48, (w & 2) ? 11 : 5), p2 = CPAIR(cs48, (w & 4) ? 12 : 5);
      c256[0] = a[0]; c256[1] = a[1]; cpw[0][0] = p0[0]; cpw[0][1] = p0[1]; cpw[1][0] = p1[0]; cpw[1][1] = p1[1]; cpw[2][0] = p2[0]; cpw[2][1] = p2[1];
    }
    if constexpr (sl >= SFIN - 6 && sl < SFIN - 3) {
      constexpr int k = sl - (SFIN - 6);
      unsigned lz = l16; tie(lz);
      const f32x2 a1 = CPAIR(csa + k * 2048, 0), pq = CPAIR(csa + k * 2048, 5 + (lz >> 8)), a16 = CPAIR(csa + k * 2048, 9);
      cc1[k][0] = a1[0]; cc1[k][1] = a1[1]; cin[k][0] = pq[0]; cin[k][1] = pq[1]; ca4[k][0] = a16[0]; ca4[k][1] = a16[1];
    }
    if constexpr (sl >= SFIN && sl < SFIN + 6) {
      constexpr int r = sl - SFIN + 1;
      float n = __uint_as_float(eg[r][0]); cmac(n, c256[0], c256[1], crt); crt = n;
    }
    if constexpr (sl == SFIN + 6) { float n; cmul(n, cpw[0][0], cpw[0][1], crt); crt = n; }
    if constexpr (sl == SFIN + 7) { float n; cmul(n, cpw[1][0], cpw[1][1], crt); crt = n; }
    if constexpr (sl == SFIN + 8) cmac(wpre, cpw[2][0], cpw[2][1], crt);
    if constexpr (sl == SFIN + 9) {
      unsigned lz = l16; tie(lz);
      const unsigned scr = lds0 + LDS_SCR + w * 1024, q = lz >> 8, li = (lz >> 4) & 15;
      fwa = scr + li * 32 + q * 8;
      fra = scr + (q >> 1) * 512 + (8 * (q & 1) + (li >> 2)) * 32 + (li & 3) * 8;
      fvo = (q >> 1) * HTB + ((q & 1) * 16 + li) * 16;
      tie(fwa); tie(fra); tie(fvo);
    }
    if constexpr (sl == SFIN + 10) {
      const unsigned pu = __float_as_uint(wpre);
      const auto s16 = __builtin_amdgcn_permlane16_swap(pu, pu, false, false);
      cz[0] = __uint_as_float(s16[0]); cz[1] = __uint_as_float(s16[1]);
    }
    if constexpr (sl == SFIN + 11) { const unsigned pu = __float_as_uint(cz[0]); const auto sw = __builtin_amdgcn_permlane32_swap(pu, pu, false, false); cz[0] = __uint_as_float(sw[0]); cz[2] = __uint_as_float(sw[1]); }
    if constexpr (sl == SFIN + 12) { const unsigned pu = __float_as_uint(cz[1]); const auto sw = __builtin_amdgcn_permlane32_swap(pu, pu, false, false); cz[1] = __uint_as_float(sw[0]); }
    if constexpr (sl >= SFIN + 13 && sl < SFIN + 16) { constexpr int k = (sl - (SFIN + 13)) == 1 ? 2 : ((sl - (SFIN + 13)) == 2 ? 1 : 0); float n; cmul(n, cin[k][0], cin[k][1], cz[k]); cz[k] = n; }
    if constexpr (sl >= SFIN + 16 && sl < SFIN + 19) { constexpr int k = (sl - (SFIN + 16)) == 1 ? 2 : ((sl - (SFIN + 16)) == 2 ? 1 : 0); cmac(spre[k][1], ca4[k][0], ca4[k][1], cz[k]); }
    if constexpr (sl == SFIN + 19) { asm volatile("v_add_f32 %0, %0, %1" : "+v"(spre[0][0]) : "v"(cz[0])); asm volatile("v_add_f32 %0, %0, %1" : "+v"(spre[1][0]) : "v"(cz[1])); asm volatile("v_add_f32 %0, %0, %1" : "+v"(spre[2][0]) : "v"(cz[2])); }
    if constexpr (sl >= SFIN + 20 && sl < SFIN + 44) {
      constexpr int r = sl - (SFIN + 20), j = r / 6, k = (r % 6) >> 1, t = (r & 1) ^ 1;
      cmac(sv[k][t][j], cc1[k][0], cc1[k][1], j == 0 ? spre[k][t] : sv[k][t][j > 0 ? j - 1 : 0]);
    }
    if constexpr (sl >= SFIN + 44 && sl < SFIN + 53) {
      constexpr int r = sl - (SFIN + 44), k = r / 3, ph = r % 3;
      if constexpr (ph < 2) {
        v_cvt(pkd[0], sv[k][ph][0], sv[k][ph][1]); v_cvt(pkd[1], sv[k][ph][2], sv[k][ph][3]);
        *(LAS u32x2*)(fwa + ph * 512) = (u32x2){pkd[0], pkd[1]};
      } else {
        const u32x4 o = __builtin_bit_cast(u32x4, trfrag<128>(fra));
        st16_sc1(rsW, (k == 2 && cgl) ? 0x70000000u : fvo, htw + (k < 2 ? (unsigned)((18 + cgl) * 1024 + k * 512) : 22u * 1024u), o);
      }
    }
    if constexpr (sl >= 2 * late_g(9) && sl < 2 * GEND) {
      constexpr int r = sl - 2 * late_g(9), db = r / 14, ph = r % 14;
      if constexpr (db < 8) {
        if constexpr (ph == 0) { unsigned a, c; v_cvt(a, ac2[db][0][0], ac2[db][0][1]); v_cvt(c, ac2[db][0][2], ac2[db][0][3]); pkd[0] = a; pkd[1] = c; }
        if constexpr (ph == 1) { unsigned a, c; v_cvt(a, ac2[db][1][0], ac2[db][1][1]); v_cvt(c, ac2[db][1][2], ac2[db][1][3]); st16_sc1(rsW, l16, pb + db * 1024, (u32x4){pkd[0], pkd[1], a, c}); }
      }
    }
  };

  unsigned fa[2], eb[2], a2b[3];
  s16x4 flo;
  auto trl = [&](unsigned a) { return __builtin_amdgcn_ds_read_tr16_b64_v4i16((LAS s16x4*)(a)); };
  auto join = [&](s16x4 a, s16x4 c) { return __builtin_bit_cast(bf16x8, __builtin_shufflevector(a, c, 0, 1, 2, 3, 4, 5, 6, 7)); };
  auto frag = [&](auto Gc, auto Pc) {
    constexpr int G = decltype(Gc)::value, part = decltype(Pc)::value;
    if constexpr (G < GT) {
      constexpr LF f = l_frag(G);
      constexpr int o = (f.ks % 3) * 8192 + (f.blk >> 2) * 4096;
      if constexpr (part == 0) flo = trl(ab[f.blk & 3] + o); else fr[G % FR] = join(flo, trl(ab[f.blk & 3] + o + 512));
    } else if constexpr (G < G2) {
      constexpr int ks = t_ks(G - GT), blk = t_blk(G - GT), uo = (ks / 6) * 30720, k6 = ks % 6;
      if constexpr (blk < 2) {
        constexpr int o = uo - (ks >= 12 ? 30720 : 0) + (k6 * 2 + blk) * 1024;
        if constexpr (part == 0) flo = trl(fa[0] + o); else fr[G % FR] = join(flo, trl(fa[1] + o));
      } else if constexpr (part == 1) fr[G % FR] = ldsfrag(eb[k6 & 1] + uo - (ks >= 12 ? 30720 : 0) + 12288 + (k6 >> 1) * 6144 + (blk - 2) * 2048);
    } else if constexpr (part == 1) {
      constexpr int U = (G - G2) / 18, g = (G - G2) % 18;
      fr[G % FR] = ldsfrag(p2_off(U, g) >= 65536 ? (p2_off(U, g) >= 131072 ? a2b[2] + (p2_off(U, g) - 131072) : a2b[1] + (p2_off(U, g) - 65536)) : a2b[0] + p2_off(U, g));
    }
  };
  auto addr_step = [&](auto Gc) {
    constexpr int G = decltype(Gc)::value;
    if constexpr (G < GT) {
      constexpr int u = l_frag(G).unit, pu = l_prev_unit(G), k = l_frag(G).blk & 3;
      if constexpr (u != pu) { ab[k] += (unsigned)(p1_off(u) - p1_off(pu)); tie(ab[k]); }
    }
    if constexpr (G == GT) {
      unsigned l_ = l16; tie(l_); const int tl = (int)(l_ >> 4) + 64 * w;
      const int lane = tl & 63, q = lane >> 4, li = lane & 15;
      const unsigned e0 = li * 128 + ((q ^ (li >> 1)) << 4);
      eb[0] = lds0 + p1_off(12) + e0; eb[1] = lds0 + p1_off(12) + (e0 ^ 64u); tie(eb[0]); tie(eb[1]);
    }
    if constexpr (G == GT + 36) { eb[0] += 30720; eb[1] += 30720; tie(eb[0]); tie(eb[1]); }
    if constexpr (G == GT + 78) { fa[0] += 30720; fa[1] += 30720; tie(fa[0]); tie(fa[1]); }
    if constexpr (G == G2) { a2b[0] = lds0 + l16; a2b[1] = lds0 + l16 + 65536; a2b[2] = lds0 + l16 + 131072; tie(a2b[0]); tie(a2b[1]); tie(a2b[2]); }
  };
  auto mma = [&](auto Gc, auto Tc) {
    constexpr int G = decltype(Gc)::value, t = decltype(Tc)::value;
    const bf16x8 a = fr[G % FR];
    if constexpr (G < GT) {
      constexpr LF f = l_frag(G);
      mfma<f.ks == 0>(acc[f.blk][t], a, xf[f.ks][t]);
    } else if constexpr (G < G2) {
      constexpr int ks = t_ks(G - GT), blk = t_blk(G - GT);
      if constexpr (blk < 2) mfma<ks == 0>(tac[blk][t], a, xf[ks][t]);
      else mfma<ks == 0>(tac[blk][t], xf[ks][t], a);
    } else if constexpr (G < late_g(0)) {
      constexpr int U = (G - G2) / 18, g = (G - G2) % 18, kk = g / 9, db = g % 9;
      mfma<(U == 0 && kk == 0)>(ac2[db][t], a, hf[U % HR][kk][t]);
    } else {
      constexpr int f = G - late_g(0), db = f / 7, kk = f % 7;
      mfma<false>(ac2[db][t], a, hl[kk][t]);
    }
  };

  auto sync = [&](auto Gc) {
    constexpr int G = decltype(Gc)::value;
    if constexpr (G < GT && G % 24 == (G < 24 ? 16 : SY1)) {
      constexpr int V = G / 24;
      constexpr int need = V + 1 < 12 ? (l_dbl(V + 1) ? (((V + 1) & 1) ? -1 : V + 2) : V + 1) : 12;
      constexpr int ng = need >= 0 ? S1.after(OP_G, need, G) : 63;
      constexpr int ng2 = V == 2 ? S1.after(OP_G, 4, G) : (V == 8 ? S1.after(OP_G, 11, G) : 63);
      constexpr int ns = V == 10 ? cmin(S1.after(OP_ST, 0, G), S1.after(OP_ST, 9, G)) : 63;
      waitv<cmin(ng, ng2, ns)>();
      if constexpr (V == 3 || V == 9) asm volatile("s_waitcnt lgkmcnt(0)" ::: "memory");
      if constexpr (V == 10) setflag(rsW, l16 == 0, flw + cgl * 4, tag);
      bar();
      if constexpr (V == 10) setflag(rsW, l16 == 0 && w == 1, (unsigned)FL_W + (unsigned)(sx * 32 + idx) * 4u, tag);
      SCHED0;
    }
    if constexpr (G == gt(0, 18)) { waitv<S1.after(OP_FL, 4, G)>(); chk_flag(IC<4>{}); SCHED0; }
    if constexpr (G == gl(2, 14)) {
      waitv<S1.after(OP_SG, 0, G)>();
      for (unsigned it = 0; it < (1u << 18); ++it) {
        tie(sgr);
        if (__all(sgr[1] == tag)) break;
        __builtin_amdgcn_s_sleep(1);
        asm volatile("global_load_dwordx2 %0, %1, %2 sc1" : "+v"(sgr) : "v"((l16 >> 1) & 248), "s"(ws + FL_S + (size_t)(rt * 256 + 32 * w) * 8) : "memory");
        waitv<0>();
      }
      tie(sgr);
      const float den = bhi(sgr[0]), rs = __builtin_amdgcn_rcpf(den);
      *(LAS f32x4*)(lds0 + LDS_ST + w * 512 + (l16 & 496)) = (f32x4){rs * -1.44269504089f, den * den, -blo(sgr[0]), rs};
      SCHED0;
    }
    if constexpr (G == gl(3, 1)) { waitv<S1.after(OP_G, 5, G)>(); bar(); SCHED0; }
    if constexpr (G == GSYC) { asm volatile("s_waitcnt lgkmcnt(0)" ::: "memory"); bar(); SCHED0; }
    if constexpr (G == gt(0, SYT)) { waitv<S1.after(OP_G, 13, G)>(); bar(); SCHED0; }
    if constexpr (G == gt(1, SYT)) { waitv<cmin(S1.after(OP_G, 14, G), S1.after(OP_FL, 0, G))>(); bar(); chk_flag(IC<0>{}); SCHED0; }
    if constexpr (G == gt(2, 2)) { waitv<S1.after(OP_ST, 1, G)>(); setflag(rsW, l16 == 0, flw + 128 + cgl * 4, tag); SCHED0; }
    if constexpr (G == gt(2, 27)) {
      waitv<cmin(S1.after(OP_A, 0, G), S1.after(OP_B, 0, G))>(); htie(IC<0>{});
      bar(); SCHED0;
    }
    if constexpr (G >= G2 && (G - G2) % 18 == SY2 && (G - G2) / 18 < 11) {
      constexpr int U = (G - G2) / 18;
      constexpr int nab = cmin(S1.after(OP_A, U + 1, G), U + 1 < 8 ? S1.after(OP_B, U + 1, G) : (U == 7 ? S1.after(OP_HL, 0, G) : 63));
      constexpr int nfl = U == 1 ? S1.after(OP_FL, 1, G) : (U == 3 ? cmin(S1.after(OP_ST, 2, G), S1.after(OP_EG, 0, G)) : (U == 5 ? S1.after(OP_FL, 2, G) : 63));
      static_assert(UFIN == 3, "");
      if constexpr (U == 0) asm volatile("s_waitcnt lgkmcnt(0)" ::: "memory");
      waitv<cmin(nab, nfl)>();
      if constexpr (U + 1 < 8) htie(IC<U + 1>{});
      if constexpr (U == 7) {
        tie(hl[0][0]); tie(hl[0][1]); tie(hl[1][0]); tie(hl[1][1]);
        for (unsigned it = 0; it < (1u << 16); ++it) {
          unsigned m = 0;
#pragma unroll
          for (int n = 4; n < 14; n++) { tie(hl[n >> 1][n & 1]); const u32x4 v = __builtin_bit_cast(u32x4, hl[n >> 1][n & 1]); asm("v_max3_u32 %0, %0, %1, %2" : "+v"(m) : "v"(v[0]), "v"(v[1])); asm("v_max3_u32 %0, %0, %1, %2" : "+v"(m) : "v"(v[2]), "v"(v[3])); }
          if (__all(m != ~0u)) break;
          __builtin_amdgcn_s_sleep(4);
          sfor<4, 14>([&](auto Nc) { constexpr int n = decltype(Nc)::value;
            gld16o_io<((n >> 1) - 4) * 1024>(hl[n >> 1][n & 1], hb0 + (size_t)(n & 1) * HTB + 16384 + 4096, l16); });
          waitv<0>();
        }
#pragma unroll
        for (int n = 4; n < 14; n++) tie(hl[n >> 1][n & 1]);
      }
      if constexpr (U == UFIN) {
        for (unsigned it = 0; it < (1u << 18); ++it) {
          unsigned d = 0;
#pragma unroll
          for (int r = 0; r < 7; r++) { tie(eg[r]); d |= eg[r][1] ^ tag; }
          if (__all(d == 0)) break;
          __builtin_amdgcn_s_sleep(1);
          sfor<0, 7>([&](auto Rc) { gld8o<decltype(Rc)::value * 384, true>(eg[decltype(Rc)::value], ws + FL_E + (size_t)(cg * 16 + rt + 1) * 384, cla2); });
          waitv<0>();
        }
#pragma unroll
        for (int r = 0; r < 7; r++) tie(eg[r]);
        crt = __uint_as_float(eg[0][0]);
        setflag(rsW, l16 == 0, flw + 256 + cgl * 4, tag);
      }
      bar();
      if constexpr (U == 1) chk_flag(IC<1>{});
      if constexpr (U == 5) chk_flag(IC<2>{});
      if constexpr (U == 0) {
        const f32x2 c = CPAIR(cs48, 10);
        c32[0] = c[0]; c32[1] = c[1];
#pragma unroll
        for (int k = 0; k < 8; k++) tw[k] = *(LAS float*)(lds0 + LDS_WTOT + (w + 1 + k) * 192 + cla);
      }
      SCHED0;
    }
  };

  unsigned wm[3];
  {
    unsigned l_ = l16; tie(l_); const int tl = (int)(l_ >> 4) + 64 * w;
    const int lane = tl & 63, q = lane >> 4, li = lane & 15;
    unsigned ep; u32x4 xr0, xr1;
    issue_at(IC<-3>{});
    bar();
    gld4_sc1(ep, ws + FL_EPOCH, l16 & 0);
    const int row = b * 8 + w;
    gld16(xr0, x + (size_t)row * DM, l16);
    gld16(xr1, x + (size_t)row * DM, 1024u + (l16 & 112));
    {
      const int chn = 8 * (9 * sx + (lane < 16 ? 2 * cgl + (lane >> 3) : 8)) + (lane & 7);
      asm volatile("global_load_ushort %0, %1, %2" : "=v"(pl[0]) : "v"(chn * 2), "s"(lre) : "memory");
      asm volatile("global_load_ushort %0, %1, %2" : "=v"(pl[1]) : "v"(chn * 2), "s"(lim) : "memory");
      asm volatile("global_load_ushort %0, %1, %2" : "=v"(pl[2]) : "v"(chn * 2), "s"(logdt) : "memory");
    }
    SCHED0;
    issue_at(IC<-1>{});
    {
      const int n = tl, r = 72 * rt + n / 6, c6 = n % 6;
      const char* p0 = (const char*)((c6 & 1) ? wu : wg) + (size_t)r * (FF * 2) + (c6 < 4 ? cg * 256 + (c6 >> 1) * 128 : fcol);
      const int m = rt * 54 + (tl - 432 < 54 ? tl - 432 : 0), rb = m / 9;
      const char* p1 = (const char*)((rb & 8) ? bim : bre) + (size_t)(8 * (9 * sx + ((rb >> 4) < 2 ? 2 * cgl + (rb >> 4) : 8)) + (rb & 7)) * (DM * 2) + (m - 9 * rb) * 128;
      asm volatile("global_load_dword %0, %1, off" : "=v"(wm[0]) : "v"(tl < 432 ? p0 : p1) : "memory");
      asm volatile("global_load_dword %0, %1, %2" : "=v"(wm[2]) : "v"((unsigned)((8 * cgl + (lane >> 3)) * (DM * 2) + 128 + (lane & 7) * 128)), "s"(xb) : "memory");
    }
    SCHED0;
    waitv<7>();
    tie(ep); tie(xr0); tie(xr1); tie(pl[0]); tie(pl[1]); tie(pl[2]);
    tag = (unsigned)__builtin_amdgcn_readfirstlane(ep) + 1u;
    STAMP(12);
    {
      float v[16];
      const bool ex = lane < 8;
#pragma unroll
      for (int k = 0; k < 4; k++) { v[2 * k] = blo(xr0[k]); v[2 * k + 1] = bhi(xr0[k]); v[8 + 2 * k] = ex ? blo(xr1[k]) : 0.f; v[9 + 2 * k] = ex ? bhi(xr1[k]) : 0.f; }
      float sm = 0.f;
#pragma unroll
      for (int k = 0; k < 16; k++) sm += v[k];
      sm = wave_sum(sm);
      const float mean = sm * (1.0f / DM);
      float qq = 0.f;
#pragma unroll
      for (int k = 0; k < 16; k++) { const float d = (k < 8 || ex) ? v[k] - mean : 0.f; qq += d * d; }
      qq = wave_sum(qq);
      const unsigned md = pk2(mean, sqrtf(rbf(rbf(qq * (1.0f / DM)) + 1e-5f)));
      const unsigned zo = (w == 1 && lane < 48) ? (unsigned)FL_E + (unsigned)((cg * 16 + rt) * 384 + lane * 8) : 0x7ffffff0u;
      __builtin_amdgcn_raw_buffer_store_b64((u32x2){lane == 63 ? md : 0u, tag}, rsW, lane == 63 ? (unsigned)FL_S + row * 8u : zo, 0, 16);
    }
    if (w == 0 && lane < 24) {
      const float lr = bf2f((u16)pl[0]), lm = bf2f((u16)pl[1]);
      const float dt = rbf(__builtin_amdgcn_exp2f(bf2f((u16)pl[2]) * 1.44269504089f));
      const float rev = __builtin_amdgcn_fractf(dt * lm * 0.15915494309f);
      const float er = __builtin_amdgcn_exp2f(dt * lr * 1.44269504089f), ar = er * __builtin_amdgcn_cosf(rev), ai = er * __builtin_amdgcn_sinf(rev);
      const float nr = ar - 1.0f, ni = ai, dn = lr * lr + lm * lm;
      const unsigned tre = lds0 + LDS_CST + (lane >> 3) * 2048 + (lane & 7) * 8;
      auto put = [&](int k, float pr, float pi) {
        if (k >= 1 && k <= 4) { *(LAS f32x2*)(tre + k * 128) = (f32x2){2.f * pr, -2.f * pi}; *(LAS f32x2*)(tre + k * 128 + 64) = (f32x2){-2.f * pr, -2.f * pi}; }
        else { *(LAS f32x2*)(tre + k * 128) = (f32x2){pr, pi}; *(LAS f32x2*)(tre + k * 128 + 64) = (f32x2){pr, -pi}; }
      };
      put(0, ar, ai); put(5, 1.f, 0.f);
      float cr = (nr * lr + ni * lm) / dn, cm_ = (ni * lr - nr * lm) / dn;
#pragma unroll
      for (int k = 1; k < 5; k++) { put(k, cr, cm_); const float t = cr * ar - cm_ * ai; cm_ = cr * ai + cm_ * ar; cr = t; }
      const float a2r = ar * ar - ai * ai, a2i = 2.f * ar * ai;
      const float a4r = a2r * a2r - a2i * a2i, a4i = 2.f * a2r * a2i;
      float pr = a4r * a4r - a4i * a4i, pi = 2.f * a4r * a4i;
      put(6, a4r, a4i); put(7, pr, pi); put(8, pr * a4r - pi * a4i, pr * a4i + pi * a4r);
#pragma unroll
      for (int k = 9; k < 14; k++) { const float t = pr * pr - pi * pi; pi = 2.f * pr * pi; pr = t; put(k, pr, pi); }
    }
    SCHED0;
    STAMP(13);
  }
  auto pack = [&]() {
    unsigned l_ = l16; tie(l_); const int tl = (int)(l_ >> 4) + 64 * w;
    const int lane = tl & 63, q = lane >> 4, li = lane & 15;
        const unsigned ra = lds0 + LDS_PK + (w < 6 ? w : 0) * 4096 + (4 * q + (li >> 2)) * 128 + (lane & 3) * 8;
    const int fa_ = pj < 16 ? 1 : 7, fb_ = (pj < 16 ? 9 * pj : 128 + pj) + sx * (KS2 * 9);
    const unsigned vo = widx < 162 ? l_ : 0x70000000u;
#pragma unroll
    for (int n = 0; n < 4; n++) {
      const u32x4 f = __builtin_bit_cast(u32x4, trfrag<2048>(ra + n * 32));
      const int d16 = 4 * pln + n, dt = (d16 * 57) >> 9, db = d16 - 9 * dt;
      st16_sc1(rsW, vo, (unsigned)WS_WT + (unsigned)(dt * (NKG * 9) + fb_ + fa_ * db) * 1024u, f);
    }
    asm volatile("s_waitcnt lgkmcnt(0)" ::: "memory");
  };
  auto pack_c = [&]() {
    unsigned l_ = l16; tie(l_); tie(cfr);
    const int dt = (cd16 * 57) >> 9, db = cd16 - 9 * dt;
    if (2 * cjj + (l_ >> 9) >= 9) cfr = (u32x4){0, 0, 0, 0};
    st16_sc1(rsW, widx < 180 ? l_ : 0x70000000u, (unsigned)WS_WT + (unsigned)((dt * NKG + sx * KS2) * 9 + 146 + cjj + 7 * db) * 1024u, cfr);
  };
  waitv<S1.after(OP_G, 0, 0)>(); xconv(IC<0>{}); xconv(IC<1>{});
  STAMP(14);
  bar();
  SCHED0;
  STAMP(1);
  frag(IC<0>{}, IC<0>{}); frag(IC<0>{}, IC<1>{}); frag(IC<1>{}, IC<0>{}); frag(IC<1>{}, IC<1>{});
  SCHED0;

  bf16x8 cpb[2];
  unsigned csp = 0, csq = 0, csr = 0;
  auto extra = [&](auto Gc) {
    constexpr int G = decltype(Gc)::value;
    if constexpr (xt_at(G) >= 0) { waitv<S1.after(OP_X, xt_at(G), G + 1)>(); xconv(IC<xt_at(G)>{}); SCHED0; }
    if constexpr (G == csx_g(0, 0) - 3 || G == csx_g(6, 0) - 3) {
      constexpr int cur = p1_off(l_unit0(G + PD));
      csp = (ab[0] ^ (unsigned)((w & 3) << 5)) + (unsigned)((w >> 2) * 4096 - cur); tie(csp);
      const u32x4 so = {0x3f803f80u, 0x3f803f80u, 0x3f803f80u, 0x3f803f80u};
      selA = __builtin_bit_cast(bf16x8, so); tie(selA);
      SCHED0;
    }
    if constexpr (G == csx_g(12, 0) - 3) {
      unsigned l_ = l16; tie(l_); const int tl = (int)(l_ >> 4) + 64 * w;
      const int lane = tl & 63, q = lane >> 4, li = lane & 15;
      const u32x4 so = {0x3f803f80u, 0x3f803f80u, 0x3f803f80u, 0x3f803f80u}, sz = {0, 0, 0, 0};
      selA = __builtin_bit_cast(bf16x8, so); selB = __builtin_bit_cast(bf16x8, w < 6 ? so : sz); tie(selA); tie(selB);
      csp = lds0 + p1_off(12) + 12288 + (w < 6 ? w : 0) * 1024 + (li & 7) * 128 + (2 * q + (li >> 3)) * 16;
      const unsigned f0 = lds0 + p1_off(12) + (unsigned)((((8 * q + (li >> 2)) ^ ((q & 1) << 2)) << 5) + 8 * (lane & 3));
      csq = f0 + w * 1024; csr = f0 + (w < 6 ? (w >> 1) * 30720 : 0) + (8 + (w & 1)) * 1024;
      fa[0] = f0; fa[1] = f0 ^ 128u;
      tie(csp); tie(csq); tie(csr); tie(fa[0]); tie(fa[1]);
      SCHED0;
    }
    if constexpr (csx_at(G) >= 0) {
      constexpr int v = csx_at(G) >> 2, n = csx_at(G) & 3;
      const bf16x8 cpc = cpb[G & 1];
      if constexpr (v < 12) mfma<(v % 6 == 0 && n == 0)>(csa0, cpc, selA);
      else if constexpr (v < 15) mfma<(v == 12 && n == 0)>(csa0, selA, cpc);
      else mfma<(v == 15 && n == 0)>(csa1, cpc, v == 16 ? selB : selA);
      SCHED0;
    }
    if constexpr (csx_at(G + 2) >= 0) {
      constexpr int v = csx_at(G + 2) >> 2, n = csx_at(G + 2) & 3;
      bf16x8& cpc = cpb[G & 1];
      if constexpr (v < 12) { unsigned a = csp + p1_off(v); cpc = trfrag<512>(a + n * 8192); }
      else if constexpr (v < 15) { unsigned a = csp + (v - 12) * 30720; cpc = ldsfrag(a + n * 6144); }
      else {
        const unsigned o = v == 15 ? csq + n * 30720 : csr + n * 2048;
        s16x4 a = __builtin_amdgcn_ds_read_tr16_b64_v4i16((LAS s16x4*)(o));
        s16x4 c = __builtin_amdgcn_ds_read_tr16_b64_v4i16((LAS s16x4*)(o ^ 128u));
        cpc = __builtin_bit_cast(bf16x8, __builtin_shufflevector(a, c, 0, 1, 2, 3, 4, 5, 6, 7));
      }
      SCHED0;
    }
  };
  auto cs_flush = [&](auto Gc) {
    constexpr int G = decltype(Gc)::value;
    if constexpr (G == CSF[0] || G == CSF[1]) {
      unsigned lz = l16; tie(lz);
      if ((lz & 240) == 0) *(LAS f32x4*)(lds0 + LDS_CS + (G == CSF[1] ? 512 : 0) + w * 64 + (lz >> 8) * 16) = csa0;
      SCHED0;
    }
    if constexpr (G == CSF[2]) {
      unsigned lz = l16; tie(lz); tie(csa0);
      const float rsum = csa0[0] + dpp_ror8(csa0[0]);
      if (lz < 128) *(LAS float*)(lds0 + LDS_CS + 1024 + w * 32 + (lz >> 2)) = rsum;
      if ((lz & 240) == 0) *(LAS f32x4*)(lds0 + LDS_CS + 1280 + w * 64 + (lz >> 8) * 16) = csa1;
      SCHED0;
    }
  };

  auto group = [&](auto Gc) {
    constexpr int G = decltype(Gc)::value;
    if constexpr (G == 144) { STAMP(2); }
    if constexpr (G == GT) { STAMP(3); }
    if constexpr (G == G2) { STAMP(4); STAMP(5); }
    if constexpr (G == g2(3, 0)) { STAMP(6); }
    if constexpr (G == g2(8, 0)) { STAMP(7); }
    if constexpr (G == GPK) { waitv<S1.after(OP_PK, 0, G)>(); pack(); SCHED0; }
    if constexpr (G == GPC) { waitv<S1.after(OP_PK, 1, G)>(); pack_c(); SCHED0; }
    if constexpr (G == gl(1, 4)) { tie(wm[0]); tie(wm[2]); }
    cs_flush(Gc);
    sync(Gc);
    issue_at(IC<2 * G>{});
    extra(Gc);
    mma(Gc, IC<0>{}); SCHED0;
    if constexpr (G + PD < GEND) { addr_step(IC<G + PD>{}); frag(IC<G + PD>{}, IC<0>{}); SCHED0; }
    side(IC<2 * G>{}); SCHED0;
    if constexpr (G + PD < GEND) { frag(IC<G + PD>{}, IC<1>{}); SCHED0; }
    mma(Gc, IC<1>{}); SCHED0;
    side(IC<2 * G + 1>{}); SCHED0;
  };
  sfor<0, 13>([&](auto Bc) { sfor<0, 45>([&](auto Jc) { group(IC<decltype(Bc)::value * 45 + decltype(Jc)::value>{}); }); });
  STAMPC(27);
  STAMP(8);

  asm volatile("s_nop 7\n\ts_nop 1" : "+v"(ac2[8][0]), "+v"(ac2[8][1]));
  unsigned l_ = l16; tie(l_); const int tz = (int)(l_ >> 4) + 64 * w;
  const int lane = tz & 63, q = lane >> 4, li = lane & 15;
  st16_sc1(rsW, l16, pb + 8 * 1024, (u32x4){pk2(ac2[8][0][0], ac2[8][0][1]), pk2(ac2[8][0][2], ac2[8][0][3]), pk2(ac2[8][1][0], ac2[8][1][1]), pk2(ac2[8][1][2], ac2[8][1][3])});
  SCHED0;
  const char* ib = ws + (size_t)pobase + (size_t)w * 9216 + 4096;
  waitv<0>();
  setflag(rsW, l16 == 0, (unsigned)FL_P + (unsigned)(((rt * 4 + cgl) * 8 + w) * 8 + sx) * 64u, tag);
  bar();
  STAMP(9);
  {
    constexpr int LROW = 304, LSRC = 32 * LROW;
    spin_flag(ws + FL_P + (size_t)(((rt * 4 + cgl) * 8 + sx) * 8 + w) * 64, l16 & 0, tag);
    sfor<0, 8>([&](auto Dc) { gld16o_sc1<(decltype(Dc)::value - 4) * 1024>(pv[decltype(Dc)::value], ib, l16); });
    gld16o_sc1<0>(pv[8], ib + 4096, l16);
    waitv<0>();
#pragma unroll
    for (int db = 0; db < 9; db++) {
      tie(pv[db]);
      *(LAS u32x2*)(lds0 + w * LSRC + li * LROW + db * 32 + q * 8) = (u32x2){pv[db][0], pv[db][1]};
      *(LAS u32x2*)(lds0 + w * LSRC + (16 + li) * LROW + db * 32 + q * 8) = (u32x2){pv[db][2], pv[db][3]};
    }
    asm volatile("s_waitcnt vmcnt(0) lgkmcnt(0)" ::: "memory");
    bar();
    STAMP(10);
#pragma unroll
    for (int r = 0; r < 3; r++) {
      tie(epv[2 * r]); tie(epv[2 * r + 1]);
      const int e = tz + 512 * r;
      if (e < 1152) {
        const unsigned t = ept[r], d8 = epd[r];
        f32x4 sum = zero4;
#pragma unroll
        for (int sr = 0; sr < 8; sr++) {
          const u32x2 p = *(LAS u32x2*)(lds0 + sr * LSRC + t * LROW + d8);
          sum += (f32x4){blo(p[0]), bhi(p[0]), blo(p[1]), bhi(p[1])};
        }
        const f32x4 st = *(LAS f32x4*)(lds0 + LDS_ST + sx * 512 + t * 16);
        f32x4 o;
#pragma unroll
        for (int j = 0; j < 4; j++) {
          const float xe = (j & 1) ? bhi(epv[2 * r][j >> 1]) : blo(epv[2 * r][j >> 1]);
          const float de = (j & 1) ? bhi(epv[2 * r + 1][j >> 1]) : blo(epv[2 * r + 1][j >> 1]);
          const float xn = rbf(rbf(xe + st[2]) * st[3]);
          o[j] = xe + rbf(xn * de) + sum[j];
        }
        *(f32x4*)((char*)(out + (size_t)erow * DM + cgl * 144) + t * (DM * 4) + d8 * 2) = o;
      }
    }
    if (b == 0 && w == 0) {
#pragma unroll
      for (int k = 0; k < 4; k++) spin_flag(ws + FL_W + k * 256, l16 >> 2, tag);
      setflag(rsW, l16 == 0, (unsigned)FL_EPOCH, tag);
    }
    STAMP(11);
    STAMP_DUMP();
  }
}

extern "C" void kernel_launch(void* const* d_in, const int* in_sizes, int n_in, void* d_out, int out_size, void* d_ws, size_t ws_size,
                              hipStream_t stream) {
  const u16* x = (const u16*)d_in[0]; const u16* lre = (const u16*)d_in[1]; const u16* lim = (const u16*)d_in[2];
  const u16* bre = (const u16*)d_in[3]; const u16* bim = (const u16*)d_in[4]; const u16* cre = (const u16*)d_in[5]; const u16* cim = (const u16*)d_in[6];
  const u16* logdt = (const u16*)d_in[7]; const u16* dsk = (const u16*)d_in[8];
  const u16* wg = (const u16*)d_in[10]; const u16* wu = (const u16*)d_in[11]; const u16* wd = (const u16*)d_in[12];
  char* ws = (char*)d_ws;
  (void)hipFuncSetAttribute((const void*)k_main, hipFuncAttributeMaxDynamicSharedMemorySize, LDS_TOT);
  k_main<<<256, 512, LDS_TOT, stream>>>(x, lre, lim, bre, bim, cre, cim, logdt, dsk, wg, wu, wd, ws, (float*)d_out);
}
```

```cpp
#include <hip/hip_runtime.h>
#include <stdint.h>
#include <type_traits>

typedef unsigned short u16;
typedef unsigned long long u64;
typedef __bf16 bf16x8 __attribute__((ext_vector_type(8)));
typedef __bf16 bf16x2 __attribute__((ext_vector_type(2)));
typedef float f32x4 __attribute__((ext_vector_type(4)));
typedef float f32x2 __attribute__((ext_vector_type(2)));
typedef short s16x4 __attribute__((ext_vector_type(4)));
typedef unsigned u32x4 __attribute__((ext_vector_type(4)));
typedef unsigned u32x2 __attribute__((ext_vector_type(2)));
#define LAS __attribute__((address_space(3)))
#define GAS __attribute__((address_space(1)))
#define DI __device__ __forceinline__

constexpr int SEQ = 2048, DM = 576, FF = 4608;
constexpr int KS2 = 23;
constexpr int NKG = 8 * KS2;
constexpr size_t FL_H = 0;
constexpr size_t FL_P = 40960;
constexpr size_t FL_W = 36864;
constexpr size_t FL_EPOCH = 38912;
constexpr size_t FL_E = 262144;
constexpr size_t FL_S = 180224;
constexpr size_t WS_HT = 524288;
constexpr size_t WS_WT = WS_HT + (size_t)128 * NKG * 1024;
constexpr size_t WS_PART = WS_WT + (size_t)4 * NKG * 9 * 1024;
constexpr size_t PART_BYTES = (size_t)8 * 4 * 8 * 8 * 9 * 1024;
constexpr size_t WS_END = WS_PART + PART_BYTES;

DI float bf2f(u16 v) { return __uint_as_float(((unsigned)v) << 16); }
DI float rbf(float f) { return (float)(__bf16)f; }
DI unsigned pk2(float lo, float hi) { f32x2 v = {lo, hi}; bf16x2 c = __builtin_convertvector(v, bf16x2); return __builtin_bit_cast(unsigned, c); }
DI float blo(unsigned u) { return __uint_as_float(u << 16); }
DI float bhi(unsigned u) { return __uint_as_float(u & 0xffff0000u); }

__host__ __device__ inline int wt_frag(int j, int db) { return j < 16 ? j * 9 + db : 144 + db * 7 + (j - 16); }
DI int kg_f0b(int s, int j) {
  const int a = 512 * s + 128 * ((j & 7) >> 1) + 64 * (j >> 3) + 32 * (j & 1), c = 4096 + 64 * s + 32 * (j & 1);
  return j < 16 ? a : c;
}
__host__ __device__ inline int kg_f0(int s, int j) {
  if (j < 16) { int p = j >> 3, cg = 4 * s + ((j & 7) >> 1); return 64 * (2 * cg + p) + 32 * (j & 1); }
  return 4096 + 32 * (2 * s + (j - 16));
}

template <int N> using IC = std::integral_constant<int, N>;
template <int B, int E, class F> DI void sfor(F&& f) { if constexpr (B < E) { f(IC<B>{}); sfor<B + 1, E>(f); } }

DI void glds_sc1(const void* sbase, unsigned voff, unsigned ldsaddr) {
  asm volatile("s_mov_b32 m0, %2\n\ts_nop 0\n\tglobal_load_lds_dwordx4 %0, %1 sc1" :: "v"(voff), "s"(sbase), "s"(ldsaddr) : "memory");
}
DI void gld4(unsigned& dst, const void* sbase, unsigned voff) {
  asm volatile("global_load_dword %0, %1, %2" : "=v"(dst) : "v"(voff), "s"(sbase) : "memory");
}
template <int C, int L, bool SC1> DI void glds_c(const void* sbase, unsigned voff, unsigned ldsw) {
  unsigned t;
  if constexpr (SC1) asm volatile("s_add_u32 m0, %3, %5\n\tv_add_u32 %0, %4, %1\n\tglobal_load_lds_dwordx4 %0, %2 sc1" : "=&v"(t) : "v"(voff), "s"(sbase), "s"(ldsw), "i"(C), "i"(L) : "memory", "scc");
  else asm volatile("s_add_u32 m0, %3, %5\n\tv_add_u32 %0, %4, %1\n\tglobal_load_lds_dwordx4 %0, %2" : "=&v"(t) : "v"(voff), "s"(sbase), "s"(ldsw), "i"(C), "i"(L) : "memory", "scc");
}
template <int OFF, class T> DI void gld16o_sc1(T& dst, const void* sbase, unsigned voff) {
  asm volatile("global_load_dwordx4 %0, %1, %2 offset:%3 sc1" : "=v"(dst) : "v"(voff), "s"(sbase), "i"(OFF) : "memory");
}
template <int OFF, class T> DI void gld16o_io(T& dst, const void* sbase, unsigned voff) {
  asm volatile("global_load_dwordx4 %0, %1, %2 offset:%3 sc1" : "+v"(dst) : "v"(voff), "s"(sbase), "i"(OFF) : "memory");
}
template <int OFF, bool IO> DI void gld8o(u32x2& dst, const void* sbase, unsigned voff) {
  if constexpr (IO) asm volatile("global_load_dwordx2 %0, %1, %2 offset:%3 sc1" : "+v"(dst) : "v"(voff), "s"(sbase), "i"(OFF) : "memory");
  else asm volatile("global_load_dwordx2 %0, %1, %2 offset:%3 sc1" : "=v"(dst) : "v"(voff), "s"(sbase), "i"(OFF) : "memory");
}
template <int L> DI void glds_run(unsigned& voff, const void* sbase, unsigned step, unsigned ldsa) {
  asm volatile("s_add_u32 m0, %3, %4\n\ts_nop 0\n\tglobal_load_lds_dwordx4 %0, %1\n\tv_add_u32 %0, %2, %0" : "+v"(voff) : "s"(sbase), "s"(step), "s"(ldsa), "i"(L) : "memory", "scc");
}
DI void glds_s(const void* sbase, unsigned voff, unsigned ldsaddr) {
  asm volatile("s_mov_b32 m0, %2\n\ts_nop 0\n\tglobal_load_lds_dwordx4 %0, %1" :: "v"(voff), "s"(sbase), "s"(ldsaddr) : "memory");
}
template <class T> DI void gld16(T& dst, const void* sbase, unsigned voff) {
  asm volatile("global_load_dwordx4 %0, %1, %2" : "=v"(dst) : "v"(voff), "s"(sbase) : "memory");
}
template <class T> DI void gld16_sc1(T& dst, const void* sbase, unsigned voff) {
  asm volatile("global_load_dwordx4 %0, %1, %2 sc1" : "=v"(dst) : "v"(voff), "s"(sbase) : "memory");
}
template <class T> DI void gld16_sc1_io(T& dst, const void* sbase, unsigned voff) {
  asm volatile("global_load_dwordx4 %0, %1, %2 sc1" : "+v"(dst) : "v"(voff), "s"(sbase) : "memory");
}
DI void gld4_sc1(unsigned& dst, const void* sbase, unsigned voff) {
  asm volatile("global_load_dword %0, %1, %2 sc1" : "=v"(dst) : "v"(voff), "s"(sbase) : "memory");
}
DI void gld8_sc1(u32x2& dst, const void* sbase, unsigned voff) {
  asm volatile("global_load_dwordx2 %0, %1, %2 sc1" : "=v"(dst) : "v"(voff), "s"(sbase) : "memory");
}
DI void gld8(u32x2& dst, const void* sbase, unsigned voff) {
  asm volatile("global_load_dwordx2 %0, %1, %2" : "=v"(dst) : "v"(voff), "s"(sbase) : "memory");
}
template <int N> DI void waitv() { static_assert(N >= 0 && N < 64, ""); asm volatile("s_waitcnt vmcnt(%0)" ::"i"(N) : "memory"); }
#define SCHED0 __builtin_amdgcn_sched_barrier(0)
DI void bar() { asm volatile("" ::: "memory"); __builtin_amdgcn_s_barrier(); asm volatile("" ::: "memory"); }
template <class T> DI void tie(T& a) { asm volatile("" : "+v"(a)); }
template <int O2> DI bf16x8 trfrag(unsigned addr) {
  s16x4 a = __builtin_amdgcn_ds_read_tr16_b64_v4i16((LAS s16x4*)(addr));
  s16x4 b = __builtin_amdgcn_ds_read_tr16_b64_v4i16((LAS s16x4*)(addr + O2));
  return __builtin_bit_cast(bf16x8, __builtin_shufflevector(a, b, 0, 1, 2, 3, 4, 5, 6, 7));
}
DI bf16x8 ldsfrag(unsigned addr) { return *(LAS bf16x8*)(addr); }
template <bool FIRST> DI void mfma(f32x4& c, bf16x8 a, bf16x8 b) {
  if constexpr (FIRST) asm volatile("v_mfma_f32_16x16x32_bf16 %0, %1, %2, 0" : "=v"(c) : "v"(a), "v"(b));
  else asm volatile("v_mfma_f32_16x16x32_bf16 %0, %1, %2, %0" : "+v"(c) : "v"(a), "v"(b));
}
DI float silu_mul(float g, float u) {
  const float e = __builtin_amdgcn_exp2f(g * -1.44269504089f);
  return g * u * __builtin_amdgcn_rcpf(1.0f + e);
}
DI unsigned silu_mul2(f32x2 g, f32x2 u) {
  const f32x2 t = g * -1.44269504089f;
  f32x2 e; e.x = __builtin_amdgcn_exp2f(t.x); e.y = __builtin_amdgcn_exp2f(t.y);
  const f32x2 d = e + 1.0f;
  f32x2 r; r.x = __builtin_amdgcn_rcpf(d.x); r.y = __builtin_amdgcn_rcpf(d.y);
  const f32x2 h = (g * u) * r;
  return pk2(h.x, h.y);
}
DI float dpp_ror8(float v) { return __int_as_float(__builtin_amdgcn_update_dpp(0, __float_as_int(v), 0x128, 0xf, 0xf, true)); }
DI void cmac(float& d, float P, float Q, float x) {
  asm volatile("v_fmac_f32 %0, %1, %3\n\tv_fmac_f32_dpp %0, %3, %2 row_ror:8 row_mask:0xf bank_mask:0xf" : "+v"(d) : "v"(P), "v"(Q), "v"(x));
}
DI void cmul(float& d, float P, float Q, float x) {
  asm volatile("v_mul_f32 %0, %1, %3\n\tv_fmac_f32_dpp %0, %3, %2 row_ror:8 row_mask:0xf bank_mask:0xf" : "=&v"(d) : "v"(P), "v"(Q), "v"(x));
}
DI void cfma(float& d, float P, float Q, float x, float c) {
  asm volatile("v_fma_f32 %0, %1, %3, %4\n\tv_fmac_f32_dpp %0, %3, %2 row_ror:8 row_mask:0xf bank_mask:0xf" : "=&v"(d) : "v"(P), "v"(Q), "v"(x), "v"(c));
}
DI void v_mul(float& d, float a, float b) { asm volatile("v_mul_f32 %0, %1, %2" : "=v"(d) : "v"(a), "v"(b)); }
DI void v_mulc(float& d, float a) { asm volatile("v_mul_f32 %0, 0xbfb8aa3b, %1" : "=v"(d) : "v"(a)); }
DI void v_fmac(float& d, float a, float b) { asm volatile("v_fmac_f32 %0, %1, %2" : "+v"(d) : "v"(a), "v"(b)); }
DI void v_fmaa(float& d, float c) { asm volatile("v_fma_f32 %0, %0, %1, %1" : "+v"(d) : "v"(c)); }
DI void v_muli(float& d, float a) { asm volatile("v_mul_f32 %0, %0, %1" : "+v"(d) : "v"(a)); }
DI void lds_add(unsigned addr, float v) { asm volatile("ds_add_f32 %0, %1" :: "v"(addr), "v"(v) : "memory"); }
DI void v_exp2(float& d) { asm volatile("v_exp_f32 %0, %0" : "+v"(d)); }
DI void v_add1(float& d) { asm volatile("v_add_f32 %0, 1.0, %0" : "+v"(d)); }
DI void v_rcp(float& d) { asm volatile("v_rcp_f32 %0, %0" : "+v"(d)); }
DI void v_cvt(unsigned& d, float a, float b) { asm volatile("v_cvt_pk_bf16_f32 %0, %1, %2" : "=v"(d) : "v"(a), "v"(b)); }
DI float wave_sum(float v) {
#define DPPADD(c) v += __int_as_float(__builtin_amdgcn_update_dpp(0, __float_as_int(v), c, 0xf, 0xf, true))
  DPPADD(0x111); DPPADD(0x112); DPPADD(0x114); DPPADD(0x118);
  v += __int_as_float(__builtin_amdgcn_update_dpp(0, __float_as_int(v), 0x142, 0xa, 0xf, false));
  v += __int_as_float(__builtin_amdgcn_update_dpp(0, __float_as_int(v), 0x143, 0xc, 0xf, false));
  return __int_as_float(__builtin_amdgcn_readlane(__float_as_int(v), 63));
}
DI void dnop() { asm volatile("s_nop 1"); }
typedef __amdgpu_buffer_rsrc_t rsrc_t;
DI rsrc_t mkrsrc(const void* p, unsigned bytes) { return __builtin_amdgcn_make_buffer_rsrc((void*)p, 0, bytes, 0x00020000); }
DI unsigned vadd_s(unsigned s, unsigned v) { unsigned o; asm volatile("v_add_u32 %0, %1, %2" : "=v"(o) : "s"(s), "v"(v)); return o; }
DI void st8_sc1(rsrc_t r, unsigned voff, unsigned soff, u32x2 v) { __builtin_amdgcn_raw_buffer_store_b64(v, r, vadd_s(soff, voff), 0, 16); }
DI void st16_sc1(rsrc_t r, unsigned voff, unsigned soff, u32x4 v) { __builtin_amdgcn_raw_buffer_store_b128(v, r, vadd_s(soff, voff), 0, 16); }
DI u32x4 ones4() { u32x4 v; asm volatile("v_mov_b32 %0, -1\n\tv_mov_b32 %1, -1\n\tv_mov_b32 %2, -1\n\tv_mov_b32 %3, -1" : "=v"(v[0]), "=v"(v[1]), "=v"(v[2]), "=v"(v[3])); return v; }
DI void setflag(rsrc_t r, bool sel, unsigned off, unsigned tag) { __builtin_amdgcn_raw_buffer_store_b32(tag, r, sel ? off : 0x7ffffff0u, 0, 16); }
DI void spin_flag(const char* base, unsigned voff, unsigned tag) {
  for (unsigned it = 0; it < (1u << 18); ++it) {
    unsigned v = __hip_atomic_load((GAS unsigned*)(base + voff), __ATOMIC_RELAXED, __HIP_MEMORY_SCOPE_AGENT);
    if (__all(v == tag)) break;
    __builtin_amdgcn_s_sleep(1);
  }
  asm volatile("" ::: "memory");
}

constexpr int PD = 2, FR = PD + 1;
constexpr int SLOT1 = 24576;
constexpr int SLOT2 = 18432;
constexpr int LDS_TOT = 163840;
constexpr int LDS_SCR = 90112;
constexpr int LDS_PK = 122880;
constexpr int LDS_XS = 98304;
constexpr int LDS_ST = 147456;
constexpr int LDS_CS = 151552;
constexpr int LDS_CST = 153600;
constexpr int LDS_WTOT = LDS_CST + 6144;

#define STAMP(k)
#define STAMPC(k)
#define STAMP_DUMP()

constexpr int p1_off(int v) { return v < 8 ? (v % 4) * SLOT1 : (v < 10 ? (v - 4) * SLOT1 : (v < 12 ? (v - 10) * SLOT1 : 2 * SLOT1 + (v - 12) * 30720)); }
constexpr int p2_off(int u, int g) {
  const int k = u % 5;
  return k < 2 ? k * SLOT2 + g * 1024 : k == 2 ? (g < 12 ? 36864 + g * 1024 : 141312 + (g - 12) * 1024) : 49152 + (k - 3) * SLOT2 + g * 1024;
}

constexpr int GT = 288, G2 = 378, GEND = 585;
constexpr int gl(int v, int g) { return v * 24 + g; }
constexpr int gt(int u, int g) { return GT + u * 30 + g; }
constexpr int g2(int u, int g) { return G2 + u * 18 + g; }
constexpr int SY1 = 8, SYT = 2, SY2 = 4;
constexpr int late_g(int f) { return g2(8, f); }
constexpr int PLAG = 4;

constexpr int SCE = 2 * g2(0, SY2) + 3;
constexpr int SQC = 2 * (G2 + 13);
constexpr int SILU0[3] = {2 * (gl(4, 0) + 12), 2 * (gl(10, 0) + 12), SQC + 8};
constexpr int SILUN[3] = {32, 32, 8};
constexpr int silu_end(int s, int nel) { return SILU0[s] + 9 * (nel / 2) + 1; }
constexpr int S1R = 2 * (GT + 55);
constexpr int S1A = S1R + 12;
constexpr int S1SEL = 2 * g2(2, 0) + 2;
constexpr int SCM = S1SEL + 6;
constexpr int UFIN = 3;
constexpr int SFIN = 2 * g2(UFIN, SY2);
static_assert(S1A + 63 < 2 * g2(0, SY2) && silu_end(2, 8) < S1SEL && SCM + 24 < SFIN - 8, "");
constexpr int csx_n(int v) { return v == 16 ? 2 : 3; }
constexpr int csx_g(int v, int n) {
  const int base[17] = {gl(0, 4), gl(0, 19), gl(1, 12), gl(2, 11), gl(2, 16), gl(3, 3), gl(7, 16), gl(7, 19), gl(8, 1), gl(8, 12), gl(8, 18), gl(9, 0),
                        GT + 27, GT + 30, GT + 34, GT + 37, GT + 40};
  const int step = (v == 5 || v == 6 || v == 7 || v >= 12) ? 1 : 2;
  return base[v] + n * step;
}
constexpr int CSF[3] = {gl(3, SY1), gl(9, SY1), GT + 44};
constexpr int GPC = gl(8, 23);
constexpr int GPK = gl(6, 6);
constexpr int GSYC = GT + 46;
constexpr int xt_g(int h) {
  const int g[18] = {-1, -1, gl(0, 6), gl(0, 12), gl(0, 20), gl(1, 3), gl(1, 11), gl(1, 18), gl(2, 2), gl(2, 10), gl(2, 17), gl(3, 1), gl(3, 9), gl(3, 14), gl(3, 19), gl(4, 0), gl(4, 3), gl(4, 5)};
  return g[h];
}
constexpr int xd_g(int h) { return h < 3 ? -1 : (h < 5 ? h - 2 : xt_g(h - 3) + 3); }

constexpr int csx_at(int G) { for (int v = 0; v < 17; v++) for (int n = 0; n < csx_n(v); n++) if (csx_g(v, n) == G) return v * 4 + n; return -1; }
constexpr int xt_at(int G) { for (int c = 2; c < 18; c++) if (xt_g(c) == G) return c; return -1; }
constexpr int silu_at(int sl) { for (int k = 0; k < 3; k++) if (sl >= SILU0[k] && sl - SILU0[k] <= 9 * (SILUN[k] / 2) + 1) return k; return -1; }

enum { OP_G = 0, OP_X, OP_ST, OP_PO, OP_PH, OP_A, OP_B, OP_FL, OP_EP, OP_HL, OP_PL, OP_EG, OP_SG, OP_PK };
struct Op { int pos, kind, a, n; };
struct Sched {
  Op op[320]; int cnt;
  constexpr void add(int pos, int kind, int a, int n) {
    int i = cnt; while (i > 0 && op[i - 1].pos > pos) { op[i] = op[i - 1]; i--; }
    op[i] = Op{pos, kind, a, n}; cnt++;
  }
  constexpr void unit(int g, int v, int step) { for (int n = 0; n < (v < 12 ? 3 : 4); n++) add(2 * (g + n * step), OP_G, v, n); }
  constexpr Sched() : op{}, cnt(0) {
    for (int h = 0; h < 2; h++) for (int n = 0; n < 2; n++) add(-3, OP_X, h, n);
    for (int n = 0; n < 3; n++) add(-3, OP_G, 0, n);
    for (int n = 0; n < 6; n++) add(-2, OP_ST, 8, n);
    for (int n = 0; n < 2; n++) add(-1, OP_X, 2, n);
    for (int n = 0; n < 3; n++) add(-1, OP_G, 1, n);
    for (int n = 0; n < 2; n++) add(-1, OP_ST, 7, n);
    add(-1, OP_ST, 6, 0);
    for (int h = 3; h < 18; h++) for (int n = 0; n < 2; n++) add(2 * xd_g(h), OP_X, h, n);
    for (int n = 0; n < 4; n++) add(2 * gl(4, 10 + 3 * n), OP_PK, 0, n);
    add(2 * gl(7, 15), OP_PK, 1, 0);
    add(2 * GPC - 1, OP_ST, 9, 0);
    unit(gl(0, 4), 2, 1);
    for (int n = 0; n < 4; n++) add(2 * GPK - 1, OP_ST, 4, n);
    add(2 * gl(1, 5), OP_SG, 0, 0);
    unit(gl(0, 9), 3, 3); unit(gl(1, 9), 4, 3); unit(gl(2, 9), 5, 1); unit(gl(3, 10), 6, 3); unit(gl(4, 9), 7, 3);
    unit(gl(5, 10), 8, 4); unit(gl(6, 9), 9, 1); unit(gl(6, 13), 10, 2); unit(gl(6, 19), 11, 2); unit(gl(8, 10), 12, 3); unit(gl(9, 10), 13, 3); unit(gl(10, 10), 14, 3);
    for (int n = 0; n < 3; n++) add(2 * gl(4, 1 + 2 * n), OP_PH, 0, n);
    for (int s = 0; s < 2; s++) for (int h = 0; h < 2; h++) for (int t = 0; t < 2; t++) add((silu_end(s, 16 * (h + 1)) / 2) * 2 + 1, OP_ST, s, 2 * h + t);
    for (int t = 0; t < 2; t++) add((silu_end(2, 8) / 2) * 2 + 1, OP_ST, 2, t);
    add(2 * gl(11, 16), OP_FL, 4, 0);
    add(2 * gt(0, 6), OP_FL, 0, 0);
    for (int n = 0; n < 2; n++) { add(2 * gt(0, 20 + 4 * n), OP_A, 0, n); add(2 * gt(1, 8 + 4 * n), OP_A, 1, n); add(2 * gt(1, 16 + 4 * n), OP_A, 2, n); }
    for (int n = 0; n < 4; n++) { add(2 * gt(2, 3 + 2 * n), OP_B, 0, n); add(2 * gt(2, 11 + 2 * n), OP_B, 1, n); add(2 * gt(2, 19 + 3 * n), OP_B, 2, n); }
    add(2 * g2(0, 1), OP_FL, 1, 0);
    for (int u = 0; u < 9; u++) {
      const int U = u + 3;
      for (int n = 0; n < (U < 11 ? 2 : 1); n++) add(2 * g2(u, 5 + 2 * n), OP_A, U, n);
      if (U < 8) for (int n = 0; n < 4; n++) add(2 * g2(u, 9 + 2 * n), OP_B, U, n);
    }
    for (int n = 0; n < 7; n++) add(2 * g2(UFIN - 1, 10) + 0, OP_EG, 0, n);
    add(2 * g2(4, 6), OP_FL, 2, 0);
    for (int n = 0; n < 4; n++) add(2 * g2(5, 9 + 2 * n), OP_HL, 0, n);
    for (int n = 4; n < 14; n++) add(2 * g2(6, 8 + (n - 4)), OP_HL, 0, n);
    for (int n = 0; n < 6; n++) add(2 * g2(8, 6 + 2 * n), OP_EP, 0, n);
    for (int db = 0; db < 8; db++) add(2 * (late_g(7 * db + 6) + 3) + 1, OP_ST, 3, db);
  }
  static constexpr bool droppable(const Op& o) { return (o.kind == OP_PH && o.n == 2) || (o.kind == OP_ST && (o.a == 4 || o.a == 9)) || (o.kind == OP_PK && o.a == 0); }
  constexpr int first_at(int pos) const { int i = 0; while (i < cnt && op[i].pos < pos) i++; return i; }
  constexpr int after(int kind, int a, int g) const {
    int last = -1;
    for (int i = 0; i < cnt; i++) if (op[i].kind == kind && op[i].a == a) last = i;
    int n = 0;
    for (int i = last + 1; i < cnt; i++) if (op[i].pos < 2 * g && !droppable(op[i])) n++;
    return n;
  }
};
constexpr Sched S1{};
constexpr int cmin(int a, int b) { return a < b ? a : b; }
constexpr int cmin(int a, int b, int c) { return cmin(cmin(a, b), c); }
constexpr bool l_dbl(int v) { return v == 4 || v == 5 || v == 6 || v == 7 || v >= 10; }
struct LF { int unit, ks, blk; };
constexpr LF l_frag(int G) {
  const int v = G / 24, g = G % 24;
  if (!l_dbl(v)) return LF{v, 3 * (v % 6) + (g >> 3), g & 7};
  const int v0 = v & ~1, r = (v & 1) * 24 + g, p = r / 12, k6 = (r % 12) / 2;
  return LF{v0 + k6 / 3, 3 * (v0 % 6) + k6, p + 4 * (r & 1)};
}
constexpr int l_prev_unit(int G) {
  for (int g = G - 1; g >= 0; g--) if ((l_frag(g).blk & 3) == (l_frag(G).blk & 3)) return l_frag(g).unit;
  return 0;
}
constexpr int l_unit0(int G) { for (int g = G - 1; g >= 0; g--) if ((l_frag(g).blk & 3) == 0) return l_frag(g).unit; return 0; }
struct Corr {
  int item[40];
  constexpr Corr(int pass) : item{} {
    const int g0 = gl(6 * pass + 3, SY1 + 2);
    bool done[16] = {};
    const int order[8] = {0, 4, 1, 5, 2, 6, 3, 7};
    for (int k = 0; k < 40; k++) {
      item[k] = -1;
      const int G = g0 + k;
      for (int o = 0; o < 16 && item[k] < 0; o++) {
        const int bk = order[o >> 1], it = bk * 2 + (o & 1);
        if (done[it]) continue;
        if (l_frag(G).blk == bk || l_frag(G - 1).blk == bk || l_frag(G - 2).blk == bk || l_frag(G + 1).blk == bk) continue;
        item[k] = it; done[it] = true;
      }
    }
  }
  constexpr int last() const { int l = 0; for (int k = 0; k < 40; k++) if (item[k] >= 0) l = k; return l; }
  constexpr int count() const { int c = 0; for (int k = 0; k < 40; k++) c += item[k] >= 0; return c; }
};
constexpr Corr CR{0};
static_assert(CR.count() == 16 && gl(3, SY1 + 2) + CR.last() < gl(4, 12), "");
constexpr int t_ks(int r) { return r < 54 ? r / 3 : (r - 54) / 2; }
constexpr int t_blk(int r) { return r < 54 ? 2 + r % 3 : (r - 54) & 1; }

__global__ __launch_bounds__(512, 2) void k_main(const u16* __restrict__ x, const u16* __restrict__ lre, const u16* __restrict__ lim,
                                                 const u16* __restrict__ bre, const u16* __restrict__ bim, const u16* __restrict__ cre,
                                                 const u16* __restrict__ cim, const u16* __restrict__ logdt, const u16* __restrict__ dsk,
                                                 const u16* __restrict__ wg, const u16* __restrict__ wu, const u16* __restrict__ wd,
                                                 char* __restrict__ ws, float* __restrict__ out) {
  extern __shared__ __attribute__((aligned(16))) char lds[];
  const int tid = threadIdx.x, w = __builtin_amdgcn_readfirstlane(tid >> 6);
  const int b = blockIdx.x, sx = b & 7, idx = b >> 3;
  unsigned lds0 = (unsigned)(size_t)(LAS char*)lds;
  asm volatile("" : "+s"(lds0));
  const int rt = idx & 7, cgl = idx >> 3, cg = sx * 4 + cgl;
  const rsrc_t rsW = mkrsrc(ws, (unsigned)WS_END);
  const unsigned htw = (unsigned)WS_HT + (unsigned)(((rt * 16 + 2 * w) * 8 + sx) * KS2) * 1024u;
  constexpr unsigned HTB = NKG * 1024;
  const unsigned flw = (unsigned)FL_H + (unsigned)((((rt * 8 + sx) * 4) * 8 + w) * 4) * 4u;
  const f32x4 zero4 = {0.f, 0.f, 0.f, 0.f};
  const unsigned l16 = (tid & 63) * 16;
  unsigned tag = 0;
  unsigned ldsw = lds0 + w * 1024; asm volatile("" : "+s"(ldsw));
  STAMP(0);

  f32x4 acc[8][2];
  f32x4 tac[5][2];
  f32x4 ac2[9][2];
  bf16x8 fr[FR];
  bf16x8 xf[18][2];
  constexpr int HR = 4;
  bf16x8 hf[HR][2][2];
  bf16x8 hl[7][2];
  u32x4 pv[9];
  unsigned flv[5] = {0, 0, 0, 0, 0};
  u32x2 epv[6];
  u32x2 eg[7];
  unsigned ept[3], epd[3];
  unsigned wvoff, ab[4], tvA = 0, tvF = 0;
  const char* wsrc = (const char*)((w >> 2) ? wu : wg) + (size_t)cg * 256;
  {
    const int lane = tid & 63, q = lane >> 4, li = lane & 15;
    const int rg = w & 3, r8 = lane >> 3, ch = lane & 7;
    const int g = ((lane >> 4) & 1) | ((rg & 1) << 1);
    wvoff = (unsigned)((8 * rg + r8) * (FF * 2) + (((ch >> 1) ^ g) << 5) + ((ch & 1) << 4));
    const int r4 = li >> 2, cc = lane & 3;
    const int gr = ((r4 >> 1) & 1) | ((q & 1) << 1);
    const unsigned a0 = (unsigned)((8 * q + r4) * 128 + (gr << 5) + 8 * cc);
#pragma unroll
    for (int k = 0; k < 4; k++) { ab[k] = lds0 + (a0 ^ (k << 5)); tie(ab[k]); }
    if (tid < 384) *(LAS float*)(lds0 + LDS_WTOT + tid * 4) = 0.f;
  }
  const char* xb = (const char*)x + (size_t)(rt * 256 + 32 * w) * (DM * 2);
  unsigned xvo, xvo1, xra, xra1;
  unsigned ldsx = lds0 + w * 6144; asm volatile("" : "+s"(ldsx));
  {
    const int lane = tid & 63, q = lane >> 4, li = lane & 15, r8 = lane >> 3;
    xvo = (unsigned)(r8 * (DM * 2) + (((lane & 7) ^ (r8 >> 1)) << 4));
    xra = lds0 + LDS_XS + w * 6144 + li * 128 + ((q ^ (li >> 1)) << 4);
    xvo1 = xvo ^ 64u; xra1 = xra ^ 64u;
    tie(xvo); tie(xra); tie(xvo1); tie(xra1);
  }
  u32x2 sgr;
  const unsigned pobase = (unsigned)WS_PART + (unsigned)(((rt * 4 + cgl) * 8 + sx) * 72) * 1024u;
  const unsigned pb = (unsigned)WS_PART + (unsigned)((((rt * 4 + cgl) * 8 + w) * 8 + sx) * 9) * 1024u;
  const char* wtb = ws + WS_WT + ((size_t)cgl * NKG + sx * KS2) * 9 * 1024 + w * 1024;
  const char* hb0 = ws + htw;
  const int erow = rt * 256 + 32 * sx;
  const int blk3 = w < 4 ? 2 * cgl + (w >> 1) : 8;
  const char* tB = (const char*)((w & 1) ? bim : bre) + (size_t)(8 * (9 * sx + blk3)) * (DM * 2);
  const size_t fcol = (size_t)(4096 + 64 * sx + 16 * cgl) * 2;
  auto fsrc = [&](int fp) { return (const char*)((fp & 1) ? wu : wg) + fcol + (size_t)(fp >> 1) * 32 * (FF * 2); };
  const char* tsF = nullptr; const char* tsA = nullptr; unsigned tsn = 0, tsu = 0, tdA = 0, tdn = 0;
  unsigned cla2 = 0;
  unsigned cla = 0;

  u32x4 cfr; unsigned pl[3], pkv = 0, pkl = 0;
  const int widx = w * 32 + idx;
  const int wi = widx < 162 ? widx : widx - 162, pj = (wi * 57) >> 9, pln = wi - 9 * pj;
  const int ci = widx < 180 ? widx : widx - 180, cd16 = (ci * 205) >> 10, cjj = ci - 5 * cd16;
  auto issue = [&](auto Ic) {
    constexpr Op o = S1.op[decltype(Ic)::value];
    if constexpr (o.kind == OP_G) {
      if constexpr (o.a < 12) {
        glds_c<(o.a / 6) * 128 + (3 * (o.a % 6) + o.n) * 32 * (FF * 2), p1_off(o.a) + o.n * 8192, false>(wsrc, wvoff, ldsw);
      } else {
        constexpr int U = o.a - 12;
        if constexpr (U == 0 && o.n == 0) {
          unsigned l_ = l16; tie(l_);
          const int lane = l_ >> 4, p = lane >> 1;
          tvF = (unsigned)((p ^ (((p >> 3) & 1) << 2)) * (FF * 2) + ((lane & 1) << 4));
          const unsigned vb = (unsigned)((lane >> 3) * (DM * 2) + (((lane & 7) ^ (4 * (w & 1) + (lane >> 4))) << 4));
          tvA = w < 6 ? vb : tvF; tie(tvA); tie(tvF);
          tsF = fsrc(w); tsA = w < 6 ? tB : tsF;
          tsn = w < 6 ? 128u : 32u * (FF * 2); tsu = w < 6 ? 128u : 128u * (FF * 2);
          tdA = ldsw + (w < 6 ? 12288 : 0); tdn = w < 6 ? 6144u : 2048u;
          asm volatile("" : "+s"(tsF), "+s"(tsA), "+s"(tsn), "+s"(tsu), "+s"(tdA), "+s"(tdn));
        }
        if constexpr (o.n < 3) {
          glds_run<p1_off(o.a)>(tvA, tsA, o.n < 2 ? tsn : tsu, tdA + o.n * tdn);
        } else glds_c<U * 192 * (FF * 2), p1_off(o.a), false>(tsF, tvF, ldsw);
      }
    } else if constexpr (o.kind == OP_X) {
      glds_c<((o.a & 1) * 16 + o.n * 8) * (DM * 2) + (o.a >> 1) * 128, LDS_XS + (o.a % 3) * 2048 + o.n * 1024, false>(xb, o.n ? xvo1 : xvo, ldsx);
    } else if constexpr (o.kind == OP_PK) {
      if constexpr (o.a == 0) {
        if constexpr (o.n == 0) {
          unsigned l_ = l16; tie(l_);
          pkv = (l_ >> 7) * (DM * 2) + (l_ & 112) + (unsigned)(kg_f0b(sx, pj) * (DM * 2) + pln * 128); tie(pkv);
          pkl = lds0 + (w < 6 ? LDS_PK + w * 4096 : LDS_TOT); asm volatile("" : "+s"(pkl));
        }
        glds_c<o.n * 8 * (DM * 2), o.n * 1024, false>(wd, pkv, pkl);
      } else {
        unsigned l_ = l16; tie(l_);
        const int lane = l_ >> 4, q = lane >> 4, li = lane & 15;
        const int cbl = 2 * cjj + (q >> 1);
        const char* cp = (const char*)((q & 1) ? cim : cre) + (unsigned)((16 * cd16 + li) * (DM * 2) + 16 * (9 * sx + (cbl < 9 ? cbl : 8)));
        asm volatile("global_load_dwordx4 %0, %1, off" : "=v"(cfr) : "v"(cp) : "memory");
      }
    } else if constexpr (o.kind == OP_SG) {
      gld8_sc1(sgr, ws + FL_S + (size_t)(rt * 256 + 32 * w) * 8, (l16 >> 1) & 248);
    } else if constexpr (o.kind == OP_PO) {
      st16_sc1(rsW, l16, pobase + (unsigned)(o.n * 8 + w) * 1024u, ones4());
    } else if constexpr (o.kind == OP_PH) {
      if constexpr (o.n < 2) st16_sc1(rsW, l16, htw + o.n * HTB + (unsigned)(18 + cgl) * 1024u, ones4());
      else {
        const unsigned f = (l16 & 512) ? 0u : ~0u;
        st16_sc1(rsW, cgl < 2 ? l16 : 0x70000000u, htw + (cgl & 1) * HTB + 22u * 1024u, (u32x4){f, f, f, f});
      }
    } else if constexpr (o.kind == OP_A) {
      constexpr int U = o.a;
      if constexpr (U % 5 == 2 && o.n == 1) glds_c<U * SLOT2 + 8192, 0, true>(wtb, l16, ldsw + (w >= 4 ? p2_off(U, 12) - 4096 : p2_off(U, 8)));
      else glds_c<U * SLOT2 + o.n * 8192, p2_off(U, 8 * o.n), true>(wtb, l16, ldsw);
      if constexpr (U < 11 && o.n == 1) { if (w < 2) glds_c<U * SLOT2 + 16384, p2_off(U, 16), true>(wtb, l16, ldsw); }
      if constexpr (U == 11) { if (w < 1) glds_c<U * SLOT2 + 8192, p2_off(U, 8), true>(wtb, l16, ldsw); }
    } else if constexpr (o.kind == OP_B) {
      constexpr int kg = 2 * o.a + (o.n >> 1);
      gld16o_sc1<(kg % 8 - 4) * 1024>(hf[o.a % HR][o.n >> 1][o.n & 1], hb0 + (size_t)(o.n & 1) * HTB + (kg / 8) * 8192 + 4096, l16);
    } else if constexpr (o.kind == OP_HL) {
      gld16o_sc1<((o.n >> 1) - 4) * 1024>(hl[o.n >> 1][o.n & 1], hb0 + (size_t)(o.n & 1) * HTB + 16384 + 4096, l16);
    } else if constexpr (o.kind == OP_PL) {
      gld16o_sc1<(o.n - 4) * 1024>(pv[o.n], ws + (size_t)pobase + (size_t)w * 9216 + 4096, l16);
    } else if constexpr (o.kind == OP_EG) {
      gld8o<o.n * 384, false>(eg[o.n], ws + FL_E + (size_t)(cg * 16 + rt + 1) * 384, cla2);
    } else if constexpr (o.kind == OP_FL) {
      unsigned lz = l16; tie(lz);
      if constexpr (o.a < 3) gld4_sc1(flv[o.a], ws + flw + 128 * o.a, (lz >> 2) & 12);
      else if constexpr (o.a == 3) gld4_sc1(flv[3], ws + FL_W + (size_t)(w * 32 + idx) * 4, lz & 0);
      else gld4_sc1(flv[4], ws + FL_W + (size_t)sx * 128, (lz >> 2) & 124);
    } else if constexpr (o.kind == OP_EP) {
      constexpr int r = o.n / 2;
      if constexpr (o.n % 2 == 0) {
        unsigned l_ = l16; tie(l_);
        const unsigned e = min((l_ >> 4) + 64u * w + 512u * r, 1151u), t = (e * 1821u) >> 16;
        ept[r] = t; epd[r] = (e - 36u * t) * 8u; tie(ept[r]); tie(epd[r]);
        gld8(epv[o.n], x + (size_t)erow * DM + cgl * 144, t * (DM * 2) + epd[r]);
      } else gld8(epv[o.n], dsk + cgl * 144, epd[r]);
    }
  };
  auto issue_at = [&](auto Pc) {
    constexpr int i0 = S1.first_at(decltype(Pc)::value), i1 = S1.first_at(decltype(Pc)::value + 1);
    sfor<i0, i1>([&](auto Ic) { issue(Ic); SCHED0; });
  };
  auto xconv = [&](auto Hc) {
    constexpr int h = decltype(Hc)::value, c = h >> 1, t = h & 1;
    xf[2 * c][t] = ldsfrag(xra + (h % 3) * 2048);
    xf[2 * c + 1][t] = ldsfrag(xra1 + (h % 3) * 2048);
  };
  auto htie = [&](auto Uc) { constexpr int U = decltype(Uc)::value; tie(hf[U % HR][0][0]); tie(hf[U % HR][0][1]); tie(hf[U % HR][1][0]); tie(hf[U % HR][1][1]); };
  auto chk_flag = [&](auto Ac) { constexpr int a = decltype(Ac)::value; tie(flv[a]);
    if (!__all(flv[a] == tag)) {
      unsigned lz = l16; tie(lz);
      if constexpr (a < 3) spin_flag(ws + flw + 128 * a, (lz >> 2) & 12, tag);
      else if constexpr (a == 3) spin_flag(ws + FL_W + (size_t)(w * 32 + idx) * 4, lz & 0, tag);
      else spin_flag(ws + FL_W + (size_t)sx * 128, (lz >> 2) & 124, tag); } };

  float te = 0.f, pp = 0.f, hh[2], sc1[2], sc2[2], sng[2];
  f32x4 csw[2];
  f32x4 csv, tst[2];
  f32x4 csa0, csa1, qs[8];
  bf16x8 selA, selB; unsigned cra = 0, crb = 0;
  unsigned hp[2][4];
  float cm[3][8], ca4[3][2], se[3][2], eg4[3][2][4], spre[3][2], sv[3][2][4], cc1[3][2], cin[3][2];
  float tw[8], c32[2], wpre = 0.f, crt = 0.f, cz[3], cpw[3][2], c256[2];
  unsigned fwa = 0, fra = 0, fvo = 0;
  unsigned cs48 = 0;
#define CPAIR(base, pr) (*(LAS f32x2*)((base) + (pr) * 128))
  unsigned csa = 0, pkd[4];
  bool q1 = false, q2 = false;
  auto side = [&](auto Sc) {
    constexpr int sl = decltype(Sc)::value;
    {
      if constexpr (silu_at(sl) >= 0) {
        constexpr int s = silu_at(sl), r = sl - SILU0[s], N = SILUN[s];
        constexpr int pr2 = r / 9, ph = r % 9;
        auto G = [&](auto Ec) -> float { constexpr int e = decltype(Ec)::value; if constexpr (s < 2) return acc[e >> 3][(e >> 2) & 1][e & 3]; else return tac[0][e >> 2][e & 3]; };
        auto Uv = [&](auto Ec) -> float { constexpr int e = decltype(Ec)::value; if constexpr (s < 2) return acc[4 + (e >> 3)][(e >> 2) & 1][e & 3]; else return tac[1][e >> 2][e & 3]; };
        constexpr int e0 = 2 * pr2, e1 = e0 + 1;
        if constexpr (ph == 0) { if constexpr (pr2 > 0) v_mul(hh[1], pp, te); if constexpr (e0 < N) v_mul(te, G(IC<e0 < N ? e0 : 0>{}), sc1[(e0 >> 2) & 1]); }
        if constexpr (ph == 1) {
          if constexpr (pr2 > 0) {
            constexpr int ep = e0 - 2, t = (ep >> 2) & 1;
            v_cvt(hp[t][s < 2 ? 2 * ((ep >> 3) & 1) + ((ep >> 1) & 1) : ((ep >> 1) & 1)], hh[0], hh[1]);
            if constexpr (s < 2 && (e0 & 15) == 0) {
#pragma unroll
              for (int tt = 0; tt < 2; tt++)
                st16_sc1(rsW, l16, htw + tt * HTB + (unsigned)(8 * s + 2 * cgl + (ep >> 4)) * 1024u, (u32x4){hp[tt][0], hp[tt][1], hp[tt][2], hp[tt][3]});
            }
            if constexpr (s == 2 && e0 == 8) {
#pragma unroll
              for (int tt = 0; tt < 2; tt++) st8_sc1(rsW, l16, htw + tt * HTB + (unsigned)((16 + (cgl >> 1)) * 1024 + (cgl & 1) * 8), (u32x2){hp[tt][0], hp[tt][1]});
            }
          }
        }
        if constexpr (e0 < N) {
          if constexpr (ph == 2 || ph == 6) v_exp2(te);
          if constexpr (ph == 3) { v_fmaa(te, sc2[(e0 >> 2) & 1]); v_mul(pp, G(IC<e0>{}), Uv(IC<e0>{})); }
          if constexpr (ph == 4 || ph == 8) v_rcp(te);
          if constexpr (ph == 5) { v_mul(hh[0], pp, te); v_mul(te, G(IC<e1>{}), sc1[(e0 >> 2) & 1]); }
          if constexpr (ph == 7) { v_fmaa(te, sc2[(e0 >> 2) & 1]); v_mul(pp, G(IC<e1>{}), Uv(IC<e1>{})); }
        }
      }
    }
    if constexpr (sl == SILU0[0] - 4 || sl == SILU0[1] - 4 || sl == SILU0[2] - 4) {
      unsigned lz = l16; tie(lz);
#pragma unroll
      for (int t = 0; t < 2; t++) { const f32x2 f = *(LAS f32x2*)(lds0 + LDS_ST + w * 512 + t * 256 + (lz & 240)); sc1[t] = f[0]; sc2[t] = f[1]; }
    }
    if constexpr (sl == 2 * gl(3, SY1) || sl == 2 * gl(9, SY1) || sl == SQC - 14) {
      unsigned lz = l16; tie(lz);
#pragma unroll
      for (int t = 0; t < 2; t++) sng[t] = *(LAS float*)(lds0 + LDS_ST + w * 512 + t * 256 + (lz & 240) + 8);
      cra = lds0 + LDS_CS + (lz >> 8) * 16; tie(cra);
    }
    {
      constexpr int pass = sl / 2 >= gl(6, 0) ? 1 : 0, k = sl / 2 - gl(6 * pass + 3, SY1 + 2), half = sl & 1;
      if constexpr (k >= 0 && k < 40) {
        constexpr int it = CR.item[k];
        if constexpr (it >= 0) { constexpr int bk = it >> 1, t = it & 1; float a0 = acc[bk][t][2 * half], a1 = acc[bk][t][2 * half + 1];
          v_fmac(a0, sng[t], csw[k & 1][2 * half]); v_fmac(a1, sng[t], csw[k & 1][2 * half + 1]); acc[bk][t][2 * half] = a0; acc[bk][t][2 * half + 1] = a1; }
      }
      if constexpr (k >= -2 && k < 38 && half == 1) {
        constexpr int nx = CR.item[k + 2];
        if constexpr (nx >= 0) csw[k & 1] = *(LAS f32x4*)(cra + pass * 512 + ((nx >> 1) >> 2) * 256 + ((nx >> 1) & 3) * 64);
      }
    }
    if constexpr (sl == SQC - 12) {
#pragma unroll
      for (int k = 0; k < 8; k++) qs[k] = *(LAS f32x4*)(cra + 1280 + k * 64);
    }
    if constexpr (sl >= SQC - 8 && sl < SQC) {
      constexpr int r = sl - (SQC - 8);
#pragma unroll
      for (int e = 3 * r; e < 3 * r + 3; e++) { const int k = 2 + e / 4, j = e & 3; float a = qs[k & 1][j]; asm volatile("v_add_f32 %0, %0, %1" : "+v"(a) : "v"(qs[k][j])); qs[k & 1][j] = a; }
      if constexpr (r == 7) { csv = qs[0]; csa0 = qs[1]; }
    }
    if constexpr (sl >= SQC && sl < SQC + 8) {
      constexpr int r = sl - SQC, mt = r >> 2, t = (r >> 1) & 1, half = r & 1;
      float a0 = tac[mt][t][2 * half], a1 = tac[mt][t][2 * half + 1];
      if constexpr (mt == 0) { v_fmac(a0, sng[t], csv[2 * half]); v_fmac(a1, sng[t], csv[2 * half + 1]); }
      else { v_fmac(a0, sng[t], csa0[2 * half]); v_fmac(a1, sng[t], csa0[2 * half + 1]); }
      tac[mt][t][2 * half] = a0; tac[mt][t][2 * half + 1] = a1;
    }
    if constexpr (sl == 2 * GSYC) {
      unsigned lz = l16; tie(lz);
      crb = lds0 + LDS_ST + w * 512 + (lz >> 8) * 64; tie(crb);
#pragma unroll
      for (int t = 0; t < 2; t++)
#pragma unroll
        for (int j = 0; j < 4; j++) tst[t][j] = *(LAS float*)(crb + t * 256 + j * 16 + 8);
#pragma unroll
      for (int k = 0; k < 3; k++) csv[k] = *(LAS float*)(lds0 + LDS_CS + 1024 + k * 64 + ((lz >> 2) & 60));
    }
    if constexpr (sl >= 2 * (GT + 48) && sl < 2 * (GT + 54)) {
      constexpr int r = sl / 2 - (GT + 48), k = (r + 1) % 3, t = r / 3, half = sl & 1;
      float a0 = tac[2 + k][t][2 * half], a1 = tac[2 + k][t][2 * half + 1];
      v_fmac(a0, tst[t][2 * half], csv[k]); v_fmac(a1, tst[t][2 * half + 1], csv[k]);
      if constexpr (half) dnop();
      tac[2 + k][t][2 * half] = a0; tac[2 + k][t][2 * half + 1] = a1;
    }
    if constexpr (sl == S1R - 2) {
#pragma unroll
      for (int t = 0; t < 2; t++)
#pragma unroll
        for (int j = 0; j < 4; j++) tst[t][j] = *(LAS float*)(crb + t * 256 + j * 16 + 12);
    }
    if constexpr (sl >= S1R && sl < S1R + 12) {
      constexpr int r = sl - S1R, k = r >> 2, t = (r >> 1) & 1, half = r & 1;
      float a0 = tac[2 + k][t][2 * half], a1 = tac[2 + k][t][2 * half + 1];
      v_muli(a0, tst[t][2 * half]); v_muli(a1, tst[t][2 * half + 1]);
      tac[2 + k][t][2 * half] = a0; tac[2 + k][t][2 * half + 1] = a1;
    }
    if constexpr (sl == S1A - 12) {
      unsigned l_ = l16; tie(l_); const int tl = (int)(l_ >> 4) + 64 * w;
      const int lane = tl & 63;
      csa = lds0 + LDS_CST + (lane & 15) * 8; cla = (unsigned)(lane < 48 ? lane : lane - 16) * 4u;
      cs48 = csa + (cla >> 6) * 2048; cla2 = cla * 2; tie(cla2);
      q1 = lane & 16; q2 = lane & 32;
    }
    if constexpr (sl == S1A - 6 || sl == S1A + 2 || sl == S1A + 10) {
      constexpr int k = (sl - (S1A - 6)) / 8;
#pragma unroll
      for (int i = 0; i < 4; i++) { const f32x2 m = CPAIR(csa + k * 2048, 1 + i); cm[k][2 * i] = m[0]; cm[k][2 * i + 1] = m[1]; }
    }
    if constexpr (sl >= S1A && sl < S1A + 24) {
      constexpr int r = sl - S1A, k = r / 8, j = (r % 8) >> 1, t = r & 1;
      if constexpr (j == 0) cmul(se[k][t], cm[k][6], cm[k][7], tac[2 + k][t][0]);
      else cmac(se[k][t], cm[k][2 * (3 - j)], cm[k][2 * (3 - j) + 1], tac[2 + k][t][j]);
    }
    if constexpr (sl == S1A + 36) {
#pragma unroll
      for (int k = 0; k < 3; k++) { const f32x2 a4 = CPAIR(csa + k * 2048, 6); ca4[k][0] = a4[0]; ca4[k][1] = a4[1]; }
    }
    if constexpr (sl == SCM - 4) {
#pragma unroll
      for (int k = 0; k < 3; k++) { const f32x2 mm = CPAIR(csa + k * 2048, 1); cin[k][0] = mm[0]; cin[k][1] = mm[1]; }
    }
    if constexpr (sl >= S1A + 24 && sl < S1A + 42) {
      constexpr int r = sl - (S1A + 24), st = r / 6, k = (r % 6) >> 1, t = r & 1;
      if constexpr (st == 0) {
        const unsigned pu = __float_as_uint(se[k][t]);
        const auto s16 = __builtin_amdgcn_permlane16_swap(pu, pu, false, false);
        eg4[k][t][0] = __uint_as_float(s16[0]); eg4[k][t][1] = __uint_as_float(s16[1]);
      } else {
        const unsigned pu = __float_as_uint(eg4[k][t][st - 1]);
        const auto sw = __builtin_amdgcn_permlane32_swap(pu, pu, false, false);
        eg4[k][t][st - 1] = __uint_as_float(sw[0]); eg4[k][t][st + 1] = __uint_as_float(sw[1]);
      }
    }
    if constexpr (sl >= S1A + 42 && sl < S1A + 63) {
      constexpr int r = sl - (S1A + 42), sgm = 1 + r / 3, k = r % 3;
      cmac(eg4[k][sgm >> 2][sgm & 3], ca4[k][0], ca4[k][1], eg4[k][(sgm - 1) >> 2][(sgm - 1) & 3]);
    }
    if constexpr (sl == S1A + 63) {
      *(LAS float*)(lds0 + LDS_WTOT + (8 + w) * 192 + cla) = q2 ? eg4[2][1][3] : (q1 ? eg4[1][1][3] : eg4[0][1][3]);
    }
    if constexpr (sl >= S1SEL && sl < S1SEL + 6) {
      constexpr int r = sl - S1SEL, k = r >> 1, t = r & 1;
      const float lo = q1 ? eg4[k][t][0] : (t ? eg4[k][0][3] : 0.f), hi = q1 ? eg4[k][t][2] : eg4[k][t][1];
      spre[k][t] = q2 ? hi : lo; tie(spre[k][t]);
    }
    if constexpr (sl >= SCE && sl < SCE + 7) {
      constexpr int k = sl - SCE + 1;
      cmac(tw[k], c32[0], c32[1], tw[k - 1]);
      if constexpr (k == 7) {
        wpre = tw[6];
        __builtin_amdgcn_raw_buffer_store_b64((u32x2){__float_as_uint(tw[7]), tag}, rsW, (w == 7 && l16 < 768) ? (unsigned)FL_E + (unsigned)((cg * 16 + 8 + rt) * 384) + cla2 : 0x7ffffff0u, 0, 16);
      }
    }
    if constexpr (sl >= SCM && sl < SCM + 24) {
      constexpr int r = sl - SCM, k = r / 8, t = (r >> 2) & 1, j = r & 3;
      cmul(sv[k][t][j], cin[k][0], cin[k][1], tac[2 + k][t][j]);
    }
    if constexpr (sl == SFIN - 8) {
      const f32x2 a = CPAIR(cs48, 13), p0 = CPAIR(cs48, (w & 1) ? 10 : 5), p1 = CPAIR(cs48, (w & 2) ? 11 : 5), p2 = CPAIR(cs48, (w & 4) ? 12 : 5);
      c256[0] = a[0]; c256[1] = a[1]; cpw[0][0] = p0[0]; cpw[0][1] = p0[1]; cpw[1][0] = p1[0]; cpw[1][1] = p1[1]; cpw[2][0] = p2[0]; cpw[2][1] = p2[1];
    }
    if constexpr (sl >= SFIN - 6 && sl < SFIN - 3) {
      constexpr int k = sl - (SFIN - 6);
      unsigned lz = l16; tie(lz);
      const f32x2 a1 = CPAIR(csa + k * 2048, 0), pq = CPAIR(csa + k * 2048, 5 + (lz >> 8)), a16 = CPAIR(csa + k * 2048, 9);
      cc1[k][0] = a1[0]; cc1[k][1] = a1[1]; cin[k][0] = pq[0]; cin[k][1] = pq[1]; ca4[k][0] = a16[0]; ca4[k][1] = a16[1];
    }
    if constexpr (sl >= SFIN && sl < SFIN + 6) {
      constexpr int r = sl - SFIN + 1;
      float n = __uint_as_float(eg[r][0]); cmac(n, c256[0], c256[1], crt); crt = n;
    }
    if constexpr (sl == SFIN + 6) { float n; cmul(n, cpw[0][0], cpw[0][1], crt); crt = n; }
    if constexpr (sl == SFIN + 7) { float n; cmul(n, cpw[1][0], cpw[1][1], crt); crt = n; }
    if constexpr (sl == SFIN + 8) cmac(wpre, cpw[2][0], cpw[2][1], crt);
    if constexpr (sl == SFIN + 9) {
      unsigned lz = l16; tie(lz);
      const unsigned scr = lds0 + LDS_SCR + w * 1024, q = lz >> 8, li = (lz >> 4) & 15;
      fwa = scr + li * 32 + q * 8;
      fra = scr + (q >> 1) * 512 + (8 * (q & 1) + (li >> 2)) * 32 + (li & 3) * 8;
      fvo = (q >> 1) * HTB + ((q & 1) * 16 + li) * 16;
      tie(fwa); tie(fra); tie(fvo);
    }
    if constexpr (sl == SFIN + 10) {
      const unsigned pu = __float_as_uint(wpre);
      const auto s16 = __builtin_amdgcn_permlane16_swap(pu, pu, false, false);
      cz[0] = __uint_as_float(s16[0]); cz[1] = __uint_as_float(s16[1]);
    }
    if constexpr (sl == SFIN + 11) { const unsigned pu = __float_as_uint(cz[0]); const auto sw = __builtin_amdgcn_permlane32_swap(pu, pu, false, false); cz[0] = __uint_as_float(sw[0]); cz[2] = __uint_as_float(sw[1]); }
    if constexpr (sl == SFIN + 12) { const unsigned pu = __float_as_uint(cz[1]); const auto sw = __builtin_amdgcn_permlane32_swap(pu, pu, false, false); cz[1] = __uint_as_float(sw[0]); }
    if constexpr (sl >= SFIN + 13 && sl < SFIN + 16) { constexpr int k = (sl - (SFIN + 13)) == 1 ? 2 : ((sl - (SFIN + 13)) == 2 ? 1 : 0); float n; cmul(n, cin[k][0], cin[k][1], cz[k]); cz[k] = n; }
    if constexpr (sl >= SFIN + 16 && sl < SFIN + 19) { constexpr int k = (sl - (SFIN + 16)) == 1 ? 2 : ((sl - (SFIN + 16)) == 2 ? 1 : 0); cmac(spre[k][1], ca4[k][0], ca4[k][1], cz[k]); }
    if constexpr (sl == SFIN + 19) { asm volatile("v_add_f32 %0, %0, %1" : "+v"(spre[0][0]) : "v"(cz[0])); asm volatile("v_add_f32 %0, %0, %1" : "+v"(spre[1][0]) : "v"(cz[1])); asm volatile("v_add_f32 %0, %0, %1" : "+v"(spre[2][0]) : "v"(cz[2])); }
    if constexpr (sl >= SFIN + 20 && sl < SFIN + 44) {
      constexpr int r = sl - (SFIN + 20), j = r / 6, k = (r % 6) >> 1, t = (r & 1) ^ 1;
      cmac(sv[k][t][j], cc1[k][0], cc1[k][1], j == 0 ? spre[k][t] : sv[k][t][j > 0 ? j - 1 : 0]);
    }
    if constexpr (sl >= SFIN + 44 && sl < SFIN + 53) {
      constexpr int r = sl - (SFIN + 44), k = r / 3, ph = r % 3;
      if constexpr (ph < 2) {
        v_cvt(pkd[0], sv[k][ph][0], sv[k][ph][1]); v_cvt(pkd[1], sv[k][ph][2], sv[k][ph][3]);
        *(LAS u32x2*)(fwa + ph * 512) = (u32x2){pkd[0], pkd[1]};
      } else {
        const u32x4 o = __builtin_bit_cast(u32x4, trfrag<128>(fra));
        st16_sc1(rsW, (k == 2 && cgl) ? 0x70000000u : fvo, htw + (k < 2 ? (unsigned)((18 + cgl) * 1024 + k * 512) : 22u * 1024u), o);
      }
    }
    if constexpr (sl >= 2 * late_g(9) && sl < 2 * GEND) {
      constexpr int r = sl - 2 * late_g(9), db = r / 14, ph = r % 14;
      if constexpr (db < 8) {
        if constexpr (ph == 0) { unsigned a, c; v_cvt(a, ac2[db][0][0], ac2[db][0][1]); v_cvt(c, ac2[db][0][2], ac2[db][0][3]); pkd[0] = a; pkd[1] = c; }
        if constexpr (ph == 1) { unsigned a, c; v_cvt(a, ac2[db][1][0], ac2[db][1][1]); v_cvt(c, ac2[db][1][2], ac2[db][1][3]); st16_sc1(rsW, l16, pb + db * 1024, (u32x4){pkd[0], pkd[1], a, c}); }
      }
    }
  };

  unsigned fa[2], eb[2], a2b[3];
  s16x4 flo;
  auto trl = [&](unsigned a) { return __builtin_amdgcn_ds_read_tr16_b64_v4i16((LAS s16x4*)(a)); };
  auto join = [&](s16x4 a, s16x4 c) { return __builtin_bit_cast(bf16x8, __builtin_shufflevector(a, c, 0, 1, 2, 3, 4, 5, 6, 7)); };
  auto frag = [&](auto Gc, auto Pc) {
    constexpr int G = decltype(Gc)::value, part = decltype(Pc)::value;
    if constexpr (G < GT) {
      constexpr LF f = l_frag(G);
      constexpr int o = (f.ks % 3) * 8192 + (f.blk >> 2) * 4096;
      if constexpr (part == 0) flo = trl(ab[f.blk & 3] + o); else fr[G % FR] = join(flo, trl(ab[f.blk & 3] + o + 512));
    } else if constexpr (G < G2) {
      constexpr int ks = t_ks(G - GT), blk = t_blk(G - GT), uo = (ks / 6) * 30720, k6 = ks % 6;
      if constexpr (blk < 2) {
        constexpr int o = uo - (ks >= 12 ? 30720 : 0) + (k6 * 2 + blk) * 1024;
        if constexpr (part == 0) flo = trl(fa[0] + o); else fr[G % FR] = join(flo, trl(fa[1] + o));
      } else if constexpr (part == 1) fr[G % FR] = ldsfrag(eb[k6 & 1] + uo - (ks >= 12 ? 30720 : 0) + 12288 + (k6 >> 1) * 6144 + (blk - 2) * 2048);
    } else if constexpr (part == 1) {
      constexpr int U = (G - G2) / 18, g = (G - G2) % 18;
      fr[G % FR] = ldsfrag(p2_off(U, g) >= 65536 ? (p2_off(U, g) >= 131072 ? a2b[2] + (p2_off(U, g) - 131072) : a2b[1] + (p2_off(U, g) - 65536)) : a2b[0] + p2_off(U, g));
    }
  };
  auto addr_step = [&](auto Gc) {
    constexpr int G = decltype(Gc)::value;
    if constexpr (G < GT) {
      constexpr int u = l_frag(G).unit, pu = l_prev_unit(G), k = l_frag(G).blk & 3;
      if constexpr (u != pu) { ab[k] += (unsigned)(p1_off(u) - p1_off(pu)); tie(ab[k]); }
    }
    if constexpr (G == GT) {
      unsigned l_ = l16; tie(l_); const int tl = (int)(l_ >> 4) + 64 * w;
      const int lane = tl & 63, q = lane >> 4, li = lane & 15;
      const unsigned e0 = li * 128 + ((q ^ (li >> 1)) << 4);
      eb[0] = lds0 + p1_off(12) + e0; eb[1] = lds0 + p1_off(12) + (e0 ^ 64u); tie(eb[0]); tie(eb[1]);
    }
    if constexpr (G == GT + 36) { eb[0] += 30720; eb[1] += 30720; tie(eb[0]); tie(eb[1]); }
    if constexpr (G == GT + 78) { fa[0] += 30720; fa[1] += 30720; tie(fa[0]); tie(fa[1]); }
    if constexpr (G == G2) { a2b[0] = lds0 + l16; a2b[1] = lds0 + l16 + 65536; a2b[2] = lds0 + l16 + 131072; tie(a2b[0]); tie(a2b[1]); tie(a2b[2]); }
  };
  auto mma = [&](auto Gc, auto Tc) {
    constexpr int G = decltype(Gc)::value, t = decltype(Tc)::value;
    const bf16x8 a = fr[G % FR];
    if constexpr (G < GT) {
      constexpr LF f = l_frag(G);
      mfma<f.ks == 0>(acc[f.blk][t], a, xf[f.ks][t]);
    } else if constexpr (G < G2) {
      constexpr int ks = t_ks(G - GT), blk = t_blk(G - GT);
      if constexpr (blk < 2) mfma<ks == 0>(tac[blk][t], a, xf[ks][t]);
      else mfma<ks == 0>(tac[blk][t], xf[ks][t], a);
    } else if constexpr (G < late_g(0)) {
      constexpr int U = (G - G2) / 18, g = (G - G2) % 18, kk = g / 9, db = g % 9;
      mfma<(U == 0 && kk == 0)>(ac2[db][t], a, hf[U % HR][kk][t]);
    } else {
      constexpr int f = G - late_g(0), db = f / 7, kk = f % 7;
      mfma<false>(ac2[db][t], a, hl[kk][t]);
    }
  };

  auto sync = [&](auto Gc) {
    constexpr int G = decltype(Gc)::value;
    if constexpr (G < GT && G % 24 == (G < 24 ? 16 : SY1)) {
      constexpr int V = G / 24;
      constexpr int need = V + 1 < 12 ? (l_dbl(V + 1) ? (((V + 1) & 1) ? -1 : V + 2) : V + 1) : 12;
      constexpr int ng = need >= 0 ? S1.after(OP_G, need, G) : 63;
      constexpr int ng2 = V == 2 ? S1.after(OP_G, 4, G) : (V == 8 ? S1.after(OP_G, 11, G) : 63);
      constexpr int ns = V == 10 ? cmin(S1.after(OP_ST, 0, G), S1.after(OP_ST, 9, G)) : 63;
      waitv<cmin(ng, ng2, ns)>();
      if constexpr (V == 3 || V == 9) asm volatile("s_waitcnt lgkmcnt(0)" ::: "memory");
      if constexpr (V == 10) setflag(rsW, l16 == 0, flw + cgl * 4, tag);
      bar();
      if constexpr (V == 10) setflag(rsW, l16 == 0 && w == 1, (unsigned)FL_W + (unsigned)(sx * 32 + idx) * 4u, tag);
      SCHED0;
    }
    if constexpr (G == gt(0, 18)) { waitv<S1.after(OP_FL, 4, G)>(); chk_flag(IC<4>{}); SCHED0; }
    if constexpr (G == gl(2, 14)) {
      waitv<S1.after(OP_SG, 0, G)>();
      for (unsigned it = 0; it < (1u << 18); ++it) {
        tie(sgr);
        if (__all(sgr[1] == tag)) break;
        __builtin_amdgcn_s_sleep(1);
        asm volatile("global_load_dwordx2 %0, %1, %2 sc1" : "+v"(sgr) : "v"((l16 >> 1) & 248), "s"(ws + FL_S + (size_t)(rt * 256 + 32 * w) * 8) : "memory");
        waitv<0>();
      }
      tie(sgr);
      const float den = bhi(sgr[0]), rs = __builtin_amdgcn_rcpf(den);
      *(LAS f32x4*)(lds0 + LDS_ST + w * 512 + (l16 & 496)) = (f32x4){rs * -1.44269504089f, den * den, -blo(sgr[0]), rs};
      SCHED0;
    }
    if constexpr (G == gl(3, 1)) { waitv<S1.after(OP_G, 5, G)>(); bar(); SCHED0; }
    if constexpr (G == GSYC) { asm volatile("s_waitcnt lgkmcnt(0)" ::: "memory"); bar(); SCHED0; }
    if constexpr (G == gt(0, SYT)) { waitv<S1.after(OP_G, 13, G)>(); bar(); SCHED0; }
    if constexpr (G == gt(1, SYT)) { waitv<cmin(S1.after(OP_G, 14, G), S1.after(OP_FL, 0, G))>(); bar(); chk_flag(IC<0>{}); SCHED0; }
    if constexpr (G == gt(2, 2)) { waitv<S1.after(OP_ST, 1, G)>(); setflag(rsW, l16 == 0, flw + 128 + cgl * 4, tag); SCHED0; }
    if constexpr (G == gt(2, 27)) {
      waitv<cmin(S1.after(OP_A, 0, G), S1.after(OP_B, 0, G))>(); htie(IC<0>{});
      bar(); SCHED0;
    }
    if constexpr (G >= G2 && (G - G2) % 18 == SY2 && (G - G2) / 18 < 11) {
      constexpr int U = (G - G2) / 18;
      constexpr int nab = cmin(S1.after(OP_A, U + 1, G), U + 1 < 8 ? S1.after(OP_B, U + 1, G) : (U == 7 ? S1.after(OP_HL, 0, G) : 63));
      constexpr int nfl = U == 1 ? S1.after(OP_FL, 1, G) : (U == 3 ? cmin(S1.after(OP_ST, 2, G), S1.after(OP_EG, 0, G)) : (U == 5 ? S1.after(OP_FL, 2, G) : 63));
      static_assert(UFIN == 3, "");
      if constexpr (U == 0) asm volatile("s_waitcnt lgkmcnt(0)" ::: "memory");
      waitv<cmin(nab, nfl)>();
      if constexpr (U + 1 < 8) htie(IC<U + 1>{});
      if constexpr (U == 7) {
        tie(hl[0][0]); tie(hl[0][1]); tie(hl[1][0]); tie(hl[1][1]);
        for (unsigned it = 0; it < (1u << 16); ++it) {
          unsigned m = 0;
#pragma unroll
          for (int n = 4; n < 14; n++) { tie(hl[n >> 1][n & 1]); const u32x4 v = __builtin_bit_cast(u32x4, hl[n >> 1][n & 1]); asm("v_max3_u32 %0, %0, %1, %2" : "+v"(m) : "v"(v[0]), "v"(v[1])); asm("v_max3_u32 %0, %0, %1, %2" : "+v"(m) : "v"(v[2]), "v"(v[3])); }
          if (__all(m != ~0u)) break;
          __builtin_amdgcn_s_sleep(4);
          sfor<4, 14>([&](auto Nc) { constexpr int n = decltype(Nc)::value;
            gld16o_io<((n >> 1) - 4) * 1024>(hl[n >> 1][n & 1], hb0 + (size_t)(n & 1) * HTB + 16384 + 4096, l16); });
          waitv<0>();
        }
#pragma unroll
        for (int n = 4; n < 14; n++) tie(hl[n >> 1][n & 1]);
      }
      if constexpr (U == UFIN) {
        for (unsigned it = 0; it < (1u << 18); ++it) {
          unsigned d = 0;
#pragma unroll
          for (int r = 0; r < 7; r++) { tie(eg[r]); d |= eg[r][1] ^ tag; }
          if (__all(d == 0)) break;
          __builtin_amdgcn_s_sleep(1);
          sfor<0, 7>([&](auto Rc) { gld8o<decltype(Rc)::value * 384, true>(eg[decltype(Rc)::value], ws + FL_E + (size_t)(cg * 16 + rt + 1) * 384, cla2); });
          waitv<0>();
        }
#pragma unroll
        for (int r = 0; r < 7; r++) tie(eg[r]);
        crt = __uint_as_float(eg[0][0]);
        setflag(rsW, l16 == 0, flw + 256 + cgl * 4, tag);
      }
      bar();
      if constexpr (U == 1) chk_flag(IC<1>{});
      if constexpr (U == 5) chk_flag(IC<2>{});
      if constexpr (U == 0) {
        const f32x2 c = CPAIR(cs48, 10);
        c32[0] = c[0]; c32[1] = c[1];
#pragma unroll
        for (int k = 0; k < 8; k++) tw[k] = *(LAS float*)(lds0 + LDS_WTOT + (w + 1 + k) * 192 + cla);
      }
      SCHED0;
    }
  };

  unsigned wm[3];
  {
    unsigned l_ = l16; tie(l_); const int tl = (int)(l_ >> 4) + 64 * w;
    const int lane = tl & 63, q = lane >> 4, li = lane & 15;
    unsigned ep; u32x4 xr0, xr1;
    issue_at(IC<-3>{});
    bar();
    gld4_sc1(ep, ws + FL_EPOCH, l16 & 0);
    const int row = b * 8 + w;
    gld16(xr0, x + (size_t)row * DM, l16);
    gld16(xr1, x + (size_t)row * DM, 1024u + (l16 & 112));
    {
      const int chn = 8 * (9 * sx + (lane < 16 ? 2 * cgl + (lane >> 3) : 8)) + (lane & 7);
      asm volatile("global_load_ushort %0, %1, %2" : "=v"(pl[0]) : "v"(chn * 2), "s"(lre) : "memory");
      asm volatile("global_load_ushort %0, %1, %2" : "=v"(pl[1]) : "v"(chn * 2), "s"(lim) : "memory");
      asm volatile("global_load_ushort %0, %1, %2" : "=v"(pl[2]) : "v"(chn * 2), "s"(logdt) : "memory");
    }
    SCHED0;
    issue_at(IC<-1>{});
    {
      const int n = tl, r = 72 * rt + n / 6, c6 = n % 6;
      const char* p0 = (const char*)((c6 & 1) ? wu : wg) + (size_t)r * (FF * 2) + (c6 < 4 ? cg * 256 + (c6 >> 1) * 128 : fcol);
      const int m = rt * 54 + (tl - 432 < 54 ? tl - 432 : 0), rb = m / 9;
      const char* p1 = (const char*)((rb & 8) ? bim : bre) + (size_t)(8 * (9 * sx + ((rb >> 4) < 2 ? 2 * cgl + (rb >> 4) : 8)) + (rb & 7)) * (DM * 2) + (m - 9 * rb) * 128;
      u64 pc; unsigned clen;
      asm volatile("s_getpc_b64 %0\n.Lk_code_from:\n\ts_mov_b32 %1, .Lk_code_to-.Lk_code_from" : "=s"(pc), "=s"(clen));
      const char* p2 = (const char*)pc + min((unsigned)(idx * 16 + (tl & 15)) * 128u, clen);
      asm volatile("global_load_dword %0, %1, off" : "=v"(wm[0]) : "v"(tl < 432 ? p0 : (tl < 496 ? p1 : p2)) : "memory");
      asm volatile("global_load_dword %0, %1, %2" : "=v"(wm[2]) : "v"((unsigned)((8 * cgl + (lane >> 3)) * (DM * 2) + 128 + (lane & 7) * 128)), "s"(xb) : "memory");
    }
    SCHED0;
    waitv<7>();
    tie(ep); tie(xr0); tie(xr1); tie(pl[0]); tie(pl[1]); tie(pl[2]);
    tag = (unsigned)__builtin_amdgcn_readfirstlane(ep) + 1u;
    STAMP(12);
    {
      float v[16];
      const bool ex = lane < 8;
#pragma unroll
      for (int k = 0; k < 4; k++) { v[2 * k] = blo(xr0[k]); v[2 * k + 1] = bhi(xr0[k]); v[8 + 2 * k] = ex ? blo(xr1[k]) : 0.f; v[9 + 2 * k] = ex ? bhi(xr1[k]) : 0.f; }
      float sm = 0.f;
#pragma unroll
      for (int k = 0; k < 16; k++) sm += v[k];
      sm = wave_sum(sm);
      const float mean = sm * (1.0f / DM);
      float qq = 0.f;
#pragma unroll
      for (int k = 0; k < 16; k++) { const float d = (k < 8 || ex) ? v[k] - mean : 0.f; qq += d * d; }
      qq = wave_sum(qq);
      const unsigned md = pk2(mean, sqrtf(rbf(rbf(qq * (1.0f / DM)) + 1e-5f)));
      const unsigned zo = (w == 1 && lane < 48) ? (unsigned)FL_E + (unsigned)((cg * 16 + rt) * 384 + lane * 8) : 0x7ffffff0u;
      __builtin_amdgcn_raw_buffer_store_b64((u32x2){lane == 63 ? md : 0u, tag}, rsW, lane == 63 ? (unsigned)FL_S + row * 8u : zo, 0, 16);
    }
    if (w == 0 && lane < 24) {
      const float lr = bf2f((u16)pl[0]), lm = bf2f((u16)pl[1]);
      const float dt = rbf(__builtin_amdgcn_exp2f(bf2f((u16)pl[2]) * 1.44269504089f));
      const float rev = __builtin_amdgcn_fractf(dt * lm * 0.15915494309f);
      const float er = __builtin_amdgcn_exp2f(dt * lr * 1.44269504089f), ar = er * __builtin_amdgcn_cosf(rev), ai = er * __builtin_amdgcn_sinf(rev);
      const float nr = ar - 1.0f, ni = ai, dn = lr * lr + lm * lm;
      const unsigned tre = lds0 + LDS_CST + (lane >> 3) * 2048 + (lane & 7) * 8;
      auto put = [&](int k, float pr, float pi) {
        if (k >= 1 && k <= 4) { *(LAS f32x2*)(tre + k * 128) = (f32x2){2.f * pr, -2.f * pi}; *(LAS f32x2*)(tre + k * 128 + 64) = (f32x2){-2.f * pr, -2.f * pi}; }
        else { *(LAS f32x2*)(tre + k * 128) = (f32x2){pr, pi}; *(LAS f32x2*)(tre + k * 128 + 64) = (f32x2){pr, -pi}; }
      };
      put(0, ar, ai); put(5, 1.f, 0.f);
      float cr = (nr * lr + ni * lm) / dn, cm_ = (ni * lr - nr * lm) / dn;
#pragma unroll
      for (int k = 1; k < 5; k++) { put(k, cr, cm_); const float t = cr * ar - cm_ * ai; cm_ = cr * ai + cm_ * ar; cr = t; }
      const float a2r = ar * ar - ai * ai, a2i = 2.f * ar * ai;
      const float a4r = a2r * a2r - a2i * a2i, a4i = 2.f * a2r * a2i;
      float pr = a4r * a4r - a4i * a4i, pi = 2.f * a4r * a4i;
      put(6, a4r, a4i); put(7, pr, pi); put(8, pr * a4r - pi * a4i, pr * a4i + pi * a4r);
#pragma unroll
      for (int k = 9; k < 14; k++) { const float t = pr * pr - pi * pi; pi = 2.f * pr * pi; pr = t; put(k, pr, pi); }
    }
    SCHED0;
    STAMP(13);
  }
  auto pack = [&]() {
    unsigned l_ = l16; tie(l_); const int tl = (int)(l_ >> 4) + 64 * w;
    const int lane = tl & 63, q = lane >> 4, li = lane & 15;
        const unsigned ra = lds0 + LDS_PK + (w < 6 ? w : 0) * 4096 + (4 * q + (li >> 2)) * 128 + (lane & 3) * 8;
    const int fa_ = pj < 16 ? 1 : 7, fb_ = (pj < 16 ? 9 * pj : 128 + pj) + sx * (KS2 * 9);
    const unsigned vo = widx < 162 ? l_ : 0x70000000u;
#pragma unroll
    for (int n = 0; n < 4; n++) {
      const u32x4 f = __builtin_bit_cast(u32x4, trfrag<2048>(ra + n * 32));
      const int d16 = 4 * pln + n, dt = (d16 * 57) >> 9, db = d16 - 9 * dt;
      st16_sc1(rsW, vo, (unsigned)WS_WT + (unsigned)(dt * (NKG * 9) + fb_ + fa_ * db) * 1024u, f);
    }
    asm volatile("s_waitcnt lgkmcnt(0)" ::: "memory");
  };
  auto pack_c = [&]() {
    unsigned l_ = l16; tie(l_); tie(cfr);
    const int dt = (cd16 * 57) >> 9, db = cd16 - 9 * dt;
    if (2 * cjj + (l_ >> 9) >= 9) cfr = (u32x4){0, 0, 0, 0};
    st16_sc1(rsW, widx < 180 ? l_ : 0x70000000u, (unsigned)WS_WT + (unsigned)((dt * NKG + sx * KS2) * 9 + 146 + cjj + 7 * db) * 1024u, cfr);
  };
  waitv<S1.after(OP_G, 0, 0)>(); xconv(IC<0>{}); xconv(IC<1>{});
  STAMP(14);
  bar();
  SCHED0;
  STAMP(1);
  frag(IC<0>{}, IC<0>{}); frag(IC<0>{}, IC<1>{}); frag(IC<1>{}, IC<0>{}); frag(IC<1>{}, IC<1>{});
  SCHED0;

  bf16x8 cpb[2];
  unsigned csp = 0, csq = 0, csr = 0;
  auto extra = [&](auto Gc) {
    constexpr int G = decltype(Gc)::value;
    if constexpr (xt_at(G) >= 0) { waitv<S1.after(OP_X, xt_at(G), G + 1)>(); xconv(IC<xt_at(G)>{}); SCHED0; }
    if constexpr (G == csx_g(0, 0) - 3 || G == csx_g(6, 0) - 3) {
      constexpr int cur = p1_off(l_unit0(G + PD));
      csp = (ab[0] ^ (unsigned)((w & 3) << 5)) + (unsigned)((w >> 2) * 4096 - cur); tie(csp);
      const u32x4 so = {0x3f803f80u, 0x3f803f80u, 0x3f803f80u, 0x3f803f80u};
      selA = __builtin_bit_cast(bf16x8, so); tie(selA);
      SCHED0;
    }
    if constexpr (G == csx_g(12, 0) - 3) {
      unsigned l_ = l16; tie(l_); const int tl = (int)(l_ >> 4) + 64 * w;
      const int lane = tl & 63, q = lane >> 4, li = lane & 15;
      const u32x4 so = {0x3f803f80u, 0x3f803f80u, 0x3f803f80u, 0x3f803f80u}, sz = {0, 0, 0, 0};
      selA = __builtin_bit_cast(bf16x8, so); selB = __builtin_bit_cast(bf16x8, w < 6 ? so : sz); tie(selA); tie(selB);
      csp = lds0 + p1_off(12) + 12288 + (w < 6 ? w : 0) * 1024 + (li & 7) * 128 + (2 * q + (li >> 3)) * 16;
      const unsigned f0 = lds0 + p1_off(12) + (unsigned)((((8 * q + (li >> 2)) ^ ((q & 1) << 2)) << 5) + 8 * (lane & 3));
      csq = f0 + w * 1024; csr = f0 + (w < 6 ? (w >> 1) * 30720 : 0) + (8 + (w & 1)) * 1024;
      fa[0] = f0; fa[1] = f0 ^ 128u;
      tie(csp); tie(csq); tie(csr); tie(fa[0]); tie(fa[1]);
      SCHED0;
    }
    if constexpr (csx_at(G) >= 0) {
      constexpr int v = csx_at(G) >> 2, n = csx_at(G) & 3;
      const bf16x8 cpc = cpb[G & 1];
      if constexpr (v < 12) mfma<(v % 6 == 0 && n == 0)>(csa0, cpc, selA);
      else if constexpr (v < 15) mfma<(v == 12 && n == 0)>(csa0, selA, cpc);
      else mfma<(v == 15 && n == 0)>(csa1, cpc, v == 16 ? selB : selA);
      SCHED0;
    }
    if constexpr (csx_at(G + 2) >= 0) {
      constexpr int v = csx_at(G + 2) >> 2, n = csx_at(G + 2) & 3;
      bf16x8& cpc = cpb[G & 1];
      if constexpr (v < 12) { unsigned a = csp + p1_off(v); cpc = trfrag<512>(a + n * 8192); }
      else if constexpr (v < 15) { unsigned a = csp + (v - 12) * 30720; cpc = ldsfrag(a + n * 6144); }
      else {
        const unsigned o = v == 15 ? csq + n * 30720 : csr + n * 2048;
        s16x4 a = __builtin_amdgcn_ds_read_tr16_b64_v4i16((LAS s16x4*)(o));
        s16x4 c = __builtin_amdgcn_ds_read_tr16_b64_v4i16((LAS s16x4*)(o ^ 128u));
        cpc = __builtin_bit_cast(bf16x8, __builtin_shufflevector(a, c, 0, 1, 2, 3, 4, 5, 6, 7));
      }
      SCHED0;
    }
  };
  auto cs_flush = [&](auto Gc) {
    constexpr int G = decltype(Gc)::value;
    if constexpr (G == CSF[0] || G == CSF[1]) {
      unsigned lz = l16; tie(lz);
      if ((lz & 240) == 0) *(LAS f32x4*)(lds0 + LDS_CS + (G == CSF[1] ? 512 : 0) + w * 64 + (lz >> 8) * 16) = csa0;
      SCHED0;
    }
    if constexpr (G == CSF[2]) {
      unsigned lz = l16; tie(lz); tie(csa0);
      const float rsum = csa0[0] + dpp_ror8(csa0[0]);
      if (lz < 128) *(LAS float*)(lds0 + LDS_CS + 1024 + w * 32 + (lz >> 2)) = rsum;
      if ((lz & 240) == 0) *(LAS f32x4*)(lds0 + LDS_CS + 1280 + w * 64 + (lz >> 8) * 16) = csa1;
      SCHED0;
    }
  };

  auto group = [&](auto Gc) {
    constexpr int G = decltype(Gc)::value;
    if constexpr (G == 144) { STAMP(2); }
    if constexpr (G == GT) { STAMP(3); }
    if constexpr (G == G2) { STAMP(4); STAMP(5); }
    if constexpr (G == g2(3, 0)) { STAMP(6); }
    if constexpr (G == g2(8, 0)) { STAMP(7); }
    if constexpr (G == GPK) { waitv<S1.after(OP_PK, 0, G)>(); pack(); SCHED0; }
    if constexpr (G == GPC) { waitv<S1.after(OP_PK, 1, G)>(); pack_c(); SCHED0; }
    if constexpr (G == gl(1, 4)) { tie(wm[0]); tie(wm[2]); }
    cs_flush(Gc);
    sync(Gc);
    issue_at(IC<2 * G>{});
    extra(Gc);
    mma(Gc, IC<0>{}); SCHED0;
    if constexpr (G + PD < GEND) { addr_step(IC<G + PD>{}); frag(IC<G + PD>{}, IC<0>{}); SCHED0; }
    side(IC<2 * G>{}); SCHED0;
    if constexpr (G + PD < GEND) { frag(IC<G + PD>{}, IC<1>{}); SCHED0; }
    mma(Gc, IC<1>{}); SCHED0;
    side(IC<2 * G + 1>{}); SCHED0;
  };
  sfor<0, 13>([&](auto Bc) { sfor<0, 45>([&](auto Jc) { group(IC<decltype(Bc)::value * 45 + decltype(Jc)::value>{}); }); });
  STAMPC(27);
  STAMP(8);

  asm volatile("s_nop 7\n\ts_nop 1" : "+v"(ac2[8][0]), "+v"(ac2[8][1]));
  unsigned l_ = l16; tie(l_); const int tz = (int)(l_ >> 4) + 64 * w;
  const int lane = tz & 63, q = lane >> 4, li = lane & 15;
  st16_sc1(rsW, l16, pb + 8 * 1024, (u32x4){pk2(ac2[8][0][0], ac2[8][0][1]), pk2(ac2[8][0][2], ac2[8][0][3]), pk2(ac2[8][1][0], ac2[8][1][1]), pk2(ac2[8][1][2], ac2[8][1][3])});
  SCHED0;
  const char* ib = ws + (size_t)pobase + (size_t)w * 9216 + 4096;
  waitv<0>();
  setflag(rsW, l16 == 0, (unsigned)FL_P + (unsigned)(((rt * 4 + cgl) * 8 + w) * 8 + sx) * 64u, tag);
  bar();
  STAMP(9);
  {
    constexpr int LROW = 304, LSRC = 32 * LROW;
    spin_flag(ws + FL_P + (size_t)(((rt * 4 + cgl) * 8 + sx) * 8 + w) * 64, l16 & 0, tag);
    sfor<0, 8>([&](auto Dc) { gld16o_sc1<(decltype(Dc)::value - 4) * 1024>(pv[decltype(Dc)::value], ib, l16); });
    gld16o_sc1<0>(pv[8], ib + 4096, l16);
    waitv<0>();
#pragma unroll
    for (int db = 0; db < 9; db++) {
      tie(pv[db]);
      *(LAS u32x2*)(lds0 + w * LSRC + li * LROW + db * 32 + q * 8) = (u32x2){pv[db][0], pv[db][1]};
      *(LAS u32x2*)(lds0 + w * LSRC + (16 + li) * LROW + db * 32 + q * 8) = (u32x2){pv[db][2], pv[db][3]};
    }
    asm volatile("s_waitcnt vmcnt(0) lgkmcnt(0)" ::: "memory");
    bar();
    STAMP(10);
#pragma unroll
    for (int r = 0; r < 3; r++) {
      tie(epv[2 * r]); tie(epv[2 * r + 1]);
      const int e = tz + 512 * r;
      if (e < 1152) {
        const unsigned t = ept[r], d8 = epd[r];
        f32x4 sum = zero4;
#pragma unroll
        for (int sr = 0; sr < 8; sr++) {
          const u32x2 p = *(LAS u32x2*)(lds0 + sr * LSRC + t * LROW + d8);
          sum += (f32x4){blo(p[0]), bhi(p[0]), blo(p[1]), bhi(p[1])};
        }
        const f32x4 st = *(LAS f32x4*)(lds0 + LDS_ST + sx * 512 + t * 16);
        f32x4 o;
#pragma unroll
        for (int j = 0; j < 4; j++) {
          const float xe = (j & 1) ? bhi(epv[2 * r][j >> 1]) : blo(epv[2 * r][j >> 1]);
          const float de = (j & 1) ? bhi(epv[2 * r + 1][j >> 1]) : blo(epv[2 * r + 1][j >> 1]);
          const float xn = rbf(rbf(xe + st[2]) * st[3]);
          o[j] = xe + rbf(xn * de) + sum[j];
        }
        *(f32x4*)((char*)(out + (size_t)erow * DM + cgl * 144) + t * (DM * 4) + d8 * 2) = o;
      }
    }
    asm volatile(".Lk_code_to:");
    if (b == 0 && w == 0) {
#pragma unroll
      for (int k = 0; k < 4; k++) spin_flag(ws + FL_W + k * 256, l16 >> 2, tag);
      setflag(rsW, l16 == 0, (unsigned)FL_EPOCH, tag);
    }
    STAMP(11);
    STAMP_DUMP();
  }
}

extern "C" void kernel_launch(void* const* d_in, const int* in_sizes, int n_in, void* d_out, int out_size, void* d_ws, size_t ws_size,
                              hipStream_t stream) {
  const u16* x = (const u16*)d_in[0]; const u16* lre = (const u16*)d_in[1]; const u16* lim = (const u16*)d_in[2];
  const u16* bre = (const u16*)d_in[3]; const u16* bim = (const u16*)d_in[4]; const u16* cre = (const u16*)d_in[5]; const u16* cim = (const u16*)d_in[6];
  const u16* logdt = (const u16*)d_in[7]; const u16* dsk = (const u16*)d_in[8];
  const u16* wg = (const u16*)d_in[10]; const u16* wu = (const u16*)d_in[11]; const u16* wd = (const u16*)d_in[12];
  char* ws = (char*)d_ws;
  (void)hipFuncSetAttribute((const void*)k_main, hipFuncAttributeMaxDynamicSharedMemorySize, LDS_TOT);
  k_main<<<256, 512, LDS_TOT, stream>>>(x, lre, lim, bre, bim, cre, cim, logdt, dsk, wg, wu, wd, ws, (float*)d_out);
}
```

```cpp
#include <hip/hip_runtime.h>
#include <stdint.h>
#include <type_traits>

typedef unsigned short u16;
typedef unsigned long long u64;
typedef __bf16 bf16x8 __attribute__((ext_vector_type(8)));
typedef __bf16 bf16x2 __attribute__((ext_vector_type(2)));
typedef float f32x4 __attribute__((ext_vector_type(4)));
typedef float f32x2 __attribute__((ext_vector_type(2)));
typedef short s16x4 __attribute__((ext_vector_type(4)));
typedef unsigned u32x4 __attribute__((ext_vector_type(4)));
typedef unsigned u32x2 __attribute__((ext_vector_type(2)));
#define LAS __attribute__((address_space(3)))
#define GAS __attribute__((address_space(1)))
#define DI __device__ __forceinline__

constexpr int SEQ = 2048, DM = 576, FF = 4608;
constexpr int KS2 = 23;
constexpr int NKG = 8 * KS2;
constexpr size_t FL_H = 0;
constexpr size_t FL_P = 40960;
constexpr size_t FL_W = 36864;
constexpr size_t FL_EPOCH = 38912;
constexpr size_t FL_E = 262144;
constexpr size_t FL_S = 180224;
constexpr size_t WS_HT = 524288;
constexpr size_t WS_WT = WS_HT + (size_t)128 * NKG * 1024;
constexpr size_t WS_PART = WS_WT + (size_t)4 * NKG * 9 * 1024;
constexpr size_t PART_BYTES = (size_t)8 * 4 * 8 * 8 * 9 * 1024;
constexpr size_t WS_END = WS_PART + PART_BYTES;

DI float bf2f(u16 v) { return __uint_as_float(((unsigned)v) << 16); }
DI float rbf(float f) { return (float)(__bf16)f; }
DI unsigned pk2(float lo, float hi) { f32x2 v = {lo, hi}; bf16x2 c = __builtin_convertvector(v, bf16x2); return __builtin_bit_cast(unsigned, c); }
DI float blo(unsigned u) { return __uint_as_float(u << 16); }
DI float bhi(unsigned u) { return __uint_as_float(u & 0xffff0000u); }

__host__ __device__ inline int wt_frag(int j, int db) { return j < 16 ? j * 9 + db : 144 + db * 7 + (j - 16); }
DI int kg_f0b(int s, int j) {
  const int a = 512 * s + 128 * ((j & 7) >> 1) + 64 * (j >> 3) + 32 * (j & 1), c = 4096 + 64 * s + 32 * (j & 1);
  return j < 16 ? a : c;
}
__host__ __device__ inline int kg_f0(int s, int j) {
  if (j < 16) { int p = j >> 3, cg = 4 * s + ((j & 7) >> 1); return 64 * (2 * cg + p) + 32 * (j & 1); }
  return 4096 + 32 * (2 * s + (j - 16));
}

template <int N> using IC = std::integral_constant<int, N>;
template <int B, int E, class F> DI void sfor(F&& f) { if constexpr (B < E) { f(IC<B>{}); sfor<B + 1, E>(f); } }

DI void glds_sc1(const void* sbase, unsigned voff, unsigned ldsaddr) {
  asm volatile("s_mov_b32 m0, %2\n\ts_nop 0\n\tglobal_load_lds_dwordx4 %0, %1 sc1" :: "v"(voff), "s"(sbase), "s"(ldsaddr) : "memory");
}
DI void gld4(unsigned& dst, const void* sbase, unsigned voff) {
  asm volatile("global_load_dword %0, %1, %2" : "=v"(dst) : "v"(voff), "s"(sbase) : "memory");
}
template <int C, int L, bool SC1> DI void glds_c(const void* sbase, unsigned voff, unsigned ldsw) {
  unsigned t;
  if constexpr (SC1) asm volatile("s_add_u32 m0, %3, %5\n\tv_add_u32 %0, %4, %1\n\tglobal_load_lds_dwordx4 %0, %2 sc1" : "=&v"(t) : "v"(voff), "s"(sbase), "s"(ldsw), "i"(C), "i"(L) : "memory", "scc");
  else asm volatile("s_add_u32 m0, %3, %5\n\tv_add_u32 %0, %4, %1\n\tglobal_load_lds_dwordx4 %0, %2" : "=&v"(t) : "v"(voff), "s"(sbase), "s"(ldsw), "i"(C), "i"(L) : "memory", "scc");
}
template <int OFF, class T> DI void gld16o_sc1(T& dst, const void* sbase, unsigned voff) {
  asm volatile("global_load_dwordx4 %0, %1, %2 offset:%3 sc1" : "=v"(dst) : "v"(voff), "s"(sbase), "i"(OFF) : "memory");
}
template <int OFF, class T> DI void gld16o_io(T& dst, const void* sbase, unsigned voff) {
  asm volatile("global_load_dwordx4 %0, %1, %2 offset:%3 sc1" : "+v"(dst) : "v"(voff), "s"(sbase), "i"(OFF) : "memory");
}
template <int OFF, bool IO> DI void gld8o(u32x2& dst, const void* sbase, unsigned voff) {
  if constexpr (IO) asm volatile("global_load_dwordx2 %0, %1, %2 offset:%3 sc1" : "+v"(dst) : "v"(voff), "s"(sbase), "i"(OFF) : "memory");
  else asm volatile("global_load_dwordx2 %0, %1, %2 offset:%3 sc1" : "=v"(dst) : "v"(voff), "s"(sbase), "i"(OFF) : "memory");
}
template <int L> DI void glds_run(unsigned& voff, const void* sbase, unsigned step, unsigned ldsa) {
  asm volatile("s_add_u32 m0, %3, %4\n\ts_nop 0\n\tglobal_load_lds_dwordx4 %0, %1\n\tv_add_u32 %0, %2, %0" : "+v"(voff) : "s"(sbase), "s"(step), "s"(ldsa), "i"(L) : "memory", "scc");
}
DI void glds_s(const void* sbase, unsigned voff, unsigned ldsaddr) {
  asm volatile("s_mov_b32 m0, %2\n\ts_nop 0\n\tglobal_load_lds_dwordx4 %0, %1" :: "v"(voff), "s"(sbase), "s"(ldsaddr) : "memory");
}
template <class T> DI void gld16(T& dst, const void* sbase, unsigned voff) {
  asm volatile("global_load_dwordx4 %0, %1, %2" : "=v"(dst) : "v"(voff), "s"(sbase) : "memory");
}
template <class T> DI void gld16_sc1(T& dst, const void* sbase, unsigned voff) {
  asm volatile("global_load_dwordx4 %0, %1, %2 sc1" : "=v"(dst) : "v"(voff), "s"(sbase) : "memory");
}
template <class T> DI void gld16_sc1_io(T& dst, const void* sbase, unsigned voff) {
  asm volatile("global_load_dwordx4 %0, %1, %2 sc1" : "+v"(dst) : "v"(voff), "s"(sbase) : "memory");
}
DI void gld4_sc1(unsigned& dst, const void* sbase, unsigned voff) {
  asm volatile("global_load_dword %0, %1, %2 sc1" : "=v"(dst) : "v"(voff), "s"(sbase) : "memory");
}
DI void gld8_sc1(u32x2& dst, const void* sbase, unsigned voff) {
  asm volatile("global_load_dwordx2 %0, %1, %2 sc1" : "=v"(dst) : "v"(voff), "s"(sbase) : "memory");
}
DI void gld8(u32x2& dst, const void* sbase, unsigned voff) {
  asm volatile("global_load_dwordx2 %0, %1, %2" : "=v"(dst) : "v"(voff), "s"(sbase) : "memory");
}
template <int N> DI void waitv() { static_assert(N >= 0 && N < 64, ""); asm volatile("s_waitcnt vmcnt(%0)" ::"i"(N) : "memory"); }
#define SCHED0 __builtin_amdgcn_sched_barrier(0)
DI void bar() { asm volatile("" ::: "memory"); __builtin_amdgcn_s_barrier(); asm volatile("" ::: "memory"); }
template <class T> DI void tie(T& a) { asm volatile("" : "+v"(a)); }
template <int O2> DI bf16x8 trfrag(unsigned addr) {
  s16x4 a = __builtin_amdgcn_ds_read_tr16_b64_v4i16((LAS s16x4*)(addr));
  s16x4 b = __builtin_amdgcn_ds_read_tr16_b64_v4i16((LAS s16x4*)(addr + O2));
  return __builtin_bit_cast(bf16x8, __builtin_shufflevector(a, b, 0, 1, 2, 3, 4, 5, 6, 7));
}
DI bf16x8 ldsfrag(unsigned addr) { return *(LAS bf16x8*)(addr); }
template <bool FIRST> DI void mfma(f32x4& c, bf16x8 a, bf16x8 b) {
  if constexpr (FIRST) asm volatile("v_mfma_f32_16x16x32_bf16 %0, %1, %2, 0" : "=v"(c) : "v"(a), "v"(b));
  else asm volatile("v_mfma_f32_16x16x32_bf16 %0, %1, %2, %0" : "+v"(c) : "v"(a), "v"(b));
}
DI float silu_mul(float g, float u) {
  const float e = __builtin_amdgcn_exp2f(g * -1.44269504089f);
  return g * u * __builtin_amdgcn_rcpf(1.0f + e);
}
DI unsigned silu_mul2(f32x2 g, f32x2 u) {
  const f32x2 t = g * -1.44269504089f;
  f32x2 e; e.x = __builtin_amdgcn_exp2f(t.x); e.y = __builtin_amdgcn_exp2f(t.y);
  const f32x2 d = e + 1.0f;
  f32x2 r; r.x = __builtin_amdgcn_rcpf(d.x); r.y = __builtin_amdgcn_rcpf(d.y);
  const f32x2 h = (g * u) * r;
  return pk2(h.x, h.y);
}
DI float dpp_ror8(float v) { return __int_as_float(__builtin_amdgcn_update_dpp(0, __float_as_int(v), 0x128, 0xf, 0xf, true)); }
DI void cmac(float& d, float P, float Q, float x) {
  asm volatile("v_fmac_f32 %0, %1, %3\n\tv_fmac_f32_dpp %0, %3, %2 row_ror:8 row_mask:0xf bank_mask:0xf" : "+v"(d) : "v"(P), "v"(Q), "v"(x));
}
DI void cmul(float& d, float P, float Q, float x) {
  asm volatile("v_mul_f32 %0, %1, %3\n\tv_fmac_f32_dpp %0, %3, %2 row_ror:8 row_mask:0xf bank_mask:0xf" : "=&v"(d) : "v"(P), "v"(Q), "v"(x));
}
DI void cfma(float& d, float P, float Q, float x, float c) {
  asm volatile("v_fma_f32 %0, %1, %3, %4\n\tv_fmac_f32_dpp %0, %3, %2 row_ror:8 row_mask:0xf bank_mask:0xf" : "=&v"(d) : "v"(P), "v"(Q), "v"(x), "v"(c));
}
DI void v_mul(float& d, float a, float b) { asm volatile("v_mul_f32 %0, %1, %2" : "=v"(d) : "v"(a), "v"(b)); }
DI void v_mulc(float& d, float a) { asm volatile("v_mul_f32 %0, 0xbfb8aa3b, %1" : "=v"(d) : "v"(a)); }
DI void v_fmac(float& d, float a, float b) { asm volatile("v_fmac_f32 %0, %1, %2" : "+v"(d) : "v"(a), "v"(b)); }
DI void v_fmaa(float& d, float c) { asm volatile("v_fma_f32 %0, %0, %1, %1" : "+v"(d) : "v"(c)); }
DI void v_muli(float& d, float a) { asm volatile("v_mul_f32 %0, %0, %1" : "+v"(d) : "v"(a)); }
DI void lds_add(unsigned addr, float v) { asm volatile("ds_add_f32 %0, %1" :: "v"(addr), "v"(v) : "memory"); }
DI void v_exp2(float& d) { asm volatile("v_exp_f32 %0, %0" : "+v"(d)); }
DI void v_add1(float& d) { asm volatile("v_add_f32 %0, 1.0, %0" : "+v"(d)); }
DI void v_rcp(float& d) { asm volatile("v_rcp_f32 %0, %0" : "+v"(d)); }
DI void v_cvt(unsigned& d, float a, float b) { asm volatile("v_cvt_pk_bf16_f32 %0, %1, %2" : "=v"(d) : "v"(a), "v"(b)); }
DI float wave_sum(float v) {
#define DPPADD(c) v += __int_as_float(__builtin_amdgcn_update_dpp(0, __float_as_int(v), c, 0xf, 0xf, true))
  DPPADD(0x111); DPPADD(0x112); DPPADD(0x114); DPPADD(0x118);
  v += __int_as_float(__builtin_amdgcn_update_dpp(0, __float_as_int(v), 0x142, 0xa, 0xf, false));
  v += __int_as_float(__builtin_amdgcn_update_dpp(0, __float_as_int(v), 0x143, 0xc, 0xf, false));
  return __int_as_float(__builtin_amdgcn_readlane(__float_as_int(v), 63));
}
DI void dnop() { asm volatile("s_nop 1"); }
typedef __amdgpu_buffer_rsrc_t rsrc_t;
DI rsrc_t mkrsrc(const void* p, unsigned bytes) { return __builtin_amdgcn_make_buffer_rsrc((void*)p, 0, bytes, 0x00020000); }
DI unsigned vadd_s(unsigned s, unsigned v) { unsigned o; asm volatile("v_add_u32 %0, %1, %2" : "=v"(o) : "s"(s), "v"(v)); return o; }
DI void st8_sc1(rsrc_t r, unsigned voff, unsigned soff, u32x2 v) { __builtin_amdgcn_raw_buffer_store_b64(v, r, vadd_s(soff, voff), 0, 16); }
DI void st16_sc1(rsrc_t r, unsigned voff, unsigned soff, u32x4 v) { __builtin_amdgcn_raw_buffer_store_b128(v, r, vadd_s(soff, voff), 0, 16); }
DI u32x4 ones4() { u32x4 v; asm volatile("v_mov_b32 %0, -1\n\tv_mov_b32 %1, -1\n\tv_mov_b32 %2, -1\n\tv_mov_b32 %3, -1" : "=v"(v[0]), "=v"(v[1]), "=v"(v[2]), "=v"(v[3])); return v; }
DI void setflag(rsrc_t r, bool sel, unsigned off, unsigned tag) { __builtin_amdgcn_raw_buffer_store_b32(tag, r, sel ? off : 0x7ffffff0u, 0, 16); }
DI void spin_flag(const char* base, unsigned voff, unsigned tag) {
  for (unsigned it = 0; it < (1u << 18); ++it) {
    unsigned v = __hip_atomic_load((GAS unsigned*)(base + voff), __ATOMIC_RELAXED, __HIP_MEMORY_SCOPE_AGENT);
    if (__all(v == tag)) break;
    __builtin_amdgcn_s_sleep(1);
  }
  asm volatile("" ::: "memory");
}

constexpr int PD = 2, FR = PD + 1;
constexpr int SLOT1 = 24576;
constexpr int SLOT2 = 18432;
constexpr int LDS_TOT = 163840;
constexpr int LDS_SCR = 90112;
constexpr int LDS_PK = 122880;
constexpr int LDS_XS = 98304;
constexpr int LDS_ST = 147456;
constexpr int LDS_CS = 151552;
constexpr int LDS_CST = 153600;
constexpr int LDS_WTOT = LDS_CST + 6144;

#define STAMP(k)
#define STAMPC(k)
#define STAMP_DUMP()

constexpr int p1_off(int v) { return v < 8 ? (v % 4) * SLOT1 : (v < 10 ? (v - 4) * SLOT1 : (v < 12 ? (v - 10) * SLOT1 : 2 * SLOT1 + (v - 12) * 30720)); }
constexpr int p2_off(int u, int g) {
  const int k = u % 5;
  return k < 2 ? k * SLOT2 + g * 1024 : k == 2 ? (g < 12 ? 36864 + g * 1024 : 141312 + (g - 12) * 1024) : 49152 + (k - 3) * SLOT2 + g * 1024;
}

constexpr int GT = 288, G2 = 378, GEND = 585;
constexpr int gl(int v, int g) { return v * 24 + g; }
constexpr int gt(int u, int g) { return GT + u * 30 + g; }
constexpr int g2(int u, int g) { return G2 + u * 18 + g; }
constexpr int SY1 = 8, SYT = 2, SY2 = 4;
constexpr int late_g(int f) { return g2(8, f); }
constexpr int PLAG = 4;

constexpr int SCE = 2 * g2(0, SY2) + 3;
constexpr int SQC = 2 * (G2 + 13);
constexpr int SILU0[3] = {2 * (gl(4, 0) + 12), 2 * (gl(10, 0) + 12), SQC + 8};
constexpr int SILUN[3] = {32, 32, 8};
constexpr int silu_end(int s, int nel) { return SILU0[s] + 9 * (nel / 2) + 1; }
constexpr int S1R = 2 * (GT + 55);
constexpr int S1A = S1R + 12;
constexpr int S1SEL = 2 * g2(2, 0) + 2;
constexpr int SCM = S1SEL + 6;
constexpr int UFIN = 3;
constexpr int SFIN = 2 * g2(UFIN, SY2);
static_assert(S1A + 63 < 2 * g2(0, SY2) && silu_end(2, 8) < S1SEL && SCM + 24 < SFIN - 8, "");
constexpr int csx_n(int v) { return v == 16 ? 2 : 3; }
constexpr int csx_g(int v, int n) {
  const int base[17] = {gl(0, 4), gl(0, 19), gl(1, 12), gl(2, 11), gl(2, 16), gl(3, 3), gl(7, 16), gl(7, 19), gl(8, 1), gl(8, 12), gl(8, 18), gl(9, 0),
                        GT + 27, GT + 30, GT + 34, GT + 37, GT + 40};
  const int step = (v == 5 || v == 6 || v == 7 || v >= 12) ? 1 : 2;
  return base[v] + n * step;
}
constexpr int CSF[3] = {gl(3, SY1), gl(9, SY1), GT + 44};
constexpr int GPC = gl(8, 23);
constexpr int GPK = gl(6, 6);
constexpr int GSYC = GT + 46;
constexpr int xt_g(int h) {
  const int g[18] = {-1, -1, gl(0, 6), gl(0, 12), gl(0, 20), gl(1, 3), gl(1, 11), gl(1, 18), gl(2, 2), gl(2, 10), gl(2, 17), gl(3, 1), gl(3, 9), gl(3, 14), gl(3, 19), gl(4, 0), gl(4, 3), gl(4, 5)};
  return g[h];
}
constexpr int xd_g(int h) { return h < 3 ? -1 : (h < 5 ? h - 2 : xt_g(h - 3) + 3); }

constexpr int csx_at(int G) { for (int v = 0; v < 17; v++) for (int n = 0; n < csx_n(v); n++) if (csx_g(v, n) == G) return v * 4 + n; return -1; }
constexpr int xt_at(int G) { for (int c = 2; c < 18; c++) if (xt_g(c) == G) return c; return -1; }
constexpr int silu_at(int sl) { for (int k = 0; k < 3; k++) if (sl >= SILU0[k] && sl - SILU0[k] <= 9 * (SILUN[k] / 2) + 1) return k; return -1; }

enum { OP_G = 0, OP_X, OP_ST, OP_PO, OP_PH, OP_A, OP_B, OP_FL, OP_EP, OP_HL, OP_PL, OP_EG, OP_SG, OP_PK };
struct Op { int pos, kind, a, n; };
struct Sched {
  Op op[320]; int cnt;
  constexpr void add(int pos, int kind, int a, int n) {
    int i = cnt; while (i > 0 && op[i - 1].pos > pos) { op[i] = op[i - 1]; i--; }
    op[i] = Op{pos, kind, a, n}; cnt++;
  }
  constexpr void unit(int g, int v, int step) { for (int n = 0; n < (v < 12 ? 3 : 4); n++) add(2 * (g + n * step), OP_G, v, n); }
  constexpr Sched() : op{}, cnt(0) {
    for (int h = 0; h < 2; h++) for (int n = 0; n < 2; n++) add(-3, OP_X, h, n);
    for (int n = 0; n < 3; n++) add(-3, OP_G, 0, n);
    for (int n = 0; n < 6; n++) add(-2, OP_ST, 8, n);
    for (int n = 0; n < 2; n++) add(-1, OP_X, 2, n);
    for (int n = 0; n < 3; n++) add(-1, OP_G, 1, n);
    for (int n = 0; n < 1; n++) add(-1, OP_ST, 7, n);
    add(-1, OP_ST, 6, 0);
    for (int h = 3; h < 18; h++) for (int n = 0; n < 2; n++) add(2 * xd_g(h), OP_X, h, n);
    for (int n = 0; n < 4; n++) add(2 * gl(4, 10 + 3 * n), OP_PK, 0, n);
    add(2 * gl(7, 15), OP_PK, 1, 0);
    add(2 * GPC - 1, OP_ST, 9, 0);
    unit(gl(0, 4), 2, 1);
    for (int n = 0; n < 4; n++) add(2 * GPK - 1, OP_ST, 4, n);
    add(2 * gl(1, 5), OP_SG, 0, 0);
    unit(gl(0, 9), 3, 3); unit(gl(1, 9), 4, 3); unit(gl(2, 9), 5, 1); unit(gl(3, 10), 6, 3); unit(gl(4, 9), 7, 3);
    unit(gl(5, 10), 8, 4); unit(gl(6, 9), 9, 1); unit(gl(6, 13), 10, 2); unit(gl(6, 19), 11, 2); unit(gl(8, 10), 12, 3); unit(gl(9, 10), 13, 3); unit(gl(10, 10), 14, 3);
    for (int n = 0; n < 3; n++) add(2 * gl(4, 1 + 2 * n), OP_PH, 0, n);
    for (int s = 0; s < 2; s++) for (int h = 0; h < 2; h++) for (int t = 0; t < 2; t++) add((silu_end(s, 16 * (h + 1)) / 2) * 2 + 1, OP_ST, s, 2 * h + t);
    for (int t = 0; t < 2; t++) add((silu_end(2, 8) / 2) * 2 + 1, OP_ST, 2, t);
    add(2 * gl(11, 16), OP_FL, 4, 0);
    add(2 * gt(0, 6), OP_FL, 0, 0);
    for (int n = 0; n < 2; n++) { add(2 * gt(0, 20 + 4 * n), OP_A, 0, n); add(2 * gt(1, 8 + 4 * n), OP_A, 1, n); add(2 * gt(1, 16 + 4 * n), OP_A, 2, n); }
    for (int n = 0; n < 4; n++) { add(2 * gt(2, 3 + 2 * n), OP_B, 0, n); add(2 * gt(2, 11 + 2 * n), OP_B, 1, n); add(2 * gt(2, 19 + 3 * n), OP_B, 2, n); }
    add(2 * g2(0, 1), OP_FL, 1, 0);
    for (int u = 0; u < 9; u++) {
      const int U = u + 3;
      for (int n = 0; n < (U < 11 ? 2 : 1); n++) add(2 * g2(u, 5 + 2 * n), OP_A, U, n);
      if (U < 8) for (int n = 0; n < 4; n++) add(2 * g2(u, 9 + 2 * n), OP_B, U, n);
    }
    for (int n = 0; n < 7; n++) add(2 * g2(UFIN - 1, 10) + 0, OP_EG, 0, n);
    add(2 * g2(4, 6), OP_FL, 2, 0);
    for (int n = 0; n < 4; n++) add(2 * g2(5, 9 + 2 * n), OP_HL, 0, n);
    for (int n = 4; n < 14; n++) add(2 * g2(6, 8 + (n - 4)), OP_HL, 0, n);
    for (int n = 0; n < 6; n++) add(2 * g2(8, 6 + 2 * n), OP_EP, 0, n);
    for (int db = 0; db < 8; db++) add(2 * (late_g(7 * db + 6) + 3) + 1, OP_ST, 3, db);
  }
  static constexpr bool droppable(const Op& o) { return (o.kind == OP_PH && o.n == 2) || (o.kind == OP_ST && (o.a == 4 || o.a == 9)) || (o.kind == OP_PK && o.a == 0); }
  constexpr int first_at(int pos) const { int i = 0; while (i < cnt && op[i].pos < pos) i++; return i; }
  constexpr int after(int kind, int a, int g) const {
    int last = -1;
    for (int i = 0; i < cnt; i++) if (op[i].kind == kind && op[i].a == a) last = i;
    int n = 0;
    for (int i = last + 1; i < cnt; i++) if (op[i].pos < 2 * g && !droppable(op[i])) n++;
    return n;
  }
};
constexpr Sched S1{};
constexpr int cmin(int a, int b) { return a < b ? a : b; }
constexpr int cmin(int a, int b, int c) { return cmin(cmin(a, b), c); }
constexpr bool l_dbl(int v) { return v == 4 || v == 5 || v == 6 || v == 7 || v >= 10; }
struct LF { int unit, ks, blk; };
constexpr LF l_frag(int G) {
  const int v = G / 24, g = G % 24;
  if (!l_dbl(v)) return LF{v, 3 * (v % 6) + (g >> 3), g & 7};
  const int v0 = v & ~1, r = (v & 1) * 24 + g, p = r / 12, k6 = (r % 12) / 2;
  return LF{v0 + k6 / 3, 3 * (v0 % 6) + k6, p + 4 * (r & 1)};
}
constexpr int l_prev_unit(int G) {
  for (int g = G - 1; g >= 0; g--) if ((l_frag(g).blk & 3) == (l_frag(G).blk & 3)) return l_frag(g).unit;
  return 0;
}
constexpr int l_unit0(int G) { for (int g = G - 1; g >= 0; g--) if ((l_frag(g).blk & 3) == 0) return l_frag(g).unit; return 0; }
struct Corr {
  int item[40];
  constexpr Corr(int pass) : item{} {
    const int g0 = gl(6 * pass + 3, SY1 + 2);
    bool done[16] = {};
    const int order[8] = {0, 4, 1, 5, 2, 6, 3, 7};
    for (int k = 0; k < 40; k++) {
      item[k] = -1;
      const int G = g0 + k;
      for (int o = 0; o < 16 && item[k] < 0; o++) {
        const int bk = order[o >> 1], it = bk * 2 + (o & 1);
        if (done[it]) continue;
        if (l_frag(G).blk == bk || l_frag(G - 1).blk == bk || l_frag(G - 2).blk == bk || l_frag(G + 1).blk == bk) continue;
        item[k] = it; done[it] = true;
      }
    }
  }
  constexpr int last() const { int l = 0; for (int k = 0; k < 40; k++) if (item[k] >= 0) l = k; return l; }
  constexpr int count() const { int c = 0; for (int k = 0; k < 40; k++) c += item[k] >= 0; return c; }
};
constexpr Corr CR{0};
static_assert(CR.count() == 16 && gl(3, SY1 + 2) + CR.last() < gl(4, 12), "");
constexpr int t_ks(int r) { return r < 54 ? r / 3 : (r - 54) / 2; }
constexpr int t_blk(int r) { return r < 54 ? 2 + r % 3 : (r - 54) & 1; }

__global__ __launch_bounds__(512, 2) void k_main(const u16* __restrict__ x, const u16* __restrict__ lre, const u16* __restrict__ lim,
                                                 const u16* __restrict__ bre, const u16* __restrict__ bim, const u16* __restrict__ cre,
                                                 const u16* __restrict__ cim, const u16* __restrict__ logdt, const u16* __restrict__ dsk,
                                                 const u16* __restrict__ wg, const u16* __restrict__ wu, const u16* __restrict__ wd,
                                                 char* __restrict__ ws, float* __restrict__ out) {
  extern __shared__ __attribute__((aligned(16))) char lds[];
  const int tid = threadIdx.x, w = __builtin_amdgcn_readfirstlane(tid >> 6);
  unsigned cw;
  {
    u64 pc; unsigned clen;
    asm volatile("s_getpc_b64 %0\n.Lk_code_from:\n\ts_mov_b32 %1, .Lk_code_to-.Lk_code_from" : "=s"(pc), "=s"(clen));
    const unsigned off = min((((blockIdx.x >> 3) * 8 + w) * 2 + (tid & 1)) * 128u, clen);
    asm volatile("global_load_dword %0, %1, %2" : "=v"(cw) : "v"(off), "s"(pc) : "memory");
  }
  const int b = blockIdx.x, sx = b & 7, idx = b >> 3;
  unsigned lds0 = (unsigned)(size_t)(LAS char*)lds;
  asm volatile("" : "+s"(lds0));
  const int rt = idx & 7, cgl = idx >> 3, cg = sx * 4 + cgl;
  const rsrc_t rsW = mkrsrc(ws, (unsigned)WS_END);
  const unsigned htw = (unsigned)WS_HT + (unsigned)(((rt * 16 + 2 * w) * 8 + sx) * KS2) * 1024u;
  constexpr unsigned HTB = NKG * 1024;
  const unsigned flw = (unsigned)FL_H + (unsigned)((((rt * 8 + sx) * 4) * 8 + w) * 4) * 4u;
  const f32x4 zero4 = {0.f, 0.f, 0.f, 0.f};
  const unsigned l16 = (tid & 63) * 16;
  unsigned tag = 0;
  unsigned ldsw = lds0 + w * 1024; asm volatile("" : "+s"(ldsw));
  STAMP(0);

  f32x4 acc[8][2];
  f32x4 tac[5][2];
  f32x4 ac2[9][2];
  bf16x8 fr[FR];
  bf16x8 xf[18][2];
  constexpr int HR = 4;
  bf16x8 hf[HR][2][2];
  bf16x8 hl[7][2];
  u32x4 pv[9];
  unsigned flv[5] = {0, 0, 0, 0, 0};
  u32x2 epv[6];
  u32x2 eg[7];
  unsigned ept[3], epd[3];
  unsigned wvoff, ab[4], tvA = 0, tvF = 0;
  const char* wsrc = (const char*)((w >> 2) ? wu : wg) + (size_t)cg * 256;
  {
    const int lane = tid & 63, q = lane >> 4, li = lane & 15;
    const int rg = w & 3, r8 = lane >> 3, ch = lane & 7;
    const int g = ((lane >> 4) & 1) | ((rg & 1) << 1);
    wvoff = (unsigned)((8 * rg + r8) * (FF * 2) + (((ch >> 1) ^ g) << 5) + ((ch & 1) << 4));
    const int r4 = li >> 2, cc = lane & 3;
    const int gr = ((r4 >> 1) & 1) | ((q & 1) << 1);
    const unsigned a0 = (unsigned)((8 * q + r4) * 128 + (gr << 5) + 8 * cc);
#pragma unroll
    for (int k = 0; k < 4; k++) { ab[k] = lds0 + (a0 ^ (k << 5)); tie(ab[k]); }
    if (tid < 384) *(LAS float*)(lds0 + LDS_WTOT + tid * 4) = 0.f;
  }
  const char* xb = (const char*)x + (size_t)(rt * 256 + 32 * w) * (DM * 2);
  unsigned xvo, xvo1, xra, xra1;
  unsigned ldsx = lds0 + w * 6144; asm volatile("" : "+s"(ldsx));
  {
    const int lane = tid & 63, q = lane >> 4, li = lane & 15, r8 = lane >> 3;
    xvo = (unsigned)(r8 * (DM * 2) + (((lane & 7) ^ (r8 >> 1)) << 4));
    xra = lds0 + LDS_XS + w * 6144 + li * 128 + ((q ^ (li >> 1)) << 4);
    xvo1 = xvo ^ 64u; xra1 = xra ^ 64u;
    tie(xvo); tie(xra); tie(xvo1); tie(xra1);
  }
  u32x2 sgr;
  const unsigned pobase = (unsigned)WS_PART + (unsigned)(((rt * 4 + cgl) * 8 + sx) * 72) * 1024u;
  const unsigned pb = (unsigned)WS_PART + (unsigned)((((rt * 4 + cgl) * 8 + w) * 8 + sx) * 9) * 1024u;
  const char* wtb = ws + WS_WT + ((size_t)cgl * NKG + sx * KS2) * 9 * 1024 + w * 1024;
  const char* hb0 = ws + htw;
  const int erow = rt * 256 + 32 * sx;
  const int blk3 = w < 4 ? 2 * cgl + (w >> 1) : 8;
  const char* tB = (const char*)((w & 1) ? bim : bre) + (size_t)(8 * (9 * sx + blk3)) * (DM * 2);
  const size_t fcol = (size_t)(4096 + 64 * sx + 16 * cgl) * 2;
  auto fsrc = [&](int fp) { return (const char*)((fp & 1) ? wu : wg) + fcol + (size_t)(fp >> 1) * 32 * (FF * 2); };
  const char* tsF = nullptr; const char* tsA = nullptr; unsigned tsn = 0, tsu = 0, tdA = 0, tdn = 0;
  unsigned cla2 = 0;
  unsigned cla = 0;

  u32x4 cfr; unsigned pl[3], pkv = 0, pkl = 0;
  const int widx = w * 32 + idx;
  const int wi = widx < 162 ? widx : widx - 162, pj = (wi * 57) >> 9, pln = wi - 9 * pj;
  const int ci = widx < 180 ? widx : widx - 180, cd16 = (ci * 205) >> 10, cjj = ci - 5 * cd16;
  auto issue = [&](auto Ic) {
    constexpr Op o = S1.op[decltype(Ic)::value];
    if constexpr (o.kind == OP_G) {
      if constexpr (o.a < 12) {
        glds_c<(o.a / 6) * 128 + (3 * (o.a % 6) + o.n) * 32 * (FF * 2), p1_off(o.a) + o.n * 8192, false>(wsrc, wvoff, ldsw);
      } else {
        constexpr int U = o.a - 12;
        if constexpr (U == 0 && o.n == 0) {
          unsigned l_ = l16; tie(l_);
          const int lane = l_ >> 4, p = lane >> 1;
          tvF = (unsigned)((p ^ (((p >> 3) & 1) << 2)) * (FF * 2) + ((lane & 1) << 4));
          const unsigned vb = (unsigned)((lane >> 3) * (DM * 2) + (((lane & 7) ^ (4 * (w & 1) + (lane >> 4))) << 4));
          tvA = w < 6 ? vb : tvF; tie(tvA); tie(tvF);
          tsF = fsrc(w); tsA = w < 6 ? tB : tsF;
          tsn = w < 6 ? 128u : 32u * (FF * 2); tsu = w < 6 ? 128u : 128u * (FF * 2);
          tdA = ldsw + (w < 6 ? 12288 : 0); tdn = w < 6 ? 6144u : 2048u;
          asm volatile("" : "+s"(tsF), "+s"(tsA), "+s"(tsn), "+s"(tsu), "+s"(tdA), "+s"(tdn));
        }
        if constexpr (o.n < 3) {
          glds_run<p1_off(o.a)>(tvA, tsA, o.n < 2 ? tsn : tsu, tdA + o.n * tdn);
        } else glds_c<U * 192 * (FF * 2), p1_off(o.a), false>(tsF, tvF, ldsw);
      }
    } else if constexpr (o.kind == OP_X) {
      glds_c<((o.a & 1) * 16 + o.n * 8) * (DM * 2) + (o.a >> 1) * 128, LDS_XS + (o.a % 3) * 2048 + o.n * 1024, false>(xb, o.n ? xvo1 : xvo, ldsx);
    } else if constexpr (o.kind == OP_PK) {
      if constexpr (o.a == 0) {
        if constexpr (o.n == 0) {
          unsigned l_ = l16; tie(l_);
          pkv = (l_ >> 7) * (DM * 2) + (l_ & 112) + (unsigned)(kg_f0b(sx, pj) * (DM * 2) + pln * 128); tie(pkv);
          pkl = lds0 + (w < 6 ? LDS_PK + w * 4096 : LDS_TOT); asm volatile("" : "+s"(pkl));
        }
        glds_c<o.n * 8 * (DM * 2), o.n * 1024, false>(wd, pkv, pkl);
      } else {
        unsigned l_ = l16; tie(l_);
        const int lane = l_ >> 4, q = lane >> 4, li = lane & 15;
        const int cbl = 2 * cjj + (q >> 1);
        const char* cp = (const char*)((q & 1) ? cim : cre) + (unsigned)((16 * cd16 + li) * (DM * 2) + 16 * (9 * sx + (cbl < 9 ? cbl : 8)));
        asm volatile("global_load_dwordx4 %0, %1, off" : "=v"(cfr) : "v"(cp) : "memory");
      }
    } else if constexpr (o.kind == OP_SG) {
      gld8_sc1(sgr, ws + FL_S + (size_t)(rt * 256 + 32 * w) * 8, (l16 >> 1) & 248);
    } else if constexpr (o.kind == OP_PO) {
      st16_sc1(rsW, l16, pobase + (unsigned)(o.n * 8 + w) * 1024u, ones4());
    } else if constexpr (o.kind == OP_PH) {
      if constexpr (o.n < 2) st16_sc1(rsW, l16, htw + o.n * HTB + (unsigned)(18 + cgl) * 1024u, ones4());
      else {
        const unsigned f = (l16 & 512) ? 0u : ~0u;
        st16_sc1(rsW, cgl < 2 ? l16 : 0x70000000u, htw + (cgl & 1) * HTB + 22u * 1024u, (u32x4){f, f, f, f});
      }
    } else if constexpr (o.kind == OP_A) {
      constexpr int U = o.a;
      if constexpr (U % 5 == 2 && o.n == 1) glds_c<U * SLOT2 + 8192, 0, true>(wtb, l16, ldsw + (w >= 4 ? p2_off(U, 12) - 4096 : p2_off(U, 8)));
      else glds_c<U * SLOT2 + o.n * 8192, p2_off(U, 8 * o.n), true>(wtb, l16, ldsw);
      if constexpr (U < 11 && o.n == 1) { if (w < 2) glds_c<U * SLOT2 + 16384, p2_off(U, 16), true>(wtb, l16, ldsw); }
      if constexpr (U == 11) { if (w < 1) glds_c<U * SLOT2 + 8192, p2_off(U, 8), true>(wtb, l16, ldsw); }
    } else if constexpr (o.kind == OP_B) {
      constexpr int kg = 2 * o.a + (o.n >> 1);
      gld16o_sc1<(kg % 8 - 4) * 1024>(hf[o.a % HR][o.n >> 1][o.n & 1], hb0 + (size_t)(o.n & 1) * HTB + (kg / 8) * 8192 + 4096, l16);
    } else if constexpr (o.kind == OP_HL) {
      gld16o_sc1<((o.n >> 1) - 4) * 1024>(hl[o.n >> 1][o.n & 1], hb0 + (size_t)(o.n & 1) * HTB + 16384 + 4096, l16);
    } else if constexpr (o.kind == OP_PL) {
      gld16o_sc1<(o.n - 4) * 1024>(pv[o.n], ws + (size_t)pobase + (size_t)w * 9216 + 4096, l16);
    } else if constexpr (o.kind == OP_EG) {
      gld8o<o.n * 384, false>(eg[o.n], ws + FL_E + (size_t)(cg * 16 + rt + 1) * 384, cla2);
    } else if constexpr (o.kind == OP_FL) {
      unsigned lz = l16; tie(lz);
      if constexpr (o.a < 3) gld4_sc1(flv[o.a], ws + flw + 128 * o.a, (lz >> 2) & 12);
      else if constexpr (o.a == 3) gld4_sc1(flv[3], ws + FL_W + (size_t)(w * 32 + idx) * 4, lz & 0);
      else gld4_sc1(flv[4], ws + FL_W + (size_t)sx * 128, (lz >> 2) & 124);
    } else if constexpr (o.kind == OP_EP) {
      constexpr int r = o.n / 2;
      if constexpr (o.n % 2 == 0) {
        unsigned l_ = l16; tie(l_);
        const unsigned e = min((l_ >> 4) + 64u * w + 512u * r, 1151u), t = (e * 1821u) >> 16;
        ept[r] = t; epd[r] = (e - 36u * t) * 8u; tie(ept[r]); tie(epd[r]);
        gld8(epv[o.n], x + (size_t)erow * DM + cgl * 144, t * (DM * 2) + epd[r]);
      } else gld8(epv[o.n], dsk + cgl * 144, epd[r]);
    }
  };
  auto issue_at = [&](auto Pc) {
    constexpr int i0 = S1.first_at(decltype(Pc)::value), i1 = S1.first_at(decltype(Pc)::value + 1);
    sfor<i0, i1>([&](auto Ic) { issue(Ic); SCHED0; });
  };
  auto xconv = [&](auto Hc) {
    constexpr int h = decltype(Hc)::value, c = h >> 1, t = h & 1;
    xf[2 * c][t] = ldsfrag(xra + (h % 3) * 2048);
    xf[2 * c + 1][t] = ldsfrag(xra1 + (h % 3) * 2048);
  };
  auto htie = [&](auto Uc) { constexpr int U = decltype(Uc)::value; tie(hf[U % HR][0][0]); tie(hf[U % HR][0][1]); tie(hf[U % HR][1][0]); tie(hf[U % HR][1][1]); };
  auto chk_flag = [&](auto Ac) { constexpr int a = decltype(Ac)::value; tie(flv[a]);
    if (!__all(flv[a] == tag)) {
      unsigned lz = l16; tie(lz);
      if constexpr (a < 3) spin_flag(ws + flw + 128 * a, (lz >> 2) & 12, tag);
      else if constexpr (a == 3) spin_flag(ws + FL_W + (size_t)(w * 32 + idx) * 4, lz & 0, tag);
      else spin_flag(ws + FL_W + (size_t)sx * 128, (lz >> 2) & 124, tag); } };

  float te = 0.f, pp = 0.f, hh[2], sc1[2], sc2[2], sng[2];
  f32x4 csw[2];
  f32x4 csv, tst[2];
  f32x4 csa0, csa1, qs[8];
  bf16x8 selA, selB; unsigned cra = 0, crb = 0;
  unsigned hp[2][4];
  float cm[3][8], ca4[3][2], se[3][2], eg4[3][2][4], spre[3][2], sv[3][2][4], cc1[3][2], cin[3][2];
  float tw[8], c32[2], wpre = 0.f, crt = 0.f, cz[3], cpw[3][2], c256[2];
  unsigned fwa = 0, fra = 0, fvo = 0;
  unsigned cs48 = 0;
#define CPAIR(base, pr) (*(LAS f32x2*)((base) + (pr) * 128))
  unsigned csa = 0, pkd[4];
  bool q1 = false, q2 = false;
  auto side = [&](auto Sc) {
    constexpr int sl = decltype(Sc)::value;
    {
      if constexpr (silu_at(sl) >= 0) {
        constexpr int s = silu_at(sl), r = sl - SILU0[s], N = SILUN[s];
        constexpr int pr2 = r / 9, ph = r % 9;
        auto G = [&](auto Ec) -> float { constexpr int e = decltype(Ec)::value; if constexpr (s < 2) return acc[e >> 3][(e >> 2) & 1][e & 3]; else return tac[0][e >> 2][e & 3]; };
        auto Uv = [&](auto Ec) -> float { constexpr int e = decltype(Ec)::value; if constexpr (s < 2) return acc[4 + (e >> 3)][(e >> 2) & 1][e & 3]; else return tac[1][e >> 2][e & 3]; };
        constexpr int e0 = 2 * pr2, e1 = e0 + 1;
        if constexpr (ph == 0) { if constexpr (pr2 > 0) v_mul(hh[1], pp, te); if constexpr (e0 < N) v_mul(te, G(IC<e0 < N ? e0 : 0>{}), sc1[(e0 >> 2) & 1]); }
        if constexpr (ph == 1) {
          if constexpr (pr2 > 0) {
            constexpr int ep = e0 - 2, t = (ep >> 2) & 1;
            v_cvt(hp[t][s < 2 ? 2 * ((ep >> 3) & 1) + ((ep >> 1) & 1) : ((ep >> 1) & 1)], hh[0], hh[1]);
            if constexpr (s < 2 && (e0 & 15) == 0) {
#pragma unroll
              for (int tt = 0; tt < 2; tt++)
                st16_sc1(rsW, l16, htw + tt * HTB + (unsigned)(8 * s + 2 * cgl + (ep >> 4)) * 1024u, (u32x4){hp[tt][0], hp[tt][1], hp[tt][2], hp[tt][3]});
            }
            if constexpr (s == 2 && e0 == 8) {
#pragma unroll
              for (int tt = 0; tt < 2; tt++) st8_sc1(rsW, l16, htw + tt * HTB + (unsigned)((16 + (cgl >> 1)) * 1024 + (cgl & 1) * 8), (u32x2){hp[tt][0], hp[tt][1]});
            }
          }
        }
        if constexpr (e0 < N) {
          if constexpr (ph == 2 || ph == 6) v_exp2(te);
          if constexpr (ph == 3) { v_fmaa(te, sc2[(e0 >> 2) & 1]); v_mul(pp, G(IC<e0>{}), Uv(IC<e0>{})); }
          if constexpr (ph == 4 || ph == 8) v_rcp(te);
          if constexpr (ph == 5) { v_mul(hh[0], pp, te); v_mul(te, G(IC<e1>{}), sc1[(e0 >> 2) & 1]); }
          if constexpr (ph == 7) { v_fmaa(te, sc2[(e0 >> 2) & 1]); v_mul(pp, G(IC<e1>{}), Uv(IC<e1>{})); }
        }
      }
    }
    if constexpr (sl == SILU0[0] - 4 || sl == SILU0[1] - 4 || sl == SILU0[2] - 4) {
      unsigned lz = l16; tie(lz);
#pragma unroll
      for (int t = 0; t < 2; t++) { const f32x2 f = *(LAS f32x2*)(lds0 + LDS_ST + w * 512 + t * 256 + (lz & 240)); sc1[t] = f[0]; sc2[t] = f[1]; }
    }
    if constexpr (sl == 2 * gl(3, SY1) || sl == 2 * gl(9, SY1) || sl == SQC - 14) {
      unsigned lz = l16; tie(lz);
#pragma unroll
      for (int t = 0; t < 2; t++) sng[t] = *(LAS float*)(lds0 + LDS_ST + w * 512 + t * 256 + (lz & 240) + 8);
      cra = lds0 + LDS_CS + (lz >> 8) * 16; tie(cra);
    }
    {
      constexpr int pass = sl / 2 >= gl(6, 0) ? 1 : 0, k = sl / 2 - gl(6 * pass + 3, SY1 + 2), half = sl & 1;
      if constexpr (k >= 0 && k < 40) {
        constexpr int it = CR.item[k];
        if constexpr (it >= 0) { constexpr int bk = it >> 1, t = it & 1; float a0 = acc[bk][t][2 * half], a1 = acc[bk][t][2 * half + 1];
          v_fmac(a0, sng[t], csw[k & 1][2 * half]); v_fmac(a1, sng[t], csw[k & 1][2 * half + 1]); acc[bk][t][2 * half] = a0; acc[bk][t][2 * half + 1] = a1; }
      }
      if constexpr (k >= -2 && k < 38 && half == 1) {
        constexpr int nx = CR.item[k + 2];
        if constexpr (nx >= 0) csw[k & 1] = *(LAS f32x4*)(cra + pass * 512 + ((nx >> 1) >> 2) * 256 + ((nx >> 1) & 3) * 64);
      }
    }
    if constexpr (sl == SQC - 12) {
#pragma unroll
      for (int k = 0; k < 8; k++) qs[k] = *(LAS f32x4*)(cra + 1280 + k * 64);
    }
    if constexpr (sl >= SQC - 8 && sl < SQC) {
      constexpr int r = sl - (SQC - 8);
#pragma unroll
      for (int e = 3 * r; e < 3 * r + 3; e++) { const int k = 2 + e / 4, j = e & 3; float a = qs[k & 1][j]; asm volatile("v_add_f32 %0, %0, %1" : "+v"(a) : "v"(qs[k][j])); qs[k & 1][j] = a; }
      if constexpr (r == 7) { csv = qs[0]; csa0 = qs[1]; }
    }
    if constexpr (sl >= SQC && sl < SQC + 8) {
      constexpr int r = sl - SQC, mt = r >> 2, t = (r >> 1) & 1, half = r & 1;
      float a0 = tac[mt][t][2 * half], a1 = tac[mt][t][2 * half + 1];
      if constexpr (mt == 0) { v_fmac(a0, sng[t], csv[2 * half]); v_fmac(a1, sng[t], csv[2 * half + 1]); }
      else { v_fmac(a0, sng[t], csa0[2 * half]); v_fmac(a1, sng[t], csa0[2 * half + 1]); }
      tac[mt][t][2 * half] = a0; tac[mt][t][2 * half + 1] = a1;
    }
    if constexpr (sl == 2 * GSYC) {
      unsigned lz = l16; tie(lz);
      crb = lds0 + LDS_ST + w * 512 + (lz >> 8) * 64; tie(crb);
#pragma unroll
      for (int t = 0; t < 2; t++)
#pragma unroll
        for (int j = 0; j < 4; j++) tst[t][j] = *(LAS float*)(crb + t * 256 + j * 16 + 8);
#pragma unroll
      for (int k = 0; k < 3; k++) csv[k] = *(LAS float*)(lds0 + LDS_CS + 1024 + k * 64 + ((lz >> 2) & 60));
    }
    if constexpr (sl >= 2 * (GT + 48) && sl < 2 * (GT + 54)) {
      constexpr int r = sl / 2 - (GT + 48), k = (r + 1) % 3, t = r / 3, half = sl & 1;
      float a0 = tac[2 + k][t][2 * half], a1 = tac[2 + k][t][2 * half + 1];
      v_fmac(a0, tst[t][2 * half], csv[k]); v_fmac(a1, tst[t][2 * half + 1], csv[k]);
      if constexpr (half) dnop();
      tac[2 + k][t][2 * half] = a0; tac[2 + k][t][2 * half + 1] = a1;
    }
    if constexpr (sl == S1R - 2) {
#pragma unroll
      for (int t = 0; t < 2; t++)
#pragma unroll
        for (int j = 0; j < 4; j++) tst[t][j] = *(LAS float*)(crb + t * 256 + j * 16 + 12);
    }
    if constexpr (sl >= S1R && sl < S1R + 12) {
      constexpr int r = sl - S1R, k = r >> 2, t = (r >> 1) & 1, half = r & 1;
      float a0 = tac[2 + k][t][2 * half], a1 = tac[2 + k][t][2 * half + 1];
      v_muli(a0, tst[t][2 * half]); v_muli(a1, tst[t][2 * half + 1]);
      tac[2 + k][t][2 * half] = a0; tac[2 + k][t][2 * half + 1] = a1;
    }
    if constexpr (sl == S1A - 12) {
      unsigned l_ = l16; tie(l_); const int tl = (int)(l_ >> 4) + 64 * w;
      const int lane = tl & 63;
      csa = lds0 + LDS_CST + (lane & 15) * 8; cla = (unsigned)(lane < 48 ? lane : lane - 16) * 4u;
      cs48 = csa + (cla >> 6) * 2048; cla2 = cla * 2; tie(cla2);
      q1 = lane & 16; q2 = lane & 32;
    }
    if constexpr (sl == S1A - 6 || sl == S1A + 2 || sl == S1A + 10) {
      constexpr int k = (sl - (S1A - 6)) / 8;
#pragma unroll
      for (int i = 0; i < 4; i++) { const f32x2 m = CPAIR(csa + k * 2048, 1 + i); cm[k][2 * i] = m[0]; cm[k][2 * i + 1] = m[1]; }
    }
    if constexpr (sl >= S1A && sl < S1A + 24) {
      constexpr int r = sl - S1A, k = r / 8, j = (r % 8) >> 1, t = r & 1;
      if constexpr (j == 0) cmul(se[k][t], cm[k][6], cm[k][7], tac[2 + k][t][0]);
      else cmac(se[k][t], cm[k][2 * (3 - j)], cm[k][2 * (3 - j) + 1], tac[2 + k][t][j]);
    }
    if constexpr (sl == S1A + 36) {
#pragma unroll
      for (int k = 0; k < 3; k++) { const f32x2 a4 = CPAIR(csa + k * 2048, 6); ca4[k][0] = a4[0]; ca4[k][1] = a4[1]; }
    }
    if constexpr (sl == SCM - 4) {
#pragma unroll
      for (int k = 0; k < 3; k++) { const f32x2 mm = CPAIR(csa + k * 2048, 1); cin[k][0] = mm[0]; cin[k][1] = mm[1]; }
    }
    if constexpr (sl >= S1A + 24 && sl < S1A + 42) {
      constexpr int r = sl - (S1A + 24), st = r / 6, k = (r % 6) >> 1, t = r & 1;
      if constexpr (st == 0) {
        const unsigned pu = __float_as_uint(se[k][t]);
        const auto s16 = __builtin_amdgcn_permlane16_swap(pu, pu, false, false);
        eg4[k][t][0] = __uint_as_float(s16[0]); eg4[k][t][1] = __uint_as_float(s16[1]);
      } else {
        const unsigned pu = __float_as_uint(eg4[k][t][st - 1]);
        const auto sw = __builtin_amdgcn_permlane32_swap(pu, pu, false, false);
        eg4[k][t][st - 1] = __uint_as_float(sw[0]); eg4[k][t][st + 1] = __uint_as_float(sw[1]);
      }
    }
    if constexpr (sl >= S1A + 42 && sl < S1A + 63) {
      constexpr int r = sl - (S1A + 42), sgm = 1 + r / 3, k = r % 3;
      cmac(eg4[k][sgm >> 2][sgm & 3], ca4[k][0], ca4[k][1], eg4[k][(sgm - 1) >> 2][(sgm - 1) & 3]);
    }
    if constexpr (sl == S1A + 63) {
      *(LAS float*)(lds0 + LDS_WTOT + (8 + w) * 192 + cla) = q2 ? eg4[2][1][3] : (q1 ? eg4[1][1][3] : eg4[0][1][3]);
    }
    if constexpr (sl >= S1SEL && sl < S1SEL + 6) {
      constexpr int r = sl - S1SEL, k = r >> 1, t = r & 1;
      const float lo = q1 ? eg4[k][t][0] : (t ? eg4[k][0][3] : 0.f), hi = q1 ? eg4[k][t][2] : eg4[k][t][1];
      spre[k][t] = q2 ? hi : lo; tie(spre[k][t]);
    }
    if constexpr (sl >= SCE && sl < SCE + 7) {
      constexpr int k = sl - SCE + 1;
      cmac(tw[k], c32[0], c32[1], tw[k - 1]);
      if constexpr (k == 7) {
        wpre = tw[6];
        __builtin_amdgcn_raw_buffer_store_b64((u32x2){__float_as_uint(tw[7]), tag}, rsW, (w == 7 && l16 < 768) ? (unsigned)FL_E + (unsigned)((cg * 16 + 8 + rt) * 384) + cla2 : 0x7ffffff0u, 0, 16);
      }
    }
    if constexpr (sl >= SCM && sl < SCM + 24) {
      constexpr int r = sl - SCM, k = r / 8, t = (r >> 2) & 1, j = r & 3;
      cmul(sv[k][t][j], cin[k][0], cin[k][1], tac[2 + k][t][j]);
    }
    if constexpr (sl == SFIN - 8) {
      const f32x2 a = CPAIR(cs48, 13), p0 = CPAIR(cs48, (w & 1) ? 10 : 5), p1 = CPAIR(cs48, (w & 2) ? 11 : 5), p2 = CPAIR(cs48, (w & 4) ? 12 : 5);
      c256[0] = a[0]; c256[1] = a[1]; cpw[0][0] = p0[0]; cpw[0][1] = p0[1]; cpw[1][0] = p1[0]; cpw[1][1] = p1[1]; cpw[2][0] = p2[0]; cpw[2][1] = p2[1];
    }
    if constexpr (sl >= SFIN - 6 && sl < SFIN - 3) {
      constexpr int k = sl - (SFIN - 6);
      unsigned lz = l16; tie(lz);
      const f32x2 a1 = CPAIR(csa + k * 2048, 0), pq = CPAIR(csa + k * 2048, 5 + (lz >> 8)), a16 = CPAIR(csa + k * 2048, 9);
      cc1[k][0] = a1[0]; cc1[k][1] = a1[1]; cin[k][0] = pq[0]; cin[k][1] = pq[1]; ca4[k][0] = a16[0]; ca4[k][1] = a16[1];
    }
    if constexpr (sl >= SFIN && sl < SFIN + 6) {
      constexpr int r = sl - SFIN + 1;
      float n = __uint_as_float(eg[r][0]); cmac(n, c256[0], c256[1], crt); crt = n;
    }
    if constexpr (sl == SFIN + 6) { float n; cmul(n, cpw[0][0], cpw[0][1], crt); crt = n; }
    if constexpr (sl == SFIN + 7) { float n; cmul(n, cpw[1][0], cpw[1][1], crt); crt = n; }
    if constexpr (sl == SFIN + 8) cmac(wpre, cpw[2][0], cpw[2][1], crt);
    if constexpr (sl == SFIN + 9) {
      unsigned lz = l16; tie(lz);
      const unsigned scr = lds0 + LDS_SCR + w * 1024, q = lz >> 8, li = (lz >> 4) & 15;
      fwa = scr + li * 32 + q * 8;
      fra = scr + (q >> 1) * 512 + (8 * (q & 1) + (li >> 2)) * 32 + (li & 3) * 8;
      fvo = (q >> 1) * HTB + ((q & 1) * 16 + li) * 16;
      tie(fwa); tie(fra); tie(fvo);
    }
    if constexpr (sl == SFIN + 10) {
      const unsigned pu = __float_as_uint(wpre);
      const auto s16 = __builtin_amdgcn_permlane16_swap(pu, pu, false, false);
      cz[0] = __uint_as_float(s16[0]); cz[1] = __uint_as_float(s16[1]);
    }
    if constexpr (sl == SFIN + 11) { const unsigned pu = __float_as_uint(cz[0]); const auto sw = __builtin_amdgcn_permlane32_swap(pu, pu, false, false); cz[0] = __uint_as_float(sw[0]); cz[2] = __uint_as_float(sw[1]); }
    if constexpr (sl == SFIN + 12) { const unsigned pu = __float_as_uint(cz[1]); const auto sw = __builtin_amdgcn_permlane32_swap(pu, pu, false, false); cz[1] = __uint_as_float(sw[0]); }
    if constexpr (sl >= SFIN + 13 && sl < SFIN + 16) { constexpr int k = (sl - (SFIN + 13)) == 1 ? 2 : ((sl - (SFIN + 13)) == 2 ? 1 : 0); float n; cmul(n, cin[k][0], cin[k][1], cz[k]); cz[k] = n; }
    if constexpr (sl >= SFIN + 16 && sl < SFIN + 19) { constexpr int k = (sl - (SFIN + 16)) == 1 ? 2 : ((sl - (SFIN + 16)) == 2 ? 1 : 0); cmac(spre[k][1], ca4[k][0], ca4[k][1], cz[k]); }
    if constexpr (sl == SFIN + 19) { asm volatile("v_add_f32 %0, %0, %1" : "+v"(spre[0][0]) : "v"(cz[0])); asm volatile("v_add_f32 %0, %0, %1" : "+v"(spre[1][0]) : "v"(cz[1])); asm volatile("v_add_f32 %0, %0, %1" : "+v"(spre[2][0]) : "v"(cz[2])); }
    if constexpr (sl >= SFIN + 20 && sl < SFIN + 44) {
      constexpr int r = sl - (SFIN + 20), j = r / 6, k = (r % 6) >> 1, t = (r & 1) ^ 1;
      cmac(sv[k][t][j], cc1[k][0], cc1[k][1], j == 0 ? spre[k][t] : sv[k][t][j > 0 ? j - 1 : 0]);
    }
    if constexpr (sl >= SFIN + 44 && sl < SFIN + 53) {
      constexpr int r = sl - (SFIN + 44), k = r / 3, ph = r % 3;
      if constexpr (ph < 2) {
        v_cvt(pkd[0], sv[k][ph][0], sv[k][ph][1]); v_cvt(pkd[1], sv[k][ph][2], sv[k][ph][3]);
        *(LAS u32x2*)(fwa + ph * 512) = (u32x2){pkd[0], pkd[1]};
      } else {
        const u32x4 o = __builtin_bit_cast(u32x4, trfrag<128>(fra));
        st16_sc1(rsW, (k == 2 && cgl) ? 0x70000000u : fvo, htw + (k < 2 ? (unsigned)((18 + cgl) * 1024 + k * 512) : 22u * 1024u), o);
      }
    }
    if constexpr (sl >= 2 * late_g(9) && sl < 2 * GEND) {
      constexpr int r = sl - 2 * late_g(9), db = r / 14, ph = r % 14;
      if constexpr (db < 8) {
        if constexpr (ph == 0) { unsigned a, c; v_cvt(a, ac2[db][0][0], ac2[db][0][1]); v_cvt(c, ac2[db][0][2], ac2[db][0][3]); pkd[0] = a; pkd[1] = c; }
        if constexpr (ph == 1) { unsigned a, c; v_cvt(a, ac2[db][1][0], ac2[db][1][1]); v_cvt(c, ac2[db][1][2], ac2[db][1][3]); st16_sc1(rsW, l16, pb + db * 1024, (u32x4){pkd[0], pkd[1], a, c}); }
      }
    }
  };

  unsigned fa[2], eb[2], a2b[3];
  s16x4 flo;
  auto trl = [&](unsigned a) { return __builtin_amdgcn_ds_read_tr16_b64_v4i16((LAS s16x4*)(a)); };
  auto join = [&](s16x4 a, s16x4 c) { return __builtin_bit_cast(bf16x8, __builtin_shufflevector(a, c, 0, 1, 2, 3, 4, 5, 6, 7)); };
  auto frag = [&](auto Gc, auto Pc) {
    constexpr int G = decltype(Gc)::value, part = decltype(Pc)::value;
    if constexpr (G < GT) {
      constexpr LF f = l_frag(G);
      constexpr int o = (f.ks % 3) * 8192 + (f.blk >> 2) * 4096;
      if constexpr (part == 0) flo = trl(ab[f.blk & 3] + o); else fr[G % FR] = join(flo, trl(ab[f.blk & 3] + o + 512));
    } else if constexpr (G < G2) {
      constexpr int ks = t_ks(G - GT), blk = t_blk(G - GT), uo = (ks / 6) * 30720, k6 = ks % 6;
      if constexpr (blk < 2) {
        constexpr int o = uo - (ks >= 12 ? 30720 : 0) + (k6 * 2 + blk) * 1024;
        if constexpr (part == 0) flo = trl(fa[0] + o); else fr[G % FR] = join(flo, trl(fa[1] + o));
      } else if constexpr (part == 1) fr[G % FR] = ldsfrag(eb[k6 & 1] + uo - (ks >= 12 ? 30720 : 0) + 12288 + (k6 >> 1) * 6144 + (blk - 2) * 2048);
    } else if constexpr (part == 1) {
      constexpr int U = (G - G2) / 18, g = (G - G2) % 18;
      fr[G % FR] = ldsfrag(p2_off(U, g) >= 65536 ? (p2_off(U, g) >= 131072 ? a2b[2] + (p2_off(U, g) - 131072) : a2b[1] + (p2_off(U, g) - 65536)) : a2b[0] + p2_off(U, g));
    }
  };
  auto addr_step = [&](auto Gc) {
    constexpr int G = decltype(Gc)::value;
    if constexpr (G < GT) {
      constexpr int u = l_frag(G).unit, pu = l_prev_unit(G), k = l_frag(G).blk & 3;
      if constexpr (u != pu) { ab[k] += (unsigned)(p1_off(u) - p1_off(pu)); tie(ab[k]); }
    }
    if constexpr (G == GT) {
      unsigned l_ = l16; tie(l_); const int tl = (int)(l_ >> 4) + 64 * w;
      const int lane = tl & 63, q = lane >> 4, li = lane & 15;
      const unsigned e0 = li * 128 + ((q ^ (li >> 1)) << 4);
      eb[0] = lds0 + p1_off(12) + e0; eb[1] = lds0 + p1_off(12) + (e0 ^ 64u); tie(eb[0]); tie(eb[1]);
    }
    if constexpr (G == GT + 36) { eb[0] += 30720; eb[1] += 30720; tie(eb[0]); tie(eb[1]); }
    if constexpr (G == GT + 78) { fa[0] += 30720; fa[1] += 30720; tie(fa[0]); tie(fa[1]); }
    if constexpr (G == G2) { a2b[0] = lds0 + l16; a2b[1] = lds0 + l16 + 65536; a2b[2] = lds0 + l16 + 131072; tie(a2b[0]); tie(a2b[1]); tie(a2b[2]); }
  };
  auto mma = [&](auto Gc, auto Tc) {
    constexpr int G = decltype(Gc)::value, t = decltype(Tc)::value;
    const bf16x8 a = fr[G % FR];
    if constexpr (G < GT) {
      constexpr LF f = l_frag(G);
      mfma<f.ks == 0>(acc[f.blk][t], a, xf[f.ks][t]);
    } else if constexpr (G < G2) {
      constexpr int ks = t_ks(G - GT), blk = t_blk(G - GT);
      if constexpr (blk < 2) mfma<ks == 0>(tac[blk][t], a, xf[ks][t]);
      else mfma<ks == 0>(tac[blk][t], xf[ks][t], a);
    } else if constexpr (G < late_g(0)) {
      constexpr int U = (G - G2) / 18, g = (G - G2) % 18, kk = g / 9, db = g % 9;
      mfma<(U == 0 && kk == 0)>(ac2[db][t], a, hf[U % HR][kk][t]);
    } else {
      constexpr int f = G - late_g(0), db = f / 7, kk = f % 7;
      mfma<false>(ac2[db][t], a, hl[kk][t]);
    }
  };

  auto sync = [&](auto Gc) {
    constexpr int G = decltype(Gc)::value;
    if constexpr (G < GT && G % 24 == (G < 24 ? 16 : SY1)) {
      constexpr int V = G / 24;
      constexpr int need = V + 1 < 12 ? (l_dbl(V + 1) ? (((V + 1) & 1) ? -1 : V + 2) : V + 1) : 12;
      constexpr int ng = need >= 0 ? S1.after(OP_G, need, G) : 63;
      constexpr int ng2 = V == 2 ? S1.after(OP_G, 4, G) : (V == 8 ? S1.after(OP_G, 11, G) : 63);
      constexpr int ns = V == 10 ? cmin(S1.after(OP_ST, 0, G), S1.after(OP_ST, 9, G)) : 63;
      waitv<cmin(ng, ng2, ns)>();
      if constexpr (V == 3 || V == 9) asm volatile("s_waitcnt lgkmcnt(0)" ::: "memory");
      if constexpr (V == 10) setflag(rsW, l16 == 0, flw + cgl * 4, tag);
      bar();
      if constexpr (V == 10) setflag(rsW, l16 == 0 && w == 1, (unsigned)FL_W + (unsigned)(sx * 32 + idx) * 4u, tag);
      SCHED0;
    }
    if constexpr (G == gt(0, 18)) { waitv<S1.after(OP_FL, 4, G)>(); chk_flag(IC<4>{}); SCHED0; }
    if constexpr (G == gl(2, 14)) {
      waitv<S1.after(OP_SG, 0, G)>();
      for (unsigned it = 0; it < (1u << 18); ++it) {
        tie(sgr);
        if (__all(sgr[1] == tag)) break;
        __builtin_amdgcn_s_sleep(1);
        asm volatile("global_load_dwordx2 %0, %1, %2 sc1" : "+v"(sgr) : "v"((l16 >> 1) & 248), "s"(ws + FL_S + (size_t)(rt * 256 + 32 * w) * 8) : "memory");
        waitv<0>();
      }
      tie(sgr);
      const float den = bhi(sgr[0]), rs = __builtin_amdgcn_rcpf(den);
      *(LAS f32x4*)(lds0 + LDS_ST + w * 512 + (l16 & 496)) = (f32x4){rs * -1.44269504089f, den * den, -blo(sgr[0]), rs};
      SCHED0;
    }
    if constexpr (G == gl(3, 1)) { waitv<S1.after(OP_G, 5, G)>(); bar(); SCHED0; }
    if constexpr (G == GSYC) { asm volatile("s_waitcnt lgkmcnt(0)" ::: "memory"); bar(); SCHED0; }
    if constexpr (G == gt(0, SYT)) { waitv<S1.after(OP_G, 13, G)>(); bar(); SCHED0; }
    if constexpr (G == gt(1, SYT)) { waitv<cmin(S1.after(OP_G, 14, G), S1.after(OP_FL, 0, G))>(); bar(); chk_flag(IC<0>{}); SCHED0; }
    if constexpr (G == gt(2, 2)) { waitv<S1.after(OP_ST, 1, G)>(); setflag(rsW, l16 == 0, flw + 128 + cgl * 4, tag); SCHED0; }
    if constexpr (G == gt(2, 27)) {
      waitv<cmin(S1.after(OP_A, 0, G), S1.after(OP_B, 0, G))>(); htie(IC<0>{});
      bar(); SCHED0;
    }
    if constexpr (G >= G2 && (G - G2) % 18 == SY2 && (G - G2) / 18 < 11) {
      constexpr int U = (G - G2) / 18;
      constexpr int nab = cmin(S1.after(OP_A, U + 1, G), U + 1 < 8 ? S1.after(OP_B, U + 1, G) : (U == 7 ? S1.after(OP_HL, 0, G) : 63));
      constexpr int nfl = U == 1 ? S1.after(OP_FL, 1, G) : (U == 3 ? cmin(S1.after(OP_ST, 2, G), S1.after(OP_EG, 0, G)) : (U == 5 ? S1.after(OP_FL, 2, G) : 63));
      static_assert(UFIN == 3, "");
      if constexpr (U == 0) asm volatile("s_waitcnt lgkmcnt(0)" ::: "memory");
      waitv<cmin(nab, nfl)>();
      if constexpr (U + 1 < 8) htie(IC<U + 1>{});
      if constexpr (U == 7) {
        tie(hl[0][0]); tie(hl[0][1]); tie(hl[1][0]); tie(hl[1][1]);
        for (unsigned it = 0; it < (1u << 16); ++it) {
          unsigned m = 0;
#pragma unroll
          for (int n = 4; n < 14; n++) { tie(hl[n >> 1][n & 1]); const u32x4 v = __builtin_bit_cast(u32x4, hl[n >> 1][n & 1]); asm("v_max3_u32 %0, %0, %1, %2" : "+v"(m) : "v"(v[0]), "v"(v[1])); asm("v_max3_u32 %0, %0, %1, %2" : "+v"(m) : "v"(v[2]), "v"(v[3])); }
          if (__all(m != ~0u)) break;
          __builtin_amdgcn_s_sleep(4);
          sfor<4, 14>([&](auto Nc) { constexpr int n = decltype(Nc)::value;
            gld16o_io<((n >> 1) - 4) * 1024>(hl[n >> 1][n & 1], hb0 + (size_t)(n & 1) * HTB + 16384 + 4096, l16); });
          waitv<0>();
        }
#pragma unroll
        for (int n = 4; n < 14; n++) tie(hl[n >> 1][n & 1]);
      }
      if constexpr (U == UFIN) {
        for (unsigned it = 0; it < (1u << 18); ++it) {
          unsigned d = 0;
#pragma unroll
          for (int r = 0; r < 7; r++) { tie(eg[r]); d |= eg[r][1] ^ tag; }
          if (__all(d == 0)) break;
          __builtin_amdgcn_s_sleep(1);
          sfor<0, 7>([&](auto Rc) { gld8o<decltype(Rc)::value * 384, true>(eg[decltype(Rc)::value], ws + FL_E + (size_t)(cg * 16 + rt + 1) * 384, cla2); });
          waitv<0>();
        }
#pragma unroll
        for (int r = 0; r < 7; r++) tie(eg[r]);
        crt = __uint_as_float(eg[0][0]);
        setflag(rsW, l16 == 0, flw + 256 + cgl * 4, tag);
      }
      bar();
      if constexpr (U == 1) chk_flag(IC<1>{});
      if constexpr (U == 5) chk_flag(IC<2>{});
      if constexpr (U == 0) {
        const f32x2 c = CPAIR(cs48, 10);
        c32[0] = c[0]; c32[1] = c[1];
#pragma unroll
        for (int k = 0; k < 8; k++) tw[k] = *(LAS float*)(lds0 + LDS_WTOT + (w + 1 + k) * 192 + cla);
      }
      SCHED0;
    }
  };

  unsigned wm[3];
  {
    unsigned l_ = l16; tie(l_); const int tl = (int)(l_ >> 4) + 64 * w;
    const int lane = tl & 63, q = lane >> 4, li = lane & 15;
    unsigned ep; u32x4 xr0, xr1;
    issue_at(IC<-3>{});
    bar();
    gld4_sc1(ep, ws + FL_EPOCH, l16 & 0);
    const int row = b * 8 + w;
    gld16(xr0, x + (size_t)row * DM, l16);
    gld16(xr1, x + (size_t)row * DM, 1024u + (l16 & 112));
    {
      const int chn = 8 * (9 * sx + (lane < 16 ? 2 * cgl + (lane >> 3) : 8)) + (lane & 7);
      asm volatile("global_load_ushort %0, %1, %2" : "=v"(pl[0]) : "v"(chn * 2), "s"(lre) : "memory");
      asm volatile("global_load_ushort %0, %1, %2" : "=v"(pl[1]) : "v"(chn * 2), "s"(lim) : "memory");
      asm volatile("global_load_ushort %0, %1, %2" : "=v"(pl[2]) : "v"(chn * 2), "s"(logdt) : "memory");
    }
    SCHED0;
    issue_at(IC<-1>{});
    {
      const int n = tl, r = 72 * rt + n / 6, c6 = n % 6;
      const char* p0 = (const char*)((c6 & 1) ? wu : wg) + (size_t)r * (FF * 2) + (c6 < 4 ? cg * 256 + (c6 >> 1) * 128 : fcol);
      const int m = rt * 54 + (tl - 432 < 54 ? tl - 432 : 0), rb = m / 9;
      const char* p1 = (const char*)((rb & 8) ? bim : bre) + (size_t)(8 * (9 * sx + ((rb >> 4) < 2 ? 2 * cgl + (rb >> 4) : 8)) + (rb & 7)) * (DM * 2) + (m - 9 * rb) * 128;
      asm volatile("global_load_dword %0, %1, off" : "=v"(wm[0]) : "v"(tl < 432 ? p0 : p1) : "memory");
    }
    SCHED0;
    waitv<6>();
    tie(ep); tie(xr0); tie(xr1); tie(pl[0]); tie(pl[1]); tie(pl[2]);
    tag = (unsigned)__builtin_amdgcn_readfirstlane(ep) + 1u;
    STAMP(12);
    {
      float v[16];
      const bool ex = lane < 8;
#pragma unroll
      for (int k = 0; k < 4; k++) { v[2 * k] = blo(xr0[k]); v[2 * k + 1] = bhi(xr0[k]); v[8 + 2 * k] = ex ? blo(xr1[k]) : 0.f; v[9 + 2 * k] = ex ? bhi(xr1[k]) : 0.f; }
      float sm = 0.f;
#pragma unroll
      for (int k = 0; k < 16; k++) sm += v[k];
      sm = wave_sum(sm);
      const float mean = sm * (1.0f / DM);
      float qq = 0.f;
#pragma unroll
      for (int k = 0; k < 16; k++) { const float d = (k < 8 || ex) ? v[k] - mean : 0.f; qq += d * d; }
      qq = wave_sum(qq);
      const unsigned md = pk2(mean, sqrtf(rbf(rbf(qq * (1.0f / DM)) + 1e-5f)));
      const unsigned zo = (w == 1 && lane < 48) ? (unsigned)FL_E + (unsigned)((cg * 16 + rt) * 384 + lane * 8) : 0x7ffffff0u;
      __builtin_amdgcn_raw_buffer_store_b64((u32x2){lane == 63 ? md : 0u, tag}, rsW, lane == 63 ? (unsigned)FL_S + row * 8u : zo, 0, 16);
    }
    if (w == 0 && lane < 24) {
      const float lr = bf2f((u16)pl[0]), lm = bf2f((u16)pl[1]);
      const float dt = rbf(__builtin_amdgcn_exp2f(bf2f((u16)pl[2]) * 1.44269504089f));
      const float rev = __builtin_amdgcn_fractf(dt * lm * 0.15915494309f);
      const float er = __builtin_amdgcn_exp2f(dt * lr * 1.44269504089f), ar = er * __builtin_amdgcn_cosf(rev), ai = er * __builtin_amdgcn_sinf(rev);
      const float nr = ar - 1.0f, ni = ai, dn = lr * lr + lm * lm;
      const unsigned tre = lds0 + LDS_CST + (lane >> 3) * 2048 + (lane & 7) * 8;
      auto put = [&](int k, float pr, float pi) {
        if (k >= 1 && k <= 4) { *(LAS f32x2*)(tre + k * 128) = (f32x2){2.f * pr, -2.f * pi}; *(LAS f32x2*)(tre + k * 128 + 64) = (f32x2){-2.f * pr, -2.f * pi}; }
        else { *(LAS f32x2*)(tre + k * 128) = (f32x2){pr, pi}; *(LAS f32x2*)(tre + k * 128 + 64) = (f32x2){pr, -pi}; }
      };
      put(0, ar, ai); put(5, 1.f, 0.f);
      float cr = (nr * lr + ni * lm) / dn, cm_ = (ni * lr - nr * lm) / dn;
#pragma unroll
      for (int k = 1; k < 5; k++) { put(k, cr, cm_); const float t = cr * ar - cm_ * ai; cm_ = cr * ai + cm_ * ar; cr = t; }
      const float a2r = ar * ar - ai * ai, a2i = 2.f * ar * ai;
      const float a4r = a2r * a2r - a2i * a2i, a4i = 2.f * a2r * a2i;
      float pr = a4r * a4r - a4i * a4i, pi = 2.f * a4r * a4i;
      put(6, a4r, a4i); put(7, pr, pi); put(8, pr * a4r - pi * a4i, pr * a4i + pi * a4r);
#pragma unroll
      for (int k = 9; k < 14; k++) { const float t = pr * pr - pi * pi; pi = 2.f * pr * pi; pr = t; put(k, pr, pi); }
    }
    SCHED0;
    STAMP(13);
  }
  auto pack = [&]() {
    unsigned l_ = l16; tie(l_); const int tl = (int)(l_ >> 4) + 64 * w;
    const int lane = tl & 63, q = lane >> 4, li = lane & 15;
        const unsigned ra = lds0 + LDS_PK + (w < 6 ? w : 0) * 4096 + (4 * q + (li >> 2)) * 128 + (lane & 3) * 8;
    const int fa_ = pj < 16 ? 1 : 7, fb_ = (pj < 16 ? 9 * pj : 128 + pj) + sx * (KS2 * 9);
    const unsigned vo = widx < 162 ? l_ : 0x70000000u;
#pragma unroll
    for (int n = 0; n < 4; n++) {
      const u32x4 f = __builtin_bit_cast(u32x4, trfrag<2048>(ra + n * 32));
      const int d16 = 4 * pln + n, dt = (d16 * 57) >> 9, db = d16 - 9 * dt;
      st16_sc1(rsW, vo, (unsigned)WS_WT + (unsigned)(dt * (NKG * 9) + fb_ + fa_ * db) * 1024u, f);
    }
    asm volatile("s_waitcnt lgkmcnt(0)" ::: "memory");
  };
  auto pack_c = [&]() {
    unsigned l_ = l16; tie(l_); tie(cfr);
    const int dt = (cd16 * 57) >> 9, db = cd16 - 9 * dt;
    if (2 * cjj + (l_ >> 9) >= 9) cfr = (u32x4){0, 0, 0, 0};
    st16_sc1(rsW, widx < 180 ? l_ : 0x70000000u, (unsigned)WS_WT + (unsigned)((dt * NKG + sx * KS2) * 9 + 146 + cjj + 7 * db) * 1024u, cfr);
  };
  waitv<S1.after(OP_G, 0, 0)>(); xconv(IC<0>{}); xconv(IC<1>{});
  STAMP(14);
  bar();
  SCHED0;
  STAMP(1);
  frag(IC<0>{}, IC<0>{}); frag(IC<0>{}, IC<1>{}); frag(IC<1>{}, IC<0>{}); frag(IC<1>{}, IC<1>{});
  SCHED0;

  bf16x8 cpb[2];
  unsigned csp = 0, csq = 0, csr = 0;
  auto extra = [&](auto Gc) {
    constexpr int G = decltype(Gc)::value;
    if constexpr (xt_at(G) >= 0) { waitv<S1.after(OP_X, xt_at(G), G + 1)>(); xconv(IC<xt_at(G)>{}); SCHED0; }
    if constexpr (G == csx_g(0, 0) - 3 || G == csx_g(6, 0) - 3) {
      constexpr int cur = p1_off(l_unit0(G + PD));
      csp = (ab[0] ^ (unsigned)((w & 3) << 5)) + (unsigned)((w >> 2) * 4096 - cur); tie(csp);
      const u32x4 so = {0x3f803f80u, 0x3f803f80u, 0x3f803f80u, 0x3f803f80u};
      selA = __builtin_bit_cast(bf16x8, so); tie(selA);
      SCHED0;
    }
    if constexpr (G == csx_g(12, 0) - 3) {
      unsigned l_ = l16; tie(l_); const int tl = (int)(l_ >> 4) + 64 * w;
      const int lane = tl & 63, q = lane >> 4, li = lane & 15;
      const u32x4 so = {0x3f803f80u, 0x3f803f80u, 0x3f803f80u, 0x3f803f80u}, sz = {0, 0, 0, 0};
      selA = __builtin_bit_cast(bf16x8, so); selB = __builtin_bit_cast(bf16x8, w < 6 ? so : sz); tie(selA); tie(selB);
      csp = lds0 + p1_off(12) + 12288 + (w < 6 ? w : 0) * 1024 + (li & 7) * 128 + (2 * q + (li >> 3)) * 16;
      const unsigned f0 = lds0 + p1_off(12) + (unsigned)((((8 * q + (li >> 2)) ^ ((q & 1) << 2)) << 5) + 8 * (lane & 3));
      csq = f0 + w * 1024; csr = f0 + (w < 6 ? (w >> 1) * 30720 : 0) + (8 + (w & 1)) * 1024;
      fa[0] = f0; fa[1] = f0 ^ 128u;
      tie(csp); tie(csq); tie(csr); tie(fa[0]); tie(fa[1]);
      SCHED0;
    }
    if constexpr (csx_at(G) >= 0) {
      constexpr int v = csx_at(G) >> 2, n = csx_at(G) & 3;
      const bf16x8 cpc = cpb[G & 1];
      if constexpr (v < 12) mfma<(v % 6 == 0 && n == 0)>(csa0, cpc, selA);
      else if constexpr (v < 15) mfma<(v == 12 && n == 0)>(csa0, selA, cpc);
      else mfma<(v == 15 && n == 0)>(csa1, cpc, v == 16 ? selB : selA);
      SCHED0;
    }
    if constexpr (csx_at(G + 2) >= 0) {
      constexpr int v = csx_at(G + 2) >> 2, n = csx_at(G + 2) & 3;
      bf16x8& cpc = cpb[G & 1];
      if constexpr (v < 12) { unsigned a = csp + p1_off(v); cpc = trfrag<512>(a + n * 8192); }
      else if constexpr (v < 15) { unsigned a = csp + (v - 12) * 30720; cpc = ldsfrag(a + n * 6144); }
      else {
        const unsigned o = v == 15 ? csq + n * 30720 : csr + n * 2048;
        s16x4 a = __builtin_amdgcn_ds_read_tr16_b64_v4i16((LAS s16x4*)(o));
        s16x4 c = __builtin_amdgcn_ds_read_tr16_b64_v4i16((LAS s16x4*)(o ^ 128u));
        cpc = __builtin_bit_cast(bf16x8, __builtin_shufflevector(a, c, 0, 1, 2, 3, 4, 5, 6, 7));
      }
      SCHED0;
    }
  };
  auto cs_flush = [&](auto Gc) {
    constexpr int G = decltype(Gc)::value;
    if constexpr (G == CSF[0] || G == CSF[1]) {
      unsigned lz = l16; tie(lz);
      if ((lz & 240) == 0) *(LAS f32x4*)(lds0 + LDS_CS + (G == CSF[1] ? 512 : 0) + w * 64 + (lz >> 8) * 16) = csa0;
      SCHED0;
    }
    if constexpr (G == CSF[2]) {
      unsigned lz = l16; tie(lz); tie(csa0);
      const float rsum = csa0[0] + dpp_ror8(csa0[0]);
      if (lz < 128) *(LAS float*)(lds0 + LDS_CS + 1024 + w * 32 + (lz >> 2)) = rsum;
      if ((lz & 240) == 0) *(LAS f32x4*)(lds0 + LDS_CS + 1280 + w * 64 + (lz >> 8) * 16) = csa1;
      SCHED0;
    }
  };

  auto group = [&](auto Gc) {
    constexpr int G = decltype(Gc)::value;
    if constexpr (G == 144) { STAMP(2); }
    if constexpr (G == GT) { STAMP(3); }
    if constexpr (G == G2) { STAMP(4); STAMP(5); }
    if constexpr (G == g2(3, 0)) { STAMP(6); }
    if constexpr (G == g2(8, 0)) { STAMP(7); }
    if constexpr (G == GPK) { waitv<S1.after(OP_PK, 0, G)>(); pack(); SCHED0; }
    if constexpr (G == GPC) { waitv<S1.after(OP_PK, 1, G)>(); pack_c(); SCHED0; }
    if constexpr (G == gl(1, 4)) { tie(wm[0]); tie(cw); }
    cs_flush(Gc);
    sync(Gc);
    issue_at(IC<2 * G>{});
    extra(Gc);
    mma(Gc, IC<0>{}); SCHED0;
    if constexpr (G + PD < GEND) { addr_step(IC<G + PD>{}); frag(IC<G + PD>{}, IC<0>{}); SCHED0; }
    side(IC<2 * G>{}); SCHED0;
    if constexpr (G + PD < GEND) { frag(IC<G + PD>{}, IC<1>{}); SCHED0; }
    mma(Gc, IC<1>{}); SCHED0;
    side(IC<2 * G + 1>{}); SCHED0;
  };
  sfor<0, 13>([&](auto Bc) { sfor<0, 45>([&](auto Jc) { group(IC<decltype(Bc)::value * 45 + decltype(Jc)::value>{}); }); });
  STAMPC(27);
  STAMP(8);

  asm volatile("s_nop 7\n\ts_nop 1" : "+v"(ac2[8][0]), "+v"(ac2[8][1]));
  unsigned l_ = l16; tie(l_); const int tz = (int)(l_ >> 4) + 64 * w;
  const int lane = tz & 63, q = lane >> 4, li = lane & 15;
  st16_sc1(rsW, l16, pb + 8 * 1024, (u32x4){pk2(ac2[8][0][0], ac2[8][0][1]), pk2(ac2[8][0][2], ac2[8][0][3]), pk2(ac2[8][1][0], ac2[8][1][1]), pk2(ac2[8][1][2], ac2[8][1][3])});
  SCHED0;
  const char* ib = ws + (size_t)pobase + (size_t)w * 9216 + 4096;
  waitv<0>();
  setflag(rsW, l16 == 0, (unsigned)FL_P + (unsigned)(((rt * 4 + cgl) * 8 + w) * 8 + sx) * 64u, tag);
  bar();
  STAMP(9);
  {
    constexpr int LROW = 304, LSRC = 32 * LROW;
    spin_flag(ws + FL_P + (size_t)(((rt * 4 + cgl) * 8 + sx) * 8 + w) * 64, l16 & 0, tag);
    sfor<0, 8>([&](auto Dc) { gld16o_sc1<(decltype(Dc)::value - 4) * 1024>(pv[decltype(Dc)::value], ib, l16); });
    gld16o_sc1<0>(pv[8], ib + 4096, l16);
    waitv<0>();
#pragma unroll
    for (int db = 0; db < 9; db++) {
      tie(pv[db]);
      *(LAS u32x2*)(lds0 + w * LSRC + li * LROW + db * 32 + q * 8) = (u32x2){pv[db][0], pv[db][1]};
      *(LAS u32x2*)(lds0 + w * LSRC + (16 + li) * LROW + db * 32 + q * 8) = (u32x2){pv[db][2], pv[db][3]};
    }
    asm volatile("s_waitcnt vmcnt(0) lgkmcnt(0)" ::: "memory");
    bar();
    STAMP(10);
#pragma unroll
    for (int r = 0; r < 3; r++) {
      tie(epv[2 * r]); tie(epv[2 * r + 1]);
      const int e = tz + 512 * r;
      if (e < 1152) {
        const unsigned t = ept[r], d8 = epd[r];
        f32x4 sum = zero4;
#pragma unroll
        for (int sr = 0; sr < 8; sr++) {
          const u32x2 p = *(LAS u32x2*)(lds0 + sr * LSRC + t * LROW + d8);
          sum += (f32x4){blo(p[0]), bhi(p[0]), blo(p[1]), bhi(p[1])};
        }
        const f32x4 st = *(LAS f32x4*)(lds0 + LDS_ST + sx * 512 + t * 16);
        f32x4 o;
#pragma unroll
        for (int j = 0; j < 4; j++) {
          const float xe = (j & 1) ? bhi(epv[2 * r][j >> 1]) : blo(epv[2 * r][j >> 1]);
          const float de = (j & 1) ? bhi(epv[2 * r + 1][j >> 1]) : blo(epv[2 * r + 1][j >> 1]);
          const float xn = rbf(rbf(xe + st[2]) * st[3]);
          o[j] = xe + rbf(xn * de) + sum[j];
        }
        *(f32x4*)((char*)(out + (size_t)erow * DM + cgl * 144) + t * (DM * 4) + d8 * 2) = o;
      }
    }
    asm volatile(".Lk_code_to:");
    if (b == 0 && w == 0) {
#pragma unroll
      for (int k = 0; k < 4; k++) spin_flag(ws + FL_W + k * 256, l16 >> 2, tag);
      setflag(rsW, l16 == 0, (unsigned)FL_EPOCH, tag);
    }
    STAMP(11);
    STAMP_DUMP();
  }
}

extern "C" void kernel_launch(void* const* d_in, const int* in_sizes, int n_in, void* d_out, int out_size, void* d_ws, size_t ws_size,
                              hipStream_t stream) {
  const u16* x = (const u16*)d_in[0]; const u16* lre = (const u16*)d_in[1]; const u16* lim = (const u16*)d_in[2];
  const u16* bre = (const u16*)d_in[3]; const u16* bim = (const u16*)d_in[4]; const u16* cre = (const u16*)d_in[5]; const u16* cim = (const u16*)d_in[6];
  const u16* logdt = (const u16*)d_in[7]; const u16* dsk = (const u16*)d_in[8];
  const u16* wg = (const u16*)d_in[10]; const u16* wu = (const u16*)d_in[11]; const u16* wd = (const u16*)d_in[12];
  char* ws = (char*)d_ws;
  (void)hipFuncSetAttribute((const void*)k_main, hipFuncAttributeMaxDynamicSharedMemorySize, LDS_TOT);
  k_main<<<256, 512, LDS_TOT, stream>>>(x, lre, lim, bre, bim, cre, cim, logdt, dsk, wg, wu, wd, ws, (float*)d_out);
}
```

```cpp
#include <hip/hip_runtime.h>
#include <stdint.h>
#include <type_traits>

typedef unsigned short u16;
typedef unsigned long long u64;
typedef __bf16 bf16x8 __attribute__((ext_vector_type(8)));
typedef __bf16 bf16x2 __attribute__((ext_vector_type(2)));
typedef float f32x4 __attribute__((ext_vector_type(4)));
typedef float f32x2 __attribute__((ext_vector_type(2)));
typedef short s16x4 __attribute__((ext_vector_type(4)));
typedef unsigned u32x4 __attribute__((ext_vector_type(4)));
typedef unsigned u32x2 __attribute__((ext_vector_type(2)));
#define LAS __attribute__((address_space(3)))
#define GAS __attribute__((address_space(1)))
#define DI __device__ __forceinline__

constexpr int SEQ = 2048, DM = 576, FF = 4608;
constexpr int KS2 = 23;
constexpr int NKG = 8 * KS2;
constexpr size_t FL_H = 0;
constexpr size_t FL_P = 40960;
constexpr size_t FL_W = 36864;
constexpr size_t FL_EPOCH = 38912;
constexpr size_t FL_E = 262144;
constexpr size_t FL_S = 180224;
constexpr size_t WS_HT = 524288;
constexpr size_t WS_WT = WS_HT + (size_t)128 * NKG * 1024;
constexpr size_t WS_PART = WS_WT + (size_t)4 * NKG * 9 * 1024;
constexpr size_t PART_BYTES = (size_t)8 * 4 * 8 * 8 * 9 * 1024;
constexpr size_t WS_END = WS_PART + PART_BYTES;

DI float bf2f(u16 v) { return __uint_as_float(((unsigned)v) << 16); }
DI float rbf(float f) { return (float)(__bf16)f; }
DI unsigned pk2(float lo, float hi) { f32x2 v = {lo, hi}; bf16x2 c = __builtin_convertvector(v, bf16x2); return __builtin_bit_cast(unsigned, c); }
DI float blo(unsigned u) { return __uint_as_float(u << 16); }
DI float bhi(unsigned u) { return __uint_as_float(u & 0xffff0000u); }

__host__ __device__ inline int wt_frag(int j, int db) { return j < 16 ? j * 9 + db : 144 + db * 7 + (j - 16); }
DI int kg_f0b(int s, int j) {
  const int a = 512 * s + 128 * ((j & 7) >> 1) + 64 * (j >> 3) + 32 * (j & 1), c = 4096 + 64 * s + 32 * (j & 1);
  return j < 16 ? a : c;
}
__host__ __device__ inline int kg_f0(int s, int j) {
  if (j < 16) { int p = j >> 3, cg = 4 * s + ((j & 7) >> 1); return 64 * (2 * cg + p) + 32 * (j & 1); }
  return 4096 + 32 * (2 * s + (j - 16));
}

template <int N> using IC = std::integral_constant<int, N>;
template <int B, int E, class F> DI void sfor(F&& f) { if constexpr (B < E) { f(IC<B>{}); sfor<B + 1, E>(f); } }

DI void glds_sc1(const void* sbase, unsigned voff, unsigned ldsaddr) {
  asm volatile("s_mov_b32 m0, %2\n\ts_nop 0\n\tglobal_load_lds_dwordx4 %0, %1 sc1" :: "v"(voff), "s"(sbase), "s"(ldsaddr) : "memory");
}
DI void gld4(unsigned& dst, const void* sbase, unsigned voff) {
  asm volatile("global_load_dword %0, %1, %2" : "=v"(dst) : "v"(voff), "s"(sbase) : "memory");
}
template <int C, int L, bool SC1> DI void glds_c(const void* sbase, unsigned voff, unsigned ldsw) {
  unsigned t;
  if constexpr (SC1) asm volatile("s_add_u32 m0, %3, %5\n\tv_add_u32 %0, %4, %1\n\tglobal_load_lds_dwordx4 %0, %2 sc1" : "=&v"(t) : "v"(voff), "s"(sbase), "s"(ldsw), "i"(C), "i"(L) : "memory", "scc");
  else asm volatile("s_add_u32 m0, %3, %5\n\tv_add_u32 %0, %4, %1\n\tglobal_load_lds_dwordx4 %0, %2" : "=&v"(t) : "v"(voff), "s"(sbase), "s"(ldsw), "i"(C), "i"(L) : "memory", "scc");
}
template <int OFF, class T> DI void gld16o_sc1(T& dst, const void* sbase, unsigned voff) {
  asm volatile("global_load_dwordx4 %0, %1, %2 offset:%3 sc1" : "=v"(dst) : "v"(voff), "s"(sbase), "i"(OFF) : "memory");
}
template <int OFF, class T> DI void gld16o_io(T& dst, const void* sbase, unsigned voff) {
  asm volatile("global_load_dwordx4 %0, %1, %2 offset:%3 sc1" : "+v"(dst) : "v"(voff), "s"(sbase), "i"(OFF) : "memory");
}
template <int OFF, bool IO> DI void gld8o(u32x2& dst, const void* sbase, unsigned voff) {
  if constexpr (IO) asm volatile("global_load_dwordx2 %0, %1, %2 offset:%3 sc1" : "+v"(dst) : "v"(voff), "s"(sbase), "i"(OFF) : "memory");
  else asm volatile("global_load_dwordx2 %0, %1, %2 offset:%3 sc1" : "=v"(dst) : "v"(voff), "s"(sbase), "i"(OFF) : "memory");
}
template <int L> DI void glds_run(unsigned& voff, const void* sbase, unsigned step, unsigned ldsa) {
  asm volatile("s_add_u32 m0, %3, %4\n\ts_nop 0\n\tglobal_load_lds_dwordx4 %0, %1\n\tv_add_u32 %0, %2, %0" : "+v"(voff) : "s"(sbase), "s"(step), "s"(ldsa), "i"(L) : "memory", "scc");
}
DI void glds_s(const void* sbase, unsigned voff, unsigned ldsaddr) {
  asm volatile("s_mov_b32 m0, %2\n\ts_nop 0\n\tglobal_load_lds_dwordx4 %0, %1" :: "v"(voff), "s"(sbase), "s"(ldsaddr) : "memory");
}
template <class T> DI void gld16(T& dst, const void* sbase, unsigned voff) {
  asm volatile("global_load_dwordx4 %0, %1, %2" : "=v"(dst) : "v"(voff), "s"(sbase) : "memory");
}
template <class T> DI void gld16_sc1(T& dst, const void* sbase, unsigned voff) {
  asm volatile("global_load_dwordx4 %0, %1, %2 sc1" : "=v"(dst) : "v"(voff), "s"(sbase) : "memory");
}
template <class T> DI void gld16_sc1_io(T& dst, const void* sbase, unsigned voff) {
  asm volatile("global_load_dwordx4 %0, %1, %2 sc1" : "+v"(dst) : "v"(voff), "s"(sbase) : "memory");
}
DI void gld4_sc1(unsigned& dst, const void* sbase, unsigned voff) {
  asm volatile("global_load_dword %0, %1, %2 sc1" : "=v"(dst) : "v"(voff), "s"(sbase) : "memory");
}
DI void gld8_sc1(u32x2& dst, const void* sbase, unsigned voff) {
  asm volatile("global_load_dwordx2 %0, %1, %2 sc1" : "=v"(dst) : "v"(voff), "s"(sbase) : "memory");
}
DI void gld8(u32x2& dst, const void* sbase, unsigned voff) {
  asm volatile("global_load_dwordx2 %0, %1, %2" : "=v"(dst) : "v"(voff), "s"(sbase) : "memory");
}
template <int N> DI void waitv() { static_assert(N >= 0 && N < 64, ""); asm volatile("s_waitcnt vmcnt(%0)" ::"i"(N) : "memory"); }
#define SCHED0 __builtin_amdgcn_sched_barrier(0)
DI void bar() { asm volatile("" ::: "memory"); __builtin_amdgcn_s_barrier(); asm volatile("" ::: "memory"); }
template <class T> DI void tie(T& a) { asm volatile("" : "+v"(a)); }
template <int O2> DI bf16x8 trfrag(unsigned addr) {
  s16x4 a = __builtin_amdgcn_ds_read_tr16_b64_v4i16((LAS s16x4*)(addr));
  s16x4 b = __builtin_amdgcn_ds_read_tr16_b64_v4i16((LAS s16x4*)(addr + O2));
  return __builtin_bit_cast(bf16x8, __builtin_shufflevector(a, b, 0, 1, 2, 3, 4, 5, 6, 7));
}
DI bf16x8 ldsfrag(unsigned addr) { return *(LAS bf16x8*)(addr); }
template <bool FIRST> DI void mfma(f32x4& c, bf16x8 a, bf16x8 b) {
  if constexpr (FIRST) asm volatile("v_mfma_f32_16x16x32_bf16 %0, %1, %2, 0" : "=v"(c) : "v"(a), "v"(b));
  else asm volatile("v_mfma_f32_16x16x32_bf16 %0, %1, %2, %0" : "+v"(c) : "v"(a), "v"(b));
}
DI float silu_mul(float g, float u) {
  const float e = __builtin_amdgcn_exp2f(g * -1.44269504089f);
  return g * u * __builtin_amdgcn_rcpf(1.0f + e);
}
DI unsigned silu_mul2(f32x2 g, f32x2 u) {
  const f32x2 t = g * -1.44269504089f;
  f32x2 e; e.x = __builtin_amdgcn_exp2f(t.x); e.y = __builtin_amdgcn_exp2f(t.y);
  const f32x2 d = e + 1.0f;
  f32x2 r; r.x = __builtin_amdgcn_rcpf(d.x); r.y = __builtin_amdgcn_rcpf(d.y);
  const f32x2 h = (g * u) * r;
  return pk2(h.x, h.y);
}
DI float dpp_ror8(float v) { return __int_as_float(__builtin_amdgcn_update_dpp(0, __float_as_int(v), 0x128, 0xf, 0xf, true)); }
DI void cmac(float& d, float P, float Q, float x) {
  asm volatile("v_fmac_f32 %0, %1, %3\n\tv_fmac_f32_dpp %0, %3, %2 row_ror:8 row_mask:0xf bank_mask:0xf" : "+v"(d) : "v"(P), "v"(Q), "v"(x));
}
DI void cmul(float& d, float P, float Q, float x) {
  asm volatile("v_mul_f32 %0, %1, %3\n\tv_fmac_f32_dpp %0, %3, %2 row_ror:8 row_mask:0xf bank_mask:0xf" : "=&v"(d) : "v"(P), "v"(Q), "v"(x));
}
DI void cfma(float& d, float P, float Q, float x, float c) {
  asm volatile("v_fma_f32 %0, %1, %3, %4\n\tv_fmac_f32_dpp %0, %3, %2 row_ror:8 row_mask:0xf bank_mask:0xf" : "=&v"(d) : "v"(P), "v"(Q), "v"(x), "v"(c));
}
DI void v_mul(float& d, float a, float b) { asm volatile("v_mul_f32 %0, %1, %2" : "=v"(d) : "v"(a), "v"(b)); }
DI void v_mulc(float& d, float a) { asm volatile("v_mul_f32 %0, 0xbfb8aa3b, %1" : "=v"(d) : "v"(a)); }
DI void v_fmac(float& d, float a, float b) { asm volatile("v_fmac_f32 %0, %1, %2" : "+v"(d) : "v"(a), "v"(b)); }
DI void v_fmaa(float& d, float c) { asm volatile("v_fma_f32 %0, %0, %1, %1" : "+v"(d) : "v"(c)); }
DI void v_muli(float& d, float a) { asm volatile("v_mul_f32 %0, %0, %1" : "+v"(d) : "v"(a)); }
DI void lds_add(unsigned addr, float v) { asm volatile("ds_add_f32 %0, %1" :: "v"(addr), "v"(v) : "memory"); }
DI void v_exp2(float& d) { asm volatile("v_exp_f32 %0, %0" : "+v"(d)); }
DI void v_add1(float& d) { asm volatile("v_add_f32 %0, 1.0, %0" : "+v"(d)); }
DI void v_rcp(float& d) { asm volatile("v_rcp_f32 %0, %0" : "+v"(d)); }
DI void v_cvt(unsigned& d, float a, float b) { asm volatile("v_cvt_pk_bf16_f32 %0, %1, %2" : "=v"(d) : "v"(a), "v"(b)); }
DI float wave_sum(float v) {
#define DPPADD(c) v += __int_as_float(__builtin_amdgcn_update_dpp(0, __float_as_int(v), c, 0xf, 0xf, true))
  DPPADD(0x111); DPPADD(0x112); DPPADD(0x114); DPPADD(0x118);
  v += __int_as_float(__builtin_amdgcn_update_dpp(0, __float_as_int(v), 0x142, 0xa, 0xf, false));
  v += __int_as_float(__builtin_amdgcn_update_dpp(0, __float_as_int(v), 0x143, 0xc, 0xf, false));
  return __int_as_float(__builtin_amdgcn_readlane(__float_as_int(v), 63));
}
DI void dnop() { asm volatile("s_nop 1"); }
typedef __amdgpu_buffer_rsrc_t rsrc_t;
DI rsrc_t mkrsrc(const void* p, unsigned bytes) { return __builtin_amdgcn_make_buffer_rsrc((void*)p, 0, bytes, 0x00020000); }
DI unsigned vadd_s(unsigned s, unsigned v) { unsigned o; asm volatile("v_add_u32 %0, %1, %2" : "=v"(o) : "s"(s), "v"(v)); return o; }
DI void st8_sc1(rsrc_t r, unsigned voff, unsigned soff, u32x2 v) { __builtin_amdgcn_raw_buffer_store_b64(v, r, vadd_s(soff, voff), 0, 16); }
DI void st16_sc1(rsrc_t r, unsigned voff, unsigned soff, u32x4 v) { __builtin_amdgcn_raw_buffer_store_b128(v, r, vadd_s(soff, voff), 0, 16); }
DI u32x4 ones4() { u32x4 v; asm volatile("v_mov_b32 %0, -1\n\tv_mov_b32 %1, -1\n\tv_mov_b32 %2, -1\n\tv_mov_b32 %3, -1" : "=v"(v[0]), "=v"(v[1]), "=v"(v[2]), "=v"(v[3])); return v; }
DI void setflag(rsrc_t r, bool sel, unsigned off, unsigned tag) { __builtin_amdgcn_raw_buffer_store_b32(tag, r, sel ? off : 0x7ffffff0u, 0, 16); }
DI void spin_flag(const char* base, unsigned voff, unsigned tag) {
  for (unsigned it = 0; it < (1u << 18); ++it) {
    unsigned v = __hip_atomic_load((GAS unsigned*)(base + voff), __ATOMIC_RELAXED, __HIP_MEMORY_SCOPE_AGENT);
    if (__all(v == tag)) break;
    __builtin_amdgcn_s_sleep(1);
  }
  asm volatile("" ::: "memory");
}

constexpr int PD = 2, FR = PD + 1;
constexpr int SLOT1 = 24576;
constexpr int SLOT2 = 18432;
constexpr int LDS_TOT = 163840;
constexpr int LDS_SCR = 98304;
constexpr int LDS_PK = 122880;
constexpr int LDS_XS = 98304;
constexpr int LDS_ST = 147456;
constexpr int LDS_CS = 151552;
constexpr int LDS_CST = 153600;
constexpr int LDS_WTOT = LDS_CST + 6144;

#define STAMP(k)
#define STAMPC(k)
#define STAMP_DUMP()

constexpr int p1_off(int v) { return v < 8 ? (v % 4) * SLOT1 : (v < 10 ? (v - 4) * SLOT1 : (v < 12 ? (v - 10) * SLOT1 : 2 * SLOT1 + (v - 12) * 30720)); }
constexpr int p2_off(int u, int g) {
  const int k = u % 5;
  return k < 2 ? k * SLOT2 + g * 1024 : k == 2 ? (g < 12 ? 36864 + g * 1024 : 141312 + (g - 12) * 1024) : 49152 + (k - 3) * SLOT2 + g * 1024;
}

constexpr int GT = 288, G2 = 378, GEND = 585;
constexpr int gl(int v, int g) { return v * 24 + g; }
constexpr int gt(int u, int g) { return GT + u * 30 + g; }
constexpr int g2(int u, int g) { return G2 + u * 18 + g; }
constexpr int SY1 = 8, SYT = 2, SY2 = 4;
constexpr int late_g(int f) { return g2(8, f); }
constexpr int PLAG = 4;

constexpr int SCE = 2 * g2(0, SY2) + 3;
constexpr int SQC = 2 * (G2 + 13);
constexpr int SILU0[3] = {2 * (gl(4, 0) + 12), 2 * (gl(10, 0) + 12), SQC + 8};
constexpr int SILUN[3] = {32, 32, 8};
constexpr int silu_end(int s, int nel) { return SILU0[s] + 9 * (nel / 2) + 1; }
constexpr int S1R = 2 * (GT + 55);
constexpr int S1A = S1R + 12;
constexpr int S1SEL = 2 * g2(2, 0) + 2;
constexpr int SCM = S1SEL + 6;
constexpr int UFIN = 3;
constexpr int SFIN = 2 * g2(UFIN, SY2);
static_assert(S1A + 63 < 2 * g2(0, SY2) && silu_end(2, 8) < S1SEL && SCM + 24 < SFIN - 8, "");
constexpr int csx_n(int v) { return v == 16 ? 2 : 3; }
constexpr int csx_g(int v, int n) {
  const int base[17] = {gl(0, 4), gl(0, 19), gl(1, 12), gl(2, 11), gl(2, 16), gl(3, 3), gl(7, 16), gl(7, 19), gl(8, 1), gl(8, 12), gl(8, 18), gl(9, 0),
                        GT + 27, GT + 30, GT + 34, GT + 37, GT + 40};
  const int step = (v == 5 || v == 6 || v == 7 || v >= 12) ? 1 : 2;
  return base[v] + n * step;
}
constexpr int CSF[3] = {gl(3, SY1), gl(9, SY1), GT + 44};
constexpr int GPC = gl(8, 23);
constexpr int GPK = gl(6, 6);
constexpr int GSYC = GT + 46;
constexpr int xt_g(int h) {
  const int g[18] = {-1, -1, gl(0, 6), gl(0, 12), gl(0, 20), gl(1, 3), gl(1, 11), gl(1, 18), gl(2, 2), gl(2, 10), gl(2, 17), gl(3, 1), gl(3, 9), gl(3, 14), gl(3, 19), gl(4, 0), gl(4, 3), gl(4, 5)};
  return g[h];
}
constexpr int xd_g(int h) { return h < 3 ? -1 : (h < 5 ? h - 2 : xt_g(h - 3) + 3); }

constexpr int csx_at(int G) { for (int v = 0; v < 17; v++) for (int n = 0; n < csx_n(v); n++) if (csx_g(v, n) == G) return v * 4 + n; return -1; }
constexpr int xt_at(int G) { for (int c = 2; c < 18; c++) if (xt_g(c) == G) return c; return -1; }
constexpr int SRES = 2 * g2(9, 6);
constexpr int res_at(int sl) {
  if (sl < SRES || !(sl & 1)) return -1;
  int n = 0;
  for (int g = SRES / 2; g < GEND; g++) { const bool busy = (g - late_g(9)) % 7 == 0; if (g == sl / 2) return (busy || n >= 30) ? -1 : n; if (!busy) n++; }
  return -1;
}
constexpr int silu_at(int sl) { for (int k = 0; k < 3; k++) if (sl >= SILU0[k] && sl - SILU0[k] <= 9 * (SILUN[k] / 2) + 1) return k; return -1; }

enum { OP_G = 0, OP_X, OP_ST, OP_PO, OP_PH, OP_A, OP_B, OP_FL, OP_EP, OP_HL, OP_PL, OP_EG, OP_SG, OP_PK };
struct Op { int pos, kind, a, n; };
struct Sched {
  Op op[320]; int cnt;
  constexpr void add(int pos, int kind, int a, int n) {
    int i = cnt; while (i > 0 && op[i - 1].pos > pos) { op[i] = op[i - 1]; i--; }
    op[i] = Op{pos, kind, a, n}; cnt++;
  }
  constexpr void unit(int g, int v, int step) { for (int n = 0; n < (v < 12 ? 3 : 4); n++) add(2 * (g + n * step), OP_G, v, n); }
  constexpr Sched() : op{}, cnt(0) {
    for (int h = 0; h < 2; h++) for (int n = 0; n < 2; n++) add(-3, OP_X, h, n);
    for (int n = 0; n < 3; n++) add(-3, OP_G, 0, n);
    for (int n = 0; n < 6; n++) add(-2, OP_ST, 8, n);
    for (int n = 0; n < 2; n++) add(-1, OP_X, 2, n);
    for (int n = 0; n < 3; n++) add(-1, OP_G, 1, n);
    for (int n = 0; n < 1; n++) add(-1, OP_ST, 7, n);
    add(-1, OP_ST, 6, 0);
    for (int h = 3; h < 18; h++) for (int n = 0; n < 2; n++) add(2 * xd_g(h), OP_X, h, n);
    for (int n = 0; n < 4; n++) add(2 * gl(4, 10 + 3 * n), OP_PK, 0, n);
    add(2 * gl(7, 15), OP_PK, 1, 0);
    add(2 * GPC - 1, OP_ST, 9, 0);
    unit(gl(0, 4), 2, 1);
    for (int n = 0; n < 4; n++) add(2 * GPK - 1, OP_ST, 4, n);
    add(2 * gl(1, 5), OP_SG, 0, 0);
    unit(gl(0, 9), 3, 3); unit(gl(1, 9), 4, 3); unit(gl(2, 9), 5, 1); unit(gl(3, 10), 6, 3); unit(gl(4, 9), 7, 3);
    unit(gl(5, 10), 8, 4); unit(gl(6, 9), 9, 1); unit(gl(6, 13), 10, 2); unit(gl(6, 19), 11, 2); unit(gl(8, 10), 12, 3); unit(gl(9, 10), 13, 3); unit(gl(10, 10), 14, 3);
    for (int n = 0; n < 3; n++) add(2 * gl(4, 1 + 2 * n), OP_PH, 0, n);
    for (int s = 0; s < 2; s++) for (int h = 0; h < 2; h++) for (int t = 0; t < 2; t++) add((silu_end(s, 16 * (h + 1)) / 2) * 2 + 1, OP_ST, s, 2 * h + t);
    for (int t = 0; t < 2; t++) add((silu_end(2, 8) / 2) * 2 + 1, OP_ST, 2, t);
    add(2 * gl(11, 16), OP_FL, 4, 0);
    add(2 * gt(0, 6), OP_FL, 0, 0);
    for (int n = 0; n < 2; n++) { add(2 * gt(0, 20 + 4 * n), OP_A, 0, n); add(2 * gt(1, 8 + 4 * n), OP_A, 1, n); add(2 * gt(1, 16 + 4 * n), OP_A, 2, n); }
    for (int n = 0; n < 4; n++) { add(2 * gt(2, 3 + 2 * n), OP_B, 0, n); add(2 * gt(2, 11 + 2 * n), OP_B, 1, n); add(2 * gt(2, 19 + 3 * n), OP_B, 2, n); }
    add(2 * g2(0, 1), OP_FL, 1, 0);
    for (int u = 0; u < 9; u++) {
      const int U = u + 3;
      for (int n = 0; n < (U < 11 ? 2 : 1); n++) add(2 * g2(u, 5 + 2 * n), OP_A, U, n);
      if (U < 8) for (int n = 0; n < 4; n++) add(2 * g2(u, 9 + 2 * n), OP_B, U, n);
    }
    for (int n = 0; n < 7; n++) add(2 * (g2(UFIN - 1, 0) + (n < 4 ? n : 2 * n - 2)), OP_EG, 0, n);
    add(2 * g2(4, 6), OP_FL, 2, 0);
    for (int n = 0; n < 4; n++) add(2 * g2(5, 9 + 2 * n), OP_HL, 0, n);
    for (int n = 4; n < 14; n++) add(2 * (g2(6, 9) + (n - 4)), OP_HL, 0, n);
    for (int n = 0; n < 6; n++) add(2 * g2(7, 6 + n), OP_EP, 0, n);
    for (int db = 0; db < 8; db++) add(2 * (late_g(7 * db + 6) + 3) + 1, OP_ST, 3, db);
  }
  static constexpr bool droppable(const Op& o) { return (o.kind == OP_PH && o.n == 2) || (o.kind == OP_ST && (o.a == 4 || o.a == 9)) || (o.kind == OP_PK && o.a == 0); }
  constexpr int first_at(int pos) const { int i = 0; while (i < cnt && op[i].pos < pos) i++; return i; }
  constexpr int after(int kind, int a, int g) const {
    int last = -1;
    for (int i = 0; i < cnt; i++) if (op[i].kind == kind && op[i].a == a) last = i;
    int n = 0;
    for (int i = last + 1; i < cnt; i++) if (op[i].pos < 2 * g && !droppable(op[i])) n++;
    return n;
  }
};
constexpr Sched S1{};
constexpr int cmin(int a, int b) { return a < b ? a : b; }
constexpr int cmin(int a, int b, int c) { return cmin(cmin(a, b), c); }
constexpr bool l_dbl(int v) { return v == 4 || v == 5 || v == 6 || v == 7 || v >= 10; }
struct LF { int unit, ks, blk; };
constexpr LF l_frag(int G) {
  const int v = G / 24, g = G % 24;
  if (!l_dbl(v)) return LF{v, 3 * (v % 6) + (g >> 3), g & 7};
  const int v0 = v & ~1, r = (v & 1) * 24 + g, p = r / 12, k6 = (r % 12) / 2;
  return LF{v0 + k6 / 3, 3 * (v0 % 6) + k6, p + 4 * (r & 1)};
}
constexpr int l_prev_unit(int G) {
  for (int g = G - 1; g >= 0; g--) if ((l_frag(g).blk & 3) == (l_frag(G).blk & 3)) return l_frag(g).unit;
  return 0;
}
constexpr int l_unit0(int G) { for (int g = G - 1; g >= 0; g--) if ((l_frag(g).blk & 3) == 0) return l_frag(g).unit; return 0; }
struct Corr {
  int item[40];
  constexpr Corr(int pass) : item{} {
    const int g0 = gl(6 * pass + 3, SY1 + 2);
    bool done[16] = {};
    const int order[8] = {0, 4, 1, 5, 2, 6, 3, 7};
    for (int k = 0; k < 40; k++) {
      item[k] = -1;
      const int G = g0 + k;
      for (int o = 0; o < 16 && item[k] < 0; o++) {
        const int bk = order[o >> 1], it = bk * 2 + (o & 1);
        if (done[it]) continue;
        if (l_frag(G).blk == bk || l_frag(G - 1).blk == bk || l_frag(G - 2).blk == bk || l_frag(G + 1).blk == bk) continue;
        item[k] = it; done[it] = true;
      }
    }
  }
  constexpr int last() const { int l = 0; for (int k = 0; k < 40; k++) if (item[k] >= 0) l = k; return l; }
  constexpr int count() const { int c = 0; for (int k = 0; k < 40; k++) c += item[k] >= 0; return c; }
};
constexpr Corr CR{0};
static_assert(CR.count() == 16 && gl(3, SY1 + 2) + CR.last() < gl(4, 12), "");
constexpr int t_ks(int r) { return r < 54 ? r / 3 : (r - 54) / 2; }
constexpr int t_blk(int r) { return r < 54 ? 2 + r % 3 : (r - 54) & 1; }

__global__ __launch_bounds__(512, 2) void k_main(const u16* __restrict__ x, const u16* __restrict__ lre, const u16* __restrict__ lim,
                                                 const u16* __restrict__ bre, const u16* __restrict__ bim, const u16* __restrict__ cre,
                                                 const u16* __restrict__ cim, const u16* __restrict__ logdt, const u16* __restrict__ dsk,
                                                 const u16* __restrict__ wg, const u16* __restrict__ wu, const u16* __restrict__ wd,
                                                 char* __restrict__ ws, float* __restrict__ out) {
  extern __shared__ __attribute__((aligned(16))) char lds[];
  const int tid = threadIdx.x, w = __builtin_amdgcn_readfirstlane(tid >> 6);
  unsigned cw;
  {
    u64 pc; unsigned clen;
    asm volatile("s_getpc_b64 %0\n.Lk_code_from:\n\ts_mov_b32 %1, .Lk_code_to-.Lk_code_from" : "=s"(pc), "=s"(clen));
    const unsigned off = min((((blockIdx.x >> 3) * 8 + w) * 2 + (tid & 1)) * 128u, clen);
    asm volatile("global_load_dword %0, %1, %2" : "=v"(cw) : "v"(off), "s"(pc) : "memory");
  }
  const int b = blockIdx.x, sx = b & 7, idx = b >> 3;
  unsigned lds0 = (unsigned)(size_t)(LAS char*)lds;
  asm volatile("" : "+s"(lds0));
  const int rt = idx & 7, cgl = idx >> 3, cg = sx * 4 + cgl;
  const rsrc_t rsW = mkrsrc(ws, (unsigned)WS_END);
  const unsigned htw = (unsigned)WS_HT + (unsigned)(((rt * 16 + 2 * w) * 8 + sx) * KS2) * 1024u;
  constexpr unsigned HTB = NKG * 1024;
  const unsigned flw = (unsigned)FL_H + (unsigned)((((rt * 8 + sx) * 4) * 8 + w) * 4) * 4u;
  const f32x4 zero4 = {0.f, 0.f, 0.f, 0.f};
  const unsigned l16 = (tid & 63) * 16;
  unsigned tag = 0;
  unsigned ldsw = lds0 + w * 1024; asm volatile("" : "+s"(ldsw));
  STAMP(0);

  f32x4 acc[8][2];
  f32x4 tac[5][2];
  f32x4 ac2[9][2];
  bf16x8 fr[FR];
  bf16x8 xf[18][2];
  constexpr int HR = 4;
  bf16x8 hf[HR][2][2];
  bf16x8 hl[7][2];
  u32x4 pv[9];
  unsigned flv[5] = {0, 0, 0, 0, 0};
  u32x2 epv[6];
  u32x2 eg[7];
  unsigned ept[3], epd[3];
  unsigned wvoff, ab[4], tvA = 0, tvF = 0;
  const char* wsrc = (const char*)((w >> 2) ? wu : wg) + (size_t)cg * 256;
  {
    const int lane = tid & 63, q = lane >> 4, li = lane & 15;
    const int rg = w & 3, r8 = lane >> 3, ch = lane & 7;
    const int g = ((lane >> 4) & 1) | ((rg & 1) << 1);
    wvoff = (unsigned)((8 * rg + r8) * (FF * 2) + (((ch >> 1) ^ g) << 5) + ((ch & 1) << 4));
    const int r4 = li >> 2, cc = lane & 3;
    const int gr = ((r4 >> 1) & 1) | ((q & 1) << 1);
    const unsigned a0 = (unsigned)((8 * q + r4) * 128 + (gr << 5) + 8 * cc);
#pragma unroll
    for (int k = 0; k < 4; k++) { ab[k] = lds0 + (a0 ^ (k << 5)); tie(ab[k]); }
    if (tid < 384) *(LAS float*)(lds0 + LDS_WTOT + tid * 4) = 0.f;
  }
  const char* xb = (const char*)x + (size_t)(rt * 256 + 32 * w) * (DM * 2);
  unsigned xvo, xvo1, xra, xra1;
  unsigned ldsx = lds0 + w * 6144; asm volatile("" : "+s"(ldsx));
  {
    const int lane = tid & 63, q = lane >> 4, li = lane & 15, r8 = lane >> 3;
    xvo = (unsigned)(r8 * (DM * 2) + (((lane & 7) ^ (r8 >> 1)) << 4));
    xra = lds0 + LDS_XS + w * 6144 + li * 128 + ((q ^ (li >> 1)) << 4);
    xvo1 = xvo ^ 64u; xra1 = xra ^ 64u;
    tie(xvo); tie(xra); tie(xvo1); tie(xra1);
  }
  u32x2 sgr;
  const unsigned pobase = (unsigned)WS_PART + (unsigned)(((rt * 4 + cgl) * 8 + sx) * 72) * 1024u;
  const unsigned pb = (unsigned)WS_PART + (unsigned)((((rt * 4 + cgl) * 8 + w) * 8 + sx) * 9) * 1024u;
  const char* wtb = ws + WS_WT + ((size_t)cgl * NKG + sx * KS2) * 9 * 1024 + w * 1024;
  const char* hb0 = ws + htw;
  const int erow = rt * 256 + 32 * sx;
  const int blk3 = w < 4 ? 2 * cgl + (w >> 1) : 8;
  const char* tB = (const char*)((w & 1) ? bim : bre) + (size_t)(8 * (9 * sx + blk3)) * (DM * 2);
  const size_t fcol = (size_t)(4096 + 64 * sx + 16 * cgl) * 2;
  auto fsrc = [&](int fp) { return (const char*)((fp & 1) ? wu : wg) + fcol + (size_t)(fp >> 1) * 32 * (FF * 2); };
  const char* tsF = nullptr; const char* tsA = nullptr; unsigned tsn = 0, tsu = 0, tdA = 0, tdn = 0;
  unsigned cla2 = 0;
  unsigned cla = 0;

  u32x4 cfr; unsigned pl[3], pkv = 0, pkl = 0;
  const int widx = w * 32 + idx;
  const int wi = widx < 162 ? widx : widx - 162, pj = (wi * 57) >> 9, pln = wi - 9 * pj;
  const int ci = widx < 180 ? widx : widx - 180, cd16 = (ci * 205) >> 10, cjj = ci - 5 * cd16;
  auto issue = [&](auto Ic) {
    constexpr Op o = S1.op[decltype(Ic)::value];
    if constexpr (o.kind == OP_G) {
      if constexpr (o.a < 12) {
        glds_c<(o.a / 6) * 128 + (3 * (o.a % 6) + o.n) * 32 * (FF * 2), p1_off(o.a) + o.n * 8192, false>(wsrc, wvoff, ldsw);
      } else {
        constexpr int U = o.a - 12;
        if constexpr (U == 0 && o.n == 0) {
          unsigned l_ = l16; tie(l_);
          const int lane = l_ >> 4, p = lane >> 1;
          tvF = (unsigned)((p ^ (((p >> 3) & 1) << 2)) * (FF * 2) + ((lane & 1) << 4));
          const unsigned vb = (unsigned)((lane >> 3) * (DM * 2) + (((lane & 7) ^ (4 * (w & 1) + (lane >> 4))) << 4));
          tvA = w < 6 ? vb : tvF; tie(tvA); tie(tvF);
          tsF = fsrc(w); tsA = w < 6 ? tB : tsF;
          tsn = w < 6 ? 128u : 32u * (FF * 2); tsu = w < 6 ? 128u : 128u * (FF * 2);
          tdA = ldsw + (w < 6 ? 12288 : 0); tdn = w < 6 ? 6144u : 2048u;
          asm volatile("" : "+s"(tsF), "+s"(tsA), "+s"(tsn), "+s"(tsu), "+s"(tdA), "+s"(tdn));
        }
        if constexpr (o.n < 3) {
          glds_run<p1_off(o.a)>(tvA, tsA, o.n < 2 ? tsn : tsu, tdA + o.n * tdn);
        } else glds_c<U * 192 * (FF * 2), p1_off(o.a), false>(tsF, tvF, ldsw);
      }
    } else if constexpr (o.kind == OP_X) {
      glds_c<((o.a & 1) * 16 + o.n * 8) * (DM * 2) + (o.a >> 1) * 128, LDS_XS + (o.a % 3) * 2048 + o.n * 1024, false>(xb, o.n ? xvo1 : xvo, ldsx);
    } else if constexpr (o.kind == OP_PK) {
      if constexpr (o.a == 0) {
        if constexpr (o.n == 0) {
          unsigned l_ = l16; tie(l_);
          pkv = (l_ >> 7) * (DM * 2) + (l_ & 112) + (unsigned)(kg_f0b(sx, pj) * (DM * 2) + pln * 128); tie(pkv);
          pkl = lds0 + (w < 6 ? LDS_PK + w * 4096 : LDS_TOT); asm volatile("" : "+s"(pkl));
        }
        glds_c<o.n * 8 * (DM * 2), o.n * 1024, false>(wd, pkv, pkl);
      } else {
        unsigned l_ = l16; tie(l_);
        const int lane = l_ >> 4, q = lane >> 4, li = lane & 15;
        const int cbl = 2 * cjj + (q >> 1);
        const char* cp = (const char*)((q & 1) ? cim : cre) + (unsigned)((16 * cd16 + li) * (DM * 2) + 16 * (9 * sx + (cbl < 9 ? cbl : 8)));
        asm volatile("global_load_dwordx4 %0, %1, off" : "=v"(cfr) : "v"(cp) : "memory");
      }
    } else if constexpr (o.kind == OP_SG) {
      gld8_sc1(sgr, ws + FL_S + (size_t)(rt * 256 + 32 * w) * 8, (l16 >> 1) & 248);
    } else if constexpr (o.kind == OP_PO) {
      st16_sc1(rsW, l16, pobase + (unsigned)(o.n * 8 + w) * 1024u, ones4());
    } else if constexpr (o.kind == OP_PH) {
      if constexpr (o.n < 2) st16_sc1(rsW, l16, htw + o.n * HTB + (unsigned)(18 + cgl) * 1024u, ones4());
      else {
        const unsigned f = (l16 & 512) ? 0u : ~0u;
        st16_sc1(rsW, cgl < 2 ? l16 : 0x70000000u, htw + (cgl & 1) * HTB + 22u * 1024u, (u32x4){f, f, f, f});
      }
    } else if constexpr (o.kind == OP_A) {
      constexpr int U = o.a;
      if constexpr (U % 5 == 2 && o.n == 1) glds_c<U * SLOT2 + 8192, 0, true>(wtb, l16, ldsw + (w >= 4 ? p2_off(U, 12) - 4096 : p2_off(U, 8)));
      else glds_c<U * SLOT2 + o.n * 8192, p2_off(U, 8 * o.n), true>(wtb, l16, ldsw);
      if constexpr (U < 11 && o.n == 1) { if (w < 2) glds_c<U * SLOT2 + 16384, p2_off(U, 16), true>(wtb, l16, ldsw); }
      if constexpr (U == 11) { if (w < 1) glds_c<U * SLOT2 + 8192, p2_off(U, 8), true>(wtb, l16, ldsw); }
    } else if constexpr (o.kind == OP_B) {
      constexpr int kg = 2 * o.a + (o.n >> 1);
      gld16o_sc1<(kg % 8 - 4) * 1024>(hf[o.a % HR][o.n >> 1][o.n & 1], hb0 + (size_t)(o.n & 1) * HTB + (kg / 8) * 8192 + 4096, l16);
    } else if constexpr (o.kind == OP_HL) {
      gld16o_sc1<((o.n >> 1) - 4) * 1024>(hl[o.n >> 1][o.n & 1], hb0 + (size_t)(o.n & 1) * HTB + 16384 + 4096, l16);
    } else if constexpr (o.kind == OP_PL) {
      gld16o_sc1<(o.n - 4) * 1024>(pv[o.n], ws + (size_t)pobase + (size_t)w * 9216 + 4096, l16);
    } else if constexpr (o.kind == OP_EG) {
      gld8o<o.n * 384, false>(eg[o.n], ws + FL_E + (size_t)(cg * 16 + rt + 1) * 384, cla2);
    } else if constexpr (o.kind == OP_FL) {
      unsigned lz = l16; tie(lz);
      if constexpr (o.a < 3) gld4_sc1(flv[o.a], ws + flw + 128 * o.a, (lz >> 2) & 12);
      else if constexpr (o.a == 3) gld4_sc1(flv[3], ws + FL_W + (size_t)(w * 32 + idx) * 4, lz & 0);
      else gld4_sc1(flv[4], ws + FL_W + (size_t)sx * 128, (lz >> 2) & 124);
    } else if constexpr (o.kind == OP_EP) {
      constexpr int r = o.n / 2;
      if constexpr (o.n % 2 == 0) {
        unsigned l_ = l16; tie(l_);
        const unsigned e = min((l_ >> 4) + 64u * w + 512u * r, 1151u), t = (e * 1821u) >> 16;
        ept[r] = t; epd[r] = (e - 36u * t) * 8u; tie(ept[r]); tie(epd[r]);
        gld8(epv[o.n], x + (size_t)erow * DM + cgl * 144, t * (DM * 2) + epd[r]);
      } else gld8(epv[o.n], dsk + cgl * 144, epd[r]);
    }
  };
  auto issue_at = [&](auto Pc) {
    constexpr int i0 = S1.first_at(decltype(Pc)::value), i1 = S1.first_at(decltype(Pc)::value + 1);
    sfor<i0, i1>([&](auto Ic) { issue(Ic); SCHED0; });
  };
  auto xconv = [&](auto Hc) {
    constexpr int h = decltype(Hc)::value, c = h >> 1, t = h & 1;
    xf[2 * c][t] = ldsfrag(xra + (h % 3) * 2048);
    xf[2 * c + 1][t] = ldsfrag(xra1 + (h % 3) * 2048);
  };
  auto htie = [&](auto Uc) { constexpr int U = decltype(Uc)::value; tie(hf[U % HR][0][0]); tie(hf[U % HR][0][1]); tie(hf[U % HR][1][0]); tie(hf[U % HR][1][1]); };
  auto chk_flag = [&](auto Ac) { constexpr int a = decltype(Ac)::value; tie(flv[a]);
    if (!__all(flv[a] == tag)) {
      unsigned lz = l16; tie(lz);
      if constexpr (a < 3) spin_flag(ws + flw + 128 * a, (lz >> 2) & 12, tag);
      else if constexpr (a == 3) spin_flag(ws + FL_W + (size_t)(w * 32 + idx) * 4, lz & 0, tag);
      else spin_flag(ws + FL_W + (size_t)sx * 128, (lz >> 2) & 124, tag); } };

  float te = 0.f, pp = 0.f, hh[2], sc1[2], sc2[2], sng[2];
  f32x4 csw[2];
  f32x4 csv, tst[2];
  f32x4 csa0, csa1, qs[8];
  bf16x8 selA, selB; unsigned cra = 0, crb = 0;
  unsigned hp[2][4];
  float cm[3][8], ca4[3][2], se[3][2], eg4[3][2][4], spre[3][2], sv[3][2][4], cc1[3][2], cin[3][2];
  float tw[8], c32[2], wpre = 0.f, crt = 0.f, cz[3], cpw[3][2], c256[2];
  float res[3][4], rst[3][2];
  unsigned fwa = 0, fra = 0, fvo = 0;
  unsigned cs48 = 0;
#define CPAIR(base, pr) (*(LAS f32x2*)((base) + (pr) * 128))
  unsigned csa = 0, pkd[4];
  bool q1 = false, q2 = false;
  auto side = [&](auto Sc) {
    constexpr int sl = decltype(Sc)::value;
    {
      if constexpr (silu_at(sl) >= 0) {
        constexpr int s = silu_at(sl), r = sl - SILU0[s], N = SILUN[s];
        constexpr int pr2 = r / 9, ph = r % 9;
        auto G = [&](auto Ec) -> float { constexpr int e = decltype(Ec)::value; if constexpr (s < 2) return acc[e >> 3][(e >> 2) & 1][e & 3]; else return tac[0][e >> 2][e & 3]; };
        auto Uv = [&](auto Ec) -> float { constexpr int e = decltype(Ec)::value; if constexpr (s < 2) return acc[4 + (e >> 3)][(e >> 2) & 1][e & 3]; else return tac[1][e >> 2][e & 3]; };
        constexpr int e0 = 2 * pr2, e1 = e0 + 1;
        if constexpr (ph == 0) { if constexpr (pr2 > 0) v_mul(hh[1], pp, te); if constexpr (e0 < N) v_mul(te, G(IC<e0 < N ? e0 : 0>{}), sc1[(e0 >> 2) & 1]); }
        if constexpr (ph == 1) {
          if constexpr (pr2 > 0) {
            constexpr int ep = e0 - 2, t = (ep >> 2) & 1;
            v_cvt(hp[t][s < 2 ? 2 * ((ep >> 3) & 1) + ((ep >> 1) & 1) : ((ep >> 1) & 1)], hh[0], hh[1]);
            if constexpr (s < 2 && (e0 & 15) == 0) {
#pragma unroll
              for (int tt = 0; tt < 2; tt++)
                st16_sc1(rsW, l16, htw + tt * HTB + (unsigned)(8 * s + 2 * cgl + (ep >> 4)) * 1024u, (u32x4){hp[tt][0], hp[tt][1], hp[tt][2], hp[tt][3]});
            }
            if constexpr (s == 2 && e0 == 8) {
#pragma unroll
              for (int tt = 0; tt < 2; tt++) st8_sc1(rsW, l16, htw + tt * HTB + (unsigned)((16 + (cgl >> 1)) * 1024 + (cgl & 1) * 8), (u32x2){hp[tt][0], hp[tt][1]});
            }
          }
        }
        if constexpr (e0 < N) {
          if constexpr (ph == 2 || ph == 6) v_exp2(te);
          if constexpr (ph == 3) { v_fmaa(te, sc2[(e0 >> 2) & 1]); v_mul(pp, G(IC<e0>{}), Uv(IC<e0>{})); }
          if constexpr (ph == 4 || ph == 8) v_rcp(te);
          if constexpr (ph == 5) { v_mul(hh[0], pp, te); v_mul(te, G(IC<e1>{}), sc1[(e0 >> 2) & 1]); }
          if constexpr (ph == 7) { v_fmaa(te, sc2[(e0 >> 2) & 1]); v_mul(pp, G(IC<e1>{}), Uv(IC<e1>{})); }
        }
      }
    }
    if constexpr (sl == SILU0[0] - 4 || sl == SILU0[1] - 4 || sl == SILU0[2] - 4) {
      unsigned lz = l16; tie(lz);
#pragma unroll
      for (int t = 0; t < 2; t++) { const f32x2 f = *(LAS f32x2*)(lds0 + LDS_ST + w * 512 + t * 256 + (lz & 240)); sc1[t] = f[0]; sc2[t] = f[1]; }
    }
    if constexpr (sl == 2 * gl(3, SY1) || sl == 2 * gl(9, SY1) || sl == SQC - 14) {
      unsigned lz = l16; tie(lz);
#pragma unroll
      for (int t = 0; t < 2; t++) sng[t] = *(LAS float*)(lds0 + LDS_ST + w * 512 + t * 256 + (lz & 240) + 8);
      cra = lds0 + LDS_CS + (lz >> 8) * 16; tie(cra);
    }
    {
      constexpr int pass = sl / 2 >= gl(6, 0) ? 1 : 0, k = sl / 2 - gl(6 * pass + 3, SY1 + 2), half = sl & 1;
      if constexpr (k >= 0 && k < 40) {
        constexpr int it = CR.item[k];
        if constexpr (it >= 0) { constexpr int bk = it >> 1, t = it & 1; float a0 = acc[bk][t][2 * half], a1 = acc[bk][t][2 * half + 1];
          v_fmac(a0, sng[t], csw[k & 1][2 * half]); v_fmac(a1, sng[t], csw[k & 1][2 * half + 1]); acc[bk][t][2 * half] = a0; acc[bk][t][2 * half + 1] = a1; }
      }
      if constexpr (k >= -2 && k < 38 && half == 1) {
        constexpr int nx = CR.item[k + 2];
        if constexpr (nx >= 0) csw[k & 1] = *(LAS f32x4*)(cra + pass * 512 + ((nx >> 1) >> 2) * 256 + ((nx >> 1) & 3) * 64);
      }
    }
    if constexpr (sl == SQC - 12) {
#pragma unroll
      for (int k = 0; k < 8; k++) qs[k] = *(LAS f32x4*)(cra + 1280 + k * 64);
    }
    if constexpr (sl >= SQC - 8 && sl < SQC) {
      constexpr int r = sl - (SQC - 8);
#pragma unroll
      for (int e = 3 * r; e < 3 * r + 3; e++) { const int k = 2 + e / 4, j = e & 3; float a = qs[k & 1][j]; asm volatile("v_add_f32 %0, %0, %1" : "+v"(a) : "v"(qs[k][j])); qs[k & 1][j] = a; }
      if constexpr (r == 7) { csv = qs[0]; csa0 = qs[1]; }
    }
    if constexpr (sl >= SQC && sl < SQC + 8) {
      constexpr int r = sl - SQC, mt = r >> 2, t = (r >> 1) & 1, half = r & 1;
      float a0 = tac[mt][t][2 * half], a1 = tac[mt][t][2 * half + 1];
      if constexpr (mt == 0) { v_fmac(a0, sng[t], csv[2 * half]); v_fmac(a1, sng[t], csv[2 * half + 1]); }
      else { v_fmac(a0, sng[t], csa0[2 * half]); v_fmac(a1, sng[t], csa0[2 * half + 1]); }
      tac[mt][t][2 * half] = a0; tac[mt][t][2 * half + 1] = a1;
    }
    if constexpr (sl == 2 * GSYC) {
      unsigned lz = l16; tie(lz);
      crb = lds0 + LDS_ST + w * 512 + (lz >> 8) * 64; tie(crb);
#pragma unroll
      for (int t = 0; t < 2; t++)
#pragma unroll
        for (int j = 0; j < 4; j++) tst[t][j] = *(LAS float*)(crb + t * 256 + j * 16 + 8);
#pragma unroll
      for (int k = 0; k < 3; k++) csv[k] = *(LAS float*)(lds0 + LDS_CS + 1024 + k * 64 + ((lz >> 2) & 60));
    }
    if constexpr (sl >= 2 * (GT + 48) && sl < 2 * (GT + 54)) {
      constexpr int r = sl / 2 - (GT + 48), k = (r + 1) % 3, t = r / 3, half = sl & 1;
      float a0 = tac[2 + k][t][2 * half], a1 = tac[2 + k][t][2 * half + 1];
      v_fmac(a0, tst[t][2 * half], csv[k]); v_fmac(a1, tst[t][2 * half + 1], csv[k]);
      if constexpr (half) dnop();
      tac[2 + k][t][2 * half] = a0; tac[2 + k][t][2 * half + 1] = a1;
    }
    if constexpr (sl == S1R - 2) {
#pragma unroll
      for (int t = 0; t < 2; t++)
#pragma unroll
        for (int j = 0; j < 4; j++) tst[t][j] = *(LAS float*)(crb + t * 256 + j * 16 + 12);
    }
    if constexpr (sl >= S1R && sl < S1R + 12) {
      constexpr int r = sl - S1R, k = r >> 2, t = (r >> 1) & 1, half = r & 1;
      float a0 = tac[2 + k][t][2 * half], a1 = tac[2 + k][t][2 * half + 1];
      v_muli(a0, tst[t][2 * half]); v_muli(a1, tst[t][2 * half + 1]);
      tac[2 + k][t][2 * half] = a0; tac[2 + k][t][2 * half + 1] = a1;
    }
    if constexpr (sl == S1A - 12) {
      unsigned l_ = l16; tie(l_); const int tl = (int)(l_ >> 4) + 64 * w;
      const int lane = tl & 63;
      csa = lds0 + LDS_CST + (lane & 15) * 8; cla = (unsigned)(lane < 48 ? lane : lane - 16) * 4u;
      cs48 = csa + (cla >> 6) * 2048; cla2 = cla * 2; tie(cla2);
      q1 = lane & 16; q2 = lane & 32;
    }
    if constexpr (sl == S1A - 6 || sl == S1A + 2 || sl == S1A + 10) {
      constexpr int k = (sl - (S1A - 6)) / 8;
#pragma unroll
      for (int i = 0; i < 4; i++) { const f32x2 m = CPAIR(csa + k * 2048, 1 + i); cm[k][2 * i] = m[0]; cm[k][2 * i + 1] = m[1]; }
    }
    if constexpr (sl >= S1A && sl < S1A + 24) {
      constexpr int r = sl - S1A, k = r / 8, j = (r % 8) >> 1, t = r & 1;
      if constexpr (j == 0) cmul(se[k][t], cm[k][6], cm[k][7], tac[2 + k][t][0]);
      else cmac(se[k][t], cm[k][2 * (3 - j)], cm[k][2 * (3 - j) + 1], tac[2 + k][t][j]);
    }
    if constexpr (sl == S1A + 36) {
#pragma unroll
      for (int k = 0; k < 3; k++) { const f32x2 a4 = CPAIR(csa + k * 2048, 6); ca4[k][0] = a4[0]; ca4[k][1] = a4[1]; }
    }
    if constexpr (sl == SCM - 4) {
#pragma unroll
      for (int k = 0; k < 3; k++) { const f32x2 mm = CPAIR(csa + k * 2048, 1); cin[k][0] = mm[0]; cin[k][1] = mm[1]; }
    }
    if constexpr (sl >= S1A + 24 && sl < S1A + 42) {
      constexpr int r = sl - (S1A + 24), st = r / 6, k = (r % 6) >> 1, t = r & 1;
      if constexpr (st == 0) {
        const unsigned pu = __float_as_uint(se[k][t]);
        const auto s16 = __builtin_amdgcn_permlane16_swap(pu, pu, false, false);
        eg4[k][t][0] = __uint_as_float(s16[0]); eg4[k][t][1] = __uint_as_float(s16[1]);
      } else {
        const unsigned pu = __float_as_uint(eg4[k][t][st - 1]);
        const auto sw = __builtin_amdgcn_permlane32_swap(pu, pu, false, false);
        eg4[k][t][st - 1] = __uint_as_float(sw[0]); eg4[k][t][st + 1] = __uint_as_float(sw[1]);
      }
    }
    if constexpr (sl >= S1A + 42 && sl < S1A + 63) {
      constexpr int r = sl - (S1A + 42), sgm = 1 + r / 3, k = r % 3;
      cmac(eg4[k][sgm >> 2][sgm & 3], ca4[k][0], ca4[k][1], eg4[k][(sgm - 1) >> 2][(sgm - 1) & 3]);
    }
    if constexpr (sl == S1A + 63) {
      *(LAS float*)(lds0 + LDS_WTOT + (8 + w) * 192 + cla) = q2 ? eg4[2][1][3] : (q1 ? eg4[1][1][3] : eg4[0][1][3]);
    }
    if constexpr (sl >= S1SEL && sl < S1SEL + 6) {
      constexpr int r = sl - S1SEL, k = r >> 1, t = r & 1;
      const float lo = q1 ? eg4[k][t][0] : (t ? eg4[k][0][3] : 0.f), hi = q1 ? eg4[k][t][2] : eg4[k][t][1];
      spre[k][t] = q2 ? hi : lo; tie(spre[k][t]);
    }
    if constexpr (sl >= SCE && sl < SCE + 7) {
      constexpr int k = sl - SCE + 1;
      cmac(tw[k], c32[0], c32[1], tw[k - 1]);
      if constexpr (k == 7) {
        wpre = tw[6];
        __builtin_amdgcn_raw_buffer_store_b64((u32x2){__float_as_uint(tw[7]), tag}, rsW, (w == 7 && l16 < 768) ? (unsigned)FL_E + (unsigned)((cg * 16 + 8 + rt) * 384) + cla2 : 0x7ffffff0u, 0, 16);
      }
    }
    if constexpr (sl >= SCM && sl < SCM + 24) {
      constexpr int r = sl - SCM, k = r / 8, t = (r >> 2) & 1, j = r & 3;
      cmul(sv[k][t][j], cin[k][0], cin[k][1], tac[2 + k][t][j]);
    }
    if constexpr (sl == SFIN - 8) {
      const f32x2 a = CPAIR(cs48, 13), p0 = CPAIR(cs48, (w & 1) ? 10 : 5), p1 = CPAIR(cs48, (w & 2) ? 11 : 5), p2 = CPAIR(cs48, (w & 4) ? 12 : 5);
      c256[0] = a[0]; c256[1] = a[1]; cpw[0][0] = p0[0]; cpw[0][1] = p0[1]; cpw[1][0] = p1[0]; cpw[1][1] = p1[1]; cpw[2][0] = p2[0]; cpw[2][1] = p2[1];
    }
    if constexpr (sl >= SFIN - 6 && sl < SFIN - 3) {
      constexpr int k = sl - (SFIN - 6);
      unsigned lz = l16; tie(lz);
      const f32x2 a1 = CPAIR(csa + k * 2048, 0), pq = CPAIR(csa + k * 2048, 5 + (lz >> 8)), a16 = CPAIR(csa + k * 2048, 9);
      cc1[k][0] = a1[0]; cc1[k][1] = a1[1]; cin[k][0] = pq[0]; cin[k][1] = pq[1]; ca4[k][0] = a16[0]; ca4[k][1] = a16[1];
    }
    if constexpr (sl >= SFIN && sl < SFIN + 6) {
      constexpr int r = sl - SFIN + 1;
      float n = __uint_as_float(eg[r][0]); cmac(n, c256[0], c256[1], crt); crt = n;
    }
    if constexpr (sl == SFIN + 6) { float n; cmul(n, cpw[0][0], cpw[0][1], crt); crt = n; }
    if constexpr (sl == SFIN + 7) { float n; cmul(n, cpw[1][0], cpw[1][1], crt); crt = n; }
    if constexpr (sl == SFIN + 8) cmac(wpre, cpw[2][0], cpw[2][1], crt);
    if constexpr (sl == SFIN + 9) {
      unsigned lz = l16; tie(lz);
      const unsigned scr = lds0 + LDS_SCR + w * 3072, q = lz >> 8, li = (lz >> 4) & 15;
      fwa = scr + li * 32 + q * 8;
      fra = scr + (q >> 1) * 512 + (8 * (q & 1) + (li >> 2)) * 32 + (li & 3) * 8;
      fvo = (q >> 1) * HTB + ((q & 1) * 16 + li) * 16;
      tie(fwa); tie(fra); tie(fvo);
    }
    if constexpr (sl == SFIN + 10) {
      const unsigned pu = __float_as_uint(wpre);
      const auto s16 = __builtin_amdgcn_permlane16_swap(pu, pu, false, false);
      cz[0] = __uint_as_float(s16[0]); cz[1] = __uint_as_float(s16[1]);
    }
    if constexpr (sl == SFIN + 11) { const unsigned pu = __float_as_uint(cz[0]); const auto sw = __builtin_amdgcn_permlane32_swap(pu, pu, false, false); cz[0] = __uint_as_float(sw[0]); cz[2] = __uint_as_float(sw[1]); }
    if constexpr (sl == SFIN + 12) { const unsigned pu = __float_as_uint(cz[1]); const auto sw = __builtin_amdgcn_permlane32_swap(pu, pu, false, false); cz[1] = __uint_as_float(sw[0]); }
    if constexpr (sl >= SFIN + 13 && sl < SFIN + 16) { constexpr int k = (sl - (SFIN + 13)) == 1 ? 2 : ((sl - (SFIN + 13)) == 2 ? 1 : 0); float n; cmul(n, cin[k][0], cin[k][1], cz[k]); cz[k] = n; }
    if constexpr (sl >= SFIN + 16 && sl < SFIN + 19) { constexpr int k = (sl - (SFIN + 16)) == 1 ? 2 : ((sl - (SFIN + 16)) == 2 ? 1 : 0); cmac(spre[k][1], ca4[k][0], ca4[k][1], cz[k]); }
    if constexpr (sl == SFIN + 19) { asm volatile("v_add_f32 %0, %0, %1" : "+v"(spre[0][0]) : "v"(cz[0])); asm volatile("v_add_f32 %0, %0, %1" : "+v"(spre[1][0]) : "v"(cz[1])); asm volatile("v_add_f32 %0, %0, %1" : "+v"(spre[2][0]) : "v"(cz[2])); }
    if constexpr (sl >= SFIN + 20 && sl < SFIN + 44) {
      constexpr int r = sl - (SFIN + 20), j = r / 6, k = (r % 6) >> 1, t = (r & 1) ^ 1;
      cmac(sv[k][t][j], cc1[k][0], cc1[k][1], j == 0 ? spre[k][t] : sv[k][t][j > 0 ? j - 1 : 0]);
    }
    if constexpr (sl >= SFIN + 44 && sl < SFIN + 50) {
      constexpr int r = sl - (SFIN + 44), k = r >> 1, ph = r & 1;
      v_cvt(pkd[0], sv[k][ph][0], sv[k][ph][1]); v_cvt(pkd[1], sv[k][ph][2], sv[k][ph][3]);
      *(LAS u32x2*)(fwa + k * 1024 + ph * 512) = (u32x2){pkd[0], pkd[1]};
    }
    if constexpr (sl >= SFIN + 50 && sl < SFIN + 53) { constexpr int k = sl - (SFIN + 50); qs[k] = __builtin_bit_cast(f32x4, trfrag<128>(fra + k * 1024)); }
    if constexpr (sl >= SFIN + 55 && sl < SFIN + 58) {
      constexpr int k = sl - (SFIN + 55);
      st16_sc1(rsW, (k == 2 && cgl) ? 0x70000000u : fvo, htw + (k < 2 ? (unsigned)((18 + cgl) * 1024 + k * 512) : 22u * 1024u), __builtin_bit_cast(u32x4, qs[k]));
    }
    if constexpr (sl == SRES - 4) {
#pragma unroll
      for (int r = 0; r < 3; r++) { const f32x2 f = *(LAS f32x2*)(lds0 + LDS_ST + sx * 512 + ept[r] * 16 + 8); rst[r][0] = f[0]; rst[r][1] = f[1]; }
    }
    if constexpr (res_at(sl) >= 0) {
      constexpr int e = res_at(sl) / 5, ph = res_at(sl) % 5, r = e >> 1, h = e & 1;
      if constexpr (ph == 0) {
        asm volatile("v_lshlrev_b32 %0, 16, %1" : "=v"(res[r][2 * h]) : "v"(epv[2 * r][h])); asm volatile("v_and_b32 %0, 0xffff0000, %1" : "=v"(res[r][2 * h + 1]) : "v"(epv[2 * r][h]));
      }
      if constexpr (ph == 1) {
        asm volatile("v_add_f32 %0, %1, %2" : "=v"(hh[0]) : "v"(res[r][2 * h]), "v"(rst[r][0])); asm volatile("v_add_f32 %0, %1, %2" : "=v"(hh[1]) : "v"(res[r][2 * h + 1]), "v"(rst[r][0]));
        v_cvt(pkd[2], hh[0], hh[1]);
      }
      if constexpr (ph == 2) {
        asm volatile("v_lshlrev_b32 %0, 16, %1" : "=v"(hh[0]) : "v"(pkd[2])); asm volatile("v_and_b32 %0, 0xffff0000, %1" : "=v"(hh[1]) : "v"(pkd[2]));
        v_muli(hh[0], rst[r][1]); v_muli(hh[1], rst[r][1]); v_cvt(pkd[2], hh[0], hh[1]);
      }
      if constexpr (ph == 3) {
        asm volatile("v_lshlrev_b32 %0, 16, %1" : "=v"(hh[0]) : "v"(pkd[2])); asm volatile("v_and_b32 %0, 0xffff0000, %1" : "=v"(hh[1]) : "v"(pkd[2]));
        asm volatile("v_lshlrev_b32 %0, 16, %1" : "=v"(te) : "v"(epv[2 * r + 1][h])); asm volatile("v_and_b32 %0, 0xffff0000, %1" : "=v"(pp) : "v"(epv[2 * r + 1][h]));
        v_muli(hh[0], te); v_muli(hh[1], pp);
      }
      if constexpr (ph == 4) {
        v_cvt(pkd[2], hh[0], hh[1]);
        asm volatile("v_lshlrev_b32 %0, 16, %1" : "=v"(hh[0]) : "v"(pkd[2])); asm volatile("v_and_b32 %0, 0xffff0000, %1" : "=v"(hh[1]) : "v"(pkd[2]));
        asm volatile("v_add_f32 %0, %0, %1" : "+v"(res[r][2 * h]) : "v"(hh[0])); asm volatile("v_add_f32 %0, %0, %1" : "+v"(res[r][2 * h + 1]) : "v"(hh[1]));
      }
    }
    if constexpr (sl >= 2 * late_g(9) && sl < 2 * GEND) {
      constexpr int r = sl - 2 * late_g(9), db = r / 14, ph = r % 14;
      if constexpr (db < 8) {
        if constexpr (ph == 0) { unsigned a, c; v_cvt(a, ac2[db][0][0], ac2[db][0][1]); v_cvt(c, ac2[db][0][2], ac2[db][0][3]); pkd[0] = a; pkd[1] = c; }
        if constexpr (ph == 1) { unsigned a, c; v_cvt(a, ac2[db][1][0], ac2[db][1][1]); v_cvt(c, ac2[db][1][2], ac2[db][1][3]); st16_sc1(rsW, l16, pb + db * 1024, (u32x4){pkd[0], pkd[1], a, c}); }
      }
    }
  };

  unsigned fa[2], eb[2], a2b[3];
  s16x4 flo;
  auto trl = [&](unsigned a) { return __builtin_amdgcn_ds_read_tr16_b64_v4i16((LAS s16x4*)(a)); };
  auto join = [&](s16x4 a, s16x4 c) { return __builtin_bit_cast(bf16x8, __builtin_shufflevector(a, c, 0, 1, 2, 3, 4, 5, 6, 7)); };
  auto frag = [&](auto Gc, auto Pc) {
    constexpr int G = decltype(Gc)::value, part = decltype(Pc)::value;
    if constexpr (G < GT) {
      constexpr LF f = l_frag(G);
      constexpr int o = (f.ks % 3) * 8192 + (f.blk >> 2) * 4096;
      if constexpr (part == 0) flo = trl(ab[f.blk & 3] + o); else fr[G % FR] = join(flo, trl(ab[f.blk & 3] + o + 512));
    } else if constexpr (G < G2) {
      constexpr int ks = t_ks(G - GT), blk = t_blk(G - GT), uo = (ks / 6) * 30720, k6 = ks % 6;
      if constexpr (blk < 2) {
        constexpr int o = uo - (ks >= 12 ? 30720 : 0) + (k6 * 2 + blk) * 1024;
        if constexpr (part == 0) flo = trl(fa[0] + o); else fr[G % FR] = join(flo, trl(fa[1] + o));
      } else if constexpr (part == 1) fr[G % FR] = ldsfrag(eb[k6 & 1] + uo - (ks >= 12 ? 30720 : 0) + 12288 + (k6 >> 1) * 6144 + (blk - 2) * 2048);
    } else if constexpr (part == 1) {
      constexpr int U = (G - G2) / 18, g = (G - G2) % 18;
      fr[G % FR] = ldsfrag(p2_off(U, g) >= 65536 ? (p2_off(U, g) >= 131072 ? a2b[2] + (p2_off(U, g) - 131072) : a2b[1] + (p2_off(U, g) - 65536)) : a2b[0] + p2_off(U, g));
    }
  };
  auto addr_step = [&](auto Gc) {
    constexpr int G = decltype(Gc)::value;
    if constexpr (G < GT) {
      constexpr int u = l_frag(G).unit, pu = l_prev_unit(G), k = l_frag(G).blk & 3;
      if constexpr (u != pu) { ab[k] += (unsigned)(p1_off(u) - p1_off(pu)); tie(ab[k]); }
    }
    if constexpr (G == GT) {
      unsigned l_ = l16; tie(l_); const int tl = (int)(l_ >> 4) + 64 * w;
      const int lane = tl & 63, q = lane >> 4, li = lane & 15;
      const unsigned e0 = li * 128 + ((q ^ (li >> 1)) << 4);
      eb[0] = lds0 + p1_off(12) + e0; eb[1] = lds0 + p1_off(12) + (e0 ^ 64u); tie(eb[0]); tie(eb[1]);
    }
    if constexpr (G == GT + 36) { eb[0] += 30720; eb[1] += 30720; tie(eb[0]); tie(eb[1]); }
    if constexpr (G == GT + 78) { fa[0] += 30720; fa[1] += 30720; tie(fa[0]); tie(fa[1]); }
    if constexpr (G == G2) { a2b[0] = lds0 + l16; a2b[1] = lds0 + l16 + 65536; a2b[2] = lds0 + l16 + 131072; tie(a2b[0]); tie(a2b[1]); tie(a2b[2]); }
  };
  auto mma = [&](auto Gc, auto Tc) {
    constexpr int G = decltype(Gc)::value, t = decltype(Tc)::value;
    const bf16x8 a = fr[G % FR];
    if constexpr (G < GT) {
      constexpr LF f = l_frag(G);
      mfma<f.ks == 0>(acc[f.blk][t], a, xf[f.ks][t]);
    } else if constexpr (G < G2) {
      constexpr int ks = t_ks(G - GT), blk = t_blk(G - GT);
      if constexpr (blk < 2) mfma<ks == 0>(tac[blk][t], a, xf[ks][t]);
      else mfma<ks == 0>(tac[blk][t], xf[ks][t], a);
    } else if constexpr (G < late_g(0)) {
      constexpr int U = (G - G2) / 18, g = (G - G2) % 18, kk = g / 9, db = g % 9;
      mfma<(U == 0 && kk == 0)>(ac2[db][t], a, hf[U % HR][kk][t]);
    } else {
      constexpr int f = G - late_g(0), db = f / 7, kk = f % 7;
      mfma<false>(ac2[db][t], a, hl[kk][t]);
    }
  };

  auto sync = [&](auto Gc) {
    constexpr int G = decltype(Gc)::value;
    if constexpr (G < GT && G % 24 == (G < 24 ? 16 : SY1)) {
      constexpr int V = G / 24;
      constexpr int need = V + 1 < 12 ? (l_dbl(V + 1) ? (((V + 1) & 1) ? -1 : V + 2) : V + 1) : 12;
      constexpr int ng = need >= 0 ? S1.after(OP_G, need, G) : 63;
      constexpr int ng2 = V == 2 ? S1.after(OP_G, 4, G) : (V == 8 ? S1.after(OP_G, 11, G) : 63);
      constexpr int ns = V == 10 ? cmin(S1.after(OP_ST, 0, G), S1.after(OP_ST, 9, G)) : 63;
      waitv<cmin(ng, ng2, ns)>();
      if constexpr (V == 3 || V == 9) asm volatile("s_waitcnt lgkmcnt(0)" ::: "memory");
      if constexpr (V == 10) setflag(rsW, l16 == 0, flw + cgl * 4, tag);
      bar();
      if constexpr (V == 10) setflag(rsW, l16 == 0 && w == 1, (unsigned)FL_W + (unsigned)(sx * 32 + idx) * 4u, tag);
      SCHED0;
    }
    if constexpr (G == gt(0, 18)) { waitv<S1.after(OP_FL, 4, G)>(); chk_flag(IC<4>{}); SCHED0; }
    if constexpr (G == gl(2, 14)) {
      waitv<S1.after(OP_SG, 0, G)>();
      for (unsigned it = 0; it < (1u << 18); ++it) {
        tie(sgr);
        if (__all(sgr[1] == tag)) break;
        __builtin_amdgcn_s_sleep(1);
        asm volatile("global_load_dwordx2 %0, %1, %2 sc1" : "+v"(sgr) : "v"((l16 >> 1) & 248), "s"(ws + FL_S + (size_t)(rt * 256 + 32 * w) * 8) : "memory");
        waitv<0>();
      }
      tie(sgr);
      const float den = bhi(sgr[0]), rs = __builtin_amdgcn_rcpf(den);
      *(LAS f32x4*)(lds0 + LDS_ST + w * 512 + (l16 & 496)) = (f32x4){rs * -1.44269504089f, den * den, -blo(sgr[0]), rs};
      SCHED0;
    }
    if constexpr (G == gl(3, 1)) { waitv<S1.after(OP_G, 5, G)>(); bar(); SCHED0; }
    if constexpr (G == g2(7, 16)) {
        waitv<S1.after(OP_HL, 0, G)>();
        tie(hl[0][0]); tie(hl[0][1]); tie(hl[1][0]); tie(hl[1][1]);
        for (unsigned it = 0; it < (1u << 16); ++it) {
          unsigned m = 0;
#pragma unroll
          for (int n = 4; n < 14; n++) { tie(hl[n >> 1][n & 1]); const u32x4 v = __builtin_bit_cast(u32x4, hl[n >> 1][n & 1]); asm("v_max3_u32 %0, %0, %1, %2" : "+v"(m) : "v"(v[0]), "v"(v[1])); asm("v_max3_u32 %0, %0, %1, %2" : "+v"(m) : "v"(v[2]), "v"(v[3])); }
          if (__all(m != ~0u)) break;
          __builtin_amdgcn_s_sleep(4);
          sfor<4, 14>([&](auto Nc) { constexpr int n = decltype(Nc)::value;
            gld16o_io<((n >> 1) - 4) * 1024>(hl[n >> 1][n & 1], hb0 + (size_t)(n & 1) * HTB + 16384 + 4096, l16); });
          waitv<0>();
        }
#pragma unroll
        for (int n = 4; n < 14; n++) tie(hl[n >> 1][n & 1]);
        SCHED0;
    }
    if constexpr (G == SRES / 2 - 2) { waitv<S1.after(OP_EP, 0, G)>();
#pragma unroll
      for (int n = 0; n < 6; n++) tie(epv[n]);
      SCHED0; }
    if constexpr (G == GSYC) { asm volatile("s_waitcnt lgkmcnt(0)" ::: "memory"); bar(); SCHED0; }
    if constexpr (G == gt(0, SYT)) { waitv<S1.after(OP_G, 13, G)>(); bar(); SCHED0; }
    if constexpr (G == gt(1, SYT)) { waitv<cmin(S1.after(OP_G, 14, G), S1.after(OP_FL, 0, G))>(); bar(); chk_flag(IC<0>{}); SCHED0; }
    if constexpr (G == gt(2, 2)) { waitv<S1.after(OP_ST, 1, G)>(); setflag(rsW, l16 == 0, flw + 128 + cgl * 4, tag); SCHED0; }
    if constexpr (G == gt(2, 27)) {
      waitv<cmin(S1.after(OP_A, 0, G), S1.after(OP_B, 0, G))>(); htie(IC<0>{});
      bar(); SCHED0;
    }
    if constexpr (G >= G2 && (G - G2) % 18 == SY2 && (G - G2) / 18 < 11) {
      constexpr int U = (G - G2) / 18;
      constexpr int nab = cmin(S1.after(OP_A, U + 1, G), U + 1 < 8 ? S1.after(OP_B, U + 1, G) : 63);
      constexpr int nfl = U == 1 ? S1.after(OP_FL, 1, G) : (U == 3 ? cmin(S1.after(OP_ST, 2, G), S1.after(OP_EG, 0, G)) : (U == 5 ? S1.after(OP_FL, 2, G) : 63));
      static_assert(UFIN == 3, "");
      if constexpr (U == 0) asm volatile("s_waitcnt lgkmcnt(0)" ::: "memory");
      waitv<cmin(nab, nfl)>();
      if constexpr (U + 1 < 8) htie(IC<U + 1>{});
      if constexpr (U == UFIN) {
        for (unsigned it = 0; it < (1u << 18); ++it) {
          unsigned d = 0;
#pragma unroll
          for (int r = 0; r < 7; r++) { tie(eg[r]); d |= eg[r][1] ^ tag; }
          if (__all(d == 0)) break;
          __builtin_amdgcn_s_sleep(1);
          sfor<0, 7>([&](auto Rc) { gld8o<decltype(Rc)::value * 384, true>(eg[decltype(Rc)::value], ws + FL_E + (size_t)(cg * 16 + rt + 1) * 384, cla2); });
          waitv<0>();
        }
#pragma unroll
        for (int r = 0; r < 7; r++) tie(eg[r]);
        crt = __uint_as_float(eg[0][0]);
        setflag(rsW, l16 == 0, flw + 256 + cgl * 4, tag);
      }
      bar();
      if constexpr (U == 1) chk_flag(IC<1>{});
      if constexpr (U == 5) chk_flag(IC<2>{});
      if constexpr (U == 0) {
        const f32x2 c = CPAIR(cs48, 10);
        c32[0] = c[0]; c32[1] = c[1];
#pragma unroll
        for (int k = 0; k < 8; k++) tw[k] = *(LAS float*)(lds0 + LDS_WTOT + (w + 1 + k) * 192 + cla);
      }
      SCHED0;
    }
  };

  unsigned wm[3];
  {
    unsigned l_ = l16; tie(l_); const int tl = (int)(l_ >> 4) + 64 * w;
    const int lane = tl & 63, q = lane >> 4, li = lane & 15;
    unsigned ep; u32x4 xr0, xr1;
    issue_at(IC<-3>{});
    bar();
    gld4_sc1(ep, ws + FL_EPOCH, l16 & 0);
    const int row = b * 8 + w;
    gld16(xr0, x + (size_t)row * DM, l16);
    gld16(xr1, x + (size_t)row * DM, 1024u + (l16 & 112));
    {
      const int chn = 8 * (9 * sx + (lane < 16 ? 2 * cgl + (lane >> 3) : 8)) + (lane & 7);
      asm volatile("global_load_ushort %0, %1, %2" : "=v"(pl[0]) : "v"(chn * 2), "s"(lre) : "memory");
      asm volatile("global_load_ushort %0, %1, %2" : "=v"(pl[1]) : "v"(chn * 2), "s"(lim) : "memory");
      asm volatile("global_load_ushort %0, %1, %2" : "=v"(pl[2]) : "v"(chn * 2), "s"(logdt) : "memory");
    }
    SCHED0;
    issue_at(IC<-1>{});
    {
      const int n = tl, r = 72 * rt + n / 6, c6 = n % 6;
      const char* p0 = (const char*)((c6 & 1) ? wu : wg) + (size_t)r * (FF * 2) + (c6 < 4 ? cg * 256 + (c6 >> 1) * 128 : fcol);
      const int m = rt * 54 + (tl - 432 < 54 ? tl - 432 : 0), rb = m / 9;
      const char* p1 = (const char*)((rb & 8) ? bim : bre) + (size_t)(8 * (9 * sx + ((rb >> 4) < 2 ? 2 * cgl + (rb >> 4) : 8)) + (rb & 7)) * (DM * 2) + (m - 9 * rb) * 128;
      asm volatile("global_load_dword %0, %1, off" : "=v"(wm[0]) : "v"(tl < 432 ? p0 : p1) : "memory");
    }
    SCHED0;
    waitv<6>();
    tie(ep); tie(xr0); tie(xr1); tie(pl[0]); tie(pl[1]); tie(pl[2]);
    tag = (unsigned)__builtin_amdgcn_readfirstlane(ep) + 1u;
    STAMP(12);
    {
      float v[16];
      const bool ex = lane < 8;
#pragma unroll
      for (int k = 0; k < 4; k++) { v[2 * k] = blo(xr0[k]); v[2 * k + 1] = bhi(xr0[k]); v[8 + 2 * k] = ex ? blo(xr1[k]) : 0.f; v[9 + 2 * k] = ex ? bhi(xr1[k]) : 0.f; }
      float sm = 0.f;
#pragma unroll
      for (int k = 0; k < 16; k++) sm += v[k];
      sm = wave_sum(sm);
      const float mean = sm * (1.0f / DM);
      float qq = 0.f;
#pragma unroll
      for (int k = 0; k < 16; k++) { const float d = (k < 8 || ex) ? v[k] - mean : 0.f; qq += d * d; }
      qq = wave_sum(qq);
      const unsigned md = pk2(mean, sqrtf(rbf(rbf(qq * (1.0f / DM)) + 1e-5f)));
      const unsigned zo = (w == 1 && lane < 48) ? (unsigned)FL_E + (unsigned)((cg * 16 + rt) * 384 + lane * 8) : 0x7ffffff0u;
      __builtin_amdgcn_raw_buffer_store_b64((u32x2){lane == 63 ? md : 0u, tag}, rsW, lane == 63 ? (unsigned)FL_S + row * 8u : zo, 0, 16);
    }
    if (w == 0 && lane < 24) {
      const float lr = bf2f((u16)pl[0]), lm = bf2f((u16)pl[1]);
      const float dt = rbf(__builtin_amdgcn_exp2f(bf2f((u16)pl[2]) * 1.44269504089f));
      const float rev = __builtin_amdgcn_fractf(dt * lm * 0.15915494309f);
      const float er = __builtin_amdgcn_exp2f(dt * lr * 1.44269504089f), ar = er * __builtin_amdgcn_cosf(rev), ai = er * __builtin_amdgcn_sinf(rev);
      const float nr = ar - 1.0f, ni = ai, dn = lr * lr + lm * lm;
      const unsigned tre = lds0 + LDS_CST + (lane >> 3) * 2048 + (lane & 7) * 8;
      auto put = [&](int k, float pr, float pi) {
        if (k >= 1 && k <= 4) { *(LAS f32x2*)(tre + k * 128) = (f32x2){2.f * pr, -2.f * pi}; *(LAS f32x2*)(tre + k * 128 + 64) = (f32x2){-2.f * pr, -2.f * pi}; }
        else { *(LAS f32x2*)(tre + k * 128) = (f32x2){pr, pi}; *(LAS f32x2*)(tre + k * 128 + 64) = (f32x2){pr, -pi}; }
      };
      put(0, ar, ai); put(5, 1.f, 0.f);
      float cr = (nr * lr + ni * lm) / dn, cm_ = (ni * lr - nr * lm) / dn;
#pragma unroll
      for (int k = 1; k < 5; k++) { put(k, cr, cm_); const float t = cr * ar - cm_ * ai; cm_ = cr * ai + cm_ * ar; cr = t; }
      const float a2r = ar * ar - ai * ai, a2i = 2.f * ar * ai;
      const float a4r = a2r * a2r - a2i * a2i, a4i = 2.f * a2r * a2i;
      float pr = a4r * a4r - a4i * a4i, pi = 2.f * a4r * a4i;
      put(6, a4r, a4i); put(7, pr, pi); put(8, pr * a4r - pi * a4i, pr * a4i + pi * a4r);
#pragma unroll
      for (int k = 9; k < 14; k++) { const float t = pr * pr - pi * pi; pi = 2.f * pr * pi; pr = t; put(k, pr, pi); }
    }
    SCHED0;
    STAMP(13);
  }
  auto pack = [&]() {
    unsigned l_ = l16; tie(l_); const int tl = (int)(l_ >> 4) + 64 * w;
    const int lane = tl & 63, q = lane >> 4, li = lane & 15;
        const unsigned ra = lds0 + LDS_PK + (w < 6 ? w : 0) * 4096 + (4 * q + (li >> 2)) * 128 + (lane & 3) * 8;
    const int fa_ = pj < 16 ? 1 : 7, fb_ = (pj < 16 ? 9 * pj : 128 + pj) + sx * (KS2 * 9);
    const unsigned vo = widx < 162 ? l_ : 0x70000000u;
#pragma unroll
    for (int n = 0; n < 4; n++) {
      const u32x4 f = __builtin_bit_cast(u32x4, trfrag<2048>(ra + n * 32));
      const int d16 = 4 * pln + n, dt = (d16 * 57) >> 9, db = d16 - 9 * dt;
      st16_sc1(rsW, vo, (unsigned)WS_WT + (unsigned)(dt * (NKG * 9) + fb_ + fa_ * db) * 1024u, f);
    }
    asm volatile("s_waitcnt lgkmcnt(0)" ::: "memory");
  };
  auto pack_c = [&]() {
    unsigned l_ = l16; tie(l_); tie(cfr);
    const int dt = (cd16 * 57) >> 9, db = cd16 - 9 * dt;
    if (2 * cjj + (l_ >> 9) >= 9) cfr = (u32x4){0, 0, 0, 0};
    st16_sc1(rsW, widx < 180 ? l_ : 0x70000000u, (unsigned)WS_WT + (unsigned)((dt * NKG + sx * KS2) * 9 + 146 + cjj + 7 * db) * 1024u, cfr);
  };
  waitv<S1.after(OP_G, 0, 0)>(); xconv(IC<0>{}); xconv(IC<1>{});
  STAMP(14);
  bar();
  SCHED0;
  STAMP(1);
  frag(IC<0>{}, IC<0>{}); frag(IC<0>{}, IC<1>{}); frag(IC<1>{}, IC<0>{}); frag(IC<1>{}, IC<1>{});
  SCHED0;

  bf16x8 cpb[2];
  unsigned csp = 0, csq = 0, csr = 0;
  auto extra = [&](auto Gc) {
    constexpr int G = decltype(Gc)::value;
    if constexpr (xt_at(G) >= 0) { waitv<S1.after(OP_X, xt_at(G), G + 1)>(); xconv(IC<xt_at(G)>{}); SCHED0; }
    if constexpr (G == csx_g(0, 0) - 3 || G == csx_g(6, 0) - 3) {
      constexpr int cur = p1_off(l_unit0(G + PD));
      csp = (ab[0] ^ (unsigned)((w & 3) << 5)) + (unsigned)((w >> 2) * 4096 - cur); tie(csp);
      const u32x4 so = {0x3f803f80u, 0x3f803f80u, 0x3f803f80u, 0x3f803f80u};
      selA = __builtin_bit_cast(bf16x8, so); tie(selA);
      SCHED0;
    }
    if constexpr (G == csx_g(12, 0) - 3) {
      unsigned l_ = l16; tie(l_); const int tl = (int)(l_ >> 4) + 64 * w;
      const int lane = tl & 63, q = lane >> 4, li = lane & 15;
      const u32x4 so = {0x3f803f80u, 0x3f803f80u, 0x3f803f80u, 0x3f803f80u}, sz = {0, 0, 0, 0};
      selA = __builtin_bit_cast(bf16x8, so); selB = __builtin_bit_cast(bf16x8, w < 6 ? so : sz); tie(selA); tie(selB);
      csp = lds0 + p1_off(12) + 12288 + (w < 6 ? w : 0) * 1024 + (li & 7) * 128 + (2 * q + (li >> 3)) * 16;
      const unsigned f0 = lds0 + p1_off(12) + (unsigned)((((8 * q + (li >> 2)) ^ ((q & 1) << 2)) << 5) + 8 * (lane & 3));
      csq = f0 + w * 1024; csr = f0 + (w < 6 ? (w >> 1) * 30720 : 0) + (8 + (w & 1)) * 1024;
      fa[0] = f0; fa[1] = f0 ^ 128u;
      tie(csp); tie(csq); tie(csr); tie(fa[0]); tie(fa[1]);
      SCHED0;
    }
    if constexpr (csx_at(G) >= 0) {
      constexpr int v = csx_at(G) >> 2, n = csx_at(G) & 3;
      const bf16x8 cpc = cpb[G & 1];
      if constexpr (v < 12) mfma<(v % 6 == 0 && n == 0)>(csa0, cpc, selA);
      else if constexpr (v < 15) mfma<(v == 12 && n == 0)>(csa0, selA, cpc);
      else mfma<(v == 15 && n == 0)>(csa1, cpc, v == 16 ? selB : selA);
      SCHED0;
    }
    if constexpr (csx_at(G + 2) >= 0) {
      constexpr int v = csx_at(G + 2) >> 2, n = csx_at(G + 2) & 3;
      bf16x8& cpc = cpb[G & 1];
      if constexpr (v < 12) { unsigned a = csp + p1_off(v); cpc = trfrag<512>(a + n * 8192); }
      else if constexpr (v < 15) { unsigned a = csp + (v - 12) * 30720; cpc = ldsfrag(a + n * 6144); }
      else {
        const unsigned o = v == 15 ? csq + n * 30720 : csr + n * 2048;
        s16x4 a = __builtin_amdgcn_ds_read_tr16_b64_v4i16((LAS s16x4*)(o));
        s16x4 c = __builtin_amdgcn_ds_read_tr16_b64_v4i16((LAS s16x4*)(o ^ 128u));
        cpc = __builtin_bit_cast(bf16x8, __builtin_shufflevector(a, c, 0, 1, 2, 3, 4, 5, 6, 7));
      }
      SCHED0;
    }
  };
  auto cs_flush = [&](auto Gc) {
    constexpr int G = decltype(Gc)::value;
    if constexpr (G == CSF[0] || G == CSF[1]) {
      unsigned lz = l16; tie(lz);
      if ((lz & 240) == 0) *(LAS f32x4*)(lds0 + LDS_CS + (G == CSF[1] ? 512 : 0) + w * 64 + (lz >> 8) * 16) = csa0;
      SCHED0;
    }
    if constexpr (G == CSF[2]) {
      unsigned lz = l16; tie(lz); tie(csa0);
      const float rsum = csa0[0] + dpp_ror8(csa0[0]);
      if (lz < 128) *(LAS float*)(lds0 + LDS_CS + 1024 + w * 32 + (lz >> 2)) = rsum;
      if ((lz & 240) == 0) *(LAS f32x4*)(lds0 + LDS_CS + 1280 + w * 64 + (lz >> 8) * 16) = csa1;
      SCHED0;
    }
  };

  auto group = [&](auto Gc) {
    constexpr int G = decltype(Gc)::value;
    if constexpr (G == 144) { STAMP(2); }
    if constexpr (G == GT) { STAMP(3); }
    if constexpr (G == G2) { STAMP(4); STAMP(5); }
    if constexpr (G == g2(3, 0)) { STAMP(6); }
    if constexpr (G == g2(8, 0)) { STAMP(7); }
    if constexpr (G == GPK) { waitv<S1.after(OP_PK, 0, G)>(); pack(); SCHED0; }
    if constexpr (G == GPC) { waitv<S1.after(OP_PK, 1, G)>(); pack_c(); SCHED0; }
    if constexpr (G == gl(1, 4)) { tie(wm[0]); tie(cw); }
    cs_flush(Gc);
    sync(Gc);
    issue_at(IC<2 * G>{});
    extra(Gc);
    mma(Gc, IC<0>{}); SCHED0;
    if constexpr (G + PD < GEND) { addr_step(IC<G + PD>{}); frag(IC<G + PD>{}, IC<0>{}); SCHED0; }
    side(IC<2 * G>{}); SCHED0;
    if constexpr (G + PD < GEND) { frag(IC<G + PD>{}, IC<1>{}); SCHED0; }
    mma(Gc, IC<1>{}); SCHED0;
    side(IC<2 * G + 1>{}); SCHED0;
  };
  sfor<0, 13>([&](auto Bc) { sfor<0, 45>([&](auto Jc) { group(IC<decltype(Bc)::value * 45 + decltype(Jc)::value>{}); }); });
  STAMPC(27);
  STAMP(8);

  asm volatile("s_nop 7\n\ts_nop 1" : "+v"(ac2[8][0]), "+v"(ac2[8][1]));
  unsigned l_ = l16; tie(l_); const int tz = (int)(l_ >> 4) + 64 * w;
  const int lane = tz & 63, q = lane >> 4, li = lane & 15;
  st16_sc1(rsW, l16, pb + 8 * 1024, (u32x4){pk2(ac2[8][0][0], ac2[8][0][1]), pk2(ac2[8][0][2], ac2[8][0][3]), pk2(ac2[8][1][0], ac2[8][1][1]), pk2(ac2[8][1][2], ac2[8][1][3])});
  SCHED0;
  const char* ib = ws + (size_t)pobase + (size_t)w * 9216 + 4096;
  waitv<0>();
  setflag(rsW, l16 == 0, (unsigned)FL_P + (unsigned)(((rt * 4 + cgl) * 8 + w) * 8 + sx) * 64u, tag);
  bar();
  STAMP(9);
  {
    constexpr int LROW = 304, LSRC = 32 * LROW;
    spin_flag(ws + FL_P + (size_t)(((rt * 4 + cgl) * 8 + sx) * 8 + w) * 64, l16 & 0, tag);
    sfor<0, 8>([&](auto Dc) { gld16o_sc1<(decltype(Dc)::value - 4) * 1024>(pv[decltype(Dc)::value], ib, l16); });
    gld16o_sc1<0>(pv[8], ib + 4096, l16);
    waitv<0>();
#pragma unroll
    for (int db = 0; db < 9; db++) {
      tie(pv[db]);
      *(LAS u32x2*)(lds0 + w * LSRC + li * LROW + db * 32 + q * 8) = (u32x2){pv[db][0], pv[db][1]};
      *(LAS u32x2*)(lds0 + w * LSRC + (16 + li) * LROW + db * 32 + q * 8) = (u32x2){pv[db][2], pv[db][3]};
    }
    asm volatile("s_waitcnt vmcnt(0) lgkmcnt(0)" ::: "memory");
    bar();
    STAMP(10);
#pragma unroll
    for (int r = 0; r < 3; r++) {
      const int e = tz + 512 * r;
      if (e < 1152) {
        const unsigned t = ept[r], d8 = epd[r];
        f32x4 o = {res[r][0], res[r][1], res[r][2], res[r][3]};
#pragma unroll
        for (int sr = 0; sr < 8; sr++) {
          const u32x2 p = *(LAS u32x2*)(lds0 + sr * LSRC + t * LROW + d8);
          o += (f32x4){blo(p[0]), bhi(p[0]), blo(p[1]), bhi(p[1])};
        }
        *(f32x4*)((char*)(out + (size_t)erow * DM + cgl * 144) + t * (DM * 4) + d8 * 2) = o;
      }
    }
    asm volatile(".Lk_code_to:");
    if (b == 0 && w == 0) {
#pragma unroll
      for (int k = 0; k < 4; k++) spin_flag(ws + FL_W + k * 256, l16 >> 2, tag);
      setflag(rsW, l16 == 0, (unsigned)FL_EPOCH, tag);
    }
    STAMP(11);
    STAMP_DUMP();
  }
}

extern "C" void kernel_launch(void* const* d_in, const int* in_sizes, int n_in, void* d_out, int out_size, void* d_ws, size_t ws_size,
                              hipStream_t stream) {
  const u16* x = (const u16*)d_in[0]; const u16* lre = (const u16*)d_in[1]; const u16* lim = (const u16*)d_in[2];
  const u16* bre = (const u16*)d_in[3]; const u16* bim = (const u16*)d_in[4]; const u16* cre = (const u16*)d_in[5]; const u16* cim = (const u16*)d_in[6];
  const u16* logdt = (const u16*)d_in[7]; const u16* dsk = (const u16*)d_in[8];
  const u16* wg = (const u16*)d_in[10]; const u16* wu = (const u16*)d_in[11]; const u16* wd = (const u16*)d_in[12];
  char* ws = (char*)d_ws;
  (void)hipFuncSetAttribute((const void*)k_main, hipFuncAttributeMaxDynamicSharedMemorySize, LDS_TOT);
  k_main<<<256, 512, LDS_TOT, stream>>>(x, lre, lim, bre, bim, cre, cim, logdt, dsk, wg, wu, wd, ws, (float*)d_out);
}
```

```cpp
#include <hip/hip_runtime.h>
#include <stdint.h>
#include <type_traits>

typedef unsigned short u16;
typedef unsigned long long u64;
typedef __bf16 bf16x8 __attribute__((ext_vector_type(8)));
typedef __bf16 bf16x2 __attribute__((ext_vector_type(2)));
typedef float f32x4 __attribute__((ext_vector_type(4)));
typedef float f32x2 __attribute__((ext_vector_type(2)));
typedef short s16x4 __attribute__((ext_vector_type(4)));
typedef unsigned u32x4 __attribute__((ext_vector_type(4)));
typedef unsigned u32x2 __attribute__((ext_vector_type(2)));
#define LAS __attribute__((address_space(3)))
#define GAS __attribute__((address_space(1)))
#define DI __device__ __forceinline__

constexpr int SEQ = 2048, DM = 576, FF = 4608;
constexpr int KS2 = 23;
constexpr int NKG = 8 * KS2;
constexpr size_t FL_H = 0;
constexpr size_t FL_P = 40960;
constexpr size_t FL_W = 36864;
constexpr size_t FL_EPOCH = 38912;
constexpr size_t FL_E = 262144;
constexpr size_t FL_S = 180224;
constexpr size_t WS_HT = 524288;
constexpr size_t WS_WT = WS_HT + (size_t)128 * NKG * 1024;
constexpr size_t WS_PART = WS_WT + (size_t)4 * NKG * 9 * 1024;
constexpr size_t PART_BYTES = (size_t)8 * 4 * 8 * 8 * 9 * 1024;
constexpr size_t WS_END = WS_PART + PART_BYTES;

DI float bf2f(u16 v) { return __uint_as_float(((unsigned)v) << 16); }
DI float rbf(float f) { return (float)(__bf16)f; }
DI unsigned pk2(float lo, float hi) { f32x2 v = {lo, hi}; bf16x2 c = __builtin_convertvector(v, bf16x2); return __builtin_bit_cast(unsigned, c); }
DI float blo(unsigned u) { return __uint_as_float(u << 16); }
DI float bhi(unsigned u) { return __uint_as_float(u & 0xffff0000u); }

__host__ __device__ inline int wt_frag(int j, int db) { return j < 16 ? j * 9 + db : 144 + db * 7 + (j - 16); }
DI int kg_f0b(int s, int j) {
  const int a = 512 * s + 128 * ((j & 7) >> 1) + 64 * (j >> 3) + 32 * (j & 1), c = 4096 + 64 * s + 32 * (j & 1);
  return j < 16 ? a : c;
}
__host__ __device__ inline int kg_f0(int s, int j) {
  if (j < 16) { int p = j >> 3, cg = 4 * s + ((j & 7) >> 1); return 64 * (2 * cg + p) + 32 * (j & 1); }
  return 4096 + 32 * (2 * s + (j - 16));
}

template <int N> using IC = std::integral_constant<int, N>;
template <int B, int E, class F> DI void sfor(F&& f) { if constexpr (B < E) { f(IC<B>{}); sfor<B + 1, E>(f); } }

DI void glds_sc1(const void* sbase, unsigned voff, unsigned ldsaddr) {
  asm volatile("s_mov_b32 m0, %2\n\ts_nop 0\n\tglobal_load_lds_dwordx4 %0, %1 sc1" :: "v"(voff), "s"(sbase), "s"(ldsaddr) : "memory");
}
DI void gld4(unsigned& dst, const void* sbase, unsigned voff) {
  asm volatile("global_load_dword %0, %1, %2" : "=v"(dst) : "v"(voff), "s"(sbase) : "memory");
}
template <int C, int L, bool SC1> DI void glds_c(const void* sbase, unsigned voff, unsigned ldsw) {
  unsigned t;
  if constexpr (SC1) asm volatile("s_add_u32 m0, %3, %5\n\tv_add_u32 %0, %4, %1\n\tglobal_load_lds_dwordx4 %0, %2 sc1" : "=&v"(t) : "v"(voff), "s"(sbase), "s"(ldsw), "i"(C), "i"(L) : "memory", "scc");
  else asm volatile("s_add_u32 m0, %3, %5\n\tv_add_u32 %0, %4, %1\n\tglobal_load_lds_dwordx4 %0, %2" : "=&v"(t) : "v"(voff), "s"(sbase), "s"(ldsw), "i"(C), "i"(L) : "memory", "scc");
}
template <int OFF, class T> DI void gld16o_sc1(T& dst, const void* sbase, unsigned voff) {
  asm volatile("global_load_dwordx4 %0, %1, %2 offset:%3 sc1" : "=v"(dst) : "v"(voff), "s"(sbase), "i"(OFF) : "memory");
}
template <int OFF, class T> DI void gld16o_io(T& dst, const void* sbase, unsigned voff) {
  asm volatile("global_load_dwordx4 %0, %1, %2 offset:%3 sc1" : "+v"(dst) : "v"(voff), "s"(sbase), "i"(OFF) : "memory");
}
template <int OFF, bool IO> DI void gld8o(u32x2& dst, const void* sbase, unsigned voff) {
  if constexpr (IO) asm volatile("global_load_dwordx2 %0, %1, %2 offset:%3 sc1" : "+v"(dst) : "v"(voff), "s"(sbase), "i"(OFF) : "memory");
  else asm volatile("global_load_dwordx2 %0, %1, %2 offset:%3 sc1" : "=v"(dst) : "v"(voff), "s"(sbase), "i"(OFF) : "memory");
}
template <int L> DI void glds_run(unsigned& voff, const void* sbase, unsigned step, unsigned ldsa) {
  asm volatile("s_add_u32 m0, %3, %4\n\ts_nop 0\n\tglobal_load_lds_dwordx4 %0, %1\n\tv_add_u32 %0, %2, %0" : "+v"(voff) : "s"(sbase), "s"(step), "s"(ldsa), "i"(L) : "memory", "scc");
}
DI void glds_s(const void* sbase, unsigned voff, unsigned ldsaddr) {
  asm volatile("s_mov_b32 m0, %2\n\ts_nop 0\n\tglobal_load_lds_dwordx4 %0, %1" :: "v"(voff), "s"(sbase), "s"(ldsaddr) : "memory");
}
template <class T> DI void gld16(T& dst, const void* sbase, unsigned voff) {
  asm volatile("global_load_dwordx4 %0, %1, %2" : "=v"(dst) : "v"(voff), "s"(sbase) : "memory");
}
template <class T> DI void gld16_sc1(T& dst, const void* sbase, unsigned voff) {
  asm volatile("global_load_dwordx4 %0, %1, %2 sc1" : "=v"(dst) : "v"(voff), "s"(sbase) : "memory");
}
template <class T> DI void gld16_sc1_io(T& dst, const void* sbase, unsigned voff) {
  asm volatile("global_load_dwordx4 %0, %1, %2 sc1" : "+v"(dst) : "v"(voff), "s"(sbase) : "memory");
}
DI void gld4_sc1(unsigned& dst, const void* sbase, unsigned voff) {
  asm volatile("global_load_dword %0, %1, %2 sc1" : "=v"(dst) : "v"(voff), "s"(sbase) : "memory");
}
DI void gld8_sc1(u32x2& dst, const void* sbase, unsigned voff) {
  asm volatile("global_load_dwordx2 %0, %1, %2 sc1" : "=v"(dst) : "v"(voff), "s"(sbase) : "memory");
}
DI void gld8(u32x2& dst, const void* sbase, unsigned voff) {
  asm volatile("global_load_dwordx2 %0, %1, %2" : "=v"(dst) : "v"(voff), "s"(sbase) : "memory");
}
template <int N> DI void waitv() { static_assert(N >= 0 && N < 64, ""); asm volatile("s_waitcnt vmcnt(%0)" ::"i"(N) : "memory"); }
#define SCHED0 __builtin_amdgcn_sched_barrier(0)
DI void bar() { asm volatile("" ::: "memory"); __builtin_amdgcn_s_barrier(); asm volatile("" ::: "memory"); }
template <class T> DI void tie(T& a) { asm volatile("" : "+v"(a)); }
template <int O2> DI bf16x8 trfrag(unsigned addr) {
  s16x4 a = __builtin_amdgcn_ds_read_tr16_b64_v4i16((LAS s16x4*)(addr));
  s16x4 b = __builtin_amdgcn_ds_read_tr16_b64_v4i16((LAS s16x4*)(addr + O2));
  return __builtin_bit_cast(bf16x8, __builtin_shufflevector(a, b, 0, 1, 2, 3, 4, 5, 6, 7));
}
DI bf16x8 ldsfrag(unsigned addr) { return *(LAS bf16x8*)(addr); }
template <bool FIRST> DI void mfma(f32x4& c, bf16x8 a, bf16x8 b) {
  if constexpr (FIRST) asm volatile("v_mfma_f32_16x16x32_bf16 %0, %1, %2, 0" : "=v"(c) : "v"(a), "v"(b));
  else asm volatile("v_mfma_f32_16x16x32_bf16 %0, %1, %2, %0" : "+v"(c) : "v"(a), "v"(b));
}
DI float silu_mul(float g, float u) {
  const float e = __builtin_amdgcn_exp2f(g * -1.44269504089f);
  return g * u * __builtin_amdgcn_rcpf(1.0f + e);
}
DI unsigned silu_mul2(f32x2 g, f32x2 u) {
  const f32x2 t = g * -1.44269504089f;
  f32x2 e; e.x = __builtin_amdgcn_exp2f(t.x); e.y = __builtin_amdgcn_exp2f(t.y);
  const f32x2 d = e + 1.0f;
  f32x2 r; r.x = __builtin_amdgcn_rcpf(d.x); r.y = __builtin_amdgcn_rcpf(d.y);
  const f32x2 h = (g * u) * r;
  return pk2(h.x, h.y);
}
DI float dpp_ror8(float v) { return __int_as_float(__builtin_amdgcn_update_dpp(0, __float_as_int(v), 0x128, 0xf, 0xf, true)); }
DI void cmac(float& d, float P, float Q, float x) {
  asm volatile("v_fmac_f32 %0, %1, %3\n\tv_fmac_f32_dpp %0, %3, %2 row_ror:8 row_mask:0xf bank_mask:0xf" : "+v"(d) : "v"(P), "v"(Q), "v"(x));
}
DI void cmul(float& d, float P, float Q, float x) {
  asm volatile("v_mul_f32 %0, %1, %3\n\tv_fmac_f32_dpp %0, %3, %2 row_ror:8 row_mask:0xf bank_mask:0xf" : "=&v"(d) : "v"(P), "v"(Q), "v"(x));
}
DI void cfma(float& d, float P, float Q, float x, float c) {
  asm volatile("v_fma_f32 %0, %1, %3, %4\n\tv_fmac_f32_dpp %0, %3, %2 row_ror:8 row_mask:0xf bank_mask:0xf" : "=&v"(d) : "v"(P), "v"(Q), "v"(x), "v"(c));
}
DI void v_mul(float& d, float a, float b) { asm volatile("v_mul_f32 %0, %1, %2" : "=v"(d) : "v"(a), "v"(b)); }
DI void v_mulc(float& d, float a) { asm volatile("v_mul_f32 %0, 0xbfb8aa3b, %1" : "=v"(d) : "v"(a)); }
DI void v_fmac(float& d, float a, float b) { asm volatile("v_fmac_f32 %0, %1, %2" : "+v"(d) : "v"(a), "v"(b)); }
DI void v_fmaa(float& d, float c) { asm volatile("v_fma_f32 %0, %0, %1, %1" : "+v"(d) : "v"(c)); }
DI void v_muli(float& d, float a) { asm volatile("v_mul_f32 %0, %0, %1" : "+v"(d) : "v"(a)); }
DI void lds_add(unsigned addr, float v) { asm volatile("ds_add_f32 %0, %1" :: "v"(addr), "v"(v) : "memory"); }
DI void v_exp2(float& d) { asm volatile("v_exp_f32 %0, %0" : "+v"(d)); }
DI void v_add1(float& d) { asm volatile("v_add_f32 %0, 1.0, %0" : "+v"(d)); }
DI void v_rcp(float& d) { asm volatile("v_rcp_f32 %0, %0" : "+v"(d)); }
DI void v_cvt(unsigned& d, float a, float b) { asm volatile("v_cvt_pk_bf16_f32 %0, %1, %2" : "=v"(d) : "v"(a), "v"(b)); }
DI float wave_sum(float v) {
#define DPPADD(c) v += __int_as_float(__builtin_amdgcn_update_dpp(0, __float_as_int(v), c, 0xf, 0xf, true))
  DPPADD(0x111); DPPADD(0x112); DPPADD(0x114); DPPADD(0x118);
  v += __int_as_float(__builtin_amdgcn_update_dpp(0, __float_as_int(v), 0x142, 0xa, 0xf, false));
  v += __int_as_float(__builtin_amdgcn_update_dpp(0, __float_as_int(v), 0x143, 0xc, 0xf, false));
  return __int_as_float(__builtin_amdgcn_readlane(__float_as_int(v), 63));
}
DI void dnop() { asm volatile("s_nop 1"); }
typedef __amdgpu_buffer_rsrc_t rsrc_t;
DI rsrc_t mkrsrc(const void* p, unsigned bytes) { return __builtin_amdgcn_make_buffer_rsrc((void*)p, 0, bytes, 0x00020000); }
DI unsigned vadd_s(unsigned s, unsigned v) { unsigned o; asm volatile("v_add_u32 %0, %1, %2" : "=v"(o) : "s"(s), "v"(v)); return o; }
DI void st8_sc1(rsrc_t r, unsigned voff, unsigned soff, u32x2 v) { __builtin_amdgcn_raw_buffer_store_b64(v, r, vadd_s(soff, voff), 0, 16); }
DI void st16_sc1(rsrc_t r, unsigned voff, unsigned soff, u32x4 v) { __builtin_amdgcn_raw_buffer_store_b128(v, r, vadd_s(soff, voff), 0, 16); }
DI u32x4 ones4() { u32x4 v; asm volatile("v_mov_b32 %0, -1\n\tv_mov_b32 %1, -1\n\tv_mov_b32 %2, -1\n\tv_mov_b32 %3, -1" : "=v"(v[0]), "=v"(v[1]), "=v"(v[2]), "=v"(v[3])); return v; }
DI void setflag(rsrc_t r, bool sel, unsigned off, unsigned tag) { __builtin_amdgcn_raw_buffer_store_b32(tag, r, sel ? off : 0x7ffffff0u, 0, 16); }
DI void spin_flag(const char* base, unsigned voff, unsigned tag) {
  for (unsigned it = 0; it < (1u << 18); ++it) {
    unsigned v = __hip_atomic_load((GAS unsigned*)(base + voff), __ATOMIC_RELAXED, __HIP_MEMORY_SCOPE_AGENT);
    if (__all(v == tag)) break;
    __builtin_amdgcn_s_sleep(1);
  }
  asm volatile("" ::: "memory");
}

constexpr int PD = 2, FR = PD + 1;
constexpr int SLOT1 = 24576;
constexpr int SLOT2 = 18432;
constexpr int LDS_TOT = 163840;
constexpr int LDS_SCR = 98304;
constexpr int LDS_PK = 122880;
constexpr int LDS_XS = 98304;
constexpr int LDS_ST = 147456;
constexpr int LDS_CS = 151552;
constexpr int LDS_CST = 153600;
constexpr int LDS_WTOT = LDS_CST + 6144;

#define STAMP(k)
#define STAMPC(k)
#define STAMP_DUMP()

constexpr int p1_off(int v) { return v < 8 ? (v % 4) * SLOT1 : (v < 10 ? (v - 4) * SLOT1 : (v < 12 ? (v - 10) * SLOT1 : 2 * SLOT1 + (v - 12) * 30720)); }
constexpr int p2_off(int u, int g) {
  const int k = u % 5;
  return k < 2 ? k * SLOT2 + g * 1024 : k == 2 ? (g < 12 ? 36864 + g * 1024 : 141312 + (g - 12) * 1024) : 49152 + (k - 3) * SLOT2 + g * 1024;
}

constexpr int GT = 288, G2 = 378, GEND = 585;
constexpr int gl(int v, int g) { return v * 24 + g; }
constexpr int gt(int u, int g) { return GT + u * 30 + g; }
constexpr int g2(int u, int g) { return G2 + u * 18 + g; }
constexpr int SY1 = 8, SYT = 2, SY2 = 4;
constexpr int late_g(int f) { return g2(8, f); }
constexpr int PLAG = 4;

constexpr int SCE = 2 * g2(0, SY2) + 3;
constexpr int SQC = 2 * (G2 + 13);
constexpr int SILU0[3] = {2 * (gl(4, 0) + 12), 2 * (gl(10, 0) + 12), SQC + 8};
constexpr int SILUN[3] = {32, 32, 8};
constexpr int silu_end(int s, int nel) { return SILU0[s] + 9 * (nel / 2) + 1; }
constexpr int S1R = 2 * (GT + 55);
constexpr int S1A = S1R + 12;
constexpr int S1SEL = 2 * g2(2, 0) + 2;
constexpr int SCM = S1SEL + 6;
constexpr int UFIN = 3;
constexpr int SFIN = 2 * g2(UFIN, SY2);
static_assert(S1A + 63 < 2 * g2(0, SY2) && silu_end(2, 8) < S1SEL && SCM + 24 < SFIN - 8, "");
constexpr int csx_n(int v) { return v == 16 ? 2 : 3; }
constexpr int csx_g(int v, int n) {
  const int base[17] = {gl(0, 4), gl(0, 19), gl(1, 12), gl(2, 11), gl(2, 16), gl(3, 3), gl(7, 16), gl(7, 19), gl(8, 1), gl(8, 12), gl(8, 18), gl(9, 0),
                        GT + 27, GT + 30, GT + 34, GT + 37, GT + 40};
  const int step = (v == 5 || v == 6 || v == 7 || v >= 12) ? 1 : 2;
  return base[v] + n * step;
}
constexpr int CSF[3] = {gl(3, SY1), gl(9, SY1), GT + 44};
constexpr int GPC = gl(8, 23);
constexpr int GPK = gl(6, 6);
constexpr int GSYC = GT + 46;
constexpr int xt_g(int h) {
  const int g[18] = {-1, -1, gl(0, 6), gl(0, 12), gl(0, 20), gl(1, 3), gl(1, 11), gl(1, 18), gl(2, 2), gl(2, 10), gl(2, 17), gl(3, 1), gl(3, 9), gl(3, 14), gl(3, 19), gl(4, 0), gl(4, 3), gl(4, 5)};
  return g[h];
}
constexpr int xd_g(int h) { return h < 3 ? -1 : (h < 5 ? h - 2 : xt_g(h - 3) + 3); }

constexpr int csx_at(int G) { for (int v = 0; v < 17; v++) for (int n = 0; n < csx_n(v); n++) if (csx_g(v, n) == G) return v * 4 + n; return -1; }
constexpr int xt_at(int G) { for (int c = 2; c < 18; c++) if (xt_g(c) == G) return c; return -1; }
constexpr int SRES = 2 * g2(9, 6);
constexpr int res_at(int sl) {
  if (sl < SRES || !(sl & 1)) return -1;
  int n = 0;
  for (int g = SRES / 2; g < GEND; g++) { const bool busy = (g - late_g(9)) % 7 == 0; if (g == sl / 2) return (busy || n >= 30) ? -1 : n; if (!busy) n++; }
  return -1;
}
constexpr int silu_at(int sl) { for (int k = 0; k < 3; k++) if (sl >= SILU0[k] && sl - SILU0[k] <= 9 * (SILUN[k] / 2) + 1) return k; return -1; }

enum { OP_G = 0, OP_X, OP_ST, OP_PO, OP_PH, OP_A, OP_B, OP_FL, OP_EP, OP_HL, OP_PL, OP_EG, OP_SG, OP_PK };
struct Op { int pos, kind, a, n; };
struct Sched {
  Op op[320]; int cnt;
  constexpr void add(int pos, int kind, int a, int n) {
    int i = cnt; while (i > 0 && op[i - 1].pos > pos) { op[i] = op[i - 1]; i--; }
    op[i] = Op{pos, kind, a, n}; cnt++;
  }
  constexpr void unit(int g, int v, int step) { for (int n = 0; n < (v < 12 ? 3 : 4); n++) add(2 * (g + n * step), OP_G, v, n); }
  constexpr Sched() : op{}, cnt(0) {
    for (int h = 0; h < 2; h++) for (int n = 0; n < 2; n++) add(-3, OP_X, h, n);
    for (int n = 0; n < 3; n++) add(-3, OP_G, 0, n);
    for (int n = 0; n < 6; n++) add(-2, OP_ST, 8, n);
    for (int n = 0; n < 2; n++) add(-1, OP_X, 2, n);
    for (int n = 0; n < 3; n++) add(-1, OP_G, 1, n);
    for (int n = 0; n < 1; n++) add(-1, OP_ST, 7, n);
    add(-1, OP_ST, 6, 0);
    for (int h = 3; h < 18; h++) for (int n = 0; n < 2; n++) add(2 * xd_g(h), OP_X, h, n);
    for (int n = 0; n < 4; n++) add(2 * gl(4, 16 + 2 * n), OP_PK, 0, n);
    add(2 * gl(7, 15), OP_PK, 1, 0);
    add(2 * GPC - 1, OP_ST, 9, 0);
    unit(gl(0, 4), 2, 1);
    for (int n = 0; n < 4; n++) add(2 * GPK - 1, OP_ST, 4, n);
    add(2 * gl(1, 5), OP_SG, 0, 0);
    unit(gl(0, 9), 3, 3); unit(gl(1, 9), 4, 3); unit(gl(2, 9), 5, 1); unit(gl(3, 10), 6, 3); unit(gl(4, 9), 7, 2);
    unit(gl(5, 10), 8, 4); unit(gl(6, 9), 9, 1); unit(gl(6, 13), 10, 2); unit(gl(6, 19), 11, 2); unit(gl(8, 10), 12, 3); unit(gl(9, 10), 13, 3); unit(gl(10, 10), 14, 3);
    for (int n = 0; n < 3; n++) add(2 * gl(4, 1 + 2 * n), OP_PH, 0, n);
    for (int n = 0; n < 3; n++) add(2 * gl(8, 2 + 2 * n), OP_PO, 0, n);
    for (int s = 0; s < 2; s++) for (int h = 0; h < 2; h++) for (int t = 0; t < 2; t++) add((silu_end(s, 16 * (h + 1)) / 2) * 2 + 1, OP_ST, s, 2 * h + t);
    for (int t = 0; t < 2; t++) add((silu_end(2, 8) / 2) * 2 + 1, OP_ST, 2, t);
    add(2 * gl(11, 16), OP_FL, 4, 0);
    add(2 * gt(0, 6), OP_FL, 0, 0);
    for (int n = 0; n < 2; n++) { add(2 * gt(0, 20 + 4 * n), OP_A, 0, n); add(2 * gt(1, 8 + 4 * n), OP_A, 1, n); add(2 * gt(1, 16 + 4 * n), OP_A, 2, n); }
    for (int n = 0; n < 4; n++) { add(2 * gt(2, 3 + 2 * n), OP_B, 0, n); add(2 * gt(2, 11 + 2 * n), OP_B, 1, n); add(2 * gt(2, 19 + 3 * n), OP_B, 2, n); }
    add(2 * g2(0, 1), OP_FL, 1, 0);
    for (int u = 0; u < 9; u++) {
      const int U = u + 3;
      for (int n = 0; n < (U < 11 ? 2 : 1); n++) add(2 * g2(u, 5 + 2 * n), OP_A, U, n);
      if (U < 8) for (int n = 0; n < 4; n++) add(2 * g2(u, 9 + 2 * n), OP_B, U, n);
    }
    for (int n = 0; n < 7; n++) add(2 * (g2(UFIN - 1, 0) + (n < 4 ? n : 2 * n - 2)), OP_EG, 0, n);
    add(2 * g2(4, 6), OP_FL, 2, 0);
    add(2 * g2(4, 8), OP_FL, 3, 0);
    add(2 * late_g(56), OP_FL, 5, 0);
    for (int n = 0; n < 4; n++) add(2 * g2(5, 9 + 2 * n), OP_HL, 0, n);
    for (int n = 4; n < 14; n++) add(2 * (g2(6, 9) + (n - 4)), OP_HL, 0, n);
    for (int n = 0; n < 6; n++) add(2 * g2(7, 6 + n), OP_EP, 0, n);
    for (int db = 0; db < 8; db++) add(2 * (late_g(7 * db + 6) + 3) + 1, OP_ST, db < 6 ? 3 : 10, db);
  }
  static constexpr bool droppable(const Op& o) { return (o.kind == OP_PH && o.n == 2) || (o.kind == OP_ST && (o.a == 4 || o.a == 9)) || (o.kind == OP_PK && o.a == 0); }
  constexpr int first_at(int pos) const { int i = 0; while (i < cnt && op[i].pos < pos) i++; return i; }
  constexpr int after(int kind, int a, int g) const {
    int last = -1;
    for (int i = 0; i < cnt; i++) if (op[i].kind == kind && op[i].a == a) last = i;
    int n = 0;
    for (int i = last + 1; i < cnt; i++) if (op[i].pos < 2 * g && !droppable(op[i])) n++;
    return n;
  }
};
constexpr Sched S1{};
constexpr int cmin(int a, int b) { return a < b ? a : b; }
constexpr int cmin(int a, int b, int c) { return cmin(cmin(a, b), c); }
constexpr bool l_dbl(int v) { return v == 4 || v == 5 || v == 6 || v == 7 || v >= 10; }
struct LF { int unit, ks, blk; };
constexpr LF l_frag(int G) {
  const int v = G / 24, g = G % 24;
  if (!l_dbl(v)) return LF{v, 3 * (v % 6) + (g >> 3), g & 7};
  const int v0 = v & ~1, r = (v & 1) * 24 + g, p = r / 12, k6 = (r % 12) / 2;
  return LF{v0 + k6 / 3, 3 * (v0 % 6) + k6, p + 4 * (r & 1)};
}
constexpr int l_prev_unit(int G) {
  for (int g = G - 1; g >= 0; g--) if ((l_frag(g).blk & 3) == (l_frag(G).blk & 3)) return l_frag(g).unit;
  return 0;
}
constexpr int l_unit0(int G) { for (int g = G - 1; g >= 0; g--) if ((l_frag(g).blk & 3) == 0) return l_frag(g).unit; return 0; }
struct Corr {
  int item[40];
  constexpr Corr(int pass) : item{} {
    const int g0 = gl(6 * pass + 3, SY1 + 2);
    bool done[16] = {};
    const int order[8] = {0, 4, 1, 5, 2, 6, 3, 7};
    for (int k = 0; k < 40; k++) {
      item[k] = -1;
      const int G = g0 + k;
      for (int o = 0; o < 16 && item[k] < 0; o++) {
        const int bk = order[o >> 1], it = bk * 2 + (o & 1);
        if (done[it]) continue;
        if (l_frag(G).blk == bk || l_frag(G - 1).blk == bk || l_frag(G - 2).blk == bk || l_frag(G + 1).blk == bk) continue;
        item[k] = it; done[it] = true;
      }
    }
  }
  constexpr int last() const { int l = 0; for (int k = 0; k < 40; k++) if (item[k] >= 0) l = k; return l; }
  constexpr int count() const { int c = 0; for (int k = 0; k < 40; k++) c += item[k] >= 0; return c; }
};
constexpr Corr CR{0};
static_assert(CR.count() == 16 && gl(3, SY1 + 2) + CR.last() < gl(4, 12), "");
constexpr int t_ks(int r) { return r < 54 ? r / 3 : (r - 54) / 2; }
constexpr int t_blk(int r) { return r < 54 ? 2 + r % 3 : (r - 54) & 1; }

__global__ __launch_bounds__(512, 2) void k_main(const u16* __restrict__ x, const u16* __restrict__ lre, const u16* __restrict__ lim,
                                                 const u16* __restrict__ bre, const u16* __restrict__ bim, const u16* __restrict__ cre,
                                                 const u16* __restrict__ cim, const u16* __restrict__ logdt, const u16* __restrict__ dsk,
                                                 const u16* __restrict__ wg, const u16* __restrict__ wu, const u16* __restrict__ wd,
                                                 char* __restrict__ ws, float* __restrict__ out) {
  extern __shared__ __attribute__((aligned(16))) char lds[];
  const int tid = threadIdx.x, w = __builtin_amdgcn_readfirstlane(tid >> 6);
  unsigned cw;
  {
    u64 pc; unsigned clen;
    asm volatile("s_getpc_b64 %0\n.Lk_code_from:\n\ts_mov_b32 %1, .Lk_code_to-.Lk_code_from" : "=s"(pc), "=s"(clen));
    const unsigned off = min((((blockIdx.x >> 3) * 8 + w) * 2 + (tid & 1)) * 128u, clen);
    asm volatile("global_load_dword %0, %1, %2" : "=v"(cw) : "v"(off), "s"(pc) : "memory");
  }
  const int b = blockIdx.x, sx = b & 7, idx = b >> 3;
  unsigned lds0 = (unsigned)(size_t)(LAS char*)lds;
  asm volatile("" : "+s"(lds0));
  const int rt = idx & 7, cgl = idx >> 3, cg = sx * 4 + cgl;
  const rsrc_t rsW = mkrsrc(ws, (unsigned)WS_END);
  const unsigned htw = (unsigned)WS_HT + (unsigned)(((rt * 16 + 2 * w) * 8 + sx) * KS2) * 1024u;
  constexpr unsigned HTB = NKG * 1024;
  const unsigned flw = (unsigned)FL_H + (unsigned)((((rt * 8 + sx) * 4) * 8 + w) * 4) * 4u;
  const f32x4 zero4 = {0.f, 0.f, 0.f, 0.f};
  const unsigned l16 = (tid & 63) * 16;
  unsigned tag = 0;
  unsigned ldsw = lds0 + w * 1024; asm volatile("" : "+s"(ldsw));
  STAMP(0);

  f32x4 acc[8][2];
  f32x4 tac[5][2];
  f32x4 ac2[9][2];
  bf16x8 fr[FR];
  bf16x8 xf[18][2];
  constexpr int HR = 4;
  bf16x8 hf[HR][2][2];
  bf16x8 hl[7][2];
  u32x4 pv[9];
  unsigned flv[6] = {0, 0, 0, 0, 0, 0};
  u32x2 epv[6];
  u32x2 eg[7];
  unsigned ept[3], epd[3];
  unsigned wvoff, ab[4], tvA = 0, tvF = 0;
  const char* wsrc = (const char*)((w >> 2) ? wu : wg) + (size_t)cg * 256;
  {
    const int lane = tid & 63, q = lane >> 4, li = lane & 15;
    const int rg = w & 3, r8 = lane >> 3, ch = lane & 7;
    const int g = ((lane >> 4) & 1) | ((rg & 1) << 1);
    wvoff = (unsigned)((8 * rg + r8) * (FF * 2) + (((ch >> 1) ^ g) << 5) + ((ch & 1) << 4));
    const int r4 = li >> 2, cc = lane & 3;
    const int gr = ((r4 >> 1) & 1) | ((q & 1) << 1);
    const unsigned a0 = (unsigned)((8 * q + r4) * 128 + (gr << 5) + 8 * cc);
#pragma unroll
    for (int k = 0; k < 4; k++) { ab[k] = lds0 + (a0 ^ (k << 5)); tie(ab[k]); }
    if (tid < 384) *(LAS float*)(lds0 + LDS_WTOT + tid * 4) = 0.f;
  }
  const char* xb = (const char*)x + (size_t)(rt * 256 + 32 * w) * (DM * 2);
  unsigned xvo, xvo1, xra, xra1;
  unsigned ldsx = lds0 + w * 6144; asm volatile("" : "+s"(ldsx));
  {
    const int lane = tid & 63, q = lane >> 4, li = lane & 15, r8 = lane >> 3;
    xvo = (unsigned)(r8 * (DM * 2) + (((lane & 7) ^ (r8 >> 1)) << 4));
    xra = lds0 + LDS_XS + w * 6144 + li * 128 + ((q ^ (li >> 1)) << 4);
    xvo1 = xvo ^ 64u; xra1 = xra ^ 64u;
    tie(xvo); tie(xra); tie(xvo1); tie(xra1);
  }
  u32x2 sgr;
  const unsigned pobase = (unsigned)WS_PART + (unsigned)(((rt * 4 + cgl) * 8 + sx) * 72) * 1024u;
  const unsigned pb = (unsigned)WS_PART + (unsigned)((((rt * 4 + cgl) * 8 + w) * 8 + sx) * 9) * 1024u;
  const char* wtb = ws + WS_WT + ((size_t)cgl * NKG + sx * KS2) * 9 * 1024 + w * 1024;
  const char* hb0 = ws + htw;
  const int erow = rt * 256 + 32 * sx;
  const int blk3 = w < 4 ? 2 * cgl + (w >> 1) : 8;
  const char* tB = (const char*)((w & 1) ? bim : bre) + (size_t)(8 * (9 * sx + blk3)) * (DM * 2);
  const size_t fcol = (size_t)(4096 + 64 * sx + 16 * cgl) * 2;
  auto fsrc = [&](int fp) { return (const char*)((fp & 1) ? wu : wg) + fcol + (size_t)(fp >> 1) * 32 * (FF * 2); };
  const char* tsF = nullptr; const char* tsA = nullptr; unsigned tsn = 0, tsu = 0, tdA = 0, tdn = 0;
  unsigned cla2 = 0;
  unsigned cla = 0;

  u32x4 cfr; unsigned pl[3], pkv = 0, pkl = 0;
  const int widx = w * 32 + idx;
  const int wi = widx < 162 ? widx : widx - 162, pj = (wi * 57) >> 9, pln = wi - 9 * pj;
  const int ci = widx < 180 ? widx : widx - 180, cd16 = (ci * 205) >> 10, cjj = ci - 5 * cd16;
  auto issue = [&](auto Ic) {
    constexpr Op o = S1.op[decltype(Ic)::value];
    if constexpr (o.kind == OP_G) {
      if constexpr (o.a < 12) {
        glds_c<(o.a / 6) * 128 + (3 * (o.a % 6) + o.n) * 32 * (FF * 2), p1_off(o.a) + o.n * 8192, false>(wsrc, wvoff, ldsw);
      } else {
        constexpr int U = o.a - 12;
        if constexpr (U == 0 && o.n == 0) {
          unsigned l_ = l16; tie(l_);
          const int lane = l_ >> 4, p = lane >> 1;
          tvF = (unsigned)((p ^ (((p >> 3) & 1) << 2)) * (FF * 2) + ((lane & 1) << 4));
          const unsigned vb = (unsigned)((lane >> 3) * (DM * 2) + (((lane & 7) ^ (4 * (w & 1) + (lane >> 4))) << 4));
          tvA = w < 6 ? vb : tvF; tie(tvA); tie(tvF);
          tsF = fsrc(w); tsA = w < 6 ? tB : tsF;
          tsn = w < 6 ? 128u : 32u * (FF * 2); tsu = w < 6 ? 128u : 128u * (FF * 2);
          tdA = ldsw + (w < 6 ? 12288 : 0); tdn = w < 6 ? 6144u : 2048u;
          asm volatile("" : "+s"(tsF), "+s"(tsA), "+s"(tsn), "+s"(tsu), "+s"(tdA), "+s"(tdn));
        }
        if constexpr (o.n < 3) {
          glds_run<p1_off(o.a)>(tvA, tsA, o.n < 2 ? tsn : tsu, tdA + o.n * tdn);
        } else glds_c<U * 192 * (FF * 2), p1_off(o.a), false>(tsF, tvF, ldsw);
      }
    } else if constexpr (o.kind == OP_X) {
      glds_c<((o.a & 1) * 16 + o.n * 8) * (DM * 2) + (o.a >> 1) * 128, LDS_XS + (o.a % 3) * 2048 + o.n * 1024, false>(xb, o.n ? xvo1 : xvo, ldsx);
    } else if constexpr (o.kind == OP_PK) {
      if constexpr (o.a == 0) {
        if constexpr (o.n == 0) {
          unsigned l_ = l16; tie(l_);
          pkv = (l_ >> 7) * (DM * 2) + (l_ & 112) + (unsigned)(kg_f0b(sx, pj) * (DM * 2) + pln * 128); tie(pkv);
          pkl = lds0 + (w < 6 ? LDS_PK + w * 4096 : LDS_TOT); asm volatile("" : "+s"(pkl));
        }
        glds_c<o.n * 8 * (DM * 2), o.n * 1024, false>(wd, pkv, pkl);
      } else {
        unsigned l_ = l16; tie(l_);
        const int lane = l_ >> 4, q = lane >> 4, li = lane & 15;
        const int cbl = 2 * cjj + (q >> 1);
        const char* cp = (const char*)((q & 1) ? cim : cre) + (unsigned)((16 * cd16 + li) * (DM * 2) + 16 * (9 * sx + (cbl < 9 ? cbl : 8)));
        asm volatile("global_load_dwordx4 %0, %1, off" : "=v"(cfr) : "v"(cp) : "memory");
      }
    } else if constexpr (o.kind == OP_SG) {
      gld8_sc1(sgr, ws + FL_S + (size_t)(rt * 256 + 32 * w) * 8, (l16 >> 1) & 248);
    } else if constexpr (o.kind == OP_PO) {
      st16_sc1(rsW, l16, pobase + (unsigned)(w * 9 + 6 + o.n) * 1024u, ones4());
    } else if constexpr (o.kind == OP_PH) {
      if constexpr (o.n < 2) st16_sc1(rsW, l16, htw + o.n * HTB + (unsigned)(18 + cgl) * 1024u, ones4());
      else {
        const unsigned f = (l16 & 512) ? 0u : ~0u;
        st16_sc1(rsW, cgl < 2 ? l16 : 0x70000000u, htw + (cgl & 1) * HTB + 22u * 1024u, (u32x4){f, f, f, f});
      }
    } else if constexpr (o.kind == OP_A) {
      constexpr int U = o.a;
      if constexpr (U % 5 == 2 && o.n == 1) glds_c<U * SLOT2 + 8192, 0, true>(wtb, l16, ldsw + (w >= 4 ? p2_off(U, 12) - 4096 : p2_off(U, 8)));
      else glds_c<U * SLOT2 + o.n * 8192, p2_off(U, 8 * o.n), true>(wtb, l16, ldsw);
      if constexpr (U < 11 && o.n == 1) { if (w < 2) glds_c<U * SLOT2 + 16384, p2_off(U, 16), true>(wtb, l16, ldsw); }
      if constexpr (U == 11) { if (w < 1) glds_c<U * SLOT2 + 8192, p2_off(U, 8), true>(wtb, l16, ldsw); }
    } else if constexpr (o.kind == OP_B) {
      constexpr int kg = 2 * o.a + (o.n >> 1);
      gld16o_sc1<(kg % 8 - 4) * 1024>(hf[o.a % HR][o.n >> 1][o.n & 1], hb0 + (size_t)(o.n & 1) * HTB + (kg / 8) * 8192 + 4096, l16);
    } else if constexpr (o.kind == OP_HL) {
      gld16o_sc1<((o.n >> 1) - 4) * 1024>(hl[o.n >> 1][o.n & 1], hb0 + (size_t)(o.n & 1) * HTB + 16384 + 4096, l16);
    } else if constexpr (o.kind == OP_PL) {
      gld16o_sc1<(o.n - 4) * 1024>(pv[o.n], ws + (size_t)pobase + (size_t)w * 9216 + 4096, l16);
    } else if constexpr (o.kind == OP_EG) {
      gld8o<o.n * 384, false>(eg[o.n], ws + FL_E + (size_t)(cg * 16 + rt + 1) * 384, cla2);
    } else if constexpr (o.kind == OP_FL) {
      unsigned lz = l16; tie(lz);
      if constexpr (o.a < 3) gld4_sc1(flv[o.a], ws + flw + 128 * o.a, (lz >> 2) & 12);
      else if constexpr (o.a == 3) gld4_sc1(flv[3], ws + FL_W + (size_t)(w * 32 + idx) * 4, lz & 0);
      else if constexpr (o.a == 4) gld4_sc1(flv[4], ws + FL_W + (size_t)sx * 128, (lz >> 2) & 124);
      else gld4_sc1(flv[5], ws + FL_P + (size_t)(((rt * 4 + cgl) * 8 + sx) * 8 + w) * 64, lz & 0);
    } else if constexpr (o.kind == OP_EP) {
      constexpr int r = o.n / 2;
      if constexpr (o.n % 2 == 0) {
        unsigned l_ = l16; tie(l_);
        const unsigned e = min((l_ >> 4) + 64u * w + 512u * r, 1151u), t = (e * 1821u) >> 16;
        ept[r] = t; epd[r] = (e - 36u * t) * 8u; tie(ept[r]); tie(epd[r]);
        gld8(epv[o.n], x + (size_t)erow * DM + cgl * 144, t * (DM * 2) + epd[r]);
      } else gld8(epv[o.n], dsk + cgl * 144, epd[r]);
    }
  };
  auto issue_at = [&](auto Pc) {
    constexpr int i0 = S1.first_at(decltype(Pc)::value), i1 = S1.first_at(decltype(Pc)::value + 1);
    sfor<i0, i1>([&](auto Ic) { issue(Ic); SCHED0; });
  };
  auto xconv = [&](auto Hc) {
    constexpr int h = decltype(Hc)::value, c = h >> 1, t = h & 1;
    xf[2 * c][t] = ldsfrag(xra + (h % 3) * 2048);
    xf[2 * c + 1][t] = ldsfrag(xra1 + (h % 3) * 2048);
  };
  auto htie = [&](auto Uc) { constexpr int U = decltype(Uc)::value; tie(hf[U % HR][0][0]); tie(hf[U % HR][0][1]); tie(hf[U % HR][1][0]); tie(hf[U % HR][1][1]); };
  auto chk_flag = [&](auto Ac) { constexpr int a = decltype(Ac)::value; tie(flv[a]);
    if (!__all(flv[a] == tag)) {
      unsigned lz = l16; tie(lz);
      if constexpr (a < 3) spin_flag(ws + flw + 128 * a, (lz >> 2) & 12, tag);
      else if constexpr (a == 3) spin_flag(ws + FL_W + (size_t)(w * 32 + idx) * 4, lz & 0, tag);
      else if constexpr (a == 4) spin_flag(ws + FL_W + (size_t)sx * 128, (lz >> 2) & 124, tag);
      else spin_flag(ws + FL_P + (size_t)(((rt * 4 + cgl) * 8 + sx) * 8 + w) * 64, lz & 0, tag); } };

  float te = 0.f, pp = 0.f, hh[2], sc1[2], sc2[2], sng[2];
  f32x4 csw[2];
  f32x4 csv, tst[2];
  f32x4 csa0, csa1, qs[8];
  bf16x8 selA, selB; unsigned cra = 0, crb = 0;
  unsigned hp[2][4];
  float cm[3][8], ca4[3][2], se[3][2], eg4[3][2][4], spre[3][2], sv[3][2][4], cc1[3][2], cin[3][2];
  float tw[8], c32[2], wpre = 0.f, crt = 0.f, cz[3], cpw[3][2], c256[2];
  float res[3][4], rst[3][2];
  unsigned fwa = 0, fra = 0, fvo = 0;
  unsigned cs48 = 0;
#define CPAIR(base, pr) (*(LAS f32x2*)((base) + (pr) * 128))
  unsigned csa = 0, pkd[4];
  bool q1 = false, q2 = false;
  auto side = [&](auto Sc) {
    constexpr int sl = decltype(Sc)::value;
    {
      if constexpr (silu_at(sl) >= 0) {
        constexpr int s = silu_at(sl), r = sl - SILU0[s], N = SILUN[s];
        constexpr int pr2 = r / 9, ph = r % 9;
        auto G = [&](auto Ec) -> float { constexpr int e = decltype(Ec)::value; if constexpr (s < 2) return acc[e >> 3][(e >> 2) & 1][e & 3]; else return tac[0][e >> 2][e & 3]; };
        auto Uv = [&](auto Ec) -> float { constexpr int e = decltype(Ec)::value; if constexpr (s < 2) return acc[4 + (e >> 3)][(e >> 2) & 1][e & 3]; else return tac[1][e >> 2][e & 3]; };
        constexpr int e0 = 2 * pr2, e1 = e0 + 1;
        if constexpr (ph == 0) { if constexpr (pr2 > 0) v_mul(hh[1], pp, te); if constexpr (e0 < N) v_mul(te, G(IC<e0 < N ? e0 : 0>{}), sc1[(e0 >> 2) & 1]); }
        if constexpr (ph == 1) {
          if constexpr (pr2 > 0) {
            constexpr int ep = e0 - 2, t = (ep >> 2) & 1;
            v_cvt(hp[t][s < 2 ? 2 * ((ep >> 3) & 1) + ((ep >> 1) & 1) : ((ep >> 1) & 1)], hh[0], hh[1]);
            if constexpr (s < 2 && (e0 & 15) == 0) {
#pragma unroll
              for (int tt = 0; tt < 2; tt++)
                st16_sc1(rsW, l16, htw + tt * HTB + (unsigned)(8 * s + 2 * cgl + (ep >> 4)) * 1024u, (u32x4){hp[tt][0], hp[tt][1], hp[tt][2], hp[tt][3]});
            }
            if constexpr (s == 2 && e0 == 8) {
#pragma unroll
              for (int tt = 0; tt < 2; tt++) st8_sc1(rsW, l16, htw + tt * HTB + (unsigned)((16 + (cgl >> 1)) * 1024 + (cgl & 1) * 8), (u32x2){hp[tt][0], hp[tt][1]});
            }
          }
        }
        if constexpr (e0 < N) {
          if constexpr (ph == 2 || ph == 6) v_exp2(te);
          if constexpr (ph == 3) { v_fmaa(te, sc2[(e0 >> 2) & 1]); v_mul(pp, G(IC<e0>{}), Uv(IC<e0>{})); }
          if constexpr (ph == 4 || ph == 8) v_rcp(te);
          if constexpr (ph == 5) { v_mul(hh[0], pp, te); v_mul(te, G(IC<e1>{}), sc1[(e0 >> 2) & 1]); }
          if constexpr (ph == 7) { v_fmaa(te, sc2[(e0 >> 2) & 1]); v_mul(pp, G(IC<e1>{}), Uv(IC<e1>{})); }
        }
      }
    }
    if constexpr (sl == SILU0[0] - 4 || sl == SILU0[1] - 4 || sl == SILU0[2] - 4) {
      unsigned lz = l16; tie(lz);
#pragma unroll
      for (int t = 0; t < 2; t++) { const f32x2 f = *(LAS f32x2*)(lds0 + LDS_ST + w * 512 + t * 256 + (lz & 240)); sc1[t] = f[0]; sc2[t] = f[1]; }
    }
    if constexpr (sl == 2 * gl(3, SY1) || sl == 2 * gl(9, SY1) || sl == SQC - 14) {
      unsigned lz = l16; tie(lz);
#pragma unroll
      for (int t = 0; t < 2; t++) sng[t] = *(LAS float*)(lds0 + LDS_ST + w * 512 + t * 256 + (lz & 240) + 8);
      cra = lds0 + LDS_CS + (lz >> 8) * 16; tie(cra);
    }
    {
      constexpr int pass = sl / 2 >= gl(6, 0) ? 1 : 0, k = sl / 2 - gl(6 * pass + 3, SY1 + 2), half = sl & 1;
      if constexpr (k >= 0 && k < 40) {
        constexpr int it = CR.item[k];
        if constexpr (it >= 0) { constexpr int bk = it >> 1, t = it & 1; float a0 = acc[bk][t][2 * half], a1 = acc[bk][t][2 * half + 1];
          v_fmac(a0, sng[t], csw[k & 1][2 * half]); v_fmac(a1, sng[t], csw[k & 1][2 * half + 1]); acc[bk][t][2 * half] = a0; acc[bk][t][2 * half + 1] = a1; }
      }
      if constexpr (k >= -2 && k < 38 && half == 1) {
        constexpr int nx = CR.item[k + 2];
        if constexpr (nx >= 0) csw[k & 1] = *(LAS f32x4*)(cra + pass * 512 + ((nx >> 1) >> 2) * 256 + ((nx >> 1) & 3) * 64);
      }
    }
    if constexpr (sl == SQC - 12) {
#pragma unroll
      for (int k = 0; k < 8; k++) qs[k] = *(LAS f32x4*)(cra + 1280 + k * 64);
    }
    if constexpr (sl >= SQC - 8 && sl < SQC) {
      constexpr int r = sl - (SQC - 8);
#pragma unroll
      for (int e = 3 * r; e < 3 * r + 3; e++) { const int k = 2 + e / 4, j = e & 3; float a = qs[k & 1][j]; asm volatile("v_add_f32 %0, %0, %1" : "+v"(a) : "v"(qs[k][j])); qs[k & 1][j] = a; }
      if constexpr (r == 7) { csv = qs[0]; csa0 = qs[1]; }
    }
    if constexpr (sl >= SQC && sl < SQC + 8) {
      constexpr int r = sl - SQC, mt = r >> 2, t = (r >> 1) & 1, half = r & 1;
      float a0 = tac[mt][t][2 * half], a1 = tac[mt][t][2 * half + 1];
      if constexpr (mt == 0) { v_fmac(a0, sng[t], csv[2 * half]); v_fmac(a1, sng[t], csv[2 * half + 1]); }
      else { v_fmac(a0, sng[t], csa0[2 * half]); v_fmac(a1, sng[t], csa0[2 * half + 1]); }
      tac[mt][t][2 * half] = a0; tac[mt][t][2 * half + 1] = a1;
    }
    if constexpr (sl == 2 * GSYC) {
      unsigned lz = l16; tie(lz);
      crb = lds0 + LDS_ST + w * 512 + (lz >> 8) * 64; tie(crb);
#pragma unroll
      for (int t = 0; t < 2; t++)
#pragma unroll
        for (int j = 0; j < 4; j++) tst[t][j] = *(LAS float*)(crb + t * 256 + j * 16 + 8);
#pragma unroll
      for (int k = 0; k < 3; k++) csv[k] = *(LAS float*)(lds0 + LDS_CS + 1024 + k * 64 + ((lz >> 2) & 60));
    }
    if constexpr (sl >= 2 * (GT + 48) && sl < 2 * (GT + 54)) {
      constexpr int r = sl / 2 - (GT + 48), k = (r + 1) % 3, t = r / 3, half = sl & 1;
      float a0 = tac[2 + k][t][2 * half], a1 = tac[2 + k][t][2 * half + 1];
      v_fmac(a0, tst[t][2 * half], csv[k]); v_fmac(a1, tst[t][2 * half + 1], csv[k]);
      if constexpr (half) dnop();
      tac[2 + k][t][2 * half] = a0; tac[2 + k][t][2 * half + 1] = a1;
    }
    if constexpr (sl == S1R - 2) {
#pragma unroll
      for (int t = 0; t < 2; t++)
#pragma unroll
        for (int j = 0; j < 4; j++) tst[t][j] = *(LAS float*)(crb + t * 256 + j * 16 + 12);
    }
    if constexpr (sl >= S1R && sl < S1R + 12) {
      constexpr int r = sl - S1R, k = r >> 2, t = (r >> 1) & 1, half = r & 1;
      float a0 = tac[2 + k][t][2 * half], a1 = tac[2 + k][t][2 * half + 1];
      v_muli(a0, tst[t][2 * half]); v_muli(a1, tst[t][2 * half + 1]);
      tac[2 + k][t][2 * half] = a0; tac[2 + k][t][2 * half + 1] = a1;
    }
    if constexpr (sl == S1A - 12) {
      unsigned l_ = l16; tie(l_); const int tl = (int)(l_ >> 4) + 64 * w;
      const int lane = tl & 63;
      csa = lds0 + LDS_CST + (lane & 15) * 8; cla = (unsigned)(lane < 48 ? lane : lane - 16) * 4u;
      cs48 = csa + (cla >> 6) * 2048; cla2 = cla * 2; tie(cla2);
      q1 = lane & 16; q2 = lane & 32;
    }
    if constexpr (sl == S1A - 6 || sl == S1A + 2 || sl == S1A + 10) {
      constexpr int k = (sl - (S1A - 6)) / 8;
#pragma unroll
      for (int i = 0; i < 4; i++) { const f32x2 m = CPAIR(csa + k * 2048, 1 + i); cm[k][2 * i] = m[0]; cm[k][2 * i + 1] = m[1]; }
    }
    if constexpr (sl >= S1A && sl < S1A + 24) {
      constexpr int r = sl - S1A, k = r / 8, j = (r % 8) >> 1, t = r & 1;
      if constexpr (j == 0) cmul(se[k][t], cm[k][6], cm[k][7], tac[2 + k][t][0]);
      else cmac(se[k][t], cm[k][2 * (3 - j)], cm[k][2 * (3 - j) + 1], tac[2 + k][t][j]);
    }
    if constexpr (sl == S1A + 36) {
#pragma unroll
      for (int k = 0; k < 3; k++) { const f32x2 a4 = CPAIR(csa + k * 2048, 6); ca4[k][0] = a4[0]; ca4[k][1] = a4[1]; }
    }
    if constexpr (sl == SCM - 4) {
#pragma unroll
      for (int k = 0; k < 3; k++) { const f32x2 mm = CPAIR(csa + k * 2048, 1); cin[k][0] = mm[0]; cin[k][1] = mm[1]; }
    }
    if constexpr (sl >= S1A + 24 && sl < S1A + 42) {
      constexpr int r = sl - (S1A + 24), st = r / 6, k = (r % 6) >> 1, t = r & 1;
      if constexpr (st == 0) {
        const unsigned pu = __float_as_uint(se[k][t]);
        const auto s16 = __builtin_amdgcn_permlane16_swap(pu, pu, false, false);
        eg4[k][t][0] = __uint_as_float(s16[0]); eg4[k][t][1] = __uint_as_float(s16[1]);
      } else {
        const unsigned pu = __float_as_uint(eg4[k][t][st - 1]);
        const auto sw = __builtin_amdgcn_permlane32_swap(pu, pu, false, false);
        eg4[k][t][st - 1] = __uint_as_float(sw[0]); eg4[k][t][st + 1] = __uint_as_float(sw[1]);
      }
    }
    if constexpr (sl >= S1A + 42 && sl < S1A + 63) {
      constexpr int r = sl - (S1A + 42), sgm = 1 + r / 3, k = r % 3;
      cmac(eg4[k][sgm >> 2][sgm & 3], ca4[k][0], ca4[k][1], eg4[k][(sgm - 1) >> 2][(sgm - 1) & 3]);
    }
    if constexpr (sl == S1A + 63) {
      *(LAS float*)(lds0 + LDS_WTOT + (8 + w) * 192 + cla) = q2 ? eg4[2][1][3] : (q1 ? eg4[1][1][3] : eg4[0][1][3]);
    }
    if constexpr (sl >= S1SEL && sl < S1SEL + 6) {
      constexpr int r = sl - S1SEL, k = r >> 1, t = r & 1;
      const float lo = q1 ? eg4[k][t][0] : (t ? eg4[k][0][3] : 0.f), hi = q1 ? eg4[k][t][2] : eg4[k][t][1];
      spre[k][t] = q2 ? hi : lo; tie(spre[k][t]);
    }
    if constexpr (sl >= SCE && sl < SCE + 7) {
      constexpr int k = sl - SCE + 1;
      cmac(tw[k], c32[0], c32[1], tw[k - 1]);
      if constexpr (k == 7) {
        wpre = tw[6];
        __builtin_amdgcn_raw_buffer_store_b64((u32x2){__float_as_uint(tw[7]), tag}, rsW, (w == 7 && l16 < 768) ? (unsigned)FL_E + (unsigned)((cg * 16 + 8 + rt) * 384) + cla2 : 0x7ffffff0u, 0, 16);
      }
    }
    if constexpr (sl >= SCM && sl < SCM + 24) {
      constexpr int r = sl - SCM, k = r / 8, t = (r >> 2) & 1, j = r & 3;
      cmul(sv[k][t][j], cin[k][0], cin[k][1], tac[2 + k][t][j]);
    }
    if constexpr (sl == SFIN - 8) {
      const f32x2 a = CPAIR(cs48, 13), p0 = CPAIR(cs48, (w & 1) ? 10 : 5), p1 = CPAIR(cs48, (w & 2) ? 11 : 5), p2 = CPAIR(cs48, (w & 4) ? 12 : 5);
      c256[0] = a[0]; c256[1] = a[1]; cpw[0][0] = p0[0]; cpw[0][1] = p0[1]; cpw[1][0] = p1[0]; cpw[1][1] = p1[1]; cpw[2][0] = p2[0]; cpw[2][1] = p2[1];
    }
    if constexpr (sl >= SFIN - 6 && sl < SFIN - 3) {
      constexpr int k = sl - (SFIN - 6);
      unsigned lz = l16; tie(lz);
      const f32x2 a1 = CPAIR(csa + k * 2048, 0), pq = CPAIR(csa + k * 2048, 5 + (lz >> 8)), a16 = CPAIR(csa + k * 2048, 9);
      cc1[k][0] = a1[0]; cc1[k][1] = a1[1]; cin[k][0] = pq[0]; cin[k][1] = pq[1]; ca4[k][0] = a16[0]; ca4[k][1] = a16[1];
    }
    if constexpr (sl >= SFIN && sl < SFIN + 6) {
      constexpr int r = sl - SFIN + 1;
      float n = __uint_as_float(eg[r][0]); cmac(n, c256[0], c256[1], crt); crt = n;
    }
    if constexpr (sl == SFIN + 6) { float n; cmul(n, cpw[0][0], cpw[0][1], crt); crt = n; }
    if constexpr (sl == SFIN + 7) { float n; cmul(n, cpw[1][0], cpw[1][1], crt); crt = n; }
    if constexpr (sl == SFIN + 8) cmac(wpre, cpw[2][0], cpw[2][1], crt);
    if constexpr (sl == SFIN + 9) {
      unsigned lz = l16; tie(lz);
      const unsigned scr = lds0 + LDS_SCR + w * 3072, q = lz >> 8, li = (lz >> 4) & 15;
      fwa = scr + li * 32 + q * 8;
      fra = scr + (q >> 1) * 512 + (8 * (q & 1) + (li >> 2)) * 32 + (li & 3) * 8;
      fvo = (q >> 1) * HTB + ((q & 1) * 16 + li) * 16;
      tie(fwa); tie(fra); tie(fvo);
    }
    if constexpr (sl == SFIN + 10) {
      const unsigned pu = __float_as_uint(wpre);
      const auto s16 = __builtin_amdgcn_permlane16_swap(pu, pu, false, false);
      cz[0] = __uint_as_float(s16[0]); cz[1] = __uint_as_float(s16[1]);
    }
    if constexpr (sl == SFIN + 11) { const unsigned pu = __float_as_uint(cz[0]); const auto sw = __builtin_amdgcn_permlane32_swap(pu, pu, false, false); cz[0] = __uint_as_float(sw[0]); cz[2] = __uint_as_float(sw[1]); }
    if constexpr (sl == SFIN + 12) { const unsigned pu = __float_as_uint(cz[1]); const auto sw = __builtin_amdgcn_permlane32_swap(pu, pu, false, false); cz[1] = __uint_as_float(sw[0]); }
    if constexpr (sl >= SFIN + 13 && sl < SFIN + 16) { constexpr int k = (sl - (SFIN + 13)) == 1 ? 2 : ((sl - (SFIN + 13)) == 2 ? 1 : 0); float n; cmul(n, cin[k][0], cin[k][1], cz[k]); cz[k] = n; }
    if constexpr (sl >= SFIN + 16 && sl < SFIN + 19) { constexpr int k = (sl - (SFIN + 16)) == 1 ? 2 : ((sl - (SFIN + 16)) == 2 ? 1 : 0); cmac(spre[k][1], ca4[k][0], ca4[k][1], cz[k]); }
    if constexpr (sl == SFIN + 19) { asm volatile("v_add_f32 %0, %0, %1" : "+v"(spre[0][0]) : "v"(cz[0])); asm volatile("v_add_f32 %0, %0, %1" : "+v"(spre[1][0]) : "v"(cz[1])); asm volatile("v_add_f32 %0, %0, %1" : "+v"(spre[2][0]) : "v"(cz[2])); }
    if constexpr (sl >= SFIN + 20 && sl < SFIN + 44) {
      constexpr int r = sl - (SFIN + 20), j = r / 6, k = (r % 6) >> 1, t = (r & 1) ^ 1;
      cmac(sv[k][t][j], cc1[k][0], cc1[k][1], j == 0 ? spre[k][t] : sv[k][t][j > 0 ? j - 1 : 0]);
    }
    if constexpr (sl >= SFIN + 44 && sl < SFIN + 50) {
      constexpr int r = sl - (SFIN + 44), k = r >> 1, ph = r & 1;
      v_cvt(pkd[0], sv[k][ph][0], sv[k][ph][1]); v_cvt(pkd[1], sv[k][ph][2], sv[k][ph][3]);
      *(LAS u32x2*)(fwa + k * 1024 + ph * 512) = (u32x2){pkd[0], pkd[1]};
    }
    if constexpr (sl >= SFIN + 50 && sl < SFIN + 53) { constexpr int k = sl - (SFIN + 50); qs[k] = __builtin_bit_cast(f32x4, trfrag<128>(fra + k * 1024)); }
    if constexpr (sl >= SFIN + 55 && sl < SFIN + 58) {
      constexpr int k = sl - (SFIN + 55);
      st16_sc1(rsW, (k == 2 && cgl) ? 0x70000000u : fvo, htw + (k < 2 ? (unsigned)((18 + cgl) * 1024 + k * 512) : 22u * 1024u), __builtin_bit_cast(u32x4, qs[k]));
    }
    if constexpr (sl == SRES - 4) {
#pragma unroll
      for (int r = 0; r < 3; r++) { const f32x2 f = *(LAS f32x2*)(lds0 + LDS_ST + sx * 512 + ept[r] * 16 + 8); rst[r][0] = f[0]; rst[r][1] = f[1]; }
    }
    if constexpr (res_at(sl) >= 0) {
      constexpr int e = res_at(sl) / 5, ph = res_at(sl) % 5, r = e >> 1, h = e & 1;
      if constexpr (ph == 0) {
        asm volatile("v_lshlrev_b32 %0, 16, %1" : "=v"(res[r][2 * h]) : "v"(epv[2 * r][h])); asm volatile("v_and_b32 %0, 0xffff0000, %1" : "=v"(res[r][2 * h + 1]) : "v"(epv[2 * r][h]));
      }
      if constexpr (ph == 1) {
        asm volatile("v_add_f32 %0, %1, %2" : "=v"(hh[0]) : "v"(res[r][2 * h]), "v"(rst[r][0])); asm volatile("v_add_f32 %0, %1, %2" : "=v"(hh[1]) : "v"(res[r][2 * h + 1]), "v"(rst[r][0]));
        v_cvt(pkd[2], hh[0], hh[1]);
      }
      if constexpr (ph == 2) {
        asm volatile("v_lshlrev_b32 %0, 16, %1" : "=v"(hh[0]) : "v"(pkd[2])); asm volatile("v_and_b32 %0, 0xffff0000, %1" : "=v"(hh[1]) : "v"(pkd[2]));
        v_muli(hh[0], rst[r][1]); v_muli(hh[1], rst[r][1]); v_cvt(pkd[2], hh[0], hh[1]);
      }
      if constexpr (ph == 3) {
        asm volatile("v_lshlrev_b32 %0, 16, %1" : "=v"(hh[0]) : "v"(pkd[2])); asm volatile("v_and_b32 %0, 0xffff0000, %1" : "=v"(hh[1]) : "v"(pkd[2]));
        asm volatile("v_lshlrev_b32 %0, 16, %1" : "=v"(te) : "v"(epv[2 * r + 1][h])); asm volatile("v_and_b32 %0, 0xffff0000, %1" : "=v"(pp) : "v"(epv[2 * r + 1][h]));
        v_muli(hh[0], te); v_muli(hh[1], pp);
      }
      if constexpr (ph == 4) {
        v_cvt(pkd[2], hh[0], hh[1]);
        asm volatile("v_lshlrev_b32 %0, 16, %1" : "=v"(hh[0]) : "v"(pkd[2])); asm volatile("v_and_b32 %0, 0xffff0000, %1" : "=v"(hh[1]) : "v"(pkd[2]));
        asm volatile("v_add_f32 %0, %0, %1" : "+v"(res[r][2 * h]) : "v"(hh[0])); asm volatile("v_add_f32 %0, %0, %1" : "+v"(res[r][2 * h + 1]) : "v"(hh[1]));
      }
    }
    if constexpr (sl >= 2 * late_g(9) && sl < 2 * GEND) {
      constexpr int r = sl - 2 * late_g(9), db = r / 14, ph = r % 14;
      if constexpr (db < 8) {
        if constexpr (ph == 0) { unsigned a, c; v_cvt(a, ac2[db][0][0], ac2[db][0][1]); v_cvt(c, ac2[db][0][2], ac2[db][0][3]); pkd[0] = a; pkd[1] = c; }
        if constexpr (ph == 1) { unsigned a, c; v_cvt(a, ac2[db][1][0], ac2[db][1][1]); v_cvt(c, ac2[db][1][2], ac2[db][1][3]); st16_sc1(rsW, l16, pb + db * 1024, (u32x4){pkd[0], pkd[1], a, c}); }
      }
    }
  };

  unsigned fa[2], eb[2], a2b[3];
  s16x4 flo;
  auto trl = [&](unsigned a) { return __builtin_amdgcn_ds_read_tr16_b64_v4i16((LAS s16x4*)(a)); };
  auto join = [&](s16x4 a, s16x4 c) { return __builtin_bit_cast(bf16x8, __builtin_shufflevector(a, c, 0, 1, 2, 3, 4, 5, 6, 7)); };
  auto frag = [&](auto Gc, auto Pc) {
    constexpr int G = decltype(Gc)::value, part = decltype(Pc)::value;
    if constexpr (G < GT) {
      constexpr LF f = l_frag(G);
      constexpr int o = (f.ks % 3) * 8192 + (f.blk >> 2) * 4096;
      if constexpr (part == 0) flo = trl(ab[f.blk & 3] + o); else fr[G % FR] = join(flo, trl(ab[f.blk & 3] + o + 512));
    } else if constexpr (G < G2) {
      constexpr int ks = t_ks(G - GT), blk = t_blk(G - GT), uo = (ks / 6) * 30720, k6 = ks % 6;
      if constexpr (blk < 2) {
        constexpr int o = uo - (ks >= 12 ? 30720 : 0) + (k6 * 2 + blk) * 1024;
        if constexpr (part == 0) flo = trl(fa[0] + o); else fr[G % FR] = join(flo, trl(fa[1] + o));
      } else if constexpr (part == 1) fr[G % FR] = ldsfrag(eb[k6 & 1] + uo - (ks >= 12 ? 30720 : 0) + 12288 + (k6 >> 1) * 6144 + (blk - 2) * 2048);
    } else if constexpr (part == 1) {
      constexpr int U = (G - G2) / 18, g = (G - G2) % 18;
      fr[G % FR] = ldsfrag(p2_off(U, g) >= 65536 ? (p2_off(U, g) >= 131072 ? a2b[2] + (p2_off(U, g) - 131072) : a2b[1] + (p2_off(U, g) - 65536)) : a2b[0] + p2_off(U, g));
    }
  };
  auto addr_step = [&](auto Gc) {
    constexpr int G = decltype(Gc)::value;
    if constexpr (G < GT) {
      constexpr int u = l_frag(G).unit, pu = l_prev_unit(G), k = l_frag(G).blk & 3;
      if constexpr (u != pu) { ab[k] += (unsigned)(p1_off(u) - p1_off(pu)); tie(ab[k]); }
    }
    if constexpr (G == GT) {
      unsigned l_ = l16; tie(l_); const int tl = (int)(l_ >> 4) + 64 * w;
      const int lane = tl & 63, q = lane >> 4, li = lane & 15;
      const unsigned e0 = li * 128 + ((q ^ (li >> 1)) << 4);
      eb[0] = lds0 + p1_off(12) + e0; eb[1] = lds0 + p1_off(12) + (e0 ^ 64u); tie(eb[0]); tie(eb[1]);
    }
    if constexpr (G == GT + 36) { eb[0] += 30720; eb[1] += 30720; tie(eb[0]); tie(eb[1]); }
    if constexpr (G == GT + 78) { fa[0] += 30720; fa[1] += 30720; tie(fa[0]); tie(fa[1]); }
    if constexpr (G == G2) { a2b[0] = lds0 + l16; a2b[1] = lds0 + l16 + 65536; a2b[2] = lds0 + l16 + 131072; tie(a2b[0]); tie(a2b[1]); tie(a2b[2]); }
  };
  auto mma = [&](auto Gc, auto Tc) {
    constexpr int G = decltype(Gc)::value, t = decltype(Tc)::value;
    const bf16x8 a = fr[G % FR];
    if constexpr (G < GT) {
      constexpr LF f = l_frag(G);
      mfma<f.ks == 0>(acc[f.blk][t], a, xf[f.ks][t]);
    } else if constexpr (G < G2) {
      constexpr int ks = t_ks(G - GT), blk = t_blk(G - GT);
      if constexpr (blk < 2) mfma<ks == 0>(tac[blk][t], a, xf[ks][t]);
      else mfma<ks == 0>(tac[blk][t], xf[ks][t], a);
    } else if constexpr (G < late_g(0)) {
      constexpr int U = (G - G2) / 18, g = (G - G2) % 18, kk = g / 9, db = g % 9;
      mfma<(U == 0 && kk == 0)>(ac2[db][t], a, hf[U % HR][kk][t]);
    } else {
      constexpr int f = G - late_g(0), db = f / 7, kk = f % 7;
      mfma<false>(ac2[db][t], a, hl[kk][t]);
    }
  };

  auto sync = [&](auto Gc) {
    constexpr int G = decltype(Gc)::value;
    if constexpr (G < GT && G % 24 == (G < 24 ? 16 : SY1)) {
      constexpr int V = G / 24;
      constexpr int need = V + 1 < 12 ? (l_dbl(V + 1) ? (((V + 1) & 1) ? -1 : V + 2) : V + 1) : 12;
      constexpr int ng = need >= 0 ? S1.after(OP_G, need, G) : 63;
      constexpr int ng2 = V == 2 ? S1.after(OP_G, 4, G) : (V == 8 ? S1.after(OP_G, 11, G) : 63);
      constexpr int ns = V == 10 ? cmin(S1.after(OP_ST, 0, G), S1.after(OP_ST, 9, G), S1.after(OP_PO, 0, G)) : 63;
      waitv<cmin(ng, ng2, ns)>();
      if constexpr (V == 3 || V == 9) asm volatile("s_waitcnt lgkmcnt(0)" ::: "memory");
      if constexpr (V == 10) setflag(rsW, l16 == 0, flw + cgl * 4, tag);
      bar();
      if constexpr (V == 10) setflag(rsW, l16 == 0 && w == 1, (unsigned)FL_W + (unsigned)(sx * 32 + idx) * 4u, tag);
      SCHED0;
    }
    if constexpr (G == gt(0, 18)) { waitv<S1.after(OP_FL, 4, G)>(); chk_flag(IC<4>{}); SCHED0; }
    if constexpr (G == gl(2, 14)) {
      waitv<S1.after(OP_SG, 0, G)>();
      for (unsigned it = 0; it < (1u << 18); ++it) {
        tie(sgr);
        if (__all(sgr[1] == tag)) break;
        __builtin_amdgcn_s_sleep(1);
        asm volatile("global_load_dwordx2 %0, %1, %2 sc1" : "+v"(sgr) : "v"((l16 >> 1) & 248), "s"(ws + FL_S + (size_t)(rt * 256 + 32 * w) * 8) : "memory");
        waitv<0>();
      }
      tie(sgr);
      const float den = bhi(sgr[0]), rs = __builtin_amdgcn_rcpf(den);
      *(LAS f32x4*)(lds0 + LDS_ST + w * 512 + (l16 & 496)) = (f32x4){rs * -1.44269504089f, den * den, -blo(sgr[0]), rs};
      SCHED0;
    }
    if constexpr (G == gl(3, 1)) { waitv<S1.after(OP_G, 5, G)>(); bar(); SCHED0; }
    if constexpr (G == g2(7, 16)) {
        waitv<S1.after(OP_HL, 0, G)>();
        tie(hl[0][0]); tie(hl[0][1]); tie(hl[1][0]); tie(hl[1][1]);
        for (unsigned it = 0; it < (1u << 16); ++it) {
          unsigned m = 0;
#pragma unroll
          for (int n = 4; n < 14; n++) { tie(hl[n >> 1][n & 1]); const u32x4 v = __builtin_bit_cast(u32x4, hl[n >> 1][n & 1]); asm("v_max3_u32 %0, %0, %1, %2" : "+v"(m) : "v"(v[0]), "v"(v[1])); asm("v_max3_u32 %0, %0, %1, %2" : "+v"(m) : "v"(v[2]), "v"(v[3])); }
          if (__all(m != ~0u)) break;
          __builtin_amdgcn_s_sleep(4);
          sfor<4, 14>([&](auto Nc) { constexpr int n = decltype(Nc)::value;
            gld16o_io<((n >> 1) - 4) * 1024>(hl[n >> 1][n & 1], hb0 + (size_t)(n & 1) * HTB + 16384 + 4096, l16); });
          waitv<0>();
        }
#pragma unroll
        for (int n = 4; n < 14; n++) tie(hl[n >> 1][n & 1]);
        chk_flag(IC<3>{});
        SCHED0;
    }
    if constexpr (G == SRES / 2 - 2) { waitv<S1.after(OP_EP, 0, G)>();
#pragma unroll
      for (int n = 0; n < 6; n++) tie(epv[n]);
      SCHED0; }
    if constexpr (G == late_g(55)) {
      waitv<S1.after(OP_ST, 3, G)>();
      setflag(rsW, l16 == 0, (unsigned)FL_P + (unsigned)(((rt * 4 + cgl) * 8 + w) * 8 + sx) * 64u, tag);
      SCHED0;
    }
    if constexpr (G == GSYC) { asm volatile("s_waitcnt lgkmcnt(0)" ::: "memory"); bar(); SCHED0; }
    if constexpr (G == gt(0, SYT)) { waitv<S1.after(OP_G, 13, G)>(); bar(); SCHED0; }
    if constexpr (G == gt(1, SYT)) { waitv<cmin(S1.after(OP_G, 14, G), S1.after(OP_FL, 0, G))>(); bar(); chk_flag(IC<0>{}); SCHED0; }
    if constexpr (G == gt(2, 2)) { waitv<S1.after(OP_ST, 1, G)>(); setflag(rsW, l16 == 0, flw + 128 + cgl * 4, tag); SCHED0; }
    if constexpr (G == gt(2, 27)) {
      waitv<cmin(S1.after(OP_A, 0, G), S1.after(OP_B, 0, G))>(); htie(IC<0>{});
      bar(); SCHED0;
    }
    if constexpr (G >= G2 && (G - G2) % 18 == SY2 && (G - G2) / 18 < 11) {
      constexpr int U = (G - G2) / 18;
      constexpr int nab = cmin(S1.after(OP_A, U + 1, G), U + 1 < 8 ? S1.after(OP_B, U + 1, G) : 63);
      constexpr int nfl = U == 1 ? S1.after(OP_FL, 1, G) : (U == 3 ? cmin(S1.after(OP_ST, 2, G), S1.after(OP_EG, 0, G)) : (U == 5 ? S1.after(OP_FL, 2, G) : 63));
      static_assert(UFIN == 3, "");
      if constexpr (U == 0) asm volatile("s_waitcnt lgkmcnt(0)" ::: "memory");
      waitv<cmin(nab, nfl)>();
      if constexpr (U + 1 < 8) htie(IC<U + 1>{});
      if constexpr (U == UFIN) {
        for (unsigned it = 0; it < (1u << 18); ++it) {
          unsigned d = 0;
#pragma unroll
          for (int r = 0; r < 7; r++) { tie(eg[r]); d |= eg[r][1] ^ tag; }
          if (__all(d == 0)) break;
          __builtin_amdgcn_s_sleep(1);
          sfor<0, 7>([&](auto Rc) { gld8o<decltype(Rc)::value * 384, true>(eg[decltype(Rc)::value], ws + FL_E + (size_t)(cg * 16 + rt + 1) * 384, cla2); });
          waitv<0>();
        }
#pragma unroll
        for (int r = 0; r < 7; r++) tie(eg[r]);
        crt = __uint_as_float(eg[0][0]);
        setflag(rsW, l16 == 0, flw + 256 + cgl * 4, tag);
      }
      bar();
      if constexpr (U == 1) chk_flag(IC<1>{});
      if constexpr (U == 5) chk_flag(IC<2>{});
      if constexpr (U == 0) {
        const f32x2 c = CPAIR(cs48, 10);
        c32[0] = c[0]; c32[1] = c[1];
#pragma unroll
        for (int k = 0; k < 8; k++) tw[k] = *(LAS float*)(lds0 + LDS_WTOT + (w + 1 + k) * 192 + cla);
      }
      SCHED0;
    }
  };

  unsigned wm[3];
  {
    unsigned l_ = l16; tie(l_); const int tl = (int)(l_ >> 4) + 64 * w;
    const int lane = tl & 63, q = lane >> 4, li = lane & 15;
    unsigned ep; u32x4 xr0, xr1;
    issue_at(IC<-3>{});
    bar();
    gld4_sc1(ep, ws + FL_EPOCH, l16 & 0);
    const int row = b * 8 + w;
    gld16(xr0, x + (size_t)row * DM, l16);
    gld16(xr1, x + (size_t)row * DM, 1024u + (l16 & 112));
    {
      const int chn = 8 * (9 * sx + (lane < 16 ? 2 * cgl + (lane >> 3) : 8)) + (lane & 7);
      asm volatile("global_load_ushort %0, %1, %2" : "=v"(pl[0]) : "v"(chn * 2), "s"(lre) : "memory");
      asm volatile("global_load_ushort %0, %1, %2" : "=v"(pl[1]) : "v"(chn * 2), "s"(lim) : "memory");
      asm volatile("global_load_ushort %0, %1, %2" : "=v"(pl[2]) : "v"(chn * 2), "s"(logdt) : "memory");
    }
    SCHED0;
    issue_at(IC<-1>{});
    {
      const int n = tl, r = 72 * rt + n / 6, c6 = n % 6;
      const char* p0 = (const char*)((c6 & 1) ? wu : wg) + (size_t)r * (FF * 2) + (c6 < 4 ? cg * 256 + (c6 >> 1) * 128 : fcol);
      const int m = rt * 54 + (tl - 432 < 54 ? tl - 432 : 0), rb = m / 9;
      const char* p1 = (const char*)((rb & 8) ? bim : bre) + (size_t)(8 * (9 * sx + ((rb >> 4) < 2 ? 2 * cgl + (rb >> 4) : 8)) + (rb & 7)) * (DM * 2) + (m - 9 * rb) * 128;
      asm volatile("global_load_dword %0, %1, off" : "=v"(wm[0]) : "v"(tl < 432 ? p0 : p1) : "memory");
    }
    SCHED0;
    waitv<6>();
    tie(ep); tie(xr0); tie(xr1); tie(pl[0]); tie(pl[1]); tie(pl[2]);
    tag = (unsigned)__builtin_amdgcn_readfirstlane(ep) + 1u;
    STAMP(12);
    {
      float v[16];
      const bool ex = lane < 8;
#pragma unroll
      for (int k = 0; k < 4; k++) { v[2 * k] = blo(xr0[k]); v[2 * k + 1] = bhi(xr0[k]); v[8 + 2 * k] = ex ? blo(xr1[k]) : 0.f; v[9 + 2 * k] = ex ? bhi(xr1[k]) : 0.f; }
      float sm = 0.f;
#pragma unroll
      for (int k = 0; k < 16; k++) sm += v[k];
      sm = wave_sum(sm);
      const float mean = sm * (1.0f / DM);
      float qq = 0.f;
#pragma unroll
      for (int k = 0; k < 16; k++) { const float d = (k < 8 || ex) ? v[k] - mean : 0.f; qq += d * d; }
      qq = wave_sum(qq);
      const unsigned md = pk2(mean, sqrtf(rbf(rbf(qq * (1.0f / DM)) + 1e-5f)));
      const unsigned zo = (w == 1 && lane < 48) ? (unsigned)FL_E + (unsigned)((cg * 16 + rt) * 384 + lane * 8) : 0x7ffffff0u;
      __builtin_amdgcn_raw_buffer_store_b64((u32x2){lane == 63 ? md : 0u, tag}, rsW, lane == 63 ? (unsigned)FL_S + row * 8u : zo, 0, 16);
    }
    if (w == 0 && lane < 24) {
      const float lr = bf2f((u16)pl[0]), lm = bf2f((u16)pl[1]);
      const float dt = rbf(__builtin_amdgcn_exp2f(bf2f((u16)pl[2]) * 1.44269504089f));
      const float rev = __builtin_amdgcn_fractf(dt * lm * 0.15915494309f);
      const float er = __builtin_amdgcn_exp2f(dt * lr * 1.44269504089f), ar = er * __builtin_amdgcn_cosf(rev), ai = er * __builtin_amdgcn_sinf(rev);
      const float nr = ar - 1.0f, ni = ai, dn = lr * lr + lm * lm;
      const unsigned tre = lds0 + LDS_CST + (lane >> 3) * 2048 + (lane & 7) * 8;
      auto put = [&](int k, float pr, float pi) {
        if (k >= 1 && k <= 4) { *(LAS f32x2*)(tre + k * 128) = (f32x2){2.f * pr, -2.f * pi}; *(LAS f32x2*)(tre + k * 128 + 64) = (f32x2){-2.f * pr, -2.f * pi}; }
        else { *(LAS f32x2*)(tre + k * 128) = (f32x2){pr, pi}; *(LAS f32x2*)(tre + k * 128 + 64) = (f32x2){pr, -pi}; }
      };
      put(0, ar, ai); put(5, 1.f, 0.f);
      float cr = (nr * lr + ni * lm) / dn, cm_ = (ni * lr - nr * lm) / dn;
#pragma unroll
      for (int k = 1; k < 5; k++) { put(k, cr, cm_); const float t = cr * ar - cm_ * ai; cm_ = cr * ai + cm_ * ar; cr = t; }
      const float a2r = ar * ar - ai * ai, a2i = 2.f * ar * ai;
      const float a4r = a2r * a2r - a2i * a2i, a4i = 2.f * a2r * a2i;
      float pr = a4r * a4r - a4i * a4i, pi = 2.f * a4r * a4i;
      put(6, a4r, a4i); put(7, pr, pi); put(8, pr * a4r - pi * a4i, pr * a4i + pi * a4r);
#pragma unroll
      for (int k = 9; k < 14; k++) { const float t = pr * pr - pi * pi; pi = 2.f * pr * pi; pr = t; put(k, pr, pi); }
    }
    SCHED0;
    STAMP(13);
  }
  auto pack = [&]() {
    unsigned l_ = l16; tie(l_); const int tl = (int)(l_ >> 4) + 64 * w;
    const int lane = tl & 63, q = lane >> 4, li = lane & 15;
        const unsigned ra = lds0 + LDS_PK + (w < 6 ? w : 0) * 4096 + (4 * q + (li >> 2)) * 128 + (lane & 3) * 8;
    const int fa_ = pj < 16 ? 1 : 7, fb_ = (pj < 16 ? 9 * pj : 128 + pj) + sx * (KS2 * 9);
    const unsigned vo = widx < 162 ? l_ : 0x70000000u;
#pragma unroll
    for (int n = 0; n < 4; n++) {
      const u32x4 f = __builtin_bit_cast(u32x4, trfrag<2048>(ra + n * 32));
      const int d16 = 4 * pln + n, dt = (d16 * 57) >> 9, db = d16 - 9 * dt;
      st16_sc1(rsW, vo, (unsigned)WS_WT + (unsigned)(dt * (NKG * 9) + fb_ + fa_ * db) * 1024u, f);
    }
    asm volatile("s_waitcnt lgkmcnt(0)" ::: "memory");
  };
  auto pack_c = [&]() {
    unsigned l_ = l16; tie(l_); tie(cfr);
    const int dt = (cd16 * 57) >> 9, db = cd16 - 9 * dt;
    if (2 * cjj + (l_ >> 9) >= 9) cfr = (u32x4){0, 0, 0, 0};
    st16_sc1(rsW, widx < 180 ? l_ : 0x70000000u, (unsigned)WS_WT + (unsigned)((dt * NKG + sx * KS2) * 9 + 146 + cjj + 7 * db) * 1024u, cfr);
  };
  waitv<S1.after(OP_G, 0, 0)>(); xconv(IC<0>{}); xconv(IC<1>{});
  STAMP(14);
  bar();
  SCHED0;
  STAMP(1);
  frag(IC<0>{}, IC<0>{}); frag(IC<0>{}, IC<1>{}); frag(IC<1>{}, IC<0>{}); frag(IC<1>{}, IC<1>{});
  SCHED0;

  bf16x8 cpb[2];
  unsigned csp = 0, csq = 0, csr = 0;
  auto extra = [&](auto Gc) {
    constexpr int G = decltype(Gc)::value;
    if constexpr (xt_at(G) >= 0) { waitv<S1.after(OP_X, xt_at(G), G + 1)>(); xconv(IC<xt_at(G)>{}); SCHED0; }
    if constexpr (G == csx_g(0, 0) - 3 || G == csx_g(6, 0) - 3) {
      constexpr int cur = p1_off(l_unit0(G + PD));
      csp = (ab[0] ^ (unsigned)((w & 3) << 5)) + (unsigned)((w >> 2) * 4096 - cur); tie(csp);
      const u32x4 so = {0x3f803f80u, 0x3f803f80u, 0x3f803f80u, 0x3f803f80u};
      selA = __builtin_bit_cast(bf16x8, so); tie(selA);
      SCHED0;
    }
    if constexpr (G == csx_g(12, 0) - 3) {
      unsigned l_ = l16; tie(l_); const int tl = (int)(l_ >> 4) + 64 * w;
      const int lane = tl & 63, q = lane >> 4, li = lane & 15;
      const u32x4 so = {0x3f803f80u, 0x3f803f80u, 0x3f803f80u, 0x3f803f80u}, sz = {0, 0, 0, 0};
      selA = __builtin_bit_cast(bf16x8, so); selB = __builtin_bit_cast(bf16x8, w < 6 ? so : sz); tie(selA); tie(selB);
      csp = lds0 + p1_off(12) + 12288 + (w < 6 ? w : 0) * 1024 + (li & 7) * 128 + (2 * q + (li >> 3)) * 16;
      const unsigned f0 = lds0 + p1_off(12) + (unsigned)((((8 * q + (li >> 2)) ^ ((q & 1) << 2)) << 5) + 8 * (lane & 3));
      csq = f0 + w * 1024; csr = f0 + (w < 6 ? (w >> 1) * 30720 : 0) + (8 + (w & 1)) * 1024;
      fa[0] = f0; fa[1] = f0 ^ 128u;
      tie(csp); tie(csq); tie(csr); tie(fa[0]); tie(fa[1]);
      SCHED0;
    }
    if constexpr (csx_at(G) >= 0) {
      constexpr int v = csx_at(G) >> 2, n = csx_at(G) & 3;
      const bf16x8 cpc = cpb[G & 1];
      if constexpr (v < 12) mfma<(v % 6 == 0 && n == 0)>(csa0, cpc, selA);
      else if constexpr (v < 15) mfma<(v == 12 && n == 0)>(csa0, selA, cpc);
      else mfma<(v == 15 && n == 0)>(csa1, cpc, v == 16 ? selB : selA);
      SCHED0;
    }
    if constexpr (csx_at(G + 2) >= 0) {
      constexpr int v = csx_at(G + 2) >> 2, n = csx_at(G + 2) & 3;
      bf16x8& cpc = cpb[G & 1];
      if constexpr (v < 12) { unsigned a = csp + p1_off(v); cpc = trfrag<512>(a + n * 8192); }
      else if constexpr (v < 15) { unsigned a = csp + (v - 12) * 30720; cpc = ldsfrag(a + n * 6144); }
      else {
        const unsigned o = v == 15 ? csq + n * 30720 : csr + n * 2048;
        s16x4 a = __builtin_amdgcn_ds_read_tr16_b64_v4i16((LAS s16x4*)(o));
        s16x4 c = __builtin_amdgcn_ds_read_tr16_b64_v4i16((LAS s16x4*)(o ^ 128u));
        cpc = __builtin_bit_cast(bf16x8, __builtin_shufflevector(a, c, 0, 1, 2, 3, 4, 5, 6, 7));
      }
      SCHED0;
    }
  };
  auto cs_flush = [&](auto Gc) {
    constexpr int G = decltype(Gc)::value;
    if constexpr (G == CSF[0] || G == CSF[1]) {
      unsigned lz = l16; tie(lz);
      if ((lz & 240) == 0) *(LAS f32x4*)(lds0 + LDS_CS + (G == CSF[1] ? 512 : 0) + w * 64 + (lz >> 8) * 16) = csa0;
      SCHED0;
    }
    if constexpr (G == CSF[2]) {
      unsigned lz = l16; tie(lz); tie(csa0);
      const float rsum = csa0[0] + dpp_ror8(csa0[0]);
      if (lz < 128) *(LAS float*)(lds0 + LDS_CS + 1024 + w * 32 + (lz >> 2)) = rsum;
      if ((lz & 240) == 0) *(LAS f32x4*)(lds0 + LDS_CS + 1280 + w * 64 + (lz >> 8) * 16) = csa1;
      SCHED0;
    }
  };

  auto group = [&](auto Gc) {
    constexpr int G = decltype(Gc)::value;
    if constexpr (G == 144) { STAMP(2); }
    if constexpr (G == GT) { STAMP(3); }
    if constexpr (G == G2) { STAMP(4); STAMP(5); }
    if constexpr (G == g2(3, 0)) { STAMP(6); }
    if constexpr (G == g2(8, 0)) { STAMP(7); }
    if constexpr (G == GPK) { waitv<S1.after(OP_PK, 0, G)>(); pack(); SCHED0; }
    if constexpr (G == GPC) { waitv<S1.after(OP_PK, 1, G)>(); pack_c(); SCHED0; }
    if constexpr (G == gl(1, 4)) { tie(wm[0]); tie(cw); }
    cs_flush(Gc);
    sync(Gc);
    issue_at(IC<2 * G>{});
    extra(Gc);
    mma(Gc, IC<0>{}); SCHED0;
    if constexpr (G + PD < GEND) { addr_step(IC<G + PD>{}); frag(IC<G + PD>{}, IC<0>{}); SCHED0; }
    side(IC<2 * G>{}); SCHED0;
    if constexpr (G + PD < GEND) { frag(IC<G + PD>{}, IC<1>{}); SCHED0; }
    mma(Gc, IC<1>{}); SCHED0;
    side(IC<2 * G + 1>{}); SCHED0;
  };
  sfor<0, 13>([&](auto Bc) { sfor<0, 45>([&](auto Jc) { group(IC<decltype(Bc)::value * 45 + decltype(Jc)::value>{}); }); });
  STAMPC(27);
  STAMP(8);

  asm volatile("s_nop 7\n\ts_nop 1" : "+v"(ac2[8][0]), "+v"(ac2[8][1]));
  unsigned l_ = l16; tie(l_); const int tz = (int)(l_ >> 4) + 64 * w;
  const int lane = tz & 63, q = lane >> 4, li = lane & 15;
  st16_sc1(rsW, l16, pb + 8 * 1024, (u32x4){pk2(ac2[8][0][0], ac2[8][0][1]), pk2(ac2[8][0][2], ac2[8][0][3]), pk2(ac2[8][1][0], ac2[8][1][1]), pk2(ac2[8][1][2], ac2[8][1][3])});
  SCHED0;
  const char* ib = ws + (size_t)pobase + (size_t)w * 9216 + 4096;
  waitv<2>();
  chk_flag(IC<5>{});
  sfor<0, 8>([&](auto Dc) { gld16o_sc1<(decltype(Dc)::value - 4) * 1024>(pv[decltype(Dc)::value], ib, l16); });
  gld16o_sc1<0>(pv[8], ib + 4096, l16);
  bar();
  STAMP(9);
  {
    constexpr int LROW = 304, LSRC = 32 * LROW;
    for (unsigned it = 0; it < (1u << 16); ++it) {
      waitv<0>();
      unsigned m = 0;
#pragma unroll
      for (int db = 6; db < 9; db++) { tie(pv[db]); m = max(max(m, max(pv[db][0], pv[db][1])), max(pv[db][2], pv[db][3])); }
      if (__all(m != ~0u)) break;
      gld16o_io<2048>(pv[6], ib, l16); gld16o_io<3072>(pv[7], ib, l16); gld16o_io<0>(pv[8], ib + 4096, l16);
    }
    waitv<0>();
#pragma unroll
    for (int db = 0; db < 9; db++) {
      tie(pv[db]);
      *(LAS u32x2*)(lds0 + w * LSRC + li * LROW + db * 32 + q * 8) = (u32x2){pv[db][0], pv[db][1]};
      *(LAS u32x2*)(lds0 + w * LSRC + (16 + li) * LROW + db * 32 + q * 8) = (u32x2){pv[db][2], pv[db][3]};
    }
    asm volatile("s_waitcnt vmcnt(0) lgkmcnt(0)" ::: "memory");
    bar();
    STAMP(10);
#pragma unroll
    for (int r = 0; r < 3; r++) {
      const int e = tz + 512 * r;
      if (e < 1152) {
        const unsigned t = ept[r], d8 = epd[r];
        f32x4 o = {res[r][0], res[r][1], res[r][2], res[r][3]};
#pragma unroll
        for (int sr = 0; sr < 8; sr++) {
          const u32x2 p = *(LAS u32x2*)(lds0 + sr * LSRC + t * LROW + d8);
          o += (f32x4){blo(p[0]), bhi(p[0]), blo(p[1]), bhi(p[1])};
        }
        *(f32x4*)((char*)(out + (size_t)erow * DM + cgl * 144) + t * (DM * 4) + d8 * 2) = o;
      }
    }
    asm volatile(".Lk_code_to:");
    if (b == 0 && w == 0) {
#pragma unroll
      for (int k = 0; k < 4; k++) spin_flag(ws + FL_W + k * 256, l16 >> 2, tag);
      setflag(rsW, l16 == 0, (unsigned)FL_EPOCH, tag);
    }
    STAMP(11);
    STAMP_DUMP();
  }
}

extern "C" void kernel_launch(void* const* d_in, const int* in_sizes, int n_in, void* d_out, int out_size, void* d_ws, size_t ws_size,
                              hipStream_t stream) {
  const u16* x = (const u16*)d_in[0]; const u16* lre = (const u16*)d_in[1]; const u16* lim = (const u16*)d_in[2];
  const u16* bre = (const u16*)d_in[3]; const u16* bim = (const u16*)d_in[4]; const u16* cre = (const u16*)d_in[5]; const u16* cim = (const u16*)d_in[6];
  const u16* logdt = (const u16*)d_in[7]; const u16* dsk = (const u16*)d_in[8];
  const u16* wg = (const u16*)d_in[10]; const u16* wu = (const u16*)d_in[11]; const u16* wd = (const u16*)d_in[12];
  char* ws = (char*)d_ws;
  (void)hipFuncSetAttribute((const void*)k_main, hipFuncAttributeMaxDynamicSharedMemorySize, LDS_TOT);
  k_main<<<256, 512, LDS_TOT, stream>>>(x, lre, lim, bre, bim, cre, cim, logdt, dsk, wg, wu, wd, ws, (float*)d_out);
}
```

```cpp
#include <hip/hip_runtime.h>
#include <stdint.h>
#include <type_traits>

typedef unsigned short u16;
typedef unsigned long long u64;
typedef __bf16 bf16x8 __attribute__((ext_vector_type(8)));
typedef __bf16 bf16x2 __attribute__((ext_vector_type(2)));
typedef float f32x4 __attribute__((ext_vector_type(4)));
typedef float f32x2 __attribute__((ext_vector_type(2)));
typedef short s16x4 __attribute__((ext_vector_type(4)));
typedef unsigned u32x4 __attribute__((ext_vector_type(4)));
typedef unsigned u32x2 __attribute__((ext_vector_type(2)));
#define LAS __attribute__((address_space(3)))
#define GAS __attribute__((address_space(1)))
#define DI __device__ __forceinline__

constexpr int DM = 576, FF = 4608;
constexpr int KS2 = 23;
constexpr int NKG = 8 * KS2;
constexpr size_t FL_H = 0;
constexpr size_t FL_P = 40960;
constexpr size_t FL_W = 36864;
constexpr size_t FL_EPOCH = 38912;
constexpr size_t FL_E = 262144;
constexpr size_t FL_S = 180224;
constexpr size_t WS_HT = 524288;
constexpr size_t WS_WT = WS_HT + (size_t)128 * NKG * 1024;
constexpr size_t WS_PART = WS_WT + (size_t)4 * NKG * 9 * 1024;
constexpr size_t PART_BYTES = (size_t)8 * 4 * 8 * 8 * 9 * 1024;
constexpr size_t WS_END = WS_PART + PART_BYTES;

DI float bf2f(u16 v) { return __uint_as_float(((unsigned)v) << 16); }
DI float rbf(float f) { return (float)(__bf16)f; }
DI unsigned pk2(float lo, float hi) { f32x2 v = {lo, hi}; bf16x2 c = __builtin_convertvector(v, bf16x2); return __builtin_bit_cast(unsigned, c); }
DI float blo(unsigned u) { return __uint_as_float(u << 16); }
DI float bhi(unsigned u) { return __uint_as_float(u & 0xffff0000u); }

DI int kg_f0b(int s, int j) {
  const int a = 512 * s + 128 * ((j & 7) >> 1) + 64 * (j >> 3) + 32 * (j & 1), c = 4096 + 64 * s + 32 * (j & 1);
  return j < 16 ? a : c;
}

template <int N> using IC = std::integral_constant<int, N>;
template <int B, int E, class F> DI void sfor(F&& f) { if constexpr (B < E) { f(IC<B>{}); sfor<B + 1, E>(f); } }

template <int C, int L, bool SC1> DI void glds_c(const void* sbase, unsigned voff, unsigned ldsw) {
  unsigned t;
  if constexpr (SC1) asm volatile("s_add_u32 m0, %3, %5\n\tv_add_u32 %0, %4, %1\n\tglobal_load_lds_dwordx4 %0, %2 sc1" : "=&v"(t) : "v"(voff), "s"(sbase), "s"(ldsw), "i"(C), "i"(L) : "memory", "scc");
  else asm volatile("s_add_u32 m0, %3, %5\n\tv_add_u32 %0, %4, %1\n\tglobal_load_lds_dwordx4 %0, %2" : "=&v"(t) : "v"(voff), "s"(sbase), "s"(ldsw), "i"(C), "i"(L) : "memory", "scc");
}
template <int OFF, class T> DI void gld16o_sc1(T& dst, const void* sbase, unsigned voff) {
  asm volatile("global_load_dwordx4 %0, %1, %2 offset:%3 sc1" : "=v"(dst) : "v"(voff), "s"(sbase), "i"(OFF) : "memory");
}
template <int OFF, class T> DI void gld16o_io(T& dst, const void* sbase, unsigned voff) {
  asm volatile("global_load_dwordx4 %0, %1, %2 offset:%3 sc1" : "+v"(dst) : "v"(voff), "s"(sbase), "i"(OFF) : "memory");
}
template <int OFF, bool IO> DI void gld8o(u32x2& dst, const void* sbase, unsigned voff) {
  if constexpr (IO) asm volatile("global_load_dwordx2 %0, %1, %2 offset:%3 sc1" : "+v"(dst) : "v"(voff), "s"(sbase), "i"(OFF) : "memory");
  else asm volatile("global_load_dwordx2 %0, %1, %2 offset:%3 sc1" : "=v"(dst) : "v"(voff), "s"(sbase), "i"(OFF) : "memory");
}
template <int L> DI void glds_run(unsigned& voff, const void* sbase, unsigned step, unsigned ldsa) {
  asm volatile("s_add_u32 m0, %3, %4\n\ts_nop 0\n\tglobal_load_lds_dwordx4 %0, %1\n\tv_add_u32 %0, %2, %0" : "+v"(voff) : "s"(sbase), "s"(step), "s"(ldsa), "i"(L) : "memory", "scc");
}
template <class T> DI void gld16(T& dst, const void* sbase, unsigned voff) {
  asm volatile("global_load_dwordx4 %0, %1, %2" : "=v"(dst) : "v"(voff), "s"(sbase) : "memory");
}
DI void gld4_sc1(unsigned& dst, const void* sbase, unsigned voff) {
  asm volatile("global_load_dword %0, %1, %2 sc1" : "=v"(dst) : "v"(voff), "s"(sbase) : "memory");
}
DI void gld8_sc1(u32x2& dst, const void* sbase, unsigned voff) {
  asm volatile("global_load_dwordx2 %0, %1, %2 sc1" : "=v"(dst) : "v"(voff), "s"(sbase) : "memory");
}
DI void gld8(u32x2& dst, const void* sbase, unsigned voff) {
  asm volatile("global_load_dwordx2 %0, %1, %2" : "=v"(dst) : "v"(voff), "s"(sbase) : "memory");
}
template <int N> DI void waitv() { static_assert(N >= 0 && N < 64, ""); asm volatile("s_waitcnt vmcnt(%0)" ::"i"(N) : "memory"); }
#define SCHED0 __builtin_amdgcn_sched_barrier(0)
DI void bar() { asm volatile("" ::: "memory"); __builtin_amdgcn_s_barrier(); asm volatile("" ::: "memory"); }
template <class T> DI void tie(T& a) { asm volatile("" : "+v"(a)); }
template <int O2> DI bf16x8 trfrag(unsigned addr) {
  s16x4 a = __builtin_amdgcn_ds_read_tr16_b64_v4i16((LAS s16x4*)(addr));
  s16x4 b = __builtin_amdgcn_ds_read_tr16_b64_v4i16((LAS s16x4*)(addr + O2));
  return __builtin_bit_cast(bf16x8, __builtin_shufflevector(a, b, 0, 1, 2, 3, 4, 5, 6, 7));
}
DI bf16x8 ldsfrag(unsigned addr) { return *(LAS bf16x8*)(addr); }
template <bool FIRST> DI void mfma(f32x4& c, bf16x8 a, bf16x8 b) {
  if constexpr (FIRST) asm volatile("v_mfma_f32_16x16x32_bf16 %0, %1, %2, 0" : "=v"(c) : "v"(a), "v"(b));
  else asm volatile("v_mfma_f32_16x16x32_bf16 %0, %1, %2, %0" : "+v"(c) : "v"(a), "v"(b));
}
DI float dpp_ror8(float v) { return __int_as_float(__builtin_amdgcn_update_dpp(0, __float_as_int(v), 0x128, 0xf, 0xf, true)); }
DI void cmac(float& d, float P, float Q, float x) {
  asm volatile("v_fmac_f32 %0, %1, %3\n\tv_fmac_f32_dpp %0, %3, %2 row_ror:8 row_mask:0xf bank_mask:0xf" : "+v"(d) : "v"(P), "v"(Q), "v"(x));
}
DI void cmul(float& d, float P, float Q, float x) {
  asm volatile("v_mul_f32 %0, %1, %3\n\tv_fmac_f32_dpp %0, %3, %2 row_ror:8 row_mask:0xf bank_mask:0xf" : "=&v"(d) : "v"(P), "v"(Q), "v"(x));
}
DI void v_mul(float& d, float a, float b) { asm volatile("v_mul_f32 %0, %1, %2" : "=v"(d) : "v"(a), "v"(b)); }
DI void v_fmac(float& d, float a, float b) { asm volatile("v_fmac_f32 %0, %1, %2" : "+v"(d) : "v"(a), "v"(b)); }
DI void v_fmaa(float& d, float c) { asm volatile("v_fma_f32 %0, %0, %1, %1" : "+v"(d) : "v"(c)); }
DI void v_muli(float& d, float a) { asm volatile("v_mul_f32 %0, %0, %1" : "+v"(d) : "v"(a)); }
DI void v_exp2(float& d) { asm volatile("v_exp_f32 %0, %0" : "+v"(d)); }
DI void v_rcp(float& d) { asm volatile("v_rcp_f32 %0, %0" : "+v"(d)); }
DI void v_cvt(unsigned& d, float a, float b) { asm volatile("v_cvt_pk_bf16_f32 %0, %1, %2" : "=v"(d) : "v"(a), "v"(b)); }
DI float wave_sum(float v) {
#define DPPADD(c) v += __int_as_float(__builtin_amdgcn_update_dpp(0, __float_as_int(v), c, 0xf, 0xf, true))
  DPPADD(0x111); DPPADD(0x112); DPPADD(0x114); DPPADD(0x118);
  v += __int_as_float(__builtin_amdgcn_update_dpp(0, __float_as_int(v), 0x142, 0xa, 0xf, false));
  v += __int_as_float(__builtin_amdgcn_update_dpp(0, __float_as_int(v), 0x143, 0xc, 0xf, false));
  return __int_as_float(__builtin_amdgcn_readlane(__float_as_int(v), 63));
}
DI void dnop() { asm volatile("s_nop 1"); }
typedef __amdgpu_buffer_rsrc_t rsrc_t;
DI rsrc_t mkrsrc(const void* p, unsigned bytes) { return __builtin_amdgcn_make_buffer_rsrc((void*)p, 0, bytes, 0x00020000); }
DI unsigned vadd_s(unsigned s, unsigned v) { unsigned o; asm volatile("v_add_u32 %0, %1, %2" : "=v"(o) : "s"(s), "v"(v)); return o; }
DI void st8_sc1(rsrc_t r, unsigned voff, unsigned soff, u32x2 v) { __builtin_amdgcn_raw_buffer_store_b64(v, r, vadd_s(soff, voff), 0, 16); }
DI void st16_sc1(rsrc_t r, unsigned voff, unsigned soff, u32x4 v) { __builtin_amdgcn_raw_buffer_store_b128(v, r, vadd_s(soff, voff), 0, 16); }
DI u32x4 ones4() { u32x4 v; asm volatile("v_mov_b32 %0, -1\n\tv_mov_b32 %1, -1\n\tv_mov_b32 %2, -1\n\tv_mov_b32 %3, -1" : "=v"(v[0]), "=v"(v[1]), "=v"(v[2]), "=v"(v[3])); return v; }
DI void setflag(rsrc_t r, bool sel, unsigned off, unsigned tag) { __builtin_amdgcn_raw_buffer_store_b32(tag, r, sel ? off : 0x7ffffff0u, 0, 16); }
DI void spin_flag(const char* base, unsigned voff, unsigned tag) {
  for (unsigned it = 0; it < (1u << 18); ++it) {
    unsigned v = __hip_atomic_load((GAS unsigned*)(base + voff), __ATOMIC_RELAXED, __HIP_MEMORY_SCOPE_AGENT);
    if (__all(v == tag)) break;
    __builtin_amdgcn_s_sleep(1);
  }
  asm volatile("" ::: "memory");
}

constexpr int PD = 2, FR = PD + 1;
constexpr int SLOT1 = 24576;
constexpr int SLOT2 = 18432;
constexpr int LDS_TOT = 163840;
constexpr int LDS_SCR = 98304;
constexpr int LDS_PK = 122880;
constexpr int LDS_XS = 98304;
constexpr int LDS_ST = 147456;
constexpr int LDS_CS = 151552;
constexpr int LDS_CST = 153600;
constexpr int LDS_WTOT = LDS_CST + 6144;

constexpr int p1_off(int v) { return v < 8 ? (v % 4) * SLOT1 : (v < 10 ? (v - 4) * SLOT1 : (v < 12 ? (v - 10) * SLOT1 : 2 * SLOT1 + (v - 12) * 30720)); }
constexpr int p2_off(int u, int g) {
  const int k = u % 5;
  return k < 2 ? k * SLOT2 + g * 1024 : k == 2 ? (g < 12 ? 36864 + g * 1024 : 141312 + (g - 12) * 1024) : 49152 + (k - 3) * SLOT2 + g * 1024;
}

constexpr int GT = 288, G2 = 378, GEND = 585;
constexpr int gl(int v, int g) { return v * 24 + g; }
constexpr int gt(int u, int g) { return GT + u * 30 + g; }
constexpr int g2(int u, int g) { return G2 + u * 18 + g; }
constexpr int SY1 = 8, SYT = 2, SY2 = 4;
constexpr int late_g(int f) { return g2(8, f); }

constexpr int SCE = 2 * g2(0, SY2) + 3;
constexpr int SQC = 2 * (G2 + 13);
constexpr int SILU0[3] = {2 * (gl(4, 0) + 12), 2 * (gl(10, 0) + 12), SQC + 8};
constexpr int SILUN[3] = {32, 32, 8};
constexpr int silu_end(int s, int nel) { return SILU0[s] + 9 * (nel / 2) + 1; }
constexpr int S1R = 2 * (GT + 55);
constexpr int S1A = S1R + 12;
constexpr int S1SEL = 2 * g2(2, 0) + 2;
constexpr int SCM = S1SEL + 6;
constexpr int UFIN = 3;
constexpr int SFIN = 2 * g2(UFIN, SY2);
static_assert(S1A + 63 < 2 * g2(0, SY2) && silu_end(2, 8) < S1SEL && SCM + 24 < SFIN - 8, "");
constexpr int csx_n(int v) { return v == 16 ? 2 : 3; }
constexpr int csx_g(int v, int n) {
  const int base[17] = {gl(0, 4), gl(0, 19), gl(1, 12), gl(2, 11), gl(2, 16), gl(3, 3), gl(7, 16), gl(7, 19), gl(8, 1), gl(8, 12), gl(8, 18), gl(9, 0),
                        GT + 27, GT + 30, GT + 34, GT + 37, GT + 40};
  const int step = (v == 5 || v == 6 || v == 7 || v >= 12) ? 1 : 2;
  return base[v] + n * step;
}
constexpr int CSF[3] = {gl(3, SY1), gl(9, SY1), GT + 44};
constexpr int GPC = gl(8, 23);
constexpr int GPK = gl(6, 6);
constexpr int GSYC = GT + 46;
constexpr int xt_g(int h) {
  const int g[18] = {-1, -1, gl(0, 6), gl(0, 12), gl(0, 20), gl(1, 3), gl(1, 11), gl(1, 18), gl(2, 2), gl(2, 10), gl(2, 17), gl(3, 1), gl(3, 9), gl(3, 14), gl(3, 19), gl(4, 0), gl(4, 3), gl(4, 5)};
  return g[h];
}
constexpr int xd_g(int h) { return h < 3 ? -1 : (h < 5 ? h - 2 : xt_g(h - 3) + 3); }

constexpr int csx_at(int G) { for (int v = 0; v < 17; v++) for (int n = 0; n < csx_n(v); n++) if (csx_g(v, n) == G) return v * 4 + n; return -1; }
constexpr int xt_at(int G) { for (int c = 2; c < 18; c++) if (xt_g(c) == G) return c; return -1; }
constexpr int SRES = 2 * g2(9, 6);
constexpr int res_at(int sl) {
  if (sl < SRES || !(sl & 1)) return -1;
  int n = 0;
  for (int g = SRES / 2; g < GEND; g++) { const bool busy = (g - late_g(9)) % 7 == 0; if (g == sl / 2) return (busy || n >= 30) ? -1 : n; if (!busy) n++; }
  return -1;
}
constexpr int silu_at(int sl) { for (int k = 0; k < 3; k++) if (sl >= SILU0[k] && sl - SILU0[k] <= 9 * (SILUN[k] / 2) + 1) return k; return -1; }

enum { OP_G = 0, OP_X, OP_ST, OP_PO, OP_PH, OP_A, OP_B, OP_FL, OP_EP, OP_HL, OP_PL, OP_EG, OP_SG, OP_PK };
struct Op { int pos, kind, a, n; };
struct Sched {
  Op op[320]; int cnt;
  constexpr void add(int pos, int kind, int a, int n) {
    int i = cnt; while (i > 0 && op[i - 1].pos > pos) { op[i] = op[i - 1]; i--; }
    op[i] = Op{pos, kind, a, n}; cnt++;
  }
  constexpr void unit(int g, int v, int step) { for (int n = 0; n < (v < 12 ? 3 : 4); n++) add(2 * (g + n * step), OP_G, v, n); }
  constexpr Sched() : op{}, cnt(0) {
    for (int h = 0; h < 2; h++) for (int n = 0; n < 2; n++) add(-3, OP_X, h, n);
    for (int n = 0; n < 3; n++) add(-3, OP_G, 0, n);
    for (int n = 0; n < 6; n++) add(-2, OP_ST, 8, n);
    for (int n = 0; n < 2; n++) add(-1, OP_X, 2, n);
    for (int n = 0; n < 3; n++) add(-1, OP_G, 1, n);
    for (int n = 0; n < 1; n++) add(-1, OP_ST, 7, n);
    add(-1, OP_ST, 6, 0);
    for (int h = 3; h < 18; h++) for (int n = 0; n < 2; n++) add(2 * xd_g(h), OP_X, h, n);
    for (int n = 0; n < 4; n++) add(2 * gl(4, 16 + 2 * n), OP_PK, 0, n);
    add(2 * gl(7, 15), OP_PK, 1, 0);
    add(2 * GPC - 1, OP_ST, 9, 0);
    unit(gl(0, 4), 2, 1);
    for (int n = 0; n < 4; n++) add(2 * GPK - 1, OP_ST, 4, n);
    add(2 * gl(1, 5), OP_SG, 0, 0);
    unit(gl(0, 9), 3, 3); unit(gl(1, 9), 4, 3); unit(gl(2, 9), 5, 1); unit(gl(3, 10), 6, 3); unit(gl(4, 9), 7, 2);
    unit(gl(5, 10), 8, 4); unit(gl(6, 9), 9, 1); unit(gl(6, 13), 10, 2); unit(gl(6, 19), 11, 2); unit(gl(8, 10), 12, 3); unit(gl(9, 10), 13, 3); unit(gl(10, 10), 14, 3);
    for (int n = 0; n < 3; n++) add(2 * gl(4, 1 + 2 * n), OP_PH, 0, n);
    for (int s = 0; s < 2; s++) for (int h = 0; h < 2; h++) for (int t = 0; t < 2; t++) add((silu_end(s, 16 * (h + 1)) / 2) * 2 + 1, OP_ST, s, 2 * h + t);
    for (int t = 0; t < 2; t++) add((silu_end(2, 8) / 2) * 2 + 1, OP_ST, 2, t);
    add(2 * gl(11, 16), OP_FL, 4, 0);
    add(2 * gt(0, 6), OP_FL, 0, 0);
    for (int n = 0; n < 2; n++) { add(2 * gt(0, 20 + 4 * n), OP_A, 0, n); add(2 * gt(1, 8 + 4 * n), OP_A, 1, n); add(2 * gt(1, 16 + 4 * n), OP_A, 2, n); }
    for (int n = 0; n < 4; n++) { add(2 * gt(2, 3 + 2 * n), OP_B, 0, n); add(2 * gt(2, 11 + 2 * n), OP_B, 1, n); add(2 * gt(2, 19 + 3 * n), OP_B, 2, n); }
    add(2 * g2(0, 1), OP_FL, 1, 0);
    for (int u = 0; u < 9; u++) {
      const int U = u + 3;
      for (int n = 0; n < (U < 11 ? 2 : 1); n++) add(2 * g2(u, 5 + 2 * n), OP_A, U, n);
      if (U < 8) for (int n = 0; n < 4; n++) add(2 * g2(u, 9 + 2 * n), OP_B, U, n);
    }
    for (int n = 0; n < 7; n++) add(2 * (g2(UFIN - 1, 0) + (n < 4 ? n : 2 * n - 2)), OP_EG, 0, n);
    add(2 * g2(4, 6), OP_FL, 2, 0);
    for (int n = 0; n < 4; n++) add(2 * g2(5, 9 + 2 * n), OP_HL, 0, n);
    for (int n = 4; n < 14; n++) add(2 * (g2(6, 9) + (n - 4)), OP_HL, 0, n);
    for (int n = 0; n < 6; n++) add(2 * g2(7, 6 + n), OP_EP, 0, n);
    for (int db = 0; db < 8; db++) add(2 * (late_g(7 * db + 6) + 3) + 1, OP_ST, 3, db);
  }
  static constexpr bool droppable(const Op& o) { return (o.kind == OP_PH && o.n == 2) || (o.kind == OP_ST && (o.a == 4 || o.a == 9)) || (o.kind == OP_PK && o.a == 0); }
  constexpr int first_at(int pos) const { int i = 0; while (i < cnt && op[i].pos < pos) i++; return i; }
  constexpr int after(int kind, int a, int g) const {
    int last = -1;
    for (int i = 0; i < cnt; i++) if (op[i].kind == kind && op[i].a == a) last = i;
    int n = 0;
    for (int i = last + 1; i < cnt; i++) if (op[i].pos < 2 * g && !droppable(op[i])) n++;
    return n;
  }
};
constexpr Sched S1{};
constexpr int cmin(int a, int b) { return a < b ? a : b; }
constexpr int cmin(int a, int b, int c) { return cmin(cmin(a, b), c); }
constexpr bool l_dbl(int v) { return v == 4 || v == 5 || v == 6 || v == 7 || v >= 10; }
struct LF { int unit, ks, blk; };
constexpr LF l_frag(int G) {
  const int v = G / 24, g = G % 24;
  if (!l_dbl(v)) return LF{v, 3 * (v % 6) + (g >> 3), g & 7};
  const int v0 = v & ~1, r = (v & 1) * 24 + g, p = r / 12, k6 = (r % 12) / 2;
  return LF{v0 + k6 / 3, 3 * (v0 % 6) + k6, p + 4 * (r & 1)};
}
constexpr int l_prev_unit(int G) {
  for (int g = G - 1; g >= 0; g--) if ((l_frag(g).blk & 3) == (l_frag(G).blk & 3)) return l_frag(g).unit;
  return 0;
}
constexpr int l_unit0(int G) { for (int g = G - 1; g >= 0; g--) if ((l_frag(g).blk & 3) == 0) return l_frag(g).unit; return 0; }
struct Corr {
  int item[40];
  constexpr Corr(int pass) : item{} {
    const int g0 = gl(6 * pass + 3, SY1 + 2);
    bool done[16] = {};
    const int order[8] = {0, 4, 1, 5, 2, 6, 3, 7};
    for (int k = 0; k < 40; k++) {
      item[k] = -1;
      const int G = g0 + k;
      for (int o = 0; o < 16 && item[k] < 0; o++) {
        const int bk = order[o >> 1], it = bk * 2 + (o & 1);
        if (done[it]) continue;
        if (l_frag(G).blk == bk || l_frag(G - 1).blk == bk || l_frag(G - 2).blk == bk || l_frag(G + 1).blk == bk) continue;
        item[k] = it; done[it] = true;
      }
    }
  }
  constexpr int last() const { int l = 0; for (int k = 0; k < 40; k++) if (item[k] >= 0) l = k; return l; }
  constexpr int count() const { int c = 0; for (int k = 0; k < 40; k++) c += item[k] >= 0; return c; }
};
constexpr Corr CR{0};
static_assert(CR.count() == 16 && gl(3, SY1 + 2) + CR.last() < gl(4, 12), "");
constexpr int t_ks(int r) { return r < 54 ? r / 3 : (r - 54) / 2; }
constexpr int t_blk(int r) { return r < 54 ? 2 + r % 3 : (r - 54) & 1; }

__global__ __launch_bounds__(512, 2) void k_main(const u16* __restrict__ x, const u16* __restrict__ lre, const u16* __restrict__ lim,
                                                 const u16* __restrict__ bre, const u16* __restrict__ bim, const u16* __restrict__ cre,
                                                 const u16* __restrict__ cim, const u16* __restrict__ logdt, const u16* __restrict__ dsk,
                                                 const u16* __restrict__ wg, const u16* __restrict__ wu, const u16* __restrict__ wd,
                                                 char* __restrict__ ws, float* __restrict__ out) {
  extern __shared__ __attribute__((aligned(16))) char lds[];
  const int tid = threadIdx.x, w = __builtin_amdgcn_readfirstlane(tid >> 6);
  unsigned cw;
  {
    u64 pc; unsigned clen;
    asm volatile("s_getpc_b64 %0\n.Lk_code_from:\n\ts_mov_b32 %1, .Lk_code_to-.Lk_code_from" : "=s"(pc), "=s"(clen));
    const unsigned off = min((((blockIdx.x >> 3) * 8 + w) * 2 + (tid & 1)) * 128u, clen);
    asm volatile("global_load_dword %0, %1, %2" : "=v"(cw) : "v"(off), "s"(pc) : "memory");
  }
  const int b = blockIdx.x, sx = b & 7, idx = b >> 3;
  unsigned lds0 = (unsigned)(size_t)(LAS char*)lds;
  asm volatile("" : "+s"(lds0));
  const int rt = idx & 7, cgl = idx >> 3, cg = sx * 4 + cgl;
  const rsrc_t rsW = mkrsrc(ws, (unsigned)WS_END);
  const unsigned htw = (unsigned)WS_HT + (unsigned)((sx * 128 + rt * 16 + 2 * w) * KS2) * 1024u;
  constexpr unsigned HTB = KS2 * 1024;
  const unsigned flw = (unsigned)FL_H + (unsigned)((((rt * 8 + sx) * 4) * 8 + w) * 4) * 4u;
  const unsigned l16 = (tid & 63) * 16;
  unsigned tag = 0;
  unsigned ldsw = lds0 + w * 1024; asm volatile("" : "+s"(ldsw));

  f32x4 acc[8][2];
  f32x4 tac[5][2];
  f32x4 ac2[9][2];
  bf16x8 fr[FR];
  bf16x8 xf[18][2];
  constexpr int HR = 4;
  bf16x8 hf[HR][2][2];
  bf16x8 hl[7][2];
  u32x4 pv[9];
  unsigned flv[5] = {0, 0, 0, 0, 0};
  u32x2 epv[6];
  u32x2 eg[7];
  u32x4 alv = {0, 0, 0, 0};
  unsigned ept[3], epd[3];
  unsigned wvoff, ab[4], tvA = 0, tvF = 0;
  const char* wsrc = (const char*)((w >> 2) ? wu : wg) + (size_t)cg * 256;
  {
    const int lane = tid & 63, q = lane >> 4, li = lane & 15;
    const int rg = w & 3, r8 = lane >> 3, ch = lane & 7;
    const int g = ((lane >> 4) & 1) | ((rg & 1) << 1);
    wvoff = (unsigned)((8 * rg + r8) * (FF * 2) + (((ch >> 1) ^ g) << 5) + ((ch & 1) << 4));
    const int r4 = li >> 2, cc = lane & 3;
    const int gr = ((r4 >> 1) & 1) | ((q & 1) << 1);
    const unsigned a0 = (unsigned)((8 * q + r4) * 128 + (gr << 5) + 8 * cc);
#pragma unroll
    for (int k = 0; k < 4; k++) { ab[k] = lds0 + (a0 ^ (k << 5)); tie(ab[k]); }
    if (tid < 384) *(LAS float*)(lds0 + LDS_WTOT + tid * 4) = 0.f;
  }
  const char* xb = (const char*)x + (size_t)(rt * 256 + 32 * w) * (DM * 2);
  unsigned xvo, xvo1, xra, xra1;
  unsigned ldsx = lds0 + w * 6144; asm volatile("" : "+s"(ldsx));
  {
    const int lane = tid & 63, q = lane >> 4, li = lane & 15, r8 = lane >> 3;
    xvo = (unsigned)(r8 * (DM * 2) + (((lane & 7) ^ (r8 >> 1)) << 4));
    xra = lds0 + LDS_XS + w * 6144 + li * 128 + ((q ^ (li >> 1)) << 4);
    xvo1 = xvo ^ 64u; xra1 = xra ^ 64u;
    tie(xvo); tie(xra); tie(xvo1); tie(xra1);
  }
  u32x2 sgr;
  const unsigned pobase = (unsigned)WS_PART + (unsigned)((sx * 32 + rt * 4 + cgl) * 72) * 1024u;
  const unsigned pb = (unsigned)WS_PART + (unsigned)(((w * 32 + rt * 4 + cgl) * 8 + sx) * 9) * 1024u;
  const char* wtb = ws + WS_WT + (size_t)((sx * 4 + cgl) * KS2) * 9 * 1024 + w * 1024;
  const char* hb0 = ws + htw;
  const int erow = rt * 256 + 32 * sx;
  const int blk3 = w < 4 ? 2 * cgl + (w >> 1) : 8;
  const char* tB = (const char*)((w & 1) ? bim : bre) + (size_t)(8 * (9 * sx + blk3)) * (DM * 2);
  const size_t fcol = (size_t)(4096 + 64 * sx + 16 * cgl) * 2;
  auto fsrc = [&](int fp) { return (const char*)((fp & 1) ? wu : wg) + fcol + (size_t)(fp >> 1) * 32 * (FF * 2); };
  const char* tsF = nullptr; const char* tsA = nullptr; unsigned tsn = 0, tsu = 0, tdA = 0, tdn = 0;
  unsigned cla2 = 0;
  unsigned cla = 0;

  u32x4 cfr; unsigned pl[3], pkv = 0, pkl = 0;
  const int widx = w * 32 + idx;
  const int wi = widx < 162 ? widx : widx - 162, pj = (wi * 57) >> 9, pln = wi - 9 * pj;
  const int ci = widx < 180 ? widx : widx - 180, cd16 = (ci * 205) >> 10, cjj = ci - 5 * cd16;
  auto issue = [&](auto Ic) {
    constexpr Op o = S1.op[decltype(Ic)::value];
    if constexpr (o.kind == OP_G) {
      if constexpr (o.a < 12) {
        glds_c<(o.a / 6) * 128 + (3 * (o.a % 6) + o.n) * 32 * (FF * 2), p1_off(o.a) + o.n * 8192, false>(wsrc, wvoff, ldsw);
      } else {
        constexpr int U = o.a - 12;
        if constexpr (U == 0 && o.n == 0) {
          unsigned l_ = l16; tie(l_);
          const int lane = l_ >> 4, p = lane >> 1;
          tvF = (unsigned)((p ^ (((p >> 3) & 1) << 2)) * (FF * 2) + ((lane & 1) << 4));
          const unsigned vb = (unsigned)((lane >> 3) * (DM * 2) + (((lane & 7) ^ (4 * (w & 1) + (lane >> 4))) << 4));
          tvA = w < 6 ? vb : tvF; tie(tvA); tie(tvF);
          tsF = fsrc(w); tsA = w < 6 ? tB : tsF;
          tsn = w < 6 ? 128u : 32u * (FF * 2); tsu = w < 6 ? 128u : 128u * (FF * 2);
          tdA = ldsw + (w < 6 ? 12288 : 0); tdn = w < 6 ? 6144u : 2048u;
          asm volatile("" : "+s"(tsF), "+s"(tsA), "+s"(tsn), "+s"(tsu), "+s"(tdA), "+s"(tdn));
        }
        if constexpr (o.n < 3) {
          glds_run<p1_off(o.a)>(tvA, tsA, o.n < 2 ? tsn : tsu, tdA + o.n * tdn);
        } else glds_c<U * 192 * (FF * 2), p1_off(o.a), false>(tsF, tvF, ldsw);
      }
    } else if constexpr (o.kind == OP_X) {
      glds_c<((o.a & 1) * 16 + o.n * 8) * (DM * 2) + (o.a >> 1) * 128, LDS_XS + (o.a % 3) * 2048 + o.n * 1024, false>(xb, o.n ? xvo1 : xvo, ldsx);
    } else if constexpr (o.kind == OP_PK) {
      if constexpr (o.a == 0) {
        if constexpr (o.n == 0) {
          unsigned l_ = l16; tie(l_);
          pkv = (l_ >> 7) * (DM * 2) + (l_ & 112) + (unsigned)(kg_f0b(sx, pj) * (DM * 2) + pln * 128); tie(pkv);
          pkl = lds0 + (w < 6 ? LDS_PK + w * 4096 : LDS_TOT); asm volatile("" : "+s"(pkl));
        }
        glds_c<o.n * 8 * (DM * 2), o.n * 1024, false>(wd, pkv, pkl);
      } else {
        unsigned l_ = l16; tie(l_);
        const int lane = l_ >> 4, q = lane >> 4, li = lane & 15;
        const int cbl = 2 * cjj + (q >> 1);
        const char* cp = (const char*)((q & 1) ? cim : cre) + (unsigned)((16 * cd16 + li) * (DM * 2) + 16 * (9 * sx + (cbl < 9 ? cbl : 8)));
        asm volatile("global_load_dwordx4 %0, %1, off" : "=v"(cfr) : "v"(cp) : "memory");
      }
    } else if constexpr (o.kind == OP_SG) {
      gld8_sc1(sgr, ws + FL_S + (size_t)(rt * 256 + 32 * w) * 8, (l16 >> 1) & 248);
    } else if constexpr (o.kind == OP_PO) {
      st16_sc1(rsW, l16, pobase + (unsigned)(o.n * 8 + w) * 1024u, ones4());
    } else if constexpr (o.kind == OP_PH) {
      if constexpr (o.n < 2) st16_sc1(rsW, l16, htw + o.n * HTB + (unsigned)(18 + cgl) * 1024u, ones4());
      else {
        const unsigned f = (l16 & 512) ? 0u : ~0u;
        st16_sc1(rsW, cgl < 2 ? l16 : 0x70000000u, htw + (cgl & 1) * HTB + 22u * 1024u, (u32x4){f, f, f, f});
      }
    } else if constexpr (o.kind == OP_A) {
      constexpr int U = o.a;
      if constexpr (U % 5 == 2 && o.n == 1) glds_c<U * SLOT2 + 8192, 0, true>(wtb, l16, ldsw + (w >= 4 ? p2_off(U, 12) - 4096 : p2_off(U, 8)));
      else glds_c<U * SLOT2 + o.n * 8192, p2_off(U, 8 * o.n), true>(wtb, l16, ldsw);
      if constexpr (U < 11 && o.n == 1) { if (w < 2) glds_c<U * SLOT2 + 16384, p2_off(U, 16), true>(wtb, l16, ldsw); }
      if constexpr (U == 11) { if (w < 1) glds_c<U * SLOT2 + 8192, p2_off(U, 8), true>(wtb, l16, ldsw); }
    } else if constexpr (o.kind == OP_B) {
      constexpr int kg = 2 * o.a + (o.n >> 1);
      gld16o_sc1<(kg % 8 - 4) * 1024>(hf[o.a % HR][o.n >> 1][o.n & 1], hb0 + (size_t)(o.n & 1) * HTB + (kg / 8) * 8192 + 4096, l16);
    } else if constexpr (o.kind == OP_HL) {
      gld16o_sc1<((o.n >> 1) - 4) * 1024>(hl[o.n >> 1][o.n & 1], hb0 + (size_t)(o.n & 1) * HTB + 16384 + 4096, l16);
    } else if constexpr (o.kind == OP_PL) {
      gld16o_sc1<(o.n - 4) * 1024>(pv[o.n], ws + (size_t)pobase + (size_t)w * 9216 + 4096, l16);
    } else if constexpr (o.kind == OP_EG) {
      gld8o<o.n * 384, false>(eg[o.n], ws + FL_E + (size_t)(cg * 16 + rt + 1) * 384, cla2);
    } else if constexpr (o.kind == OP_FL) {
      unsigned lz = l16; tie(lz);
      if constexpr (o.a < 3) gld4_sc1(flv[o.a], ws + flw + 128 * o.a, (lz >> 2) & 12);
      else if constexpr (o.a == 3) gld4_sc1(flv[3], ws + FL_W + (size_t)(w * 32 + idx) * 4, lz & 0);
      else gld4_sc1(flv[4], ws + FL_W + (size_t)sx * 128, (lz >> 2) & 124);
    } else if constexpr (o.kind == OP_EP) {
      constexpr int r = o.n / 2;
      if constexpr (o.n % 2 == 0) {
        unsigned l_ = l16; tie(l_);
        const unsigned e = min((l_ >> 4) + 64u * w + 512u * r, 1151u), t = (e * 1821u) >> 16;
        ept[r] = t; epd[r] = (e - 36u * t) * 8u; tie(ept[r]); tie(epd[r]);
        gld8(epv[o.n], x + (size_t)erow * DM + cgl * 144, t * (DM * 2) + epd[r]);
      } else gld8(epv[o.n], dsk + cgl * 144, epd[r]);
    }
  };
  auto issue_at = [&](auto Pc) {
    constexpr int i0 = S1.first_at(decltype(Pc)::value), i1 = S1.first_at(decltype(Pc)::value + 1);
    sfor<i0, i1>([&](auto Ic) { issue(Ic); SCHED0; });
  };
  auto xconv = [&](auto Hc) {
    constexpr int h = decltype(Hc)::value, c = h >> 1, t = h & 1;
    xf[2 * c][t] = ldsfrag(xra + (h % 3) * 2048);
    xf[2 * c + 1][t] = ldsfrag(xra1 + (h % 3) * 2048);
  };
  auto htie = [&](auto Uc) { constexpr int U = decltype(Uc)::value; tie(hf[U % HR][0][0]); tie(hf[U % HR][0][1]); tie(hf[U % HR][1][0]); tie(hf[U % HR][1][1]); };
  auto chk_flag = [&](auto Ac) { constexpr int a = decltype(Ac)::value; tie(flv[a]);
    if (!__all(flv[a] == tag)) {
      unsigned lz = l16; tie(lz);
      if constexpr (a < 3) spin_flag(ws + flw + 128 * a, (lz >> 2) & 12, tag);
      else if constexpr (a == 3) spin_flag(ws + FL_W + (size_t)(w * 32 + idx) * 4, lz & 0, tag);
      else spin_flag(ws + FL_W + (size_t)sx * 128, (lz >> 2) & 124, tag); } };

  float te = 0.f, pp = 0.f, hh[2], sc1[2], sc2[2], sng[2];
  f32x4 csw[2];
  f32x4 csv, tst[2];
  f32x4 csa0, csa1, qs[8];
  bf16x8 selA, selB; unsigned cra = 0, crb = 0;
  unsigned hp[2][4];
  float cm[3][8], ca4[3][2], se[3][2], eg4[3][2][4], spre[3][2], sv[3][2][4], cc1[3][2], cin[3][2];
  float tw[8], c32[2], wpre = 0.f, crt = 0.f, cz[3], cpw[3][2], c256[2];
  float res[3][4], rst[3][2];
  unsigned fwa = 0, fra = 0, fvo = 0;
  unsigned cs48 = 0;
#define CPAIR(base, pr) (*(LAS f32x2*)((base) + (pr) * 128))
  unsigned csa = 0, pkd[4];
  bool q1 = false, q2 = false;
  auto side = [&](auto Sc) {
    constexpr int sl = decltype(Sc)::value;
    {
      if constexpr (silu_at(sl) >= 0) {
        constexpr int s = silu_at(sl), r = sl - SILU0[s], N = SILUN[s];
        constexpr int pr2 = r / 9, ph = r % 9;
        auto G = [&](auto Ec) -> float { constexpr int e = decltype(Ec)::value; if constexpr (s < 2) return acc[e >> 3][(e >> 2) & 1][e & 3]; else return tac[0][e >> 2][e & 3]; };
        auto Uv = [&](auto Ec) -> float { constexpr int e = decltype(Ec)::value; if constexpr (s < 2) return acc[4 + (e >> 3)][(e >> 2) & 1][e & 3]; else return tac[1][e >> 2][e & 3]; };
        constexpr int e0 = 2 * pr2, e1 = e0 + 1;
        if constexpr (ph == 0) { if constexpr (pr2 > 0) v_mul(hh[1], pp, te); if constexpr (e0 < N) v_mul(te, G(IC<e0 < N ? e0 : 0>{}), sc1[(e0 >> 2) & 1]); }
        if constexpr (ph == 1) {
          if constexpr (pr2 > 0) {
            constexpr int ep = e0 - 2, t = (ep >> 2) & 1;
            v_cvt(hp[t][s < 2 ? 2 * ((ep >> 3) & 1) + ((ep >> 1) & 1) : ((ep >> 1) & 1)], hh[0], hh[1]);
            if constexpr (s < 2 && (e0 & 15) == 0) {
#pragma unroll
              for (int tt = 0; tt < 2; tt++)
                st16_sc1(rsW, l16, htw + tt * HTB + (unsigned)(8 * s + 2 * cgl + (ep >> 4)) * 1024u, (u32x4){hp[tt][0], hp[tt][1], hp[tt][2], hp[tt][3]});
            }
            if constexpr (s == 2 && e0 == 8) {
#pragma unroll
              for (int tt = 0; tt < 2; tt++) st8_sc1(rsW, l16, htw + tt * HTB + (unsigned)((16 + (cgl >> 1)) * 1024 + (cgl & 1) * 8), (u32x2){hp[tt][0], hp[tt][1]});
            }
          }
        }
        if constexpr (e0 < N) {
          if constexpr (ph == 2 || ph == 6) v_exp2(te);
          if constexpr (ph == 3) { v_fmaa(te, sc2[(e0 >> 2) & 1]); v_mul(pp, G(IC<e0>{}), Uv(IC<e0>{})); }
          if constexpr (ph == 4 || ph == 8) v_rcp(te);
          if constexpr (ph == 5) { v_mul(hh[0], pp, te); v_mul(te, G(IC<e1>{}), sc1[(e0 >> 2) & 1]); }
          if constexpr (ph == 7) { v_fmaa(te, sc2[(e0 >> 2) & 1]); v_mul(pp, G(IC<e1>{}), Uv(IC<e1>{})); }
        }
      }
    }
    if constexpr (sl == SILU0[0] - 4 || sl == SILU0[1] - 4 || sl == SILU0[2] - 4) {
      unsigned lz = l16; tie(lz);
#pragma unroll
      for (int t = 0; t < 2; t++) { const f32x2 f = *(LAS f32x2*)(lds0 + LDS_ST + w * 512 + t * 256 + (lz & 240)); sc1[t] = f[0]; sc2[t] = f[1]; }
    }
    if constexpr (sl == 2 * gl(3, SY1) || sl == 2 * gl(9, SY1) || sl == SQC - 14) {
      unsigned lz = l16; tie(lz);
#pragma unroll
      for (int t = 0; t < 2; t++) sng[t] = *(LAS float*)(lds0 + LDS_ST + w * 512 + t * 256 + (lz & 240) + 8);
      cra = lds0 + LDS_CS + (lz >> 8) * 16; tie(cra);
    }
    {
      constexpr int pass = sl / 2 >= gl(6, 0) ? 1 : 0, k = sl / 2 - gl(6 * pass + 3, SY1 + 2), half = sl & 1;
      if constexpr (k >= 0 && k < 40) {
        constexpr int it = CR.item[k];
        if constexpr (it >= 0) { constexpr int bk = it >> 1, t = it & 1; float a0 = acc[bk][t][2 * half], a1 = acc[bk][t][2 * half + 1];
          v_fmac(a0, sng[t], csw[k & 1][2 * half]); v_fmac(a1, sng[t], csw[k & 1][2 * half + 1]); acc[bk][t][2 * half] = a0; acc[bk][t][2 * half + 1] = a1; }
      }
      if constexpr (k >= -2 && k < 38 && half == 1) {
        constexpr int nx = CR.item[k + 2];
        if constexpr (nx >= 0) csw[k & 1] = *(LAS f32x4*)(cra + pass * 512 + ((nx >> 1) >> 2) * 256 + ((nx >> 1) & 3) * 64);
      }
    }
    if constexpr (sl == SQC - 12) {
#pragma unroll
      for (int k = 0; k < 8; k++) qs[k] = *(LAS f32x4*)(cra + 1280 + k * 64);
    }
    if constexpr (sl >= SQC - 8 && sl < SQC) {
      constexpr int r = sl - (SQC - 8);
#pragma unroll
      for (int e = 3 * r; e < 3 * r + 3; e++) { const int k = 2 + e / 4, j = e & 3; float a = qs[k & 1][j]; asm volatile("v_add_f32 %0, %0, %1" : "+v"(a) : "v"(qs[k][j])); qs[k & 1][j] = a; }
      if constexpr (r == 7) { csv = qs[0]; csa0 = qs[1]; }
    }
    if constexpr (sl >= SQC && sl < SQC + 8) {
      constexpr int r = sl - SQC, mt = r >> 2, t = (r >> 1) & 1, half = r & 1;
      float a0 = tac[mt][t][2 * half], a1 = tac[mt][t][2 * half + 1];
      if constexpr (mt == 0) { v_fmac(a0, sng[t], csv[2 * half]); v_fmac(a1, sng[t], csv[2 * half + 1]); }
      else { v_fmac(a0, sng[t], csa0[2 * half]); v_fmac(a1, sng[t], csa0[2 * half + 1]); }
      tac[mt][t][2 * half] = a0; tac[mt][t][2 * half + 1] = a1;
    }
    if constexpr (sl == 2 * GSYC) {
      unsigned lz = l16; tie(lz);
      crb = lds0 + LDS_ST + w * 512 + (lz >> 8) * 64; tie(crb);
#pragma unroll
      for (int t = 0; t < 2; t++)
#pragma unroll
        for (int j = 0; j < 4; j++) tst[t][j] = *(LAS float*)(crb + t * 256 + j * 16 + 8);
#pragma unroll
      for (int k = 0; k < 3; k++) csv[k] = *(LAS float*)(lds0 + LDS_CS + 1024 + k * 64 + ((lz >> 2) & 60));
    }
    if constexpr (sl >= 2 * (GT + 48) && sl < 2 * (GT + 54)) {
      constexpr int r = sl / 2 - (GT + 48), k = (r + 1) % 3, t = r / 3, half = sl & 1;
      float a0 = tac[2 + k][t][2 * half], a1 = tac[2 + k][t][2 * half + 1];
      v_fmac(a0, tst[t][2 * half], csv[k]); v_fmac(a1, tst[t][2 * half + 1], csv[k]);
      if constexpr (half) dnop();
      tac[2 + k][t][2 * half] = a0; tac[2 + k][t][2 * half + 1] = a1;
    }
    if constexpr (sl == S1R - 2) {
#pragma unroll
      for (int t = 0; t < 2; t++)
#pragma unroll
        for (int j = 0; j < 4; j++) tst[t][j] = *(LAS float*)(crb + t * 256 + j * 16 + 12);
    }
    if constexpr (sl >= S1R && sl < S1R + 12) {
      constexpr int r = sl - S1R, k = r >> 2, t = (r >> 1) & 1, half = r & 1;
      float a0 = tac[2 + k][t][2 * half], a1 = tac[2 + k][t][2 * half + 1];
      v_muli(a0, tst[t][2 * half]); v_muli(a1, tst[t][2 * half + 1]);
      tac[2 + k][t][2 * half] = a0; tac[2 + k][t][2 * half + 1] = a1;
    }
    if constexpr (sl == S1A - 12) {
      unsigned l_ = l16; tie(l_); const int tl = (int)(l_ >> 4) + 64 * w;
      const int lane = tl & 63;
      csa = lds0 + LDS_CST + (lane & 15) * 8; cla = (unsigned)(lane < 48 ? lane : lane - 16) * 4u;
      cs48 = csa + (cla >> 6) * 2048; cla2 = cla * 2; tie(cla2);
      q1 = lane & 16; q2 = lane & 32;
    }
    if constexpr (sl == S1A - 6 || sl == S1A + 2 || sl == S1A + 10) {
      constexpr int k = (sl - (S1A - 6)) / 8;
#pragma unroll
      for (int i = 0; i < 4; i++) { const f32x2 m = CPAIR(csa + k * 2048, 1 + i); cm[k][2 * i] = m[0]; cm[k][2 * i + 1] = m[1]; }
    }
    if constexpr (sl >= S1A && sl < S1A + 24) {
      constexpr int r = sl - S1A, k = r / 8, j = (r % 8) >> 1, t = r & 1;
      if constexpr (j == 0) cmul(se[k][t], cm[k][6], cm[k][7], tac[2 + k][t][0]);
      else cmac(se[k][t], cm[k][2 * (3 - j)], cm[k][2 * (3 - j) + 1], tac[2 + k][t][j]);
    }
    if constexpr (sl == S1A + 36) {
#pragma unroll
      for (int k = 0; k < 3; k++) { const f32x2 a4 = CPAIR(csa + k * 2048, 6); ca4[k][0] = a4[0]; ca4[k][1] = a4[1]; }
    }
    if constexpr (sl == SCM - 4) {
#pragma unroll
      for (int k = 0; k < 3; k++) { const f32x2 mm = CPAIR(csa + k * 2048, 1); cin[k][0] = mm[0]; cin[k][1] = mm[1]; }
    }
    if constexpr (sl >= S1A + 24 && sl < S1A + 42) {
      constexpr int r = sl - (S1A + 24), st = r / 6, k = (r % 6) >> 1, t = r & 1;
      if constexpr (st == 0) {
        const unsigned pu = __float_as_uint(se[k][t]);
        const auto s16 = __builtin_amdgcn_permlane16_swap(pu, pu, false, false);
        eg4[k][t][0] = __uint_as_float(s16[0]); eg4[k][t][1] = __uint_as_float(s16[1]);
      } else {
        const unsigned pu = __float_as_uint(eg4[k][t][st - 1]);
        const auto sw = __builtin_amdgcn_permlane32_swap(pu, pu, false, false);
        eg4[k][t][st - 1] = __uint_as_float(sw[0]); eg4[k][t][st + 1] = __uint_as_float(sw[1]);
      }
    }
    if constexpr (sl >= S1A + 42 && sl < S1A + 63) {
      constexpr int r = sl - (S1A + 42), sgm = 1 + r / 3, k = r % 3;
      cmac(eg4[k][sgm >> 2][sgm & 3], ca4[k][0], ca4[k][1], eg4[k][(sgm - 1) >> 2][(sgm - 1) & 3]);
    }
    if constexpr (sl == S1A + 63) {
      *(LAS float*)(lds0 + LDS_WTOT + (8 + w) * 192 + cla) = q2 ? eg4[2][1][3] : (q1 ? eg4[1][1][3] : eg4[0][1][3]);
    }
    if constexpr (sl >= S1SEL && sl < S1SEL + 6) {
      constexpr int r = sl - S1SEL, k = r >> 1, t = r & 1;
      const float lo = q1 ? eg4[k][t][0] : (t ? eg4[k][0][3] : 0.f), hi = q1 ? eg4[k][t][2] : eg4[k][t][1];
      spre[k][t] = q2 ? hi : lo; tie(spre[k][t]);
    }
    if constexpr (sl >= SCE && sl < SCE + 7) {
      constexpr int k = sl - SCE + 1;
      cmac(tw[k], c32[0], c32[1], tw[k - 1]);
      if constexpr (k == 7) {
        wpre = tw[6];
        __builtin_amdgcn_raw_buffer_store_b64((u32x2){__float_as_uint(tw[7]), tag}, rsW, (w == 7 && l16 < 768) ? (unsigned)FL_E + (unsigned)((cg * 16 + 8 + rt) * 384) + cla2 : 0x7ffffff0u, 0, 16);
      }
    }
    if constexpr (sl >= SCM && sl < SCM + 24) {
      constexpr int r = sl - SCM, k = r / 8, t = (r >> 2) & 1, j = r & 3;
      cmul(sv[k][t][j], cin[k][0], cin[k][1], tac[2 + k][t][j]);
    }
    if constexpr (sl == SFIN - 8) {
      const f32x2 a = CPAIR(cs48, 13), p0 = CPAIR(cs48, (w & 1) ? 10 : 5), p1 = CPAIR(cs48, (w & 2) ? 11 : 5), p2 = CPAIR(cs48, (w & 4) ? 12 : 5);
      c256[0] = a[0]; c256[1] = a[1]; cpw[0][0] = p0[0]; cpw[0][1] = p0[1]; cpw[1][0] = p1[0]; cpw[1][1] = p1[1]; cpw[2][0] = p2[0]; cpw[2][1] = p2[1];
    }
    if constexpr (sl >= SFIN - 6 && sl < SFIN - 3) {
      constexpr int k = sl - (SFIN - 6);
      unsigned lz = l16; tie(lz);
      const f32x2 a1 = CPAIR(csa + k * 2048, 0), pq = CPAIR(csa + k * 2048, 5 + (lz >> 8)), a16 = CPAIR(csa + k * 2048, 9);
      cc1[k][0] = a1[0]; cc1[k][1] = a1[1]; cin[k][0] = pq[0]; cin[k][1] = pq[1]; ca4[k][0] = a16[0]; ca4[k][1] = a16[1];
    }
    if constexpr (sl >= SFIN && sl < SFIN + 6) {
      constexpr int r = sl - SFIN + 1;
      float n = __uint_as_float(eg[r][0]); cmac(n, c256[0], c256[1], crt); crt = n;
    }
    if constexpr (sl == SFIN + 6) { float n; cmul(n, cpw[0][0], cpw[0][1], crt); crt = n; }
    if constexpr (sl == SFIN + 7) { float n; cmul(n, cpw[1][0], cpw[1][1], crt); crt = n; }
    if constexpr (sl == SFIN + 8) cmac(wpre, cpw[2][0], cpw[2][1], crt);
    if constexpr (sl == SFIN + 9) {
      unsigned lz = l16; tie(lz);
      const unsigned scr = lds0 + LDS_SCR + w * 3072, q = lz >> 8, li = (lz >> 4) & 15;
      fwa = scr + li * 32 + q * 8;
      fra = scr + (q >> 1) * 512 + (8 * (q & 1) + (li >> 2)) * 32 + (li & 3) * 8;
      fvo = (q >> 1) * HTB + ((q & 1) * 16 + li) * 16;
      tie(fwa); tie(fra); tie(fvo);
    }
    if constexpr (sl == SFIN + 10) {
      const unsigned pu = __float_as_uint(wpre);
      const auto s16 = __builtin_amdgcn_permlane16_swap(pu, pu, false, false);
      cz[0] = __uint_as_float(s16[0]); cz[1] = __uint_as_float(s16[1]);
    }
    if constexpr (sl == SFIN + 11) { const unsigned pu = __float_as_uint(cz[0]); const auto sw = __builtin_amdgcn_permlane32_swap(pu, pu, false, false); cz[0] = __uint_as_float(sw[0]); cz[2] = __uint_as_float(sw[1]); }
    if constexpr (sl == SFIN + 12) { const unsigned pu = __float_as_uint(cz[1]); const auto sw = __builtin_amdgcn_permlane32_swap(pu, pu, false, false); cz[1] = __uint_as_float(sw[0]); }
    if constexpr (sl >= SFIN + 13 && sl < SFIN + 16) { constexpr int k = (sl - (SFIN + 13)) == 1 ? 2 : ((sl - (SFIN + 13)) == 2 ? 1 : 0); float n; cmul(n, cin[k][0], cin[k][1], cz[k]); cz[k] = n; }
    if constexpr (sl >= SFIN + 16 && sl < SFIN + 19) { constexpr int k = (sl - (SFIN + 16)) == 1 ? 2 : ((sl - (SFIN + 16)) == 2 ? 1 : 0); cmac(spre[k][1], ca4[k][0], ca4[k][1], cz[k]); }
    if constexpr (sl == SFIN + 19) { asm volatile("v_add_f32 %0, %0, %1" : "+v"(spre[0][0]) : "v"(cz[0])); asm volatile("v_add_f32 %0, %0, %1" : "+v"(spre[1][0]) : "v"(cz[1])); asm volatile("v_add_f32 %0, %0, %1" : "+v"(spre[2][0]) : "v"(cz[2])); }
    if constexpr (sl >= SFIN + 20 && sl < SFIN + 44) {
      constexpr int r = sl - (SFIN + 20), j = r / 6, k = (r % 6) >> 1, t = (r & 1) ^ 1;
      cmac(sv[k][t][j], cc1[k][0], cc1[k][1], j == 0 ? spre[k][t] : sv[k][t][j > 0 ? j - 1 : 0]);
    }
    if constexpr (sl >= SFIN + 44 && sl < SFIN + 50) {
      constexpr int r = sl - (SFIN + 44), k = r >> 1, ph = r & 1;
      v_cvt(pkd[0], sv[k][ph][0], sv[k][ph][1]); v_cvt(pkd[1], sv[k][ph][2], sv[k][ph][3]);
      *(LAS u32x2*)(fwa + k * 1024 + ph * 512) = (u32x2){pkd[0], pkd[1]};
    }
    if constexpr (sl >= SFIN + 50 && sl < SFIN + 53) { constexpr int k = sl - (SFIN + 50); qs[k] = __builtin_bit_cast(f32x4, trfrag<128>(fra + k * 1024)); }
    if constexpr (sl >= SFIN + 55 && sl < SFIN + 58) {
      constexpr int k = sl - (SFIN + 55);
      st16_sc1(rsW, (k == 2 && cgl) ? 0x70000000u : fvo, htw + (k < 2 ? (unsigned)((18 + cgl) * 1024 + k * 512) : 22u * 1024u), __builtin_bit_cast(u32x4, qs[k]));
    }
    if constexpr (sl == SRES - 4) {
#pragma unroll
      for (int r = 0; r < 3; r++) { const f32x2 f = *(LAS f32x2*)(lds0 + LDS_ST + sx * 512 + ept[r] * 16 + 8); rst[r][0] = f[0]; rst[r][1] = f[1]; }
    }
    if constexpr (res_at(sl) >= 0) {
      constexpr int e = res_at(sl) / 5, ph = res_at(sl) % 5, r = e >> 1, h = e & 1;
      if constexpr (ph == 0) {
        asm volatile("v_lshlrev_b32 %0, 16, %1" : "=v"(res[r][2 * h]) : "v"(epv[2 * r][h])); asm volatile("v_and_b32 %0, 0xffff0000, %1" : "=v"(res[r][2 * h + 1]) : "v"(epv[2 * r][h]));
      }
      if constexpr (ph == 1) {
        asm volatile("v_add_f32 %0, %1, %2" : "=v"(hh[0]) : "v"(res[r][2 * h]), "v"(rst[r][0])); asm volatile("v_add_f32 %0, %1, %2" : "=v"(hh[1]) : "v"(res[r][2 * h + 1]), "v"(rst[r][0]));
        v_cvt(pkd[2], hh[0], hh[1]);
      }
      if constexpr (ph == 2) {
        asm volatile("v_lshlrev_b32 %0, 16, %1" : "=v"(hh[0]) : "v"(pkd[2])); asm volatile("v_and_b32 %0, 0xffff0000, %1" : "=v"(hh[1]) : "v"(pkd[2]));
        v_muli(hh[0], rst[r][1]); v_muli(hh[1], rst[r][1]); v_cvt(pkd[2], hh[0], hh[1]);
      }
      if constexpr (ph == 3) {
        asm volatile("v_lshlrev_b32 %0, 16, %1" : "=v"(hh[0]) : "v"(pkd[2])); asm volatile("v_and_b32 %0, 0xffff0000, %1" : "=v"(hh[1]) : "v"(pkd[2]));
        asm volatile("v_lshlrev_b32 %0, 16, %1" : "=v"(te) : "v"(epv[2 * r + 1][h])); asm volatile("v_and_b32 %0, 0xffff0000, %1" : "=v"(pp) : "v"(epv[2 * r + 1][h]));
        v_muli(hh[0], te); v_muli(hh[1], pp);
      }
      if constexpr (ph == 4) {
        v_cvt(pkd[2], hh[0], hh[1]);
        asm volatile("v_lshlrev_b32 %0, 16, %1" : "=v"(hh[0]) : "v"(pkd[2])); asm volatile("v_and_b32 %0, 0xffff0000, %1" : "=v"(hh[1]) : "v"(pkd[2]));
        asm volatile("v_add_f32 %0, %0, %1" : "+v"(res[r][2 * h]) : "v"(hh[0])); asm volatile("v_add_f32 %0, %0, %1" : "+v"(res[r][2 * h + 1]) : "v"(hh[1]));
      }
    }
    if constexpr (sl >= 2 * late_g(9) && sl < 2 * GEND) {
      constexpr int r = sl - 2 * late_g(9), db = r / 14, ph = r % 14;
      if constexpr (db < 8) {
        if constexpr (ph == 0) { unsigned a, c; v_cvt(a, ac2[db][0][0], ac2[db][0][1]); v_cvt(c, ac2[db][0][2], ac2[db][0][3]); pkd[0] = a; pkd[1] = c; }
        if constexpr (ph == 1) { unsigned a, c; v_cvt(a, ac2[db][1][0], ac2[db][1][1]); v_cvt(c, ac2[db][1][2], ac2[db][1][3]); st16_sc1(rsW, l16, pb + db * 1024, (u32x4){pkd[0], pkd[1], a, c}); }
      }
    }
  };

  unsigned fa[2], eb[2], a2b[3];
  s16x4 flo;
  auto trl = [&](unsigned a) { return __builtin_amdgcn_ds_read_tr16_b64_v4i16((LAS s16x4*)(a)); };
  auto join = [&](s16x4 a, s16x4 c) { return __builtin_bit_cast(bf16x8, __builtin_shufflevector(a, c, 0, 1, 2, 3, 4, 5, 6, 7)); };
  auto frag = [&](auto Gc, auto Pc) {
    constexpr int G = decltype(Gc)::value, part = decltype(Pc)::value;
    if constexpr (G < GT) {
      constexpr LF f = l_frag(G);
      constexpr int o = (f.ks % 3) * 8192 + (f.blk >> 2) * 4096;
      if constexpr (part == 0) flo = trl(ab[f.blk & 3] + o); else fr[G % FR] = join(flo, trl(ab[f.blk & 3] + o + 512));
    } else if constexpr (G < G2) {
      constexpr int ks = t_ks(G - GT), blk = t_blk(G - GT), uo = (ks / 6) * 30720, k6 = ks % 6;
      if constexpr (blk < 2) {
        constexpr int o = uo - (ks >= 12 ? 30720 : 0) + (k6 * 2 + blk) * 1024;
        if constexpr (part == 0) flo = trl(fa[0] + o); else fr[G % FR] = join(flo, trl(fa[1] + o));
      } else if constexpr (part == 1) fr[G % FR] = ldsfrag(eb[k6 & 1] + uo - (ks >= 12 ? 30720 : 0) + 12288 + (k6 >> 1) * 6144 + (blk - 2) * 2048);
    } else if constexpr (part == 1) {
      constexpr int U = (G - G2) / 18, g = (G - G2) % 18;
      fr[G % FR] = ldsfrag(p2_off(U, g) >= 65536 ? (p2_off(U, g) >= 131072 ? a2b[2] + (p2_off(U, g) - 131072) : a2b[1] + (p2_off(U, g) - 65536)) : a2b[0] + p2_off(U, g));
    }
  };
  auto addr_step = [&](auto Gc) {
    constexpr int G = decltype(Gc)::value;
    if constexpr (G < GT) {
      constexpr int u = l_frag(G).unit, pu = l_prev_unit(G), k = l_frag(G).blk & 3;
      if constexpr (u != pu) { ab[k] += (unsigned)(p1_off(u) - p1_off(pu)); tie(ab[k]); }
    }
    if constexpr (G == GT) {
      unsigned l_ = l16; tie(l_); const int tl = (int)(l_ >> 4) + 64 * w;
      const int lane = tl & 63, q = lane >> 4, li = lane & 15;
      const unsigned e0 = li * 128 + ((q ^ (li >> 1)) << 4);
      eb[0] = lds0 + p1_off(12) + e0; eb[1] = lds0 + p1_off(12) + (e0 ^ 64u); tie(eb[0]); tie(eb[1]);
    }
    if constexpr (G == GT + 36) { eb[0] += 30720; eb[1] += 30720; tie(eb[0]); tie(eb[1]); }
    if constexpr (G == GT + 78) { fa[0] += 30720; fa[1] += 30720; tie(fa[0]); tie(fa[1]); }
    if constexpr (G == G2) { a2b[0] = lds0 + l16; a2b[1] = lds0 + l16 + 65536; a2b[2] = lds0 + l16 + 131072; tie(a2b[0]); tie(a2b[1]); tie(a2b[2]); }
  };
  auto mma = [&](auto Gc, auto Tc) {
    constexpr int G = decltype(Gc)::value, t = decltype(Tc)::value;
    const bf16x8 a = fr[G % FR];
    if constexpr (G < GT) {
      constexpr LF f = l_frag(G);
      mfma<f.ks == 0>(acc[f.blk][t], a, xf[f.ks][t]);
    } else if constexpr (G < G2) {
      constexpr int ks = t_ks(G - GT), blk = t_blk(G - GT);
      if constexpr (blk < 2) mfma<ks == 0>(tac[blk][t], a, xf[ks][t]);
      else mfma<ks == 0>(tac[blk][t], xf[ks][t], a);
    } else if constexpr (G < late_g(0)) {
      constexpr int U = (G - G2) / 18, g = (G - G2) % 18, kk = g / 9, db = g % 9;
      mfma<(U == 0 && kk == 0)>(ac2[db][t], a, hf[U % HR][kk][t]);
    } else {
      constexpr int f = G - late_g(0), db = f / 7, kk = f % 7;
      mfma<false>(ac2[db][t], a, hl[kk][t]);
    }
  };

  auto sync = [&](auto Gc) {
    constexpr int G = decltype(Gc)::value;
    if constexpr (G < GT && G % 24 == (G < 24 ? 16 : SY1)) {
      constexpr int V = G / 24;
      constexpr int need = V + 1 < 12 ? (l_dbl(V + 1) ? (((V + 1) & 1) ? -1 : V + 2) : V + 1) : 12;
      constexpr int ng = need >= 0 ? S1.after(OP_G, need, G) : 63;
      constexpr int ng2 = V == 2 ? S1.after(OP_G, 4, G) : (V == 8 ? S1.after(OP_G, 11, G) : 63);
      constexpr int ns = V == 10 ? cmin(S1.after(OP_ST, 0, G), S1.after(OP_ST, 9, G)) : 63;
      waitv<cmin(ng, ng2, ns)>();
      if constexpr (V == 3 || V == 9) asm volatile("s_waitcnt lgkmcnt(0)" ::: "memory");
      if constexpr (V == 10) setflag(rsW, l16 == 0, flw + cgl * 4, tag);
      bar();
      if constexpr (V == 10) setflag(rsW, l16 == 0 && w == 1, (unsigned)FL_W + (unsigned)(sx * 32 + idx) * 4u, tag);
      SCHED0;
    }
    if constexpr (G == gt(0, 18)) { waitv<S1.after(OP_FL, 4, G)>(); chk_flag(IC<4>{}); SCHED0; }
    if constexpr (G == gl(2, 14)) {
      waitv<S1.after(OP_SG, 0, G)>();
      for (unsigned it = 0; it < (1u << 18); ++it) {
        tie(sgr);
        if (__all(sgr[1] == tag)) break;
        __builtin_amdgcn_s_sleep(1);
        asm volatile("global_load_dwordx2 %0, %1, %2 sc1" : "+v"(sgr) : "v"((l16 >> 1) & 248), "s"(ws + FL_S + (size_t)(rt * 256 + 32 * w) * 8) : "memory");
        waitv<0>();
      }
      tie(sgr);
      const float den = bhi(sgr[0]), rs = __builtin_amdgcn_rcpf(den);
      *(LAS f32x4*)(lds0 + LDS_ST + w * 512 + (l16 & 496)) = (f32x4){rs * -1.44269504089f, den * den, -blo(sgr[0]), rs};
      SCHED0;
    }
    if constexpr (G == gl(3, 1)) { waitv<S1.after(OP_G, 5, G)>(); bar(); SCHED0; }
    if constexpr (G == g2(7, 16)) {
        waitv<S1.after(OP_HL, 0, G)>();
        tie(hl[0][0]); tie(hl[0][1]); tie(hl[1][0]); tie(hl[1][1]);
        for (unsigned it = 0; it < (1u << 16); ++it) {
          unsigned m = 0;
#pragma unroll
          for (int n = 4; n < 14; n++) { tie(hl[n >> 1][n & 1]); const u32x4 v = __builtin_bit_cast(u32x4, hl[n >> 1][n & 1]); asm("v_max3_u32 %0, %0, %1, %2" : "+v"(m) : "v"(v[0]), "v"(v[1])); asm("v_max3_u32 %0, %0, %1, %2" : "+v"(m) : "v"(v[2]), "v"(v[3])); }
          if (__all(m != ~0u)) break;
          __builtin_amdgcn_s_sleep(4);
          sfor<4, 14>([&](auto Nc) { constexpr int n = decltype(Nc)::value;
            gld16o_io<((n >> 1) - 4) * 1024>(hl[n >> 1][n & 1], hb0 + (size_t)(n & 1) * HTB + 16384 + 4096, l16); });
          waitv<0>();
        }
#pragma unroll
        for (int n = 4; n < 14; n++) tie(hl[n >> 1][n & 1]);
        SCHED0;
    }
    if constexpr (G == SRES / 2 - 2) { waitv<S1.after(OP_EP, 0, G)>();
#pragma unroll
      for (int n = 0; n < 6; n++) tie(epv[n]);
      SCHED0; }
    if constexpr (G == g2(5, 2)) { if (b == 0 && w == 0) gld16o_io<0>(alv, ws + FL_W, l16); SCHED0; }
    if constexpr (G == g2(8, SY2 + 1)) {
      if (b == 0 && w == 0) {
        for (unsigned it = 0; it < (1u << 18); ++it) {
          tie(alv);
          if (__all(((alv[0] ^ tag) | (alv[1] ^ tag) | (alv[2] ^ tag) | (alv[3] ^ tag)) == 0)) break;
          __builtin_amdgcn_s_sleep(1);
          gld16o_io<0>(alv, ws + FL_W, l16); waitv<0>();
        }
        setflag(rsW, l16 == 0, (unsigned)FL_EPOCH, tag);
      }
      SCHED0;
    }
    if constexpr (G == GSYC) { asm volatile("s_waitcnt lgkmcnt(0)" ::: "memory"); bar(); SCHED0; }
    if constexpr (G == gt(0, SYT)) { waitv<S1.after(OP_G, 13, G)>(); bar(); SCHED0; }
    if constexpr (G == gt(1, SYT)) { waitv<cmin(S1.after(OP_G, 14, G), S1.after(OP_FL, 0, G))>(); bar(); chk_flag(IC<0>{}); SCHED0; }
    if constexpr (G == gt(2, 2)) { waitv<S1.after(OP_ST, 1, G)>(); setflag(rsW, l16 == 0, flw + 128 + cgl * 4, tag); SCHED0; }
    if constexpr (G == gt(2, 27)) {
      waitv<cmin(S1.after(OP_A, 0, G), S1.after(OP_B, 0, G))>(); htie(IC<0>{});
      bar(); SCHED0;
    }
    if constexpr (G >= G2 && (G - G2) % 18 == SY2 && (G - G2) / 18 < 11) {
      constexpr int U = (G - G2) / 18;
      constexpr int nab = cmin(S1.after(OP_A, U + 1, G), U + 1 < 8 ? S1.after(OP_B, U + 1, G) : 63);
      constexpr int nfl = U == 1 ? S1.after(OP_FL, 1, G) : (U == 3 ? cmin(S1.after(OP_ST, 2, G), S1.after(OP_EG, 0, G)) : (U == 5 ? S1.after(OP_FL, 2, G) : 63));
      static_assert(UFIN == 3, "");
      if constexpr (U == 0) asm volatile("s_waitcnt lgkmcnt(0)" ::: "memory");
      waitv<cmin(nab, nfl)>();
      if constexpr (U + 1 < 8) htie(IC<U + 1>{});
      if constexpr (U == UFIN) {
        for (unsigned it = 0; it < (1u << 18); ++it) {
          unsigned d = 0;
#pragma unroll
          for (int r = 0; r < 7; r++) { tie(eg[r]); d |= eg[r][1] ^ tag; }
          if (__all(d == 0)) break;
          __builtin_amdgcn_s_sleep(1);
          sfor<0, 7>([&](auto Rc) { gld8o<decltype(Rc)::value * 384, true>(eg[decltype(Rc)::value], ws + FL_E + (size_t)(cg * 16 + rt + 1) * 384, cla2); });
          waitv<0>();
        }
#pragma unroll
        for (int r = 0; r < 7; r++) tie(eg[r]);
        crt = __uint_as_float(eg[0][0]);
        setflag(rsW, l16 == 0, flw + 256 + cgl * 4, tag);
      }
      bar();
      if constexpr (U == 1) chk_flag(IC<1>{});
      if constexpr (U == 5) chk_flag(IC<2>{});
      if constexpr (U == 0) {
        const f32x2 c = CPAIR(cs48, 10);
        c32[0] = c[0]; c32[1] = c[1];
#pragma unroll
        for (int k = 0; k < 8; k++) tw[k] = *(LAS float*)(lds0 + LDS_WTOT + (w + 1 + k) * 192 + cla);
      }
      SCHED0;
    }
  };

  unsigned wm[3];
  {
    unsigned l_ = l16; tie(l_); const int tl = (int)(l_ >> 4) + 64 * w;
    const int lane = tl & 63, q = lane >> 4, li = lane & 15;
    unsigned ep; u32x4 xr0, xr1;
    issue_at(IC<-3>{});
    bar();
    gld4_sc1(ep, ws + FL_EPOCH, l16 & 0);
    const int row = b * 8 + w;
    gld16(xr0, x + (size_t)row * DM, l16);
    gld16(xr1, x + (size_t)row * DM, 1024u + (l16 & 112));
    {
      const int chn = 8 * (9 * sx + (lane < 16 ? 2 * cgl + (lane >> 3) : 8)) + (lane & 7);
      asm volatile("global_load_ushort %0, %1, %2" : "=v"(pl[0]) : "v"(chn * 2), "s"(lre) : "memory");
      asm volatile("global_load_ushort %0, %1, %2" : "=v"(pl[1]) : "v"(chn * 2), "s"(lim) : "memory");
      asm volatile("global_load_ushort %0, %1, %2" : "=v"(pl[2]) : "v"(chn * 2), "s"(logdt) : "memory");
    }
    SCHED0;
    issue_at(IC<-1>{});
    {
      const int n = tl, r = 72 * rt + n / 6, c6 = n % 6;
      const char* p0 = (const char*)((c6 & 1) ? wu : wg) + (size_t)r * (FF * 2) + (c6 < 4 ? cg * 256 + (c6 >> 1) * 128 : fcol);
      const int m = rt * 54 + (tl - 432 < 54 ? tl - 432 : 0), rb = m / 9;
      const char* p1 = (const char*)((rb & 8) ? bim : bre) + (size_t)(8 * (9 * sx + ((rb >> 4) < 2 ? 2 * cgl + (rb >> 4) : 8)) + (rb & 7)) * (DM * 2) + (m - 9 * rb) * 128;
      asm volatile("global_load_dword %0, %1, off" : "=v"(wm[0]) : "v"(tl < 432 ? p0 : p1) : "memory");
    }
    SCHED0;
    waitv<6>();
    tie(ep); tie(xr0); tie(xr1); tie(pl[0]); tie(pl[1]); tie(pl[2]);
    tag = (unsigned)__builtin_amdgcn_readfirstlane(ep) + 1u;
    {
      float v[16];
      const bool ex = lane < 8;
#pragma unroll
      for (int k = 0; k < 4; k++) { v[2 * k] = blo(xr0[k]); v[2 * k + 1] = bhi(xr0[k]); v[8 + 2 * k] = ex ? blo(xr1[k]) : 0.f; v[9 + 2 * k] = ex ? bhi(xr1[k]) : 0.f; }
      float sm = 0.f;
#pragma unroll
      for (int k = 0; k < 16; k++) sm += v[k];
      sm = wave_sum(sm);
      const float mean = sm * (1.0f / DM);
      float qq = 0.f;
#pragma unroll
      for (int k = 0; k < 16; k++) { const float d = (k < 8 || ex) ? v[k] - mean : 0.f; qq += d * d; }
      qq = wave_sum(qq);
      const unsigned md = pk2(mean, sqrtf(rbf(rbf(qq * (1.0f / DM)) + 1e-5f)));
      const unsigned zo = (w == 1 && lane < 48) ? (unsigned)FL_E + (unsigned)((cg * 16 + rt) * 384 + lane * 8) : 0x7ffffff0u;
      __builtin_amdgcn_raw_buffer_store_b64((u32x2){lane == 63 ? md : 0u, tag}, rsW, lane == 63 ? (unsigned)FL_S + row * 8u : zo, 0, 16);
    }
    if (w == 0 && lane < 24) {
      const float lr = bf2f((u16)pl[0]), lm = bf2f((u16)pl[1]);
      const float dt = rbf(__builtin_amdgcn_exp2f(bf2f((u16)pl[2]) * 1.44269504089f));
      const float rev = __builtin_amdgcn_fractf(dt * lm * 0.15915494309f);
      const float er = __builtin_amdgcn_exp2f(dt * lr * 1.44269504089f), ar = er * __builtin_amdgcn_cosf(rev), ai = er * __builtin_amdgcn_sinf(rev);
      const float nr = ar - 1.0f, ni = ai, dn = lr * lr + lm * lm;
      const unsigned tre = lds0 + LDS_CST + (lane >> 3) * 2048 + (lane & 7) * 8;
      auto put = [&](int k, float pr, float pi) {
        if (k >= 1 && k <= 4) { *(LAS f32x2*)(tre + k * 128) = (f32x2){2.f * pr, -2.f * pi}; *(LAS f32x2*)(tre + k * 128 + 64) = (f32x2){-2.f * pr, -2.f * pi}; }
        else { *(LAS f32x2*)(tre + k * 128) = (f32x2){pr, pi}; *(LAS f32x2*)(tre + k * 128 + 64) = (f32x2){pr, -pi}; }
      };
      put(0, ar, ai); put(5, 1.f, 0.f);
      float cr = (nr * lr + ni * lm) / dn, cm_ = (ni * lr - nr * lm) / dn;
#pragma unroll
      for (int k = 1; k < 5; k++) { put(k, cr, cm_); const float t = cr * ar - cm_ * ai; cm_ = cr * ai + cm_ * ar; cr = t; }
      const float a2r = ar * ar - ai * ai, a2i = 2.f * ar * ai;
      const float a4r = a2r * a2r - a2i * a2i, a4i = 2.f * a2r * a2i;
      float pr = a4r * a4r - a4i * a4i, pi = 2.f * a4r * a4i;
      put(6, a4r, a4i); put(7, pr, pi); put(8, pr * a4r - pi * a4i, pr * a4i + pi * a4r);
#pragma unroll
      for (int k = 9; k < 14; k++) { const float t = pr * pr - pi * pi; pi = 2.f * pr * pi; pr = t; put(k, pr, pi); }
    }
    SCHED0;
  }
  auto pack = [&]() {
    unsigned l_ = l16; tie(l_); const int tl = (int)(l_ >> 4) + 64 * w;
    const int lane = tl & 63, q = lane >> 4, li = lane & 15;
        const unsigned ra = lds0 + LDS_PK + (w < 6 ? w : 0) * 4096 + (4 * q + (li >> 2)) * 128 + (lane & 3) * 8;
    const int fa_ = pj < 16 ? 1 : 7, fb_ = (pj < 16 ? 9 * pj : 128 + pj) + sx * (4 * KS2 * 9);
    const unsigned vo = widx < 162 ? l_ : 0x70000000u;
#pragma unroll
    for (int n = 0; n < 4; n++) {
      const u32x4 f = __builtin_bit_cast(u32x4, trfrag<2048>(ra + n * 32));
      const int d16 = 4 * pln + n, dt = (d16 * 57) >> 9, db = d16 - 9 * dt;
      st16_sc1(rsW, vo, (unsigned)WS_WT + (unsigned)(dt * (KS2 * 9) + fb_ + fa_ * db) * 1024u, f);
    }
    asm volatile("s_waitcnt lgkmcnt(0)" ::: "memory");
  };
  auto pack_c = [&]() {
    unsigned l_ = l16; tie(l_); tie(cfr);
    const int dt = (cd16 * 57) >> 9, db = cd16 - 9 * dt;
    if (2 * cjj + (l_ >> 9) >= 9) cfr = (u32x4){0, 0, 0, 0};
    st16_sc1(rsW, widx < 180 ? l_ : 0x70000000u, (unsigned)WS_WT + (unsigned)((sx * 4 + dt) * (KS2 * 9) + 146 + cjj + 7 * db) * 1024u, cfr);
  };
  waitv<S1.after(OP_G, 0, 0)>(); xconv(IC<0>{}); xconv(IC<1>{});
  bar();
  SCHED0;
  frag(IC<0>{}, IC<0>{}); frag(IC<0>{}, IC<1>{}); frag(IC<1>{}, IC<0>{}); frag(IC<1>{}, IC<1>{});
  SCHED0;

  bf16x8 cpb[2];
  unsigned csp = 0, csq = 0, csr = 0;
  auto extra = [&](auto Gc) {
    constexpr int G = decltype(Gc)::value;
    if constexpr (xt_at(G) >= 0) { waitv<S1.after(OP_X, xt_at(G), G + 1)>(); xconv(IC<xt_at(G)>{}); SCHED0; }
    if constexpr (G == csx_g(0, 0) - 3 || G == csx_g(6, 0) - 3) {
      constexpr int cur = p1_off(l_unit0(G + PD));
      csp = (ab[0] ^ (unsigned)((w & 3) << 5)) + (unsigned)((w >> 2) * 4096 - cur); tie(csp);
      const u32x4 so = {0x3f803f80u, 0x3f803f80u, 0x3f803f80u, 0x3f803f80u};
      selA = __builtin_bit_cast(bf16x8, so); tie(selA);
      SCHED0;
    }
    if constexpr (G == csx_g(12, 0) - 3) {
      unsigned l_ = l16; tie(l_); const int tl = (int)(l_ >> 4) + 64 * w;
      const int lane = tl & 63, q = lane >> 4, li = lane & 15;
      const u32x4 so = {0x3f803f80u, 0x3f803f80u, 0x3f803f80u, 0x3f803f80u}, sz = {0, 0, 0, 0};
      selA = __builtin_bit_cast(bf16x8, so); selB = __builtin_bit_cast(bf16x8, w < 6 ? so : sz); tie(selA); tie(selB);
      csp = lds0 + p1_off(12) + 12288 + (w < 6 ? w : 0) * 1024 + (li & 7) * 128 + (2 * q + (li >> 3)) * 16;
      const unsigned f0 = lds0 + p1_off(12) + (unsigned)((((8 * q + (li >> 2)) ^ ((q & 1) << 2)) << 5) + 8 * (lane & 3));
      csq = f0 + w * 1024; csr = f0 + (w < 6 ? (w >> 1) * 30720 : 0) + (8 + (w & 1)) * 1024;
      fa[0] = f0; fa[1] = f0 ^ 128u;
      tie(csp); tie(csq); tie(csr); tie(fa[0]); tie(fa[1]);
      SCHED0;
    }
    if constexpr (csx_at(G) >= 0) {
      constexpr int v = csx_at(G) >> 2, n = csx_at(G) & 3;
      const bf16x8 cpc = cpb[G & 1];
      if constexpr (v < 12) mfma<(v % 6 == 0 && n == 0)>(csa0, cpc, selA);
      else if constexpr (v < 15) mfma<(v == 12 && n == 0)>(csa0, selA, cpc);
      else mfma<(v == 15 && n == 0)>(csa1, cpc, v == 16 ? selB : selA);
      SCHED0;
    }
    if constexpr (csx_at(G + 2) >= 0) {
      constexpr int v = csx_at(G + 2) >> 2, n = csx_at(G + 2) & 3;
      bf16x8& cpc = cpb[G & 1];
      if constexpr (v < 12) { unsigned a = csp + p1_off(v); cpc = trfrag<512>(a + n * 8192); }
      else if constexpr (v < 15) { unsigned a = csp + (v - 12) * 30720; cpc = ldsfrag(a + n * 6144); }
      else {
        const unsigned o = v == 15 ? csq + n * 30720 : csr + n * 2048;
        s16x4 a = __builtin_amdgcn_ds_read_tr16_b64_v4i16((LAS s16x4*)(o));
        s16x4 c = __builtin_amdgcn_ds_read_tr16_b64_v4i16((LAS s16x4*)(o ^ 128u));
        cpc = __builtin_bit_cast(bf16x8, __builtin_shufflevector(a, c, 0, 1, 2, 3, 4, 5, 6, 7));
      }
      SCHED0;
    }
  };
  auto cs_flush = [&](auto Gc) {
    constexpr int G = decltype(Gc)::value;
    if constexpr (G == CSF[0] || G == CSF[1]) {
      unsigned lz = l16; tie(lz);
      if ((lz & 240) == 0) *(LAS f32x4*)(lds0 + LDS_CS + (G == CSF[1] ? 512 : 0) + w * 64 + (lz >> 8) * 16) = csa0;
      SCHED0;
    }
    if constexpr (G == CSF[2]) {
      unsigned lz = l16; tie(lz); tie(csa0);
      const float rsum = csa0[0] + dpp_ror8(csa0[0]);
      if (lz < 128) *(LAS float*)(lds0 + LDS_CS + 1024 + w * 32 + (lz >> 2)) = rsum;
      if ((lz & 240) == 0) *(LAS f32x4*)(lds0 + LDS_CS + 1280 + w * 64 + (lz >> 8) * 16) = csa1;
      SCHED0;
    }
  };

  auto group = [&](auto Gc) {
    constexpr int G = decltype(Gc)::value;
    if constexpr (G == GPK) { waitv<S1.after(OP_PK, 0, G)>(); pack(); SCHED0; }
    if constexpr (G == GPC) { waitv<S1.after(OP_PK, 1, G)>(); pack_c(); SCHED0; }
    if constexpr (G == gl(1, 4)) { tie(wm[0]); tie(cw); }
    cs_flush(Gc);
    sync(Gc);
    issue_at(IC<2 * G>{});
    extra(Gc);
    mma(Gc, IC<0>{}); SCHED0;
    if constexpr (G + PD < GEND) { addr_step(IC<G + PD>{}); frag(IC<G + PD>{}, IC<0>{}); SCHED0; }
    side(IC<2 * G>{}); SCHED0;
    if constexpr (G + PD < GEND) { frag(IC<G + PD>{}, IC<1>{}); SCHED0; }
    mma(Gc, IC<1>{}); SCHED0;
    side(IC<2 * G + 1>{}); SCHED0;
  };
  sfor<0, 13>([&](auto Bc) { sfor<0, 45>([&](auto Jc) { group(IC<decltype(Bc)::value * 45 + decltype(Jc)::value>{}); }); });

  asm volatile("s_nop 7\n\ts_nop 1" : "+v"(ac2[8][0]), "+v"(ac2[8][1]));
  unsigned l_ = l16; tie(l_); const int tz = (int)(l_ >> 4) + 64 * w;
  const int lane = tz & 63, q = lane >> 4, li = lane & 15;
  st16_sc1(rsW, l16, pb + 8 * 1024, (u32x4){pk2(ac2[8][0][0], ac2[8][0][1]), pk2(ac2[8][0][2], ac2[8][0][3]), pk2(ac2[8][1][0], ac2[8][1][1]), pk2(ac2[8][1][2], ac2[8][1][3])});
  SCHED0;
  const char* ib = ws + (size_t)pobase + (size_t)w * 9216 + 4096;
  waitv<0>();
  setflag(rsW, l16 == 0, (unsigned)FL_P + (unsigned)(((rt * 4 + cgl) * 8 + w) * 8 + sx) * 64u, tag);
  bar();
  {
    constexpr int LROW = 304, LSRC = 32 * LROW;
    spin_flag(ws + FL_P + (size_t)(((rt * 4 + cgl) * 8 + sx) * 8 + w) * 64, l16 & 0, tag);
    sfor<0, 8>([&](auto Dc) { gld16o_sc1<(decltype(Dc)::value - 4) * 1024>(pv[decltype(Dc)::value], ib, l16); });
    gld16o_sc1<0>(pv[8], ib + 4096, l16);
    waitv<0>();
#pragma unroll
    for (int db = 0; db < 9; db++) {
      tie(pv[db]);
      *(LAS u32x2*)(lds0 + w * LSRC + li * LROW + db * 32 + q * 8) = (u32x2){pv[db][0], pv[db][1]};
      *(LAS u32x2*)(lds0 + w * LSRC + (16 + li) * LROW + db * 32 + q * 8) = (u32x2){pv[db][2], pv[db][3]};
    }
    asm volatile("s_waitcnt vmcnt(0) lgkmcnt(0)" ::: "memory");
    bar();
#pragma unroll
    for (int r = 0; r < 3; r++) {
      const int e = tz + 512 * r;
      if (e < 1152) {
        const unsigned t = ept[r], d8 = epd[r];
        f32x4 o = {res[r][0], res[r][1], res[r][2], res[r][3]};
#pragma unroll
        for (int sr = 0; sr < 8; sr++) {
          const u32x2 p = *(LAS u32x2*)(lds0 + sr * LSRC + t * LROW + d8);
          o += (f32x4){blo(p[0]), bhi(p[0]), blo(p[1]), bhi(p[1])};
        }
        *(f32x4*)((char*)(out + (size_t)erow * DM + cgl * 144) + t * (DM * 4) + d8 * 2) = o;
      }
    }
    asm volatile(".Lk_code_to:");
  }
}

extern "C" void kernel_launch(void* const* d_in, const int* in_sizes, int n_in, void* d_out, int out_size, void* d_ws, size_t ws_size,
                              hipStream_t stream) {
  const u16* x = (const u16*)d_in[0]; const u16* lre = (const u16*)d_in[1]; const u16* lim = (const u16*)d_in[2];
  const u16* bre = (const u16*)d_in[3]; const u16* bim = (const u16*)d_in[4]; const u16* cre = (const u16*)d_in[5]; const u16* cim = (const u16*)d_in[6];
  const u16* logdt = (const u16*)d_in[7]; const u16* dsk = (const u16*)d_in[8];
  const u16* wg = (const u16*)d_in[10]; const u16* wu = (const u16*)d_in[11]; const u16* wd = (const u16*)d_in[12];
  char* ws = (char*)d_ws;
  (void)hipFuncSetAttribute((const void*)k_main, hipFuncAttributeMaxDynamicSharedMemorySize, LDS_TOT);
  k_main<<<256, 512, LDS_TOT, stream>>>(x, lre, lim, bre, bim, cre, cim, logdt, dsk, wg, wu, wd, ws, (float*)d_out);
}
```
